```python
import torch

def _softmax(x, axis=-1):
    return torch.softmax(x, dim=axis)

def _gelu(x):
    return torch.nn.functional.gelu(x, approximate='tanh')

def _silu(x):
    return torch.nn.functional.silu(x)

def _sigmoid(x):
    return torch.sigmoid(x)

def _layernorm(x, axis=-1, eps=1e-05):
    m = x.mean(dim=axis, keepdim=True)
    v = x.var(dim=axis, keepdim=True, unbiased=False)
    return (x - m) / torch.sqrt(v + eps)

def _scatter_add(tgt, idx, src, axis=0):
    return tgt.clone().index_add_(axis, idx, src)

def _segment_sum(src, seg, n):
    out = torch.zeros((n,) + src.shape[1:], dtype=src.dtype)
    return out.index_add_(0, seg, src)

def _pad(x, pw):
    flat = tuple((v for pair in reversed(pw) for v in pair))
    return torch.nn.functional.pad(x, flat)
kernel_name = 'adaLN_cond__memory_aug_recurrent_mem_tr__swiglu_ffn__prompt'

def setup_inputs():

    _w = lambda a: (lambda t: t.to(torch.bfloat16) if t.is_floating_point() else t)(torch.as_tensor(a))

    def _A_setup():
        B, S, Dm = (1, 2048, 576)
        H, Dh = (32, 128)
        M, seg = (8, 32)
        seg = min(seg, S)
        S = S // seg * seg
        return {'x': _w((lambda *s: torch.randn(*s, dtype=torch.bfloat16) * 0.05)(B, S, Dm)), 'mem0': _w((lambda *s: torch.randn(*s, dtype=torch.bfloat16) * 0.4)(B, M, Dm)), 'Wq': _w((lambda *s: torch.randn(*s, dtype=torch.bfloat16) * 3.2)(Dm, H, Dh) * Dm ** (-0.5)), 'Wk': _w((lambda *s: torch.randn(*s, dtype=torch.bfloat16) * 3.2)(Dm, H, Dh) * Dm ** (-0.5)), 'Wv': _w((lambda *s: torch.randn(*s, dtype=torch.bfloat16) * 0.4)(Dm, H, Dh) * Dm ** (-0.5)), 'Wo': _w((lambda *s: torch.randn(*s, dtype=torch.bfloat16) * 0.4)(H, Dh, Dm) * (H * Dh) ** (-0.5)), 'seg_len': seg}
    _A_in = _A_setup()

    def _B_setup():
        B, S, D, F = (1, 2048, 576, 4608)
        return {'x': _w((lambda *s: torch.randn(*s, dtype=torch.bfloat16) * 0.05)(B, S, D)), 'w_gate': _w((lambda *s: torch.randn(*s, dtype=torch.bfloat16) * 0.05)(D, F)), 'w_up': _w((lambda *s: torch.randn(*s, dtype=torch.bfloat16) * 0.05)(D, F)), 'w_down': _w((lambda *s: torch.randn(*s, dtype=torch.bfloat16) * 0.05)(F, D))}
    _B_in = _B_setup()
    return {'A_x': _A_in['x'], 'A_mem0': _A_in['mem0'], 'A_Wq': _A_in['Wq'], 'A_Wk': _A_in['Wk'], 'A_Wv': _A_in['Wv'], 'A_Wo': _A_in['Wo'], 'A_seg_len': _A_in['seg_len'], 'B_x': _B_in['x'], 'B_w_gate': _B_in['w_gate'], 'B_w_up': _B_in['w_up'], 'B_w_down': _B_in['w_down'], 'c': _w((lambda *s: torch.randn(*s, dtype=torch.bfloat16) * 0.1)(1, 576) * 8.0), 'w_mod': _w((lambda *s: torch.randn(*s, dtype=torch.bfloat16) * 0.1)(576, 6 * 576) * 576 ** (-0.5) * 8.0)}

def reference(A_x, A_mem0, A_Wq, A_Wk, A_Wv, A_Wo, A_seg_len, B_x, B_w_gate, B_w_up, B_w_down, c, w_mod):

    def _A_ref(x, mem0, Wq, Wk, Wv, Wo, seg_len):
        B, S, Dm = x.shape
        H, Dh = (Wq.shape[1], Wq.shape[2])
        M = mem0.shape[1]
        scale = Dh ** (-0.5)
        n_seg = S // seg_len
        L = M + seg_len
        idx = torch.arange(L)
        mask = (idx[:, None] < M) | (idx[:, None] >= idx[None, :])
        mem = mem0
        outs = []
        for s in range(n_seg):
            xs = x[:, s * seg_len:(s + 1) * seg_len, :].clone()
            h = torch.concatenate([mem, xs], axis=1)
            q = torch.einsum('btd,dhe->bhte', h, Wq)
            k = torch.einsum('btd,dhe->bhte', h, Wk)
            v = torch.einsum('btd,dhe->bhte', h, Wv)
            scores = torch.einsum('bhqe,bhke->bhqk', q, k) * scale
            scores = torch.where(mask[None, None], scores, -1000000000.0)
            p = _softmax(scores, axis=-1)
            o = torch.einsum('bhqk,bhke->bhqe', p, v)
            o = torch.einsum('bhte,hed->btd', o, Wo)
            mem = o[:, :M, :].clone()
            outs.append(o[:, M:, :])
        return torch.concatenate(outs, axis=1)

    def _B_ref(x, w_gate, w_up, w_down):
        g = _silu(x @ w_gate)
        u = x @ w_up
        return g * u @ w_down
    x = A_x
    mod = (_silu(c) @ w_mod).reshape(c.shape[0], 6, 1, -1)
    sa, ca, ga, sb, cb, gb = (mod[:, 0].clone(), mod[:, 1].clone(), mod[:, 2].clone(), mod[:, 3].clone(), mod[:, 4].clone(), mod[:, 5].clone())
    h = _layernorm(x, axis=-1) * (1 + ca) + sa
    x = x + ga * _A_ref(h, A_mem0, A_Wq, A_Wk, A_Wv, A_Wo, A_seg_len)
    h = _layernorm(x, axis=-1) * (1 + cb) + sb
    x = x + gb * _B_ref(h, B_w_gate, B_w_up, B_w_down)
    return x
```

```cpp
#include <hip/hip_runtime.h>
#include <hip/hip_bf16.h>
#include <stdint.h>

typedef unsigned short u16;
static __device__ __forceinline__ float b2f(u16 v){ return __uint_as_float(((unsigned)v)<<16); }
static __device__ __forceinline__ u16 f2b(float f){ __hip_bfloat16 b = __float2bfloat16(f); return *(u16*)&b; }
static __device__ __forceinline__ float rb(float f){ return b2f(f2b(f)); }

constexpr int S=2048, D=576, H=32, DH=128, HD=4096, M=8, SEG=32, NSEG=64, F=4608;

__global__ void k_mod(const u16* c, const u16* wmod, float* mod){
  __shared__ float sc[D];
  for(int i=threadIdx.x;i<D;i+=blockDim.x){ float v=b2f(c[i]); sc[i]=rb(v/(1.f+__expf(-v))); }
  __syncthreads();
  int j=blockIdx.x*blockDim.x+threadIdx.x; if(j>=6*D) return;
  float a=0; for(int k=0;k<D;k++) a+=sc[k]*b2f(wmod[(size_t)k*6*D+j]);
  mod[j]=rb(a);
}
template<bool INF32>
__global__ void k_ln(const void* xin, const float* shift, const float* scale, u16* h){
  int r=blockIdx.x; int t=threadIdx.x;
  float v[9]; float s=0;
  for(int i=0;i<9;i++){ int c=t+64*i; v[i]= INF32? ((const float*)xin)[(size_t)r*D+c] : b2f(((const u16*)xin)[(size_t)r*D+c]); s+=v[i]; }
  for(int o=32;o;o>>=1) s+=__shfl_xor(s,o);
  float m=s/D; float q=0; for(int i=0;i<9;i++){ float d=v[i]-m; q+=d*d; }
  for(int o=32;o;o>>=1) q+=__shfl_xor(q,o);
  float var=q/D; float rs=1.f/sqrtf(var+1e-5f);
  for(int i=0;i<9;i++){ int c=t+64*i; float n=(v[i]-m)*rs; h[(size_t)r*D+c]=f2b(n*(1.f+scale[c])+shift[c]); }
}
template<bool OUTF32>
__global__ void k_gemm(const u16* A,int lda,const u16* B,int ldb,void* C,int ldc,int K){
  __shared__ float sa[16][65]; __shared__ float sb[16][65];
  int tx=threadIdx.x%16, ty=threadIdx.x/16; int m0=blockIdx.y*64, n0=blockIdx.x*64;
  float acc[4][4]={};
  for(int k0=0;k0<K;k0+=16){
    for(int i=threadIdx.x;i<64*16;i+=256){ int r=i/16,k=i%16; sa[k][r]=b2f(A[(size_t)(m0+r)*lda+k0+k]); }
    for(int i=threadIdx.x;i<64*16;i+=256){ int k=i/64,n=i%64; sb[k][n]=b2f(B[(size_t)(k0+k)*ldb+n0+n]); }
    __syncthreads();
    for(int k=0;k<16;k++){ float a[4],b[4]; for(int i=0;i<4;i++){a[i]=sa[k][ty*4+i]; b[i]=sb[k][tx*4+i];}
      for(int i=0;i<4;i++) for(int j=0;j<4;j++) acc[i][j]+=a[i]*b[j]; }
    __syncthreads();
  }
  for(int i=0;i<4;i++) for(int j=0;j<4;j++){ size_t o=(size_t)(m0+ty*4+i)*ldc+n0+tx*4+j; if(OUTF32) ((float*)C)[o]=acc[i][j]; else ((u16*)C)[o]=f2b(acc[i][j]); }
}
__global__ void __launch_bounds__(128) k_chainA(const u16* mem, const u16* Wq,const u16* Wk,const u16* Wv,const u16* Kt,const u16* Vt,int s,u16* km,u16* vm,u16* o){
  int h=blockIdx.x; int t=threadIdx.x;
  __shared__ float sm[M][D]; __shared__ float q[M][DH],k[M+SEG][DH],v[M+SEG][DH],p[M][M+SEG];
  for(int i=t;i<M*D;i+=128) sm[i/D][i%D]=b2f(mem[i]);
  __syncthreads();
  float aq[M]={},ak[M]={},av[M]={};
  for(int d=0;d<D;d++){ float wq=b2f(Wq[(size_t)d*HD+h*DH+t]),wk=b2f(Wk[(size_t)d*HD+h*DH+t]),wv=b2f(Wv[(size_t)d*HD+h*DH+t]);
    for(int i=0;i<M;i++){ float x=sm[i][d]; aq[i]+=x*wq; ak[i]+=x*wk; av[i]+=x*wv; } }
  for(int i=0;i<M;i++){ q[i][t]=rb(aq[i]); k[i][t]=rb(ak[i]); v[i][t]=rb(av[i]);
    km[((size_t)(s*H+h)*M+i)*DH+t]=f2b(ak[i]); vm[((size_t)(s*H+h)*M+i)*DH+t]=f2b(av[i]); }
  for(int j=0;j<SEG;j++){ k[M+j][t]=b2f(Kt[(size_t)(s*SEG+j)*HD+h*DH+t]); v[M+j][t]=b2f(Vt[(size_t)(s*SEG+j)*HD+h*DH+t]); }
  __syncthreads();
  const float scale=0.08838834764831845f;
  for(int e=t;e<M*(M+SEG);e+=128){ int i=e/(M+SEG),j=e%(M+SEG); float a=0; for(int d=0;d<DH;d++) a+=q[i][d]*k[j][d]; p[i][j]=a*scale; }
  __syncthreads();
  if(t<M){ float mx=-1e30f; for(int j=0;j<M+SEG;j++) mx=fmaxf(mx,p[t][j]); float sum=0; for(int j=0;j<M+SEG;j++){ float e=__expf(p[t][j]-mx); p[t][j]=e; sum+=e; } for(int j=0;j<M+SEG;j++) p[t][j]=rb(p[t][j]/sum); }
  __syncthreads();
  for(int i=0;i<M;i++){ float a=0; for(int j=0;j<M+SEG;j++) a+=p[i][j]*v[j][t]; o[(size_t)i*HD+h*DH+t]=f2b(a); }
}
__global__ void k_chainB(const u16* o,const u16* Wo,u16* memn){
  int c=blockIdx.x*64+threadIdx.x%64; int i=blockIdx.y; int part=threadIdx.x/64;
  float a=0; for(int k=part*1024;k<part*1024+1024;k++) a+=b2f(o[(size_t)i*HD+k])*b2f(Wo[(size_t)k*D+c]);
  __shared__ float red[4][64]; red[part][threadIdx.x%64]=a; __syncthreads();
  if(part==0) memn[i*D+c]=f2b(red[0][threadIdx.x]+red[1][threadIdx.x]+red[2][threadIdx.x]+red[3][threadIdx.x]);
}
__global__ void __launch_bounds__(128) k_tokattn(const u16* Qt,const u16* Kt,const u16* Vt,const u16* km,const u16* vm,u16* O){
  int s=blockIdx.x/H,h=blockIdx.x%H; int t=threadIdx.x;
  __shared__ float q[SEG][DH+1],k[M+SEG][DH+1],v[M+SEG][DH],p[SEG][M+SEG];
  for(int j=0;j<SEG;j++){ q[j][t]=b2f(Qt[(size_t)(s*SEG+j)*HD+h*DH+t]); k[M+j][t]=b2f(Kt[(size_t)(s*SEG+j)*HD+h*DH+t]); v[M+j][t]=b2f(Vt[(size_t)(s*SEG+j)*HD+h*DH+t]); }
  for(int j=0;j<M;j++){ k[j][t]=b2f(km[((size_t)(s*H+h)*M+j)*DH+t]); v[j][t]=b2f(vm[((size_t)(s*H+h)*M+j)*DH+t]); }
  __syncthreads();
  const float scale=0.08838834764831845f;
  for(int e=t;e<SEG*(M+SEG);e+=128){ int i=e/(M+SEG),j=e%(M+SEG); float a=0; for(int d=0;d<DH;d++) a+=q[i][d]*k[j][d]; p[i][j]=(j<=i+M)? a*scale : -1e9f; }
  __syncthreads();
  if(t<SEG){ float mx=-1e30f; for(int j=0;j<M+SEG;j++) mx=fmaxf(mx,p[t][j]); float sum=0; for(int j=0;j<M+SEG;j++){ float e=__expf(p[t][j]-mx); p[t][j]=e; sum+=e; } for(int j=0;j<M+SEG;j++) p[t][j]=rb(p[t][j]/sum); }
  __syncthreads();
  for(int i=0;i<SEG;i++){ float a=0; for(int j=0;j<M+SEG;j++) a+=p[i][j]*v[j][t]; O[(size_t)(s*SEG+i)*HD+h*DH+t]=f2b(a); }
}
__global__ void k_res1(const u16* x,const float* y,const float* g,float* x1){ size_t i=(size_t)blockIdx.x*256+threadIdx.x; int c=i%D; x1[i]=b2f(x[i])+g[c]*y[i]; }
__global__ void k_act(const float* g,const float* u,u16* a){ size_t i=(size_t)blockIdx.x*256+threadIdx.x; float x=g[i]; a[i]=f2b(x/(1.f+__expf(-x))*u[i]); }
__global__ void k_res2(const float* x1,const float* y,const float* g,u16* out){ size_t i=(size_t)blockIdx.x*256+threadIdx.x; int c=i%D; out[i]=f2b(x1[i]+g[c]*y[i]); }

extern "C" void kernel_launch(void* const* d_in, const int* in_sizes, int n_in, void* d_out, int out_size, void* d_ws, size_t ws_size, hipStream_t stream){
  const u16* x=(const u16*)d_in[0]; const u16* mem0=(const u16*)d_in[1]; const u16* Wq=(const u16*)d_in[2]; const u16* Wk=(const u16*)d_in[3];
  const u16* Wv=(const u16*)d_in[4]; const u16* Wo=(const u16*)d_in[5]; const u16* wg=(const u16*)d_in[8]; const u16* wu=(const u16*)d_in[9]; const u16* wd=(const u16*)d_in[10];
  const u16* c=(const u16*)d_in[11]; const u16* wmod=(const u16*)d_in[12];
  char* w=(char*)d_ws; size_t off=0; auto al=[&](size_t n){ char* p=w+off; off+=(n+255)&~(size_t)255; return p; };
  float* mod=(float*)al(6*D*4); u16* h1=(u16*)al((size_t)S*D*2); u16* Qt=(u16*)al((size_t)S*HD*2); u16* Kt=(u16*)al((size_t)S*HD*2); u16* Vt=(u16*)al((size_t)S*HD*2);
  u16* mems=(u16*)al((size_t)(NSEG+1)*M*D*2); u16* km=(u16*)al((size_t)NSEG*H*M*DH*2); u16* vm=(u16*)al((size_t)NSEG*H*M*DH*2); u16* o=(u16*)al((size_t)M*HD*2);
  u16* O=(u16*)al((size_t)S*HD*2); float* y=(float*)al((size_t)S*D*4); float* x1=(float*)al((size_t)S*D*4); u16* h2=(u16*)al((size_t)S*D*2);
  float* g=(float*)al((size_t)S*F*4); float* u=(float*)al((size_t)S*F*4); u16* act=(u16*)al((size_t)S*F*2);
  k_mod<<<(6*D+255)/256,256,0,stream>>>(c,wmod,mod);
  k_ln<false><<<S,64,0,stream>>>(x,mod,mod+D,h1);
  k_gemm<false><<<dim3(HD/64,S/64),256,0,stream>>>(h1,D,Wq,HD,Qt,HD,D);
  k_gemm<false><<<dim3(HD/64,S/64),256,0,stream>>>(h1,D,Wk,HD,Kt,HD,D);
  k_gemm<false><<<dim3(HD/64,S/64),256,0,stream>>>(h1,D,Wv,HD,Vt,HD,D);
  hipMemcpyAsync(mems,mem0,M*D*2,hipMemcpyDeviceToDevice,stream);
  for(int s=0;s<NSEG;s++){
    k_chainA<<<H,128,0,stream>>>(mems+(size_t)s*M*D,Wq,Wk,Wv,Kt,Vt,s,km,vm,o);
    if(s<NSEG-1) k_chainB<<<dim3(D/64,M),256,0,stream>>>(o,Wo,mems+(size_t)(s+1)*M*D);
  }
  k_tokattn<<<NSEG*H,128,0,stream>>>(Qt,Kt,Vt,km,vm,O);
  k_gemm<true><<<dim3(D/64,S/64),256,0,stream>>>(O,HD,Wo,D,y,D,HD);
  k_res1<<<S*D/256,256,0,stream>>>(x,y,mod+2*D,x1);
  k_ln<true><<<S,64,0,stream>>>(x1,mod+3*D,mod+4*D,h2);
  k_gemm<true><<<dim3(F/64,S/64),256,0,stream>>>(h2,D,wg,F,g,F,D);
  k_gemm<true><<<dim3(F/64,S/64),256,0,stream>>>(h2,D,wu,F,u,F,D);
  k_act<<<S*F/256,256,0,stream>>>(g,u,act);
  k_gemm<true><<<dim3(D/64,S/64),256,0,stream>>>(act,F,wd,D,y,D,F);
  k_res2<<<S*D/256,256,0,stream>>>(x1,y,mod+5*D,(u16*)d_out);
}
```

```cpp
#include <hip/hip_runtime.h>
#include <stdint.h>
#include <utility>
typedef unsigned short u16;
typedef unsigned v4u __attribute__((ext_vector_type(4)));
typedef unsigned v2u __attribute__((ext_vector_type(2)));
typedef float f4 __attribute__((ext_vector_type(4)));
typedef float f2 __attribute__((ext_vector_type(2)));
typedef float f16v __attribute__((ext_vector_type(16)));
typedef __bf16 bf8 __attribute__((ext_vector_type(8)));
typedef __bf16 bf2 __attribute__((ext_vector_type(2)));
typedef short s4 __attribute__((ext_vector_type(4)));
typedef __amdgpu_buffer_rsrc_t rsrc_t;
#define DEVI static __device__ __forceinline__
#define LDSP __attribute__((address_space(3)))

constexpr int D_=576, HD_=4096, F_=4608, NSEG=64;
constexpr int FL_K=0, FL_V=8192, FL_TMO=16384, FL_EPOCH=20480, FL_WIN=20608, FL_PRB=20864, FL_Q=24576, FL_H1=32768, FL_O=FL_H1+64*128, FL_Y=FL_O+64*32*128, FL_H2=FL_Y+64*24*128, FL_ACT=FL_H2+64*8*128, FL_Z=FL_ACT+64*36*128;
constexpr size_t SZ_FLAGS=FL_Z+64*24*128;
constexpr unsigned L_TAG=163836;
constexpr size_t O_FLAGS=0;
constexpr size_t O_PROBE=O_FLAGS+((SZ_FLAGS+4095)&~(size_t)4095);
constexpr size_t O_MOD=O_PROBE+4096;
constexpr size_t O_H1=O_MOD+32768;
constexpr size_t SZ_T576=(size_t)64*36864;
constexpr size_t SZ_QKV=(size_t)64*32*8192;
constexpr size_t O_Q=O_H1+SZ_T576, O_K=O_Q+SZ_QKV, O_V=O_K+SZ_QKV;
constexpr size_t SZ_KM=(size_t)64*32*2048;
constexpr size_t O_KM=O_V+SZ_QKV, O_VM=O_KM+SZ_KM;
constexpr size_t O_O=O_VM+SZ_KM;
constexpr size_t SZ_O=(size_t)64*262144;
constexpr size_t SZ_PART=(size_t)64*294912;
constexpr size_t O_YP=O_O+SZ_O;
constexpr size_t O_X1=O_YP+SZ_PART;
constexpr size_t SZ_X1=(size_t)64*73728;
constexpr size_t O_H2=O_X1+SZ_X1;
constexpr size_t O_ACT=O_H2+SZ_T576;
constexpr size_t SZ_ACT=(size_t)64*294912;
constexpr size_t O_ZP=O_ACT+SZ_ACT;
constexpr size_t O_WIN=O_ZP+SZ_PART;
constexpr size_t WS_NEED=O_WIN+(4u<<20)+4096;
constexpr int CH_LDS=160*1024;
constexpr int PG_M=216, PG_MT=240;
#define POLL_SLEEP 3
constexpr unsigned SPIN_MAX=1u<<15;

struct Args {
  const u16* Wq; const u16* Wk; const u16* Wv; const u16* Wo;
  const char* Kt; const char* Vt; char* km; char* vm;
  char* win; unsigned* flags; unsigned long long* dbg;
  int rsv; int dA; int dB; int dC;
  char* ws; const u16* x; const u16* mem0; const u16* wg; const u16* wu; const u16* wd; const u16* c; const u16* wmod; u16* out;
};

typedef const __attribute__((address_space(4))) Args CArgs;
DEVI CArgs& kargs(){ CArgs* p=(CArgs*)__builtin_amdgcn_kernarg_segment_ptr(); asm volatile("" : "+s"(p)); return *p; }
DEVI float b2f(u16 v){ return __uint_as_float(((unsigned)v)<<16); }
DEVI float blo(unsigned v){ return __uint_as_float(v<<16); }
DEVI float bhi(unsigned v){ return __uint_as_float(v&0xffff0000u); }
DEVI unsigned pk2(float a,float b){ f2 t={a,b}; bf2 r=__builtin_convertvector(t,bf2); return __builtin_bit_cast(unsigned,r); }
DEVI rsrc_t mkr(const void* p,size_t n){ return __builtin_amdgcn_make_buffer_rsrc((void*)p,0,(int)n,0x00020000); }
DEVI v4u ld16c(rsrc_t r,unsigned vo,unsigned so){ return __builtin_amdgcn_raw_buffer_load_b128(r,vo,so,16); }
DEVI v2u ld8c(rsrc_t r,unsigned vo,unsigned so){ return __builtin_amdgcn_raw_buffer_load_b64(r,vo,so,16); }
DEVI v4u ld16(rsrc_t r,unsigned vo,unsigned so){ return __builtin_amdgcn_raw_buffer_load_b128(r,vo,so,0); }
DEVI unsigned ld4c(rsrc_t r,unsigned vo,unsigned so){ return __builtin_amdgcn_raw_buffer_load_b32(r,vo,so,16); }
DEVI void st16c(v4u v,rsrc_t r,unsigned vo,unsigned so){ __builtin_amdgcn_raw_buffer_store_b128(v,r,vo+so,0,16); }
DEVI void st4c(unsigned v,rsrc_t r,unsigned vo,unsigned so){ __builtin_amdgcn_raw_buffer_store_b32(v,r,vo,so,16); }
template<int IL,int IG> DEVI void dma16c(rsrc_t r,unsigned lds,unsigned vo,unsigned so){
  unsigned t;
  asm volatile("s_add_u32 m0, %1, %5\n\ts_add_u32 %0, %4, %6\n\tbuffer_load_dwordx4 %2, %3, %0 offen sc1 lds" : "=&s"(t) : "s"(lds),"v"(vo),"s"(r),"s"(so),"n"(IL),"n"(IG) : "memory","scc"); }
template<int I,int N,int GS> struct DmaSeq { static __device__ __forceinline__ void run(rsrc_t r,unsigned lds,unsigned vo,unsigned so){ dma16c<I*4096,I*GS>(r,lds,vo,so); DmaSeq<I+1,N,GS>::run(r,lds,vo,so); } };
template<int N,int GS> struct DmaSeq<N,N,GS> { static __device__ __forceinline__ void run(rsrc_t,unsigned,unsigned,unsigned){} };
DEVI void wait_vm0(){ asm volatile("s_waitcnt vmcnt(0)" ::: "memory"); }
DEVI void bar_all(){ asm volatile("s_waitcnt vmcnt(0) lgkmcnt(0)\n\ts_barrier" ::: "memory"); }
DEVI void bar_lds(){ asm volatile("s_waitcnt lgkmcnt(0)\n\ts_barrier" ::: "memory"); }
DEVI int fresh_lane(){ int l; asm volatile("v_mbcnt_lo_u32_b32 %0, -1, 0\n\tv_mbcnt_hi_u32_b32 %0, -1, %0" : "=v"(l)); return l; }
DEVI int ufl(int v){ return __builtin_amdgcn_readfirstlane(v); }
DEVI v2u swap32(unsigned a,unsigned b){ auto r=__builtin_amdgcn_permlane32_swap(a,b,false,false); v2u o={r[0],r[1]}; return o; }
DEVI v2u swap16(unsigned a,unsigned b){ auto r=__builtin_amdgcn_permlane16_swap(a,b,false,false); v2u o={r[0],r[1]}; return o; }
#define PIN(x) asm volatile("" : "+v"(x))
DEVI bf8 asbf8(v4u v){ return __builtin_bit_cast(bf8,v); }

struct Ctx { rsrc_t fl; int lane; int wave; bool dead; bool rec; unsigned tag; };
constexpr unsigned L_STAMP=155648, L_CTL=86016;
#define FST(j) do{}while(0)
#define GST(j) do{}while(0)
#define LST(j) do{}while(0)
#define RST(j) do{}while(0)
#define WST(j) do{}while(0)
#define STAMP(c,slot) do{}while(0)
#define PFEN 0
#define DEFER 1
#define MIDBACK 8
#define WARMF 1
#define WARMSL 30
#define MIDTH 6
#define DEFTH 3
#define POLLTH 6
#define K0PF 0
#define LN2L 1
#define SMARG 170
#define SENTN 24
#define SENT0 58
#define UA0 15
#define UA1 19
#define UB0 21
#define UB1 21
#define LATE1 0
#define LATE2 0
#define LN2SL 44
#define MFSL 4
#define MFD 0
#define PRBN 2
#define PERIOD 240
#define MARGIN 40
#define CSTC false
#define CST(j) do{}while(0)
#define CST2(j) do{}while(0)
DEVI unsigned now_t(){ return (unsigned)__builtin_amdgcn_s_memrealtime(); }
DEVI bool flags_ready(Ctx& c,unsigned off,int n,unsigned want){
  asm volatile("" ::: "memory");
  unsigned v=ld4c(c.fl,(c.lane<n)? off+c.lane*128 : 0x7ffffff0u,0);
  return __builtin_amdgcn_ballot_w64(c.lane<n && v!=want)==0ull;
}
DEVI void give_up(Ctx& c,unsigned off){ c.dead=true; if(c.lane==0 && c.rec){ st4c(5000u+off,c.fl,FL_TMO,0);
} }
DEVI void flags_wait(Ctx& c,unsigned off,int n,unsigned want){
  if(c.dead) return;
  unsigned sp=0;
  while(!flags_ready(c,off,n,want)){
    __builtin_amdgcn_s_sleep(8);
    if(++sp>SPIN_MAX){ give_up(c,off); break; }
  }
}
struct Pred { unsigned t; int s; };
DEVI bool wait_pred(Ctx& c,Pred& p,int s,unsigned off,int n,unsigned want){
  if(c.dead) return true;
  bool slept=false;
  if(p.s>=0 && s-p.s<=3 && s<62){
    const unsigned target=p.t+(unsigned)(s-p.s)*PERIOD-MARGIN; unsigned g=0;
    while((int)(now_t()-target)<0 && ++g<SPIN_MAX){ __builtin_amdgcn_s_sleep(2); slept=true; }
  }
  unsigned sp=0;
  while(!flags_ready(c,off,n,want)){
    __builtin_amdgcn_s_sleep(POLL_SLEEP);
    if(++sp>SPIN_MAX){ give_up(c,off); break; }
  }
  __builtin_amdgcn_s_waitcnt(0x0F70);
  if(sp==0){ p.s=-1; return !slept; }
  p.t=now_t(); p.s=s; return false;
}
DEVI void flag_set(Ctx& c,unsigned off){ if(c.lane==0) st4c(c.tag,c.fl,off,0); }
DEVI void flag_set_if(Ctx& c,unsigned off,bool on){ st4c(c.tag,c.fl,(on && c.lane==0)? off : 0x7ffffff0u,0); }

extern __shared__ __attribute__((aligned(16))) char smem[];
DEVI bf8 lds_frag(unsigned off){ return *(const LDSP bf8*)(uintptr_t)off; }
DEVI s4 lds_tr(unsigned off){ typedef short v4i16 __attribute__((ext_vector_type(4))); return __builtin_amdgcn_ds_read_tr16_b64_v4i16((LDSP v4i16*)(uintptr_t)off); }

template<int... I,class F> DEVI void sfor_impl(std::integer_sequence<int,I...>,F f){ (f(std::integral_constant<int,I>{}),...); }
template<int N,class F> DEVI void sfor(F f){ sfor_impl(std::make_integer_sequence<int,N>{},f); }
DEVI void poll_lds_at(Ctx& c,unsigned vo,unsigned lds){
  asm volatile("s_mov_b32 m0, %0\n\ts_nop 0\n\tbuffer_load_dword %1, %2, 0 offen sc1 lds" :: "s"(lds),"v"(vo),"s"(c.fl) : "memory");
}
DEVI void poll_lds(Ctx& c,unsigned off,int n,unsigned lds){
  const unsigned vo=off+c.lane*128;
  asm volatile("s_mov_b32 m0, %0\n\ts_nop 0\n\tbuffer_load_dword %1, %2, 0 offen sc1 lds" :: "s"(lds),"v"(vo),"s"(c.fl) : "memory");
}
template<int NF> DEVI void apoll_issue(Ctx& c,bool go,unsigned off,int p){
  if(c.wave==2){
    if(go && c.lane<NF) poll_lds(c,off,NF,L_CTL+p*256);
    if(c.lane==0) *(LDSP volatile unsigned*)(uintptr_t)(L_CTL+p*256+63*4)=go? c.tag : ~c.tag;
  }
}
template<int NF> DEVI bool apoll_eval(Ctx& c,int p){
  const unsigned fv=*(LDSP volatile unsigned*)(uintptr_t)(L_CTL+p*256+c.lane*4);
  return __builtin_amdgcn_ballot_w64((c.lane<NF || c.lane==63) && fv!=c.tag)==0ull;
}
template<class R> DEVI void mpoll_issue(Ctx& c,R& r,int t0,int n,int row){
  if(c.wave==2){
    const int d=c.lane/R::NF, p=c.lane-d*R::NF;
    if(d<R::PD && t0+d<n) poll_lds_at(c,r.in_off(r.seg(t0+d))+p*128,L_CTL+row*256);
  }
}
template<class R> DEVI int mpoll_eval(Ctx& c,int t0,int n,int row){
  const unsigned fv=*(LDSP volatile unsigned*)(uintptr_t)(L_CTL+row*256+c.lane*4);
  unsigned long long m=__builtin_amdgcn_ballot_w64(fv==c.tag);
  int cnt=0; const int lim=(n-t0<R::PD)? n-t0 : R::PD;
  while(cnt<lim && (m&((1ull<<R::NF)-1))==((1ull<<R::NF)-1)){ cnt++; m>>=R::NF; }
  return cnt;
}
template<class R> DEVI void tile_loop(Ctx& c,R& r,int slot0){
  STAMP(c,slot0-1);
  constexpr unsigned TILE=R::TILE;
  const int n=r.nseg();
  Pred pr{0u,-1};
  auto dma_all=[&](int seg,unsigned buf){ typename R::Src q=r.src(seg,buf); sfor<R::NDMA>([&](auto I){ dma16c<I*4096,I*R::GS>(r.ri,q.lds,q.vo,q.so); }); r.extra(seg,buf); };
  __builtin_amdgcn_s_waitcnt(0);
  if(c.wave==0) wait_pred(c,pr,0,r.in_off(r.seg(0)),R::NF,c.tag);
  apoll_issue<R::NF>(c,true,r.in_off(r.seg(1)),0);
  bar_lds();
  dma_all(r.seg(0),0);
  bool prev=false;
  for(int k=0;k<n;k++){
    const unsigned buf=(k&1)*TILE; const int p=k&1;
    RST(0);
    if(prev) asm volatile("s_waitcnt vmcnt(%0) lgkmcnt(0)\n\ts_barrier" :: "n"(R::NST) : "memory"); else bar_all();
    RST(1);
    STAMP(c,136+k);
    const bool pre=apoll_eval<R::NF>(c,p) && (k+1<n);
    apoll_issue<R::NF>(c,pre && (k+2<n),r.in_off(r.seg(k+2)),p^1);
    const int psig=prev? r.seg(k-1) : -1;
    RST(2);
    if(pre){ r.template work<true>(r.seg(k),buf,r.seg(k+1),TILE-buf,psig,-1); if(prev) STAMP(c,slot0+k-1); prev=true; }
    else {
      r.template work<false>(r.seg(k),buf,0,0,psig,(k+1<n)? (int)r.in_off(r.seg(k+1)) : -1);
      if(prev) STAMP(c,slot0+k-1);
      asm volatile("s_waitcnt vmcnt(%0) lgkmcnt(0)\n\ts_barrier" :: "n"(R::NST) : "memory");
      const bool rdy=apoll_eval<R::NF>(c,2);
      if(rdy){
        apoll_issue<R::NF>(c,k+2<n,r.in_off(r.seg(k+2)),p^1);
        dma_all(r.seg(k+1),TILE-buf);
        asm volatile("s_waitcnt vmcnt(%0) lgkmcnt(0)\n\ts_barrier" :: "n"(R::NDMA+1) : "memory");
        if(c.wave==1) r.signal(r.seg(k));
        STAMP(c,slot0+k);
        prev=false;
      } else {
        bar_all();
        RST(8);
        if(c.wave==1) r.signal(r.seg(k));
        STAMP(c,slot0+k);
        if(k+1<n){
          if(c.wave==0) wait_pred(c,pr,k+1,r.in_off(r.seg(k+1)),R::NF,c.tag);
          RST(9);
          if(k+1>=40) STAMP(c,200+k+1-40);
          apoll_issue<R::NF>(c,false,0u,p^1);
          bar_lds();
          dma_all(r.seg(k+1),TILE-buf);
          if(k+1>=40) STAMP(c,224+k+1-40);
        }
        prev=false;
      }
    }
  }
}
struct TileSrc { unsigned lds,vo,so; };
DEVI TileSrc src_linear(unsigned gbase,unsigned lds,int lane,int wave){ return TileSrc{lds+wave*1024,(unsigned)lane*16,gbase+wave*1024}; }
DEVI TileSrc src_tg(unsigned gbase,unsigned lds,int lane,int wave){
  const int tok=lane&31; return TileSrc{lds+wave*1024,(unsigned)((tok>>2)*4608+(lane>>5)*64+(tok&3)*16),gbase+wave*128}; }

constexpr unsigned L_PAR=L_CTL+1024, L_TP=90112;
constexpr unsigned OOBO=0x7ffffff0u;
struct GP { const char* ri; unsigned ri_n; const char* ro; unsigned ro_n; int n; unsigned inb,ins,outb,outs,sigb,sigs,wstr,slot0;
            const char* p0; const char* p1; unsigned dv,sv[4],sgv,segv,tabv; int nf,pd; };
DEVI void gemm_params(Ctx& c,CArgs& a,const GP& g){
  const int lane=c.lane, wave=c.wave;
  if(wave==0 && lane<20){
    const unsigned long long ri=(unsigned long long)g.ri, ro=(unsigned long long)g.ro, fl=(unsigned long long)a.flags;
    unsigned v= lane==0? (unsigned)ri : lane==1? (unsigned)(ri>>32) : lane==2? g.ri_n : lane==3? (unsigned)ro : lane==4? (unsigned)(ro>>32) : lane==5? g.ro_n :
                lane==6? (unsigned)fl : lane==7? (unsigned)(fl>>32) : lane==8? (unsigned)SZ_FLAGS : lane==9? (unsigned)g.n : lane==10? g.inb : lane==11? g.ins :
                lane==12? g.outb : lane==13? g.outs : lane==14? g.sigb : lane==15? g.sigs : lane==16? g.wstr : lane==17? g.slot0 : lane==18? c.tag : (g.slot0==8? 1u : 0u);
    *(LDSP unsigned*)(uintptr_t)(L_PAR+lane*4)=v;
  }
  const int h=lane>>5, blk=(lane>>4)&1, q=(lane&15)>>2, p=lane&3;
  unsigned ta[2];
  #pragma unroll
  for(int t=0;t<2;t++){ const int r=8*h+4*t+q; const int ch=4*wave+2*blk+(p>>1); ta[t]=256*r+16*(ch^(((r&3)<<2)|((r>>2)&3)))+8*(p&1); }
  const int d=lane/g.nf, pp=lane-d*g.nf;
  const unsigned long long p0=(unsigned long long)g.p0, p1=(unsigned long long)g.p1;
  LDSP v4u* tp=(LDSP v4u*)(uintptr_t)(L_TP+(wave*64+lane)*64);
  tp[0]=(v4u){(unsigned)p0,(unsigned)(p0>>32),(unsigned)p1,(unsigned)(p1>>32)};
  tp[1]=(v4u){g.dv,g.sv[0],g.sv[1],g.sv[2]};
  tp[2]=(v4u){g.sv[3],g.sgv,(unsigned)((d<g.pd)? d*4 : 0),(unsigned)(pp*128)};
  tp[3]=(v4u){ta[0],ta[1],g.segv,g.tabv};
  bar_all();
}
DEVI int w_row(int lane,int wave){ return 4*wave+(lane>>4); }
DEVI int w_ch(int lane,int wave){ return (lane&15)^(((lane>>4)<<2)|wave); }
#define GEMM_ASM_BEGIN asm volatile(
#define GEMM_ASM_END :: "{s35}"(c.wave) :
#define MEMF __device__ __forceinline__
DEVI float dror(float v,int n){
  return __uint_as_float(n==8? __builtin_amdgcn_update_dpp(0u,__float_as_uint(v),0x128,0xf,0xf,false) : __builtin_amdgcn_update_dpp(0u,__float_as_uint(v),0x124,0xf,0xf,false)); }
DEVI float rows_sum(float v){
  v2u t=swap16(__float_as_uint(v),__float_as_uint(v)); v=__uint_as_float(t.x)+__uint_as_float(t.y);
  t=swap32(__float_as_uint(v),__float_as_uint(v)); return __uint_as_float(t.x)+__uint_as_float(t.y); }
DEVI float wsum8(float v){ v+=dror(v,8); return rows_sum(v); }
DEVI float wsum4(float v){ v+=dror(v,8); v+=dror(v,4); return rows_sum(v); }
DEVI void unpack8(v4u v,float* o){ o[0]=blo(v.x); o[1]=bhi(v.x); o[2]=blo(v.y); o[3]=bhi(v.y); o[4]=blo(v.z); o[5]=bhi(v.z); o[6]=blo(v.w); o[7]=bhi(v.w); }
DEVI v4u pack8(const float* o){ v4u r={pk2(o[0],o[1]),pk2(o[2],o[3]),pk2(o[4],o[5]),pk2(o[6],o[7])}; return r; }
struct ModRegs { v4u wv[9]; u16 cv[9]; };
DEVI void mod_load(CArgs& a,int task,int t,ModRegs& m){
  const int cb=task*32, q=t&3, rg=t>>2;
  #pragma unroll
  for(int i=0;i<9;i++){ const int k=rg+64*i; m.wv[i]=*(const v4u*)(a.wmod+(size_t)k*3456+cb+q*8); m.cv[i]=a.c[k]; }
}
DEVI void mod_finish(Ctx& c,CArgs& a,int task,int t,const ModRegs& m){
  LDSP float* red=(LDSP float*)(uintptr_t)0;
  const int q=t&3, rg=t>>2;
  float acc[8]={0,0,0,0,0,0,0,0};
  #pragma unroll
  for(int i=0;i<9;i++){ const float cv=b2f(m.cv[i]); const float sc=cv/(1.f+__expf(-cv)); float w8[8]; unpack8(m.wv[i],w8); const float sr=b2f((u16)(pk2(sc,0.f)&0xffffu)); for(int j=0;j<8;j++) acc[j]+=sr*w8[j]; }
  for(int j=0;j<8;j++) red[rg*33+q*8+j]=acc[j];
  bar_lds();
  if(c.wave==0){
    float sm=0.f; const int tt=c.lane&31;
    for(int i=0;i<64;i++) sm+=red[i*33+tt];
    const v2u o={__float_as_uint(b2f((u16)(pk2(sm,0.f)&0xffffu))),c.tag};
    __builtin_amdgcn_raw_buffer_store_b64(o,mkr(a.ws+O_MOD,32768),(c.lane<32)? (unsigned)(task*32+tt)*8u : 0x7ffffff0u,0,16);
  }
  bar_lds();
}
constexpr unsigned L_MOD=16384;
DEVI void mod_fetch(Ctx& c,CArgs& a,int first,int n){
  rsrc_t rm=mkr(a.ws+O_MOD,32768);
  const int t=c.wave*64+c.lane;
  for(int i=t;i<n/2;i+=256){
    v4u v; unsigned sp=0;
    for(;;){
      asm volatile("" ::: "memory");
      v=ld16c(rm,(unsigned)(first+2*i)*8u,0);
      if((v.y==c.tag && v.w==c.tag) || c.dead) break;
      __builtin_amdgcn_s_sleep(MFSL);
      if(++sp>SPIN_MAX){ st4c(8000u,c.fl,FL_TMO,0); break; }
    }
    *(LDSP v2u*)(uintptr_t)(L_MOD+i*8)=(v2u){v.x,v.z};
  }
  bar_lds();
}
DEVI f4 ldmod(int idx){ return *(const LDSP f4*)(uintptr_t)(L_MOD+idx*4); }
DEVI void ln1_segment(Ctx& c,CArgs& a,int s){
  c.lane=fresh_lane();
  rsrc_t rx=mkr(a.x,(size_t)2048*1152), rh=mkr(a.ws+O_H1,SZ_T576);
  const int cg=c.lane>>3, tk=c.lane&7; const int tok=c.wave*8+tk; const int t=c.wave*64+c.lane;
  float v[9][8]; float sum=0.f;
  v4u raw[9]; ModRegs mr;
  const unsigned ep=ld4c(c.fl,FL_EPOCH,0);
  if(s<36) mod_load(a,s,t,mr);
  #pragma unroll
  for(int i=0;i<9;i++) raw[i]=ld16(rx,(unsigned)(s*32+tok)*1152u+(cg+8*i)*16,0);
  c.tag=(unsigned)ufl((int)ep)+1u;
  if(s<36) mod_finish(c,a,s,t,mr);
  #pragma unroll
  for(int i=0;i<9;i++){ unpack8(raw[i],v[i]); for(int j=0;j<8;j++) sum+=v[i][j]; }
  const float m=wsum8(sum)*(1.f/576.f); float q=0.f;
  #pragma unroll
  for(int i=0;i<9;i++) for(int j=0;j<8;j++){ const float d=v[i][j]-m; q+=d*d; }
  const float rs=1.f/sqrtf(wsum8(q)*(1.f/576.f)+1e-5f);
  for(int i=0;i<MFD;i++) __builtin_amdgcn_s_sleep(16);
  mod_fetch(c,a,0,1152);
  #pragma unroll
  for(int i=0;i<9;i++){
    const int col=(cg+8*i)*8; const f4 s0=ldmod(col), s1=ldmod(col+4), c0=ldmod(576+col), c1=ldmod(576+col+4);
    float o[8];
    for(int j=0;j<4;j++){ o[j]=(v[i][j]-m)*rs*(1.f+c0[j])+s0[j]; o[4+j]=(v[i][4+j]-m)*rs*(1.f+c1[j])+s1[j]; }
    st16c(pack8(o),rh,(cg+8*i)*512+tok*16,(unsigned)s*36864u);
  }
  bar_all();
  if(c.wave==0) flag_set(c,FL_H1+s*128);
}
DEVI void window_fill(Ctx& c,CArgs& a,int g){
  c.lane=fresh_lane();
  rsrc_t rw=mkr(a.win,2u<<20);
  const int t=c.wave*64+c.lane;
  #pragma unroll
  for(int u=0;u<18;u++){
    const unsigned o=(unsigned)(g*18+u)*4096u+(unsigned)t*16u; const unsigned p=o>>12;
    v4u v={~0u,~0u,~0u,~0u}; const int L=(p>>2)*2+(p&1); int k=-1;
    if(L>=PG_M && L<PG_M+8) k=((L-PG_M)&1)*4+((o>>10)&3); else if(L==PG_MT) k=8;
    if(k>=0){ const int kk=k*64+((o>>7)&7)*8, r=(o>>4)&7; v=*(const v4u*)(a.mem0+r*576+kk); }
    st16c(v,rw,(p<500)? o : 0x7ffffff0u,0);
  }
  {
    rsrc_t rk=mkr(a.ws+O_KM,2*SZ_KM); const v4u v={~0u,~0u,~0u,~0u};
    constexpr unsigned N16=(64-SENT0)*32*2048/16;
    for(unsigned i=g*256+t;i<2*N16;i+=28*256){ const unsigned m=i/N16, j=i-m*N16; st16c(v,rk,m*(unsigned)SZ_KM+SENT0*32*2048u+j*16u,0); }
  }
  bar_all();
  if(c.wave==0) flag_set(c,FL_WIN+g*4);
}
DEVI void numa_probe(Ctx& c,CArgs& a,int pi){
  c.lane=fresh_lane();
  const int w=c.wave;
  LDSP unsigned* res=(LDSP unsigned*)(uintptr_t)0;
  unsigned best=0xffffffffu, acc=0;
  acc+=__hip_atomic_load((const unsigned*)(a.win+(505+(w&1))*4096+(pi+(w>>1)*16)*128),__ATOMIC_RELAXED,__HIP_MEMORY_SCOPE_AGENT);
  asm volatile("s_waitcnt vmcnt(0)" : "+v"(acc) :: "memory");
  for(int i=0;i<PRBN;i++){
    const unsigned* p=(const unsigned*)(a.win+(508+w)*4096+(pi*2+(i&1))*128+(acc&0));
    const unsigned long long c0=__builtin_amdgcn_s_memtime();
    unsigned v=__hip_atomic_load(p,__ATOMIC_RELAXED,__HIP_MEMORY_SCOPE_AGENT);
    asm volatile("s_waitcnt vmcnt(0)" : "+v"(v) :: "memory");
    const unsigned long long c1=__builtin_amdgcn_s_memtime();
    acc+=v&0; asm volatile("" : "+v"(acc)); best=min(best,(unsigned)(c1-c0));
  }
  if(c.lane==0) res[w]=best;
  bar_lds();
  if(w==0 && c.lane<2){ v2u o={min(res[2*c.lane],res[2*c.lane+1]),c.tag}; __builtin_amdgcn_raw_buffer_store_b64(o,c.fl,FL_PRB+(pi*2+c.lane)*8,0,16); }
  bar_lds();
}
#define GN0 15
#define GN1 20
DEVI int qkv_seg(int k,int g,int n0,int n1){ if(n0==0) return k; if(k<n0) return 3*k+g; if(k<n1) return 3*n0+2*(k-n0)+(g-1); return 3*n0+2*(n1-n0)+(k-n1); }
#define P1DQ 22
#define P1DG 0
#define QPF 0
#define TLBW 0
DEVI void touch_pages(const char* base,size_t lo,size_t hi,unsigned step,int id,int nid){
  for(size_t o=lo+(size_t)id*step;o<hi;o+=(size_t)nid*step){
    unsigned v; const char* p=base+o;
    asm volatile("global_load_dword %0, %1, off sc1\n\ts_waitcnt vmcnt(0)" : "=&v"(v) : "v"(p) : "memory");
  }
}
DEVI void phase1(Ctx& c,CArgs& a,int mat,int slice,int g,int n0,int n1){
  { const int d=(n0==0)? P1DQ : 0; for(int i=0;i<d;i++) __builtin_amdgcn_s_sleep(16); }
  c.lane=fresh_lane();
  const u16* Wm=(mat==0)? a.Wq : (mat==1)? a.Wk : a.Wv;
  const int lane=c.lane, wave=c.wave, t=lane&31, hf=lane>>5;
  GP p; p.ri=a.ws+O_H1; p.ri_n=SZ_T576; p.ro=a.ws+((mat==0)?O_Q:(mat==1)?O_K:O_V); p.ro_n=SZ_QKV;
  p.n=(n0==0)? 64 : (g==0)? n0 : (g==1)? n1 : 64-n0-n1; p.inb=0; p.ins=36864; p.outb=2*slice*8192; p.outs=32*8192;
  p.sigb=((mat==0)? FL_Q : (mat==1)? FL_K : FL_V)+2*slice*256; p.sigs=4; p.wstr=16*8192; p.slot0=8;
  p.p0=(const char*)Wm+(size_t)w_row(lane,wave)*8192+slice*512+w_ch(lane,wave)*16; p.p1=p.p0+256;
  p.dv=lane*16+wave*1024;
  const unsigned vo=(mat<2)? (t>>4)*4096+(4*wave+hf)*256+(t&15)*16 : ((2*wave+(t>>4))*64+hf*16+(t&15))*16;
  p.sv[0]=vo; p.sv[1]=vo+512; p.sv[2]=vo+8192; p.sv[3]=vo+8192+512;
  p.sgv=(wave==1 && lane<2)? lane*256 : OOBO;
  const int sg=(lane<p.n)? qkv_seg(lane,g,n0,n1) : 0; p.segv=sg; p.tabv=(lane<p.n)? FL_H1+sg*128 : OOBO; p.nf=1; p.pd=16;
  gemm_params(c,a,p);
  if(mat<2){ GEMM_ASM_BEGIN
"v_mbcnt_lo_u32_b32 v10, -1, 0\n"
"v_mbcnt_hi_u32_b32 v10, -1, v10\n"
"v_lshlrev_b32 v0, 4, v10\n"
"v_lshlrev_b32 v192, 2, v10\n"
"v_add_u32 v192, 87040, v192\n"
"ds_read_b32 v193, v192\n"
"s_lshl_b32 s67, %0, 12\n"
"s_add_u32 s67, s67, 90112\n"
"v_lshlrev_b32 v194, 6, v10\n"
"v_add_u32 v194, s67, v194\n"
"ds_read_b128 v[160:163], v194\n"
"ds_read_b128 v[164:167], v194 offset:16\n"
"ds_read_b128 v[168:171], v194 offset:32\n"
"ds_read_b128 v[172:175], v194 offset:48\n"
"s_waitcnt lgkmcnt(0)\n"
"v_readlane_b32 s36, v193, 0\n"
"v_readlane_b32 s37, v193, 1\n"
"v_readlane_b32 s38, v193, 2\n"
"v_readlane_b32 s40, v193, 3\n"
"v_readlane_b32 s41, v193, 4\n"
"v_readlane_b32 s42, v193, 5\n"
"v_readlane_b32 s44, v193, 6\n"
"v_readlane_b32 s45, v193, 7\n"
"v_readlane_b32 s46, v193, 8\n"
"v_readlane_b32 s48, v193, 9\n"
"v_readlane_b32 s49, v193, 10\n"
"v_readlane_b32 s50, v193, 11\n"
"v_readlane_b32 s51, v193, 12\n"
"v_readlane_b32 s52, v193, 13\n"
"v_readlane_b32 s53, v193, 14\n"
"v_readlane_b32 s54, v193, 15\n"
"v_readlane_b32 s55, v193, 16\n"
"v_readlane_b32 s56, v193, 17\n"
"v_readlane_b32 s97, v193, 18\n"
"v_readlane_b32 s98, v193, 19\n"
"s_nop 1\n"
"s_and_b32 s37, s37, 0xffff\n"
"s_mov_b32 s39, 0x20000\n"
"s_and_b32 s41, s41, 0xffff\n"
"s_mov_b32 s43, 0x20000\n"
"s_and_b32 s45, s45, 0xffff\n"
"s_mov_b32 s47, 0x20000\n"
"v_mov_b32 v176, v160\n"
"v_mov_b32 v177, v161\n"
"v_mov_b32 v178, v162\n"
"v_mov_b32 v179, v163\n"
"v_mov_b32 v2, v164\n"
"v_mov_b32 v11, v165\n"
"v_mov_b32 v12, v166\n"
"v_mov_b32 v13, v167\n"
"v_mov_b32 v14, v168\n"
"v_mov_b32 v3, v169\n"
"v_mov_b32 v8, v170\n"
"v_mov_b32 v9, v171\n"
"v_mov_b32 v180, v172\n"
"v_mov_b32 v181, v173\n"
"v_mov_b32 v6, v174\n"
"v_mov_b32 v7, v175\n"
"v_mov_b32 v4, 1\n"
"v_mov_b32 v15, 0x7ffffff0\n"
"v_lshlrev_b32 v5, 7, v10\n"
"v_cmp_gt_u32_e32 vcc, 1, v10\n"
"s_nop 1\n"
"v_cndmask_b32_e32 v5, v15, v5, vcc\n"
"s_mov_b64 s[82:83], 1\n"
"s_mov_b64 s[78:79], 0\n"
"s_cmp_eq_u32 %0, 2\n"
"s_cbranch_scc0 Lnxpack36_%=\n"
"s_mov_b32 s78, 0xffff\n"
"s_mov_b32 s79, 0x0\n"
"Lnxpack36_%=:\n"
"s_mov_b32 s75, 0\n"
"s_mov_b32 s74, -1\n"
"s_mov_b32 s73, 0\n"
"s_barrier\n"
"v_mov_b32 v200, 20480\n"
"buffer_load_dword v200, v200, s[44:47], 0 offen sc1\n"
"v_add_u32 v182, 0x10000, v180\n"
"v_add_u32 v183, 0x10000, v181\n"
"v_add_u32 v184, 0x20000, v180\n"
"v_add_u32 v185, 0x20000, v181\n"
"s_lshl_b32 s69, %0, 10\n"
"s_mov_b32 s96, 0\n"
"s_add_u32 m0, s69, 0\n"
"s_nop 0\n"
"global_load_lds_dwordx4 v[176:177], off\n"
"v_add_co_u32_e32 v176, vcc, s55, v176\n"
"v_addc_co_u32_e32 v177, vcc, 0, v177, vcc\n"
"s_add_u32 m0, s69, 4096\n"
"s_nop 0\n"
"global_load_lds_dwordx4 v[176:177], off\n"
"v_add_co_u32_e32 v176, vcc, s55, v176\n"
"v_addc_co_u32_e32 v177, vcc, 0, v177, vcc\n"
"s_add_u32 m0, s69, 8192\n"
"s_nop 0\n"
"global_load_lds_dwordx4 v[176:177], off\n"
"v_add_co_u32_e32 v176, vcc, s55, v176\n"
"v_addc_co_u32_e32 v177, vcc, 0, v177, vcc\n"
"s_add_u32 m0, s69, 12288\n"
"s_nop 0\n"
"global_load_lds_dwordx4 v[176:177], off\n"
"v_add_co_u32_e32 v176, vcc, s55, v176\n"
"v_addc_co_u32_e32 v177, vcc, 0, v177, vcc\n"
"s_add_u32 m0, s69, 16384\n"
"s_nop 0\n"
"global_load_lds_dwordx4 v[176:177], off\n"
"v_add_co_u32_e32 v176, vcc, s55, v176\n"
"v_addc_co_u32_e32 v177, vcc, 0, v177, vcc\n"
"s_add_u32 m0, s69, 20480\n"
"s_nop 0\n"
"global_load_lds_dwordx4 v[176:177], off\n"
"v_add_co_u32_e32 v176, vcc, s55, v176\n"
"v_addc_co_u32_e32 v177, vcc, 0, v177, vcc\n"
"s_add_u32 m0, s69, 24576\n"
"s_nop 0\n"
"global_load_lds_dwordx4 v[176:177], off\n"
"v_add_co_u32_e32 v176, vcc, s55, v176\n"
"v_addc_co_u32_e32 v177, vcc, 0, v177, vcc\n"
"s_add_u32 m0, s69, 28672\n"
"s_nop 0\n"
"global_load_lds_dwordx4 v[176:177], off\n"
"v_add_co_u32_e32 v176, vcc, s55, v176\n"
"v_addc_co_u32_e32 v177, vcc, 0, v177, vcc\n"
"s_add_u32 m0, s69, 32768\n"
"s_nop 0\n"
"global_load_lds_dwordx4 v[176:177], off\n"
"v_add_co_u32_e32 v176, vcc, s55, v176\n"
"v_addc_co_u32_e32 v177, vcc, 0, v177, vcc\n"
"s_add_u32 m0, s69, 36864\n"
"s_nop 0\n"
"global_load_lds_dwordx4 v[176:177], off\n"
"v_add_co_u32_e32 v176, vcc, s55, v176\n"
"v_addc_co_u32_e32 v177, vcc, 0, v177, vcc\n"
"s_add_u32 m0, s69, 40960\n"
"s_nop 0\n"
"global_load_lds_dwordx4 v[176:177], off\n"
"v_add_co_u32_e32 v176, vcc, s55, v176\n"
"v_addc_co_u32_e32 v177, vcc, 0, v177, vcc\n"
"s_add_u32 m0, s69, 45056\n"
"s_nop 0\n"
"global_load_lds_dwordx4 v[176:177], off\n"
"v_add_co_u32_e32 v176, vcc, s55, v176\n"
"v_addc_co_u32_e32 v177, vcc, 0, v177, vcc\n"
"s_add_u32 m0, s69, 49152\n"
"s_nop 0\n"
"global_load_lds_dwordx4 v[176:177], off\n"
"v_add_co_u32_e32 v176, vcc, s55, v176\n"
"v_addc_co_u32_e32 v177, vcc, 0, v177, vcc\n"
"s_add_u32 m0, s69, 53248\n"
"s_nop 0\n"
"global_load_lds_dwordx4 v[176:177], off\n"
"v_add_co_u32_e32 v176, vcc, s55, v176\n"
"v_addc_co_u32_e32 v177, vcc, 0, v177, vcc\n"
"s_add_u32 m0, s69, 57344\n"
"s_nop 0\n"
"global_load_lds_dwordx4 v[176:177], off\n"
"v_add_co_u32_e32 v176, vcc, s55, v176\n"
"v_addc_co_u32_e32 v177, vcc, 0, v177, vcc\n"
"s_add_u32 m0, s69, 61440\n"
"s_nop 0\n"
"global_load_lds_dwordx4 v[176:177], off\n"
"v_add_co_u32_e32 v176, vcc, s55, v176\n"
"v_addc_co_u32_e32 v177, vcc, 0, v177, vcc\n"
"s_add_u32 m0, s69, 65536\n"
"s_nop 0\n"
"global_load_lds_dwordx4 v[176:177], off\n"
"v_add_co_u32_e32 v176, vcc, s55, v176\n"
"v_addc_co_u32_e32 v177, vcc, 0, v177, vcc\n"
"s_add_u32 m0, s69, 69632\n"
"s_nop 0\n"
"global_load_lds_dwordx4 v[176:177], off\n"
"v_add_co_u32_e32 v176, vcc, s55, v176\n"
"v_addc_co_u32_e32 v177, vcc, 0, v177, vcc\n"
"s_add_u32 m0, s69, 73728\n"
"s_nop 0\n"
"global_load_lds_dwordx4 v[176:177], off\n"
"v_add_co_u32_e32 v176, vcc, s55, v176\n"
"v_addc_co_u32_e32 v177, vcc, 0, v177, vcc\n"
"s_add_u32 m0, s69, 77824\n"
"s_nop 0\n"
"global_load_lds_dwordx4 v[176:177], off\n"
"v_add_co_u32_e32 v176, vcc, s55, v176\n"
"v_addc_co_u32_e32 v177, vcc, 0, v177, vcc\n"
"s_add_u32 m0, s69, 81920\n"
"s_nop 0\n"
"global_load_lds_dwordx4 v[176:177], off\n"
"v_add_co_u32_e32 v176, vcc, s55, v176\n"
"v_addc_co_u32_e32 v177, vcc, 0, v177, vcc\n"
"s_add_u32 m0, s69, 86016\n"
"s_nop 0\n"
"global_load_lds_dwordx4 v[176:177], off\n"
"v_add_co_u32_e32 v176, vcc, s55, v176\n"
"v_addc_co_u32_e32 v177, vcc, 0, v177, vcc\n"
"s_add_u32 m0, s69, 90112\n"
"s_nop 0\n"
"global_load_lds_dwordx4 v[176:177], off\n"
"v_add_co_u32_e32 v176, vcc, s55, v176\n"
"v_addc_co_u32_e32 v177, vcc, 0, v177, vcc\n"
"s_add_u32 m0, s69, 94208\n"
"s_nop 0\n"
"global_load_lds_dwordx4 v[176:177], off\n"
"v_add_co_u32_e32 v176, vcc, s55, v176\n"
"v_addc_co_u32_e32 v177, vcc, 0, v177, vcc\n"
"s_add_u32 m0, s69, 98304\n"
"s_nop 0\n"
"global_load_lds_dwordx4 v[176:177], off\n"
"v_add_co_u32_e32 v176, vcc, s55, v176\n"
"v_addc_co_u32_e32 v177, vcc, 0, v177, vcc\n"
"s_add_u32 m0, s69, 102400\n"
"s_nop 0\n"
"global_load_lds_dwordx4 v[176:177], off\n"
"v_add_co_u32_e32 v176, vcc, s55, v176\n"
"v_addc_co_u32_e32 v177, vcc, 0, v177, vcc\n"
"s_add_u32 m0, s69, 106496\n"
"s_nop 0\n"
"global_load_lds_dwordx4 v[176:177], off\n"
"v_add_co_u32_e32 v176, vcc, s55, v176\n"
"v_addc_co_u32_e32 v177, vcc, 0, v177, vcc\n"
"s_add_u32 m0, s69, 110592\n"
"s_nop 0\n"
"global_load_lds_dwordx4 v[176:177], off\n"
"v_add_co_u32_e32 v176, vcc, s55, v176\n"
"v_addc_co_u32_e32 v177, vcc, 0, v177, vcc\n"
"s_add_u32 m0, s69, 114688\n"
"s_nop 0\n"
"global_load_lds_dwordx4 v[176:177], off\n"
"v_add_co_u32_e32 v176, vcc, s55, v176\n"
"v_addc_co_u32_e32 v177, vcc, 0, v177, vcc\n"
"s_add_u32 m0, s69, 118784\n"
"s_nop 0\n"
"global_load_lds_dwordx4 v[176:177], off\n"
"v_add_co_u32_e32 v176, vcc, s55, v176\n"
"v_addc_co_u32_e32 v177, vcc, 0, v177, vcc\n"
"s_add_u32 m0, s69, 122880\n"
"s_nop 0\n"
"global_load_lds_dwordx4 v[176:177], off\n"
"v_add_co_u32_e32 v176, vcc, s55, v176\n"
"v_addc_co_u32_e32 v177, vcc, 0, v177, vcc\n"
"s_add_u32 m0, s69, 126976\n"
"s_nop 0\n"
"global_load_lds_dwordx4 v[176:177], off\n"
"v_add_co_u32_e32 v176, vcc, s55, v176\n"
"v_addc_co_u32_e32 v177, vcc, 0, v177, vcc\n"
"s_add_u32 m0, s69, 131072\n"
"s_nop 0\n"
"global_load_lds_dwordx4 v[176:177], off\n"
"v_add_co_u32_e32 v176, vcc, s55, v176\n"
"v_addc_co_u32_e32 v177, vcc, 0, v177, vcc\n"
"s_add_u32 m0, s69, 135168\n"
"s_nop 0\n"
"global_load_lds_dwordx4 v[176:177], off\n"
"v_add_co_u32_e32 v176, vcc, s55, v176\n"
"v_addc_co_u32_e32 v177, vcc, 0, v177, vcc\n"
"s_add_u32 m0, s69, 139264\n"
"s_nop 0\n"
"global_load_lds_dwordx4 v[176:177], off\n"
"v_add_co_u32_e32 v176, vcc, s55, v176\n"
"v_addc_co_u32_e32 v177, vcc, 0, v177, vcc\n"
"s_add_u32 m0, s69, 143360\n"
"s_nop 0\n"
"global_load_lds_dwordx4 v[176:177], off\n"
"s_waitcnt vmcnt(27)\n"
"s_barrier\n"
"v_readfirstlane_b32 s90, v200\n"
"s_nop 3\n"
"s_add_u32 s90, s90, 1\n"
"s_cmp_eq_u32 s98, 1\n"
"s_cselect_b32 s97, s90, s97\n"
"v_mov_b32 v4, s97\n"
"v_mov_b32 v201, 163836\n"
"ds_write_b32 v201, v4\n"
"ds_read_b64_tr_b16 v[128:129], v180\n"
"ds_read_b64_tr_b16 v[130:131], v181\n"
"ds_read_b64_tr_b16 v[132:133], v180 offset:4096\n"
"ds_read_b64_tr_b16 v[134:135], v181 offset:4096\n"
"ds_read_b64_tr_b16 v[136:137], v180 offset:8192\n"
"ds_read_b64_tr_b16 v[138:139], v181 offset:8192\n"
"ds_read_b64_tr_b16 v[140:141], v180 offset:12288\n"
"ds_read_b64_tr_b16 v[142:143], v181 offset:12288\n"
"ds_read_b64_tr_b16 v[144:145], v180 offset:16384\n"
"ds_read_b64_tr_b16 v[146:147], v181 offset:16384\n"
"ds_read_b64_tr_b16 v[148:149], v180 offset:20480\n"
"ds_read_b64_tr_b16 v[150:151], v181 offset:20480\n"
"ds_read_b64_tr_b16 v[152:153], v180 offset:24576\n"
"ds_read_b64_tr_b16 v[154:155], v181 offset:24576\n"
"s_waitcnt lgkmcnt(8)\n"
"ds_read_b64_tr_b16 v[156:157], v180 offset:28672\n"
"ds_read_b64_tr_b16 v[158:159], v181 offset:28672\n"
"ds_read_b64_tr_b16 a[144:145], v180 offset:32768\n"
"ds_read_b64_tr_b16 a[146:147], v181 offset:32768\n"
"s_waitcnt lgkmcnt(0)\n"
"s_barrier\n"
"s_add_u32 m0, s69, 0\n"
"s_nop 0\n"
"global_load_lds_dwordx4 v[178:179], off\n"
"v_add_co_u32_e32 v178, vcc, s55, v178\n"
"v_addc_co_u32_e32 v179, vcc, 0, v179, vcc\n"
"s_add_u32 m0, s69, 4096\n"
"s_nop 0\n"
"global_load_lds_dwordx4 v[178:179], off\n"
"v_add_co_u32_e32 v178, vcc, s55, v178\n"
"v_addc_co_u32_e32 v179, vcc, 0, v179, vcc\n"
"s_add_u32 m0, s69, 8192\n"
"s_nop 0\n"
"global_load_lds_dwordx4 v[178:179], off\n"
"v_add_co_u32_e32 v178, vcc, s55, v178\n"
"v_addc_co_u32_e32 v179, vcc, 0, v179, vcc\n"
"s_add_u32 m0, s69, 12288\n"
"s_nop 0\n"
"global_load_lds_dwordx4 v[178:179], off\n"
"v_add_co_u32_e32 v178, vcc, s55, v178\n"
"v_addc_co_u32_e32 v179, vcc, 0, v179, vcc\n"
"s_add_u32 m0, s69, 16384\n"
"s_nop 0\n"
"global_load_lds_dwordx4 v[178:179], off\n"
"v_add_co_u32_e32 v178, vcc, s55, v178\n"
"v_addc_co_u32_e32 v179, vcc, 0, v179, vcc\n"
"s_add_u32 m0, s69, 20480\n"
"s_nop 0\n"
"global_load_lds_dwordx4 v[178:179], off\n"
"v_add_co_u32_e32 v178, vcc, s55, v178\n"
"v_addc_co_u32_e32 v179, vcc, 0, v179, vcc\n"
"s_add_u32 m0, s69, 24576\n"
"s_nop 0\n"
"global_load_lds_dwordx4 v[178:179], off\n"
"v_add_co_u32_e32 v178, vcc, s55, v178\n"
"v_addc_co_u32_e32 v179, vcc, 0, v179, vcc\n"
"s_add_u32 m0, s69, 28672\n"
"s_nop 0\n"
"global_load_lds_dwordx4 v[178:179], off\n"
"v_add_co_u32_e32 v178, vcc, s55, v178\n"
"v_addc_co_u32_e32 v179, vcc, 0, v179, vcc\n"
"s_add_u32 m0, s69, 32768\n"
"s_nop 0\n"
"global_load_lds_dwordx4 v[178:179], off\n"
"v_add_co_u32_e32 v178, vcc, s55, v178\n"
"v_addc_co_u32_e32 v179, vcc, 0, v179, vcc\n"
"s_waitcnt vmcnt(27)\n"
"s_barrier\n"
"ds_read_b64_tr_b16 a[148:149], v180 offset:36864\n"
"ds_read_b64_tr_b16 a[150:151], v181 offset:36864\n"
"ds_read_b64_tr_b16 a[152:153], v180 offset:40960\n"
"ds_read_b64_tr_b16 a[154:155], v181 offset:40960\n"
"ds_read_b64_tr_b16 a[156:157], v180 offset:45056\n"
"ds_read_b64_tr_b16 a[158:159], v181 offset:45056\n"
"ds_read_b64_tr_b16 a[160:161], v180 offset:49152\n"
"ds_read_b64_tr_b16 a[162:163], v181 offset:49152\n"
"ds_read_b64_tr_b16 a[164:165], v180 offset:53248\n"
"ds_read_b64_tr_b16 a[166:167], v181 offset:53248\n"
"ds_read_b64_tr_b16 a[168:169], v180 offset:57344\n"
"ds_read_b64_tr_b16 a[170:171], v181 offset:57344\n"
"ds_read_b64_tr_b16 a[172:173], v180 offset:61440\n"
"ds_read_b64_tr_b16 a[174:175], v181 offset:61440\n"
"ds_read_b64_tr_b16 a[176:177], v182\n"
"s_waitcnt lgkmcnt(8)\n"
"ds_read_b64_tr_b16 a[178:179], v183\n"
"ds_read_b64_tr_b16 a[180:181], v182 offset:4096\n"
"ds_read_b64_tr_b16 a[182:183], v183 offset:4096\n"
"s_waitcnt lgkmcnt(0)\n"
"s_barrier\n"
"s_add_u32 m0, s69, 36864\n"
"s_nop 0\n"
"global_load_lds_dwordx4 v[178:179], off\n"
"v_add_co_u32_e32 v178, vcc, s55, v178\n"
"v_addc_co_u32_e32 v179, vcc, 0, v179, vcc\n"
"s_add_u32 m0, s69, 40960\n"
"s_nop 0\n"
"global_load_lds_dwordx4 v[178:179], off\n"
"v_add_co_u32_e32 v178, vcc, s55, v178\n"
"v_addc_co_u32_e32 v179, vcc, 0, v179, vcc\n"
"s_add_u32 m0, s69, 45056\n"
"s_nop 0\n"
"global_load_lds_dwordx4 v[178:179], off\n"
"v_add_co_u32_e32 v178, vcc, s55, v178\n"
"v_addc_co_u32_e32 v179, vcc, 0, v179, vcc\n"
"s_add_u32 m0, s69, 49152\n"
"s_nop 0\n"
"global_load_lds_dwordx4 v[178:179], off\n"
"v_add_co_u32_e32 v178, vcc, s55, v178\n"
"v_addc_co_u32_e32 v179, vcc, 0, v179, vcc\n"
"s_add_u32 m0, s69, 53248\n"
"s_nop 0\n"
"global_load_lds_dwordx4 v[178:179], off\n"
"v_add_co_u32_e32 v178, vcc, s55, v178\n"
"v_addc_co_u32_e32 v179, vcc, 0, v179, vcc\n"
"s_add_u32 m0, s69, 57344\n"
"s_nop 0\n"
"global_load_lds_dwordx4 v[178:179], off\n"
"v_add_co_u32_e32 v178, vcc, s55, v178\n"
"v_addc_co_u32_e32 v179, vcc, 0, v179, vcc\n"
"s_add_u32 m0, s69, 61440\n"
"s_nop 0\n"
"global_load_lds_dwordx4 v[178:179], off\n"
"v_add_co_u32_e32 v178, vcc, s55, v178\n"
"v_addc_co_u32_e32 v179, vcc, 0, v179, vcc\n"
"s_add_u32 m0, s69, 65536\n"
"s_nop 0\n"
"global_load_lds_dwordx4 v[178:179], off\n"
"v_add_co_u32_e32 v178, vcc, s55, v178\n"
"v_addc_co_u32_e32 v179, vcc, 0, v179, vcc\n"
"s_add_u32 m0, s69, 69632\n"
"s_nop 0\n"
"global_load_lds_dwordx4 v[178:179], off\n"
"v_add_co_u32_e32 v178, vcc, s55, v178\n"
"v_addc_co_u32_e32 v179, vcc, 0, v179, vcc\n"
"s_waitcnt vmcnt(27)\n"
"s_barrier\n"
"ds_read_b64_tr_b16 a[184:185], v182 offset:8192\n"
"ds_read_b64_tr_b16 a[186:187], v183 offset:8192\n"
"ds_read_b64_tr_b16 a[188:189], v182 offset:12288\n"
"ds_read_b64_tr_b16 a[190:191], v183 offset:12288\n"
"ds_read_b64_tr_b16 a[192:193], v182 offset:16384\n"
"ds_read_b64_tr_b16 a[194:195], v183 offset:16384\n"
"ds_read_b64_tr_b16 a[196:197], v182 offset:20480\n"
"ds_read_b64_tr_b16 a[198:199], v183 offset:20480\n"
"ds_read_b64_tr_b16 a[200:201], v182 offset:24576\n"
"ds_read_b64_tr_b16 a[202:203], v183 offset:24576\n"
"ds_read_b64_tr_b16 a[204:205], v182 offset:28672\n"
"ds_read_b64_tr_b16 a[206:207], v183 offset:28672\n"
"ds_read_b64_tr_b16 a[208:209], v182 offset:32768\n"
"ds_read_b64_tr_b16 a[210:211], v183 offset:32768\n"
"ds_read_b64_tr_b16 a[212:213], v182 offset:36864\n"
"s_waitcnt lgkmcnt(8)\n"
"ds_read_b64_tr_b16 a[214:215], v183 offset:36864\n"
"ds_read_b64_tr_b16 a[216:217], v182 offset:40960\n"
"ds_read_b64_tr_b16 a[218:219], v183 offset:40960\n"
"s_waitcnt lgkmcnt(0)\n"
"s_barrier\n"
"s_add_u32 m0, s69, 73728\n"
"s_nop 0\n"
"global_load_lds_dwordx4 v[178:179], off\n"
"v_add_co_u32_e32 v178, vcc, s55, v178\n"
"v_addc_co_u32_e32 v179, vcc, 0, v179, vcc\n"
"s_add_u32 m0, s69, 77824\n"
"s_nop 0\n"
"global_load_lds_dwordx4 v[178:179], off\n"
"v_add_co_u32_e32 v178, vcc, s55, v178\n"
"v_addc_co_u32_e32 v179, vcc, 0, v179, vcc\n"
"s_add_u32 m0, s69, 81920\n"
"s_nop 0\n"
"global_load_lds_dwordx4 v[178:179], off\n"
"v_add_co_u32_e32 v178, vcc, s55, v178\n"
"v_addc_co_u32_e32 v179, vcc, 0, v179, vcc\n"
"s_add_u32 m0, s69, 86016\n"
"s_nop 0\n"
"global_load_lds_dwordx4 v[178:179], off\n"
"v_add_co_u32_e32 v178, vcc, s55, v178\n"
"v_addc_co_u32_e32 v179, vcc, 0, v179, vcc\n"
"s_add_u32 m0, s69, 90112\n"
"s_nop 0\n"
"global_load_lds_dwordx4 v[178:179], off\n"
"v_add_co_u32_e32 v178, vcc, s55, v178\n"
"v_addc_co_u32_e32 v179, vcc, 0, v179, vcc\n"
"s_add_u32 m0, s69, 94208\n"
"s_nop 0\n"
"global_load_lds_dwordx4 v[178:179], off\n"
"v_add_co_u32_e32 v178, vcc, s55, v178\n"
"v_addc_co_u32_e32 v179, vcc, 0, v179, vcc\n"
"s_add_u32 m0, s69, 98304\n"
"s_nop 0\n"
"global_load_lds_dwordx4 v[178:179], off\n"
"v_add_co_u32_e32 v178, vcc, s55, v178\n"
"v_addc_co_u32_e32 v179, vcc, 0, v179, vcc\n"
"s_add_u32 m0, s69, 102400\n"
"s_nop 0\n"
"global_load_lds_dwordx4 v[178:179], off\n"
"v_add_co_u32_e32 v178, vcc, s55, v178\n"
"v_addc_co_u32_e32 v179, vcc, 0, v179, vcc\n"
"s_add_u32 m0, s69, 106496\n"
"s_nop 0\n"
"global_load_lds_dwordx4 v[178:179], off\n"
"v_add_co_u32_e32 v178, vcc, s55, v178\n"
"v_addc_co_u32_e32 v179, vcc, 0, v179, vcc\n"
"s_waitcnt vmcnt(27)\n"
"s_barrier\n"
"ds_read_b64_tr_b16 a[220:221], v182 offset:45056\n"
"ds_read_b64_tr_b16 a[222:223], v183 offset:45056\n"
"ds_read_b64_tr_b16 a[224:225], v182 offset:49152\n"
"ds_read_b64_tr_b16 a[226:227], v183 offset:49152\n"
"ds_read_b64_tr_b16 a[228:229], v182 offset:53248\n"
"ds_read_b64_tr_b16 a[230:231], v183 offset:53248\n"
"ds_read_b64_tr_b16 a[232:233], v182 offset:57344\n"
"ds_read_b64_tr_b16 a[234:235], v183 offset:57344\n"
"ds_read_b64_tr_b16 a[236:237], v182 offset:61440\n"
"ds_read_b64_tr_b16 a[238:239], v183 offset:61440\n"
"ds_read_b64_tr_b16 a[240:241], v184\n"
"ds_read_b64_tr_b16 a[242:243], v185\n"
"ds_read_b64_tr_b16 a[244:245], v184 offset:4096\n"
"ds_read_b64_tr_b16 a[246:247], v185 offset:4096\n"
"ds_read_b64_tr_b16 a[248:249], v184 offset:8192\n"
"s_waitcnt lgkmcnt(8)\n"
"ds_read_b64_tr_b16 a[250:251], v185 offset:8192\n"
"ds_read_b64_tr_b16 a[252:253], v184 offset:12288\n"
"ds_read_b64_tr_b16 a[254:255], v185 offset:12288\n"
"s_waitcnt lgkmcnt(0)\n"
"s_barrier\n"
"s_waitcnt vmcnt(18)\n"
"s_barrier\n"
"ds_read_b64_tr_b16 a[0:1], v180\n"
"ds_read_b64_tr_b16 a[2:3], v181\n"
"ds_read_b64_tr_b16 a[4:5], v180 offset:4096\n"
"ds_read_b64_tr_b16 a[6:7], v181 offset:4096\n"
"ds_read_b64_tr_b16 a[8:9], v180 offset:8192\n"
"ds_read_b64_tr_b16 a[10:11], v181 offset:8192\n"
"ds_read_b64_tr_b16 a[12:13], v180 offset:12288\n"
"ds_read_b64_tr_b16 a[14:15], v181 offset:12288\n"
"ds_read_b64_tr_b16 a[16:17], v180 offset:16384\n"
"ds_read_b64_tr_b16 a[18:19], v181 offset:16384\n"
"ds_read_b64_tr_b16 a[20:21], v180 offset:20480\n"
"ds_read_b64_tr_b16 a[22:23], v181 offset:20480\n"
"ds_read_b64_tr_b16 a[24:25], v180 offset:24576\n"
"ds_read_b64_tr_b16 a[26:27], v181 offset:24576\n"
"ds_read_b64_tr_b16 a[28:29], v180 offset:28672\n"
"s_waitcnt lgkmcnt(8)\n"
"ds_read_b64_tr_b16 a[30:31], v181 offset:28672\n"
"ds_read_b64_tr_b16 a[32:33], v180 offset:32768\n"
"ds_read_b64_tr_b16 a[34:35], v181 offset:32768\n"
"s_waitcnt lgkmcnt(0)\n"
"s_barrier\n"
"s_waitcnt vmcnt(9)\n"
"s_barrier\n"
"ds_read_b64_tr_b16 a[36:37], v180 offset:36864\n"
"ds_read_b64_tr_b16 a[38:39], v181 offset:36864\n"
"ds_read_b64_tr_b16 a[40:41], v180 offset:40960\n"
"ds_read_b64_tr_b16 a[42:43], v181 offset:40960\n"
"ds_read_b64_tr_b16 a[44:45], v180 offset:45056\n"
"ds_read_b64_tr_b16 a[46:47], v181 offset:45056\n"
"ds_read_b64_tr_b16 a[48:49], v180 offset:49152\n"
"ds_read_b64_tr_b16 a[50:51], v181 offset:49152\n"
"ds_read_b64_tr_b16 a[52:53], v180 offset:53248\n"
"ds_read_b64_tr_b16 a[54:55], v181 offset:53248\n"
"ds_read_b64_tr_b16 a[56:57], v180 offset:57344\n"
"ds_read_b64_tr_b16 a[58:59], v181 offset:57344\n"
"ds_read_b64_tr_b16 a[60:61], v180 offset:61440\n"
"ds_read_b64_tr_b16 a[62:63], v181 offset:61440\n"
"ds_read_b64_tr_b16 a[64:65], v182\n"
"s_waitcnt lgkmcnt(8)\n"
"ds_read_b64_tr_b16 a[66:67], v183\n"
"ds_read_b64_tr_b16 a[68:69], v182 offset:4096\n"
"ds_read_b64_tr_b16 a[70:71], v183 offset:4096\n"
"s_waitcnt lgkmcnt(0)\n"
"s_barrier\n"
"v_readlane_b32 s72, v7, 0\n"
"s_nop 4\n"
"buffer_load_dword v198, v5, s[44:47], s72 offen sc1\n"
"s_add_u32 m0, s69, 110592\n"
"s_nop 0\n"
"global_load_lds_dwordx4 v[178:179], off\n"
"v_add_co_u32_e32 v178, vcc, s55, v178\n"
"v_addc_co_u32_e32 v179, vcc, 0, v179, vcc\n"
"s_add_u32 m0, s69, 114688\n"
"s_nop 0\n"
"global_load_lds_dwordx4 v[178:179], off\n"
"v_add_co_u32_e32 v178, vcc, s55, v178\n"
"v_addc_co_u32_e32 v179, vcc, 0, v179, vcc\n"
"s_add_u32 m0, s69, 118784\n"
"s_nop 0\n"
"global_load_lds_dwordx4 v[178:179], off\n"
"v_add_co_u32_e32 v178, vcc, s55, v178\n"
"v_addc_co_u32_e32 v179, vcc, 0, v179, vcc\n"
"s_add_u32 m0, s69, 122880\n"
"s_nop 0\n"
"global_load_lds_dwordx4 v[178:179], off\n"
"v_add_co_u32_e32 v178, vcc, s55, v178\n"
"v_addc_co_u32_e32 v179, vcc, 0, v179, vcc\n"
"s_add_u32 m0, s69, 126976\n"
"s_nop 0\n"
"global_load_lds_dwordx4 v[178:179], off\n"
"v_add_co_u32_e32 v178, vcc, s55, v178\n"
"v_addc_co_u32_e32 v179, vcc, 0, v179, vcc\n"
"s_add_u32 m0, s69, 131072\n"
"s_nop 0\n"
"global_load_lds_dwordx4 v[178:179], off\n"
"v_add_co_u32_e32 v178, vcc, s55, v178\n"
"v_addc_co_u32_e32 v179, vcc, 0, v179, vcc\n"
"s_add_u32 m0, s69, 135168\n"
"s_nop 0\n"
"global_load_lds_dwordx4 v[178:179], off\n"
"v_add_co_u32_e32 v178, vcc, s55, v178\n"
"v_addc_co_u32_e32 v179, vcc, 0, v179, vcc\n"
"s_add_u32 m0, s69, 139264\n"
"s_nop 0\n"
"global_load_lds_dwordx4 v[178:179], off\n"
"v_add_co_u32_e32 v178, vcc, s55, v178\n"
"v_addc_co_u32_e32 v179, vcc, 0, v179, vcc\n"
"s_add_u32 m0, s69, 143360\n"
"s_nop 0\n"
"global_load_lds_dwordx4 v[178:179], off\n"
"s_waitcnt vmcnt(9)\n"
"s_barrier\n"
"ds_read_b64_tr_b16 a[72:73], v182 offset:8192\n"
"ds_read_b64_tr_b16 a[74:75], v183 offset:8192\n"
"ds_read_b64_tr_b16 a[76:77], v182 offset:12288\n"
"ds_read_b64_tr_b16 a[78:79], v183 offset:12288\n"
"ds_read_b64_tr_b16 a[80:81], v182 offset:16384\n"
"ds_read_b64_tr_b16 a[82:83], v183 offset:16384\n"
"ds_read_b64_tr_b16 a[84:85], v182 offset:20480\n"
"ds_read_b64_tr_b16 a[86:87], v183 offset:20480\n"
"ds_read_b64_tr_b16 a[88:89], v182 offset:24576\n"
"ds_read_b64_tr_b16 a[90:91], v183 offset:24576\n"
"ds_read_b64_tr_b16 a[92:93], v182 offset:28672\n"
"ds_read_b64_tr_b16 a[94:95], v183 offset:28672\n"
"ds_read_b64_tr_b16 a[96:97], v182 offset:32768\n"
"ds_read_b64_tr_b16 a[98:99], v183 offset:32768\n"
"ds_read_b64_tr_b16 a[100:101], v182 offset:36864\n"
"s_waitcnt lgkmcnt(8)\n"
"ds_read_b64_tr_b16 a[102:103], v183 offset:36864\n"
"ds_read_b64_tr_b16 a[104:105], v182 offset:40960\n"
"ds_read_b64_tr_b16 a[106:107], v183 offset:40960\n"
"s_waitcnt lgkmcnt(0)\n"
"s_barrier\n"
"v_cmp_ne_u32_e64 s[80:81], s97, v198\n"
"s_nop 1\n"
"s_and_b64 s[80:81], s[80:81], s[82:83]\n"
"s_cbranch_scc1 Lx0npack36_%=\n"
"s_mov_b32 s96, 1\n"
"s_mov_b32 s57, 0\n"
"s_mov_b32 s77, 36864\n"
"v_readlane_b32 s67, v6, s57\n"
"s_nop 0\n"
"s_mul_i32 s67, s67, s50\n"
"s_add_u32 s65, s67, s49\n"
"s_sub_u32 s64, 36864, s77\n"
"s_lshl_b32 s68, %0, 10\n"
"s_add_u32 s64, s64, s68\n"
"s_add_u32 m0, s64, 0\n"
"s_add_u32 s70, s65, 0\n"
"buffer_load_dwordx4 v2, s[36:39], s70 offen sc1 lds\n"
"s_add_u32 m0, s64, 4096\n"
"s_add_u32 s70, s65, 4096\n"
"buffer_load_dwordx4 v2, s[36:39], s70 offen sc1 lds\n"
"s_add_u32 m0, s64, 8192\n"
"s_add_u32 s70, s65, 8192\n"
"buffer_load_dwordx4 v2, s[36:39], s70 offen sc1 lds\n"
"s_add_u32 m0, s64, 12288\n"
"s_add_u32 s70, s65, 12288\n"
"buffer_load_dwordx4 v2, s[36:39], s70 offen sc1 lds\n"
"s_add_u32 m0, s64, 16384\n"
"s_add_u32 s70, s65, 16384\n"
"buffer_load_dwordx4 v2, s[36:39], s70 offen sc1 lds\n"
"s_add_u32 m0, s64, 20480\n"
"s_add_u32 s70, s65, 20480\n"
"buffer_load_dwordx4 v2, s[36:39], s70 offen sc1 lds\n"
"s_add_u32 m0, s64, 24576\n"
"s_add_u32 s70, s65, 24576\n"
"buffer_load_dwordx4 v2, s[36:39], s70 offen sc1 lds\n"
"s_add_u32 m0, s64, 28672\n"
"s_add_u32 s70, s65, 28672\n"
"buffer_load_dwordx4 v2, s[36:39], s70 offen sc1 lds\n"
"s_add_u32 m0, s64, 32768\n"
"s_add_u32 s70, s65, 32768\n"
"buffer_load_dwordx4 v2, s[36:39], s70 offen sc1 lds\n"
"Lx0npack36_%=:\n"
"s_cmp_eq_u32 s96, 0\n"
"s_cbranch_scc1 Lw7apack36_%=\n"
"s_waitcnt vmcnt(9)\n"
"s_branch Lw7bpack36_%=\n"
"Lw7apack36_%=:\n"
"s_waitcnt vmcnt(0)\n"
"Lw7bpack36_%=:\n"
"s_barrier\n"
"ds_read_b64_tr_b16 a[108:109], v182 offset:45056\n"
"ds_read_b64_tr_b16 a[110:111], v183 offset:45056\n"
"ds_read_b64_tr_b16 a[112:113], v182 offset:49152\n"
"ds_read_b64_tr_b16 a[114:115], v183 offset:49152\n"
"ds_read_b64_tr_b16 a[116:117], v182 offset:53248\n"
"ds_read_b64_tr_b16 a[118:119], v183 offset:53248\n"
"ds_read_b64_tr_b16 a[120:121], v182 offset:57344\n"
"ds_read_b64_tr_b16 a[122:123], v183 offset:57344\n"
"ds_read_b64_tr_b16 a[124:125], v182 offset:61440\n"
"ds_read_b64_tr_b16 a[126:127], v183 offset:61440\n"
"ds_read_b64_tr_b16 a[128:129], v184\n"
"ds_read_b64_tr_b16 a[130:131], v185\n"
"ds_read_b64_tr_b16 a[132:133], v184 offset:4096\n"
"ds_read_b64_tr_b16 a[134:135], v185 offset:4096\n"
"ds_read_b64_tr_b16 a[136:137], v184 offset:8192\n"
"s_waitcnt lgkmcnt(8)\n"
"ds_read_b64_tr_b16 a[138:139], v185 offset:8192\n"
"ds_read_b64_tr_b16 a[140:141], v184 offset:12288\n"
"ds_read_b64_tr_b16 a[142:143], v185 offset:12288\n"
"s_waitcnt lgkmcnt(0)\n"
"s_barrier\n"
"s_lshl_b32 s55, %0, 10\n"
"s_mov_b32 s88, 0xffff\n"
"s_mov_b32 s89, 0x0\n"
"v_lshrrev_b32 v212, 2, v0\n"
"v_add_u32 v212, 86016, v212\n"
"v_lshlrev_b32 v192, 2, v10\n"
"v_add_u32 v192, 86528, v192\n"
"ds_write_b32 v192, v7\n"
"ds_write_b32 v192, v15 offset:256\n"
"s_mov_b32 s57, 0\n"
"s_mov_b32 s59, 0\n"
"s_mov_b32 s60, -1\n"
"s_mov_b32 s61, -1\n"
"s_mov_b32 s77, 36864\n"
"s_waitcnt lgkmcnt(0)\n"
"s_cmp_lg_u32 s96, 0\n"
"s_cbranch_scc1 Lbwipack36_%=\n"
"s_cmp_eq_u32 %0, 0\n"
"s_cbranch_scc0 Lbwipack36_%=\n"
"s_cmp_lg_u32 s75, 0\n"
"s_cbranch_scc1 Lbwipack36_%=\n"
"v_readlane_b32 s72, v7, s57\n"
"s_nop 3\n"
"s_mov_b32 s71, 0\n"
"s_cmp_lt_i32 s74, 0\n"
"s_cbranch_scc1 Lplipack36_%=\n"
"s_sub_u32 s67, s57, s74\n"
"s_cmp_gt_u32 s67, 3\n"
"s_cbranch_scc1 Lplipack36_%=\n"
"s_add_u32 s68, s57, 2\n"
"s_cmp_ge_u32 s68, s48\n"
"s_cbranch_scc1 Lplipack36_%=\n"
"s_mul_i32 s67, s67, 240\n"
"s_add_u32 s67, s67, s73\n"
"s_sub_u32 s67, s67, 40\n"
"s_mov_b32 s76, 0\n"
"Lslipack36_%=:\n"
"s_memrealtime s[84:85]\n"
"s_waitcnt lgkmcnt(0)\n"
"s_sub_u32 s68, s84, s67\n"
"s_cmp_ge_i32 s68, 0\n"
"s_cbranch_scc1 Lplipack36_%=\n"
"s_sleep 2\n"
"s_mov_b32 s71, 1\n"
"s_add_u32 s76, s76, 1\n"
"s_cmp_lt_u32 s76, 32768\n"
"s_cbranch_scc1 Lslipack36_%=\n"
"Lplipack36_%=:\n"
"s_mov_b32 s76, 0\n"
"Lppipack36_%=:\n"
"buffer_load_dword v192, v5, s[44:47], s72 offen sc1\n"
"s_waitcnt vmcnt(0)\n"
"v_cmp_ne_u32_e64 s[80:81], s97, v192\n"
"s_nop 1\n"
"s_and_b64 s[80:81], s[80:81], s[82:83]\n"
"s_cbranch_scc0 Lrdipack36_%=\n"
"s_sleep 3\n"
"s_add_u32 s76, s76, 1\n"
"s_cmp_lt_u32 s76, 32768\n"
"s_cbranch_scc1 Lppipack36_%=\n"
"s_mov_b32 s75, 1\n"
"v_mov_b32 v192, 16384\n"
"v_mov_b32 v193, 7000\n"
"buffer_store_dword v193, v192, s[44:47], 0 offen sc1\n"
"Lrdipack36_%=:\n"
"s_mov_b32 s74, -1\n"
"s_cmp_eq_u32 s76, 0\n"
"s_cbranch_scc1 Lbwipack36_%=\n"
"s_memrealtime s[84:85]\n"
"s_waitcnt lgkmcnt(0)\n"
"s_mov_b32 s73, s84\n"
"s_mov_b32 s74, s57\n"
"Lbwipack36_%=:\n"
"s_barrier\n"
"s_cmp_lg_u32 s96, 0\n"
"s_cbranch_scc1 Lx0spack36_%=\n"
"v_readlane_b32 s67, v6, s57\n"
"s_nop 0\n"
"s_mul_i32 s67, s67, s50\n"
"s_add_u32 s65, s67, s49\n"
"s_sub_u32 s64, 36864, s77\n"
"s_lshl_b32 s68, %0, 10\n"
"s_add_u32 s64, s64, s68\n"
"s_add_u32 m0, s64, 0\n"
"s_add_u32 s70, s65, 0\n"
"buffer_load_dwordx4 v2, s[36:39], s70 offen sc1 lds\n"
"s_add_u32 m0, s64, 4096\n"
"s_add_u32 s70, s65, 4096\n"
"buffer_load_dwordx4 v2, s[36:39], s70 offen sc1 lds\n"
"s_add_u32 m0, s64, 8192\n"
"s_add_u32 s70, s65, 8192\n"
"buffer_load_dwordx4 v2, s[36:39], s70 offen sc1 lds\n"
"s_add_u32 m0, s64, 12288\n"
"s_add_u32 s70, s65, 12288\n"
"buffer_load_dwordx4 v2, s[36:39], s70 offen sc1 lds\n"
"s_add_u32 m0, s64, 16384\n"
"s_add_u32 s70, s65, 16384\n"
"buffer_load_dwordx4 v2, s[36:39], s70 offen sc1 lds\n"
"s_add_u32 m0, s64, 20480\n"
"s_add_u32 s70, s65, 20480\n"
"buffer_load_dwordx4 v2, s[36:39], s70 offen sc1 lds\n"
"s_add_u32 m0, s64, 24576\n"
"s_add_u32 s70, s65, 24576\n"
"buffer_load_dwordx4 v2, s[36:39], s70 offen sc1 lds\n"
"s_add_u32 m0, s64, 28672\n"
"s_add_u32 s70, s65, 28672\n"
"buffer_load_dwordx4 v2, s[36:39], s70 offen sc1 lds\n"
"s_add_u32 m0, s64, 32768\n"
"s_add_u32 s70, s65, 32768\n"
"buffer_load_dwordx4 v2, s[36:39], s70 offen sc1 lds\n"
"Lx0spack36_%=:\n"
"s_waitcnt vmcnt(0) lgkmcnt(0)\n"
"s_barrier\n"
"s_sub_u32 s77, 36864, s77\n"
"v_add_u32 v1, s77, v0\n"
"ds_read_b128 v[80:83], v1\n"
"ds_read_b128 v[84:87], v1 offset:1024\n"
"ds_read_b128 v[88:91], v1 offset:2048\n"
"ds_read_b128 v[92:95], v1 offset:3072\n"
"s_mov_b32 s58, 1\n"
"Ltoppack36_%=:\n"
"s_cmp_eq_u32 s59, 1\n"
"s_cbranch_scc1 LPApack36_%=\n"
"s_cmp_eq_u32 s59, 2\n"
"s_cbranch_scc1 LPBpack36_%=\n"
"s_add_u32 s67, s57, 1\n"
"s_cmp_lt_u32 s67, s58\n"
"s_cbranch_scc0 Lnonpack36_%=\n"
"v_readlane_b32 s67, v6, s67\n"
"s_nop 0\n"
"s_mul_i32 s67, s67, s50\n"
"s_add_u32 s65, s67, s49\n"
"s_sub_u32 s64, 36864, s77\n"
"s_lshl_b32 s68, %0, 10\n"
"s_add_u32 s64, s64, s68\n"
"s_mov_b32 s62, -1\n"
"s_cmp_ge_u32 s58, s48\n"
"s_cbranch_scc1 Lq1pack36_%=\n"
"s_sub_u32 s68, s58, s57\n"
"s_cmp_le_u32 s68, 4\n"
"s_cselect_b32 s62, s58, -1\n"
"Lq1pack36_%=:\n"
"s_max_i32 s68, s60, s61\n"
"s_mov_b32 s63, 0\n"
"s_cmp_lt_i32 s68, 0\n"
"s_cbranch_scc1 Lq2pack36_%=\n"
"s_mov_b32 s63, 1\n"
"Lq2pack36_%=:\n"
"s_waitcnt lgkmcnt(3)\n"
"v_mfma_f32_32x32x16_bf16 v[16:31], v[128:131], v[80:83], 0\n"
"ds_read_b128 v[96:99], v1 offset:4096\n"
"v_mfma_f32_32x32x16_bf16 v[32:47], a[0:3], v[80:83], 0\n"
"s_waitcnt lgkmcnt(3)\n"
"v_mfma_f32_32x32x16_bf16 v[16:31], v[132:135], v[84:87], v[16:31]\n"
"ds_read_b128 v[100:103], v1 offset:5120\n"
"v_mfma_f32_32x32x16_bf16 v[32:47], a[4:7], v[84:87], v[32:47]\n"
"s_add_u32 m0, s64, 0\n"
"s_add_u32 s70, s65, 0\n"
"buffer_load_dwordx4 v2, s[36:39], s70 offen sc1 lds\n"
"s_waitcnt lgkmcnt(3)\n"
"v_mfma_f32_32x32x16_bf16 v[16:31], v[136:139], v[88:91], v[16:31]\n"
"ds_read_b128 v[80:83], v1 offset:6144\n"
"v_mfma_f32_32x32x16_bf16 v[32:47], a[8:11], v[88:91], v[32:47]\n"
"s_waitcnt lgkmcnt(3)\n"
"v_mfma_f32_32x32x16_bf16 v[16:31], v[140:143], v[92:95], v[16:31]\n"
"ds_read_b128 v[84:87], v1 offset:7168\n"
"v_mfma_f32_32x32x16_bf16 v[32:47], a[12:15], v[92:95], v[32:47]\n"
"s_add_u32 m0, s64, 4096\n"
"s_add_u32 s70, s65, 4096\n"
"buffer_load_dwordx4 v2, s[36:39], s70 offen sc1 lds\n"
"s_waitcnt lgkmcnt(3)\n"
"v_mfma_f32_32x32x16_bf16 v[16:31], v[144:147], v[96:99], v[16:31]\n"
"ds_read_b128 v[88:91], v1 offset:8192\n"
"v_mfma_f32_32x32x16_bf16 v[32:47], a[16:19], v[96:99], v[32:47]\n"
"s_waitcnt lgkmcnt(3)\n"
"v_mfma_f32_32x32x16_bf16 v[16:31], v[148:151], v[100:103], v[16:31]\n"
"ds_read_b128 v[92:95], v1 offset:9216\n"
"v_mfma_f32_32x32x16_bf16 v[32:47], a[20:23], v[100:103], v[32:47]\n"
"s_add_u32 m0, s64, 8192\n"
"s_add_u32 s70, s65, 8192\n"
"buffer_load_dwordx4 v2, s[36:39], s70 offen sc1 lds\n"
"s_waitcnt lgkmcnt(3)\n"
"v_mfma_f32_32x32x16_bf16 v[16:31], v[152:155], v[80:83], v[16:31]\n"
"ds_read_b128 v[96:99], v1 offset:10240\n"
"v_mfma_f32_32x32x16_bf16 v[32:47], a[24:27], v[80:83], v[32:47]\n"
"s_waitcnt lgkmcnt(3)\n"
"v_mfma_f32_32x32x16_bf16 v[16:31], v[156:159], v[84:87], v[16:31]\n"
"ds_read_b128 v[100:103], v1 offset:11264\n"
"v_mfma_f32_32x32x16_bf16 v[32:47], a[28:31], v[84:87], v[32:47]\n"
"s_add_u32 m0, s64, 12288\n"
"s_add_u32 s70, s65, 12288\n"
"buffer_load_dwordx4 v2, s[36:39], s70 offen sc1 lds\n"
"s_waitcnt lgkmcnt(3)\n"
"v_mfma_f32_32x32x16_bf16 v[16:31], a[144:147], v[88:91], v[16:31]\n"
"ds_read_b128 v[80:83], v1 offset:12288\n"
"v_mfma_f32_32x32x16_bf16 v[32:47], a[32:35], v[88:91], v[32:47]\n"
"s_waitcnt lgkmcnt(3)\n"
"v_mfma_f32_32x32x16_bf16 v[16:31], a[148:151], v[92:95], v[16:31]\n"
"ds_read_b128 v[84:87], v1 offset:13312\n"
"v_mfma_f32_32x32x16_bf16 v[32:47], a[36:39], v[92:95], v[32:47]\n"
"s_add_u32 m0, s64, 16384\n"
"s_add_u32 s70, s65, 16384\n"
"buffer_load_dwordx4 v2, s[36:39], s70 offen sc1 lds\n"
"s_waitcnt lgkmcnt(3)\n"
"v_mfma_f32_32x32x16_bf16 v[16:31], a[152:155], v[96:99], v[16:31]\n"
"ds_read_b128 v[88:91], v1 offset:14336\n"
"v_mfma_f32_32x32x16_bf16 v[32:47], a[40:43], v[96:99], v[32:47]\n"
"s_waitcnt lgkmcnt(3)\n"
"v_mfma_f32_32x32x16_bf16 v[16:31], a[156:159], v[100:103], v[16:31]\n"
"ds_read_b128 v[92:95], v1 offset:15360\n"
"v_mfma_f32_32x32x16_bf16 v[32:47], a[44:47], v[100:103], v[32:47]\n"
"s_add_u32 m0, s64, 20480\n"
"s_add_u32 s70, s65, 20480\n"
"buffer_load_dwordx4 v2, s[36:39], s70 offen sc1 lds\n"
"s_waitcnt lgkmcnt(3)\n"
"v_mfma_f32_32x32x16_bf16 v[16:31], a[160:163], v[80:83], v[16:31]\n"
"ds_read_b128 v[96:99], v1 offset:16384\n"
"v_mfma_f32_32x32x16_bf16 v[32:47], a[48:51], v[80:83], v[32:47]\n"
"s_waitcnt lgkmcnt(3)\n"
"v_mfma_f32_32x32x16_bf16 v[16:31], a[164:167], v[84:87], v[16:31]\n"
"ds_read_b128 v[100:103], v1 offset:17408\n"
"v_mfma_f32_32x32x16_bf16 v[32:47], a[52:55], v[84:87], v[32:47]\n"
"s_add_u32 m0, s64, 24576\n"
"s_add_u32 s70, s65, 24576\n"
"buffer_load_dwordx4 v2, s[36:39], s70 offen sc1 lds\n"
"s_waitcnt lgkmcnt(3)\n"
"v_mfma_f32_32x32x16_bf16 v[16:31], a[168:171], v[88:91], v[16:31]\n"
"ds_read_b128 v[80:83], v1 offset:18432\n"
"v_mfma_f32_32x32x16_bf16 v[32:47], a[56:59], v[88:91], v[32:47]\n"
"s_cmp_eq_u32 s63, 0\n"
"s_cbranch_scc1 LnmDpack36_%=\n"
"s_waitcnt vmcnt(7)\n"
"s_barrier\n"
"s_cmp_lt_i32 s60, 0\n"
"s_cbranch_scc1 LnsmaDpack36_%=\n"
"v_readlane_b32 s67, v6, s60\n"
"s_nop 0\n"
"s_mul_i32 s67, s67, s54\n"
"s_add_u32 s67, s67, s53\n"
"buffer_store_dword v4, v3, s[44:47], s67 offen sc1\n"
"LnsmaDpack36_%=:\n"
"s_cmp_lt_i32 s61, 0\n"
"s_cbranch_scc1 LnsmbDpack36_%=\n"
"v_readlane_b32 s67, v6, s61\n"
"s_nop 0\n"
"s_mul_i32 s67, s67, s54\n"
"s_add_u32 s67, s67, s53\n"
"buffer_store_dword v4, v3, s[44:47], s67 offen sc1\n"
"LnsmbDpack36_%=:\n"
"LnmDpack36_%=:\n"
"s_waitcnt lgkmcnt(3)\n"
"v_mfma_f32_32x32x16_bf16 v[16:31], a[172:175], v[92:95], v[16:31]\n"
"ds_read_b128 v[84:87], v1 offset:19456\n"
"v_mfma_f32_32x32x16_bf16 v[32:47], a[60:63], v[92:95], v[32:47]\n"
"s_add_u32 m0, s64, 28672\n"
"s_add_u32 s70, s65, 28672\n"
"buffer_load_dwordx4 v2, s[36:39], s70 offen sc1 lds\n"
"s_waitcnt lgkmcnt(3)\n"
"v_mfma_f32_32x32x16_bf16 v[16:31], a[176:179], v[96:99], v[16:31]\n"
"ds_read_b128 v[88:91], v1 offset:20480\n"
"v_mfma_f32_32x32x16_bf16 v[32:47], a[64:67], v[96:99], v[32:47]\n"
"s_waitcnt lgkmcnt(3)\n"
"v_mfma_f32_32x32x16_bf16 v[16:31], a[180:183], v[100:103], v[16:31]\n"
"ds_read_b128 v[92:95], v1 offset:21504\n"
"v_mfma_f32_32x32x16_bf16 v[32:47], a[68:71], v[100:103], v[32:47]\n"
"s_add_u32 m0, s64, 32768\n"
"s_add_u32 s70, s65, 32768\n"
"buffer_load_dwordx4 v2, s[36:39], s70 offen sc1 lds\n"
"s_waitcnt lgkmcnt(3)\n"
"v_mfma_f32_32x32x16_bf16 v[16:31], a[184:187], v[80:83], v[16:31]\n"
"ds_read_b128 v[96:99], v1 offset:22528\n"
"v_mfma_f32_32x32x16_bf16 v[32:47], a[72:75], v[80:83], v[32:47]\n"
"s_cmp_lt_i32 s62, 0\n"
"s_cbranch_scc1 LnpDpack36_%=\n"
"s_and_b32 s72, s57, 1\n"
"s_lshl_b32 s72, s72, 8\n"
"s_add_u32 s72, s72, 86016\n"
"s_waitcnt lgkmcnt(0)\n"
"s_lshl_b32 s67, s62, 2\n"
"s_add_u32 s67, s67, 86528\n"
"v_add_u32 v192, s67, v8\n"
"ds_read_b32 v192, v192\n"
"s_waitcnt lgkmcnt(0)\n"
"v_add_u32 v192, v192, v9\n"
"s_mov_b32 m0, s72\n"
"s_mov_b64 exec, s[78:79]\n"
"buffer_load_dword v192, s[44:47], 0 offen sc1 lds\n"
"s_mov_b64 exec, -1\n"
"s_waitcnt lgkmcnt(0)\n"
"LnpDpack36_%=:\n"
"s_waitcnt lgkmcnt(3)\n"
"v_mfma_f32_32x32x16_bf16 v[16:31], a[188:191], v[84:87], v[16:31]\n"
"ds_read_b128 v[100:103], v1 offset:23552\n"
"v_mfma_f32_32x32x16_bf16 v[32:47], a[76:79], v[84:87], v[32:47]\n"
"s_waitcnt lgkmcnt(3)\n"
"v_mfma_f32_32x32x16_bf16 v[16:31], a[192:195], v[88:91], v[16:31]\n"
"ds_read_b128 v[80:83], v1 offset:24576\n"
"v_mfma_f32_32x32x16_bf16 v[32:47], a[80:83], v[88:91], v[32:47]\n"
"s_waitcnt lgkmcnt(3)\n"
"v_mfma_f32_32x32x16_bf16 v[16:31], a[196:199], v[92:95], v[16:31]\n"
"ds_read_b128 v[84:87], v1 offset:25600\n"
"v_mfma_f32_32x32x16_bf16 v[32:47], a[84:87], v[92:95], v[32:47]\n"
"s_waitcnt lgkmcnt(3)\n"
"v_mfma_f32_32x32x16_bf16 v[16:31], a[200:203], v[96:99], v[16:31]\n"
"ds_read_b128 v[88:91], v1 offset:26624\n"
"v_mfma_f32_32x32x16_bf16 v[32:47], a[88:91], v[96:99], v[32:47]\n"
"s_waitcnt lgkmcnt(3)\n"
"v_mfma_f32_32x32x16_bf16 v[16:31], a[204:207], v[100:103], v[16:31]\n"
"ds_read_b128 v[92:95], v1 offset:27648\n"
"v_mfma_f32_32x32x16_bf16 v[32:47], a[92:95], v[100:103], v[32:47]\n"
"s_waitcnt lgkmcnt(3)\n"
"v_mfma_f32_32x32x16_bf16 v[16:31], a[208:211], v[80:83], v[16:31]\n"
"ds_read_b128 v[96:99], v1 offset:28672\n"
"v_mfma_f32_32x32x16_bf16 v[32:47], a[96:99], v[80:83], v[32:47]\n"
"s_waitcnt lgkmcnt(3)\n"
"v_mfma_f32_32x32x16_bf16 v[16:31], a[212:215], v[84:87], v[16:31]\n"
"ds_read_b128 v[100:103], v1 offset:29696\n"
"v_mfma_f32_32x32x16_bf16 v[32:47], a[100:103], v[84:87], v[32:47]\n"
"s_waitcnt lgkmcnt(3)\n"
"v_mfma_f32_32x32x16_bf16 v[16:31], a[216:219], v[88:91], v[16:31]\n"
"ds_read_b128 v[80:83], v1 offset:30720\n"
"v_mfma_f32_32x32x16_bf16 v[32:47], a[104:107], v[88:91], v[32:47]\n"
"s_waitcnt lgkmcnt(3)\n"
"v_mfma_f32_32x32x16_bf16 v[16:31], a[220:223], v[92:95], v[16:31]\n"
"ds_read_b128 v[84:87], v1 offset:31744\n"
"v_mfma_f32_32x32x16_bf16 v[32:47], a[108:111], v[92:95], v[32:47]\n"
"s_waitcnt lgkmcnt(3)\n"
"v_mfma_f32_32x32x16_bf16 v[16:31], a[224:227], v[96:99], v[16:31]\n"
"ds_read_b128 v[88:91], v1 offset:32768\n"
"v_mfma_f32_32x32x16_bf16 v[32:47], a[112:115], v[96:99], v[32:47]\n"
"s_waitcnt lgkmcnt(3)\n"
"v_mfma_f32_32x32x16_bf16 v[16:31], a[228:231], v[100:103], v[16:31]\n"
"ds_read_b128 v[92:95], v1 offset:33792\n"
"v_mfma_f32_32x32x16_bf16 v[32:47], a[116:119], v[100:103], v[32:47]\n"
"s_waitcnt lgkmcnt(3)\n"
"v_mfma_f32_32x32x16_bf16 v[16:31], a[232:235], v[80:83], v[16:31]\n"
"ds_read_b128 v[96:99], v1 offset:34816\n"
"v_mfma_f32_32x32x16_bf16 v[32:47], a[120:123], v[80:83], v[32:47]\n"
"s_waitcnt lgkmcnt(3)\n"
"v_mfma_f32_32x32x16_bf16 v[16:31], a[236:239], v[84:87], v[16:31]\n"
"ds_read_b128 v[100:103], v1 offset:35840\n"
"v_mfma_f32_32x32x16_bf16 v[32:47], a[124:127], v[84:87], v[32:47]\n"
"s_waitcnt lgkmcnt(3)\n"
"v_mfma_f32_32x32x16_bf16 v[16:31], a[240:243], v[88:91], v[16:31]\n"
"v_mfma_f32_32x32x16_bf16 v[32:47], a[128:131], v[88:91], v[32:47]\n"
"s_waitcnt lgkmcnt(2)\n"
"v_mfma_f32_32x32x16_bf16 v[16:31], a[244:247], v[92:95], v[16:31]\n"
"v_mfma_f32_32x32x16_bf16 v[32:47], a[132:135], v[92:95], v[32:47]\n"
"s_waitcnt lgkmcnt(1)\n"
"v_mfma_f32_32x32x16_bf16 v[16:31], a[248:251], v[96:99], v[16:31]\n"
"v_mfma_f32_32x32x16_bf16 v[32:47], a[136:139], v[96:99], v[32:47]\n"
"s_waitcnt lgkmcnt(0)\n"
"v_mfma_f32_32x32x16_bf16 v[16:31], a[252:255], v[100:103], v[16:31]\n"
"v_mfma_f32_32x32x16_bf16 v[32:47], a[140:143], v[100:103], v[32:47]\n"
"s_waitcnt vmcnt(0) lgkmcnt(0)\n"
"s_barrier\n"
"s_mov_b32 s99, 0\n"
"s_sub_u32 s77, 36864, s77\n"
"v_add_u32 v1, s77, v0\n"
"ds_read_b128 v[80:83], v1\n"
"ds_read_b128 v[84:87], v1 offset:1024\n"
"ds_read_b128 v[88:91], v1 offset:2048\n"
"ds_read_b128 v[92:95], v1 offset:3072\n"
"s_branch Le1pack36_%=\n"
"LPApack36_%=:\n"
"s_waitcnt lgkmcnt(3)\n"
"v_mfma_f32_32x32x16_bf16 v[48:63], v[128:131], v[80:83], 0\n"
"ds_read_b128 v[96:99], v1 offset:4096\n"
"s_add_u32 s92, s57, 1\n"
"v_readlane_b32 s92, v6, s92\n"
"s_sub_u32 s64, 36864, s77\n"
"v_mfma_f32_32x32x16_bf16 v[64:79], a[0:3], v[80:83], 0\n"
"s_add_u32 s64, s64, s55\n"
"s_mul_i32 s92, s92, s50\n"
"s_add_u32 s65, s92, s49\n"
"s_sub_u32 s90, s57, 1\n"
"s_and_b32 s90, s90, 1\n"
"s_waitcnt lgkmcnt(3)\n"
"v_mfma_f32_32x32x16_bf16 v[48:63], v[132:135], v[84:87], v[48:63]\n"
"ds_read_b128 v[100:103], v1 offset:5120\n"
"s_lshl_b32 s90, s90, 8\n"
"v_add_u32 v194, s90, v212\n"
"ds_read_b32 v195, v194\n"
"v_mfma_f32_32x32x16_bf16 v[64:79], a[4:7], v[84:87], v[64:79]\n"
"s_add_u32 m0, s64, 0\n"
"s_add_u32 s92, s65, 0\n"
"buffer_load_dwordx4 v2, s[36:39], s92 offen sc1 lds\n"
"s_sub_u32 s93, s57, 1\n"
"s_waitcnt lgkmcnt(4)\n"
"v_mfma_f32_32x32x16_bf16 v[48:63], v[136:139], v[88:91], v[48:63]\n"
"ds_read_b128 v[80:83], v1 offset:6144\n"
"v_readlane_b32 s93, v6, s93\n"
"v_mfma_f32_32x32x16_bf16 v[64:79], a[8:11], v[88:91], v[64:79]\n"
"s_add_u32 m0, s64, 4096\n"
"s_add_u32 s92, s65, 4096\n"
"buffer_load_dwordx4 v2, s[36:39], s92 offen sc1 lds\n"
"s_mul_i32 s93, s93, s52\n"
"s_waitcnt lgkmcnt(4)\n"
"v_mfma_f32_32x32x16_bf16 v[48:63], v[140:143], v[92:95], v[48:63]\n"
"ds_read_b128 v[84:87], v1 offset:7168\n"
"s_waitcnt lgkmcnt(2)\n"
"v_cmp_ne_u32_e64 s[80:81], s97, v195\n"
"s_add_u32 s66, s93, s51\n"
"v_readlane_b32 s93, v6, s60\n"
"v_mfma_f32_32x32x16_bf16 v[64:79], a[12:15], v[92:95], v[64:79]\n"
"s_add_u32 m0, s64, 8192\n"
"s_add_u32 s92, s65, 8192\n"
"buffer_load_dwordx4 v2, s[36:39], s92 offen sc1 lds\n"
"s_and_b64 s[80:81], s[80:81], s[88:89]\n"
"v_mfma_f32_32x32x16_bf16 v[48:63], v[144:147], v[96:99], v[48:63]\n"
"ds_read_b128 v[88:91], v1 offset:8192\n"
"s_ff1_i32_b64 s90, s[80:81]\n"
"s_cmp_lt_i32 s90, 0\n"
"s_cselect_b32 s90, 16, s90\n"
"s_mul_i32 s90, s90, 65536\n"
"v_mfma_f32_32x32x16_bf16 v[64:79], a[16:19], v[96:99], v[64:79]\n"
"s_add_u32 m0, s64, 12288\n"
"s_add_u32 s92, s65, 12288\n"
"buffer_load_dwordx4 v2, s[36:39], s92 offen sc1 lds\n"
"s_lshr_b32 s90, s90, 16\n"
"v_mfma_f32_32x32x16_bf16 v[48:63], v[148:151], v[100:103], v[48:63]\n"
"ds_read_b128 v[92:95], v1 offset:9216\n"
"s_sub_u32 s91, s48, s86\n"
"s_min_u32 s90, s90, s91\n"
"s_add_u32 s90, s90, s86\n"
"s_mul_i32 s93, s93, s54\n"
"v_mfma_f32_32x32x16_bf16 v[64:79], a[20:23], v[100:103], v[64:79]\n"
"s_add_u32 m0, s64, 16384\n"
"s_add_u32 s92, s65, 16384\n"
"buffer_load_dwordx4 v2, s[36:39], s92 offen sc1 lds\n"
"s_add_u32 s93, s93, s53\n"
"s_waitcnt lgkmcnt(3)\n"
"v_mfma_f32_32x32x16_bf16 v[48:63], v[152:155], v[80:83], v[48:63]\n"
"ds_read_b128 v[96:99], v1 offset:10240\n"
"s_cmp_lt_i32 s86, 0\n"
"s_cselect_b32 s58, s58, s90\n"
"v_mfma_f32_32x32x16_bf16 v[64:79], a[24:27], v[80:83], v[64:79]\n"
"s_add_u32 m0, s64, 20480\n"
"s_add_u32 s92, s65, 20480\n"
"buffer_load_dwordx4 v2, s[36:39], s92 offen sc1 lds\n"
"s_waitcnt lgkmcnt(3)\n"
"v_mfma_f32_32x32x16_bf16 v[48:63], v[156:159], v[84:87], v[48:63]\n"
"ds_read_b128 v[100:103], v1 offset:11264\n"
"s_cmp_lt_u32 s58, s48\n"
"s_cselect_b32 s62, s58, -1\n"
"s_sub_u32 s90, s58, s57\n"
"v_mfma_f32_32x32x16_bf16 v[64:79], a[28:31], v[84:87], v[64:79]\n"
"s_add_u32 m0, s64, 24576\n"
"s_add_u32 s92, s65, 24576\n"
"buffer_load_dwordx4 v2, s[36:39], s92 offen sc1 lds\n"
"s_waitcnt lgkmcnt(3)\n"
"v_mfma_f32_32x32x16_bf16 v[48:63], a[144:147], v[88:91], v[48:63]\n"
"ds_read_b128 v[80:83], v1 offset:12288\n"
"s_cmp_le_u32 s90, 4\n"
"s_cselect_b32 s62, s62, -1\n"
"s_max_i32 s90, s62, 0\n"
"v_mfma_f32_32x32x16_bf16 v[64:79], a[32:35], v[88:91], v[64:79]\n"
"s_add_u32 m0, s64, 28672\n"
"s_add_u32 s92, s65, 28672\n"
"buffer_load_dwordx4 v2, s[36:39], s92 offen sc1 lds\n"
"s_lshl_b32 s90, s90, 2\n"
"s_waitcnt lgkmcnt(3)\n"
"v_mfma_f32_32x32x16_bf16 v[48:63], a[148:151], v[92:95], v[48:63]\n"
"ds_read_b128 v[84:87], v1 offset:13312\n"
"s_add_u32 s90, s90, 86528\n"
"v_add_u32 v196, s90, v8\n"
"ds_read_b32 v196, v196\n"
"v_mfma_f32_32x32x16_bf16 v[64:79], a[36:39], v[92:95], v[64:79]\n"
"s_add_u32 m0, s64, 32768\n"
"s_add_u32 s92, s65, 32768\n"
"buffer_load_dwordx4 v2, s[36:39], s92 offen sc1 lds\n"
"s_waitcnt lgkmcnt(4)\n"
"v_mfma_f32_32x32x16_bf16 v[48:63], a[152:155], v[96:99], v[48:63]\n"
"ds_read_b128 v[88:91], v1 offset:14336\n"
"s_cmp_ge_i32 s62, 0\n"
"s_cselect_b64 s[94:95], s[78:79], 0\n"
"s_and_b32 s91, s57, 1\n"
"v_mfma_f32_32x32x16_bf16 v[64:79], a[40:43], v[96:99], v[64:79]\n"
"s_lshl_b32 s91, s91, 8\n"
"s_add_u32 s91, s91, 86016\n"
"s_add_u32 s90, s57, 2\n"
"s_cmp_lt_u32 s90, s58\n"
"s_cselect_b32 s87, 1, 0\n"
"s_waitcnt lgkmcnt(4)\n"
"v_mfma_f32_32x32x16_bf16 v[48:63], a[156:159], v[100:103], v[48:63]\n"
"ds_read_b128 v[92:95], v1 offset:15360\n"
"s_cmp_lt_i32 s60, 0\n"
"s_cselect_b32 s93, 0x7ffffff0, s93\n"
"v_add_u32 v197, s93, v3\n"
"v_mfma_f32_32x32x16_bf16 v[64:79], a[44:47], v[100:103], v[64:79]\n"
"v_cvt_pk_bf16_f32 v160, v16, v17\n"
"v_cvt_pk_bf16_f32 v161, v18, v19\n"
"v_cvt_pk_bf16_f32 v162, v20, v21\n"
"v_cvt_pk_bf16_f32 v163, v22, v23\n"
"v_cvt_pk_bf16_f32 v164, v24, v25\n"
"s_waitcnt lgkmcnt(4)\n"
"v_mfma_f32_32x32x16_bf16 v[48:63], a[160:163], v[80:83], v[48:63]\n"
"ds_read_b128 v[96:99], v1 offset:16384\n"
"s_waitcnt lgkmcnt(3)\n"
"v_add_u32 v196, v196, v9\n"
"v_cvt_pk_bf16_f32 v165, v26, v27\n"
"v_cvt_pk_bf16_f32 v166, v28, v29\n"
"v_mfma_f32_32x32x16_bf16 v[64:79], a[48:51], v[80:83], v[64:79]\n"
"s_mov_b32 m0, s91\n"
"s_mov_b64 exec, s[94:95]\n"
"buffer_load_dword v196, s[44:47], 0 offen sc1 lds\n"
"s_mov_b64 exec, -1\n"
"v_cvt_pk_bf16_f32 v167, v30, v31\n"
"v_mfma_f32_32x32x16_bf16 v[48:63], a[164:167], v[84:87], v[48:63]\n"
"ds_read_b128 v[100:103], v1 offset:17408\n"
"v_cvt_pk_bf16_f32 v168, v32, v33\n"
"v_cvt_pk_bf16_f32 v169, v34, v35\n"
"v_cvt_pk_bf16_f32 v170, v36, v37\n"
"v_cvt_pk_bf16_f32 v171, v38, v39\n"
"v_mfma_f32_32x32x16_bf16 v[64:79], a[52:55], v[84:87], v[64:79]\n"
"v_cvt_pk_bf16_f32 v172, v40, v41\n"
"v_cvt_pk_bf16_f32 v173, v42, v43\n"
"v_cvt_pk_bf16_f32 v174, v44, v45\n"
"v_cvt_pk_bf16_f32 v175, v46, v47\n"
"v_permlane32_swap_b32 v160, v162\n"
"s_waitcnt lgkmcnt(3)\n"
"v_mfma_f32_32x32x16_bf16 v[48:63], a[168:171], v[88:91], v[48:63]\n"
"ds_read_b128 v[80:83], v1 offset:18432\n"
"v_permlane32_swap_b32 v161, v163\n"
"v_permlane32_swap_b32 v164, v166\n"
"v_permlane32_swap_b32 v165, v167\n"
"v_mfma_f32_32x32x16_bf16 v[64:79], a[56:59], v[88:91], v[64:79]\n"
"v_permlane32_swap_b32 v168, v170\n"
"v_permlane32_swap_b32 v169, v171\n"
"v_permlane32_swap_b32 v172, v174\n"
"v_permlane32_swap_b32 v173, v175\n"
"s_waitcnt lgkmcnt(3)\n"
"v_mfma_f32_32x32x16_bf16 v[48:63], a[172:175], v[92:95], v[48:63]\n"
"ds_read_b128 v[84:87], v1 offset:19456\n"
"buffer_store_dwordx4 v[160:163], v11, s[40:43], s66 offen sc1\n"
"v_mfma_f32_32x32x16_bf16 v[64:79], a[60:63], v[92:95], v[64:79]\n"
"buffer_store_dwordx4 v[164:167], v12, s[40:43], s66 offen sc1\n"
"s_waitcnt lgkmcnt(3)\n"
"v_mfma_f32_32x32x16_bf16 v[48:63], a[176:179], v[96:99], v[48:63]\n"
"ds_read_b128 v[88:91], v1 offset:20480\n"
"buffer_store_dwordx4 v[168:171], v13, s[40:43], s66 offen sc1\n"
"v_mfma_f32_32x32x16_bf16 v[64:79], a[64:67], v[96:99], v[64:79]\n"
"buffer_store_dwordx4 v[172:175], v14, s[40:43], s66 offen sc1\n"
"s_waitcnt lgkmcnt(3)\n"
"v_mfma_f32_32x32x16_bf16 v[48:63], a[180:183], v[100:103], v[48:63]\n"
"ds_read_b128 v[92:95], v1 offset:21504\n"
"v_mfma_f32_32x32x16_bf16 v[64:79], a[68:71], v[100:103], v[64:79]\n"
"s_waitcnt lgkmcnt(3)\n"
"v_mfma_f32_32x32x16_bf16 v[48:63], a[184:187], v[80:83], v[48:63]\n"
"ds_read_b128 v[96:99], v1 offset:22528\n"
"v_mfma_f32_32x32x16_bf16 v[64:79], a[72:75], v[80:83], v[64:79]\n"
"s_waitcnt lgkmcnt(3)\n"
"v_mfma_f32_32x32x16_bf16 v[48:63], a[188:191], v[84:87], v[48:63]\n"
"ds_read_b128 v[100:103], v1 offset:23552\n"
"v_mfma_f32_32x32x16_bf16 v[64:79], a[76:79], v[84:87], v[64:79]\n"
"s_waitcnt lgkmcnt(3)\n"
"v_mfma_f32_32x32x16_bf16 v[48:63], a[192:195], v[88:91], v[48:63]\n"
"ds_read_b128 v[80:83], v1 offset:24576\n"
"v_mfma_f32_32x32x16_bf16 v[64:79], a[80:83], v[88:91], v[64:79]\n"
"s_waitcnt lgkmcnt(3)\n"
"v_mfma_f32_32x32x16_bf16 v[48:63], a[196:199], v[92:95], v[48:63]\n"
"ds_read_b128 v[84:87], v1 offset:25600\n"
"v_mfma_f32_32x32x16_bf16 v[64:79], a[84:87], v[92:95], v[64:79]\n"
"s_waitcnt lgkmcnt(3)\n"
"v_mfma_f32_32x32x16_bf16 v[48:63], a[200:203], v[96:99], v[48:63]\n"
"ds_read_b128 v[88:91], v1 offset:26624\n"
"v_mfma_f32_32x32x16_bf16 v[64:79], a[88:91], v[96:99], v[64:79]\n"
"s_waitcnt lgkmcnt(3)\n"
"v_mfma_f32_32x32x16_bf16 v[48:63], a[204:207], v[100:103], v[48:63]\n"
"ds_read_b128 v[92:95], v1 offset:27648\n"
"v_mfma_f32_32x32x16_bf16 v[64:79], a[92:95], v[100:103], v[64:79]\n"
"s_waitcnt lgkmcnt(3)\n"
"v_mfma_f32_32x32x16_bf16 v[48:63], a[208:211], v[80:83], v[48:63]\n"
"ds_read_b128 v[96:99], v1 offset:28672\n"
"v_mfma_f32_32x32x16_bf16 v[64:79], a[96:99], v[80:83], v[64:79]\n"
"s_waitcnt lgkmcnt(3)\n"
"v_mfma_f32_32x32x16_bf16 v[48:63], a[212:215], v[84:87], v[48:63]\n"
"ds_read_b128 v[100:103], v1 offset:29696\n"
"v_mfma_f32_32x32x16_bf16 v[64:79], a[100:103], v[84:87], v[64:79]\n"
"s_waitcnt lgkmcnt(3)\n"
"v_mfma_f32_32x32x16_bf16 v[48:63], a[216:219], v[88:91], v[48:63]\n"
"ds_read_b128 v[80:83], v1 offset:30720\n"
"v_mfma_f32_32x32x16_bf16 v[64:79], a[104:107], v[88:91], v[64:79]\n"
"s_waitcnt lgkmcnt(3)\n"
"v_mfma_f32_32x32x16_bf16 v[48:63], a[220:223], v[92:95], v[48:63]\n"
"ds_read_b128 v[84:87], v1 offset:31744\n"
"v_mfma_f32_32x32x16_bf16 v[64:79], a[108:111], v[92:95], v[64:79]\n"
"s_waitcnt lgkmcnt(3)\n"
"v_mfma_f32_32x32x16_bf16 v[48:63], a[224:227], v[96:99], v[48:63]\n"
"ds_read_b128 v[88:91], v1 offset:32768\n"
"v_mfma_f32_32x32x16_bf16 v[64:79], a[112:115], v[96:99], v[64:79]\n"
"s_waitcnt lgkmcnt(3)\n"
"v_mfma_f32_32x32x16_bf16 v[48:63], a[228:231], v[100:103], v[48:63]\n"
"ds_read_b128 v[92:95], v1 offset:33792\n"
"v_mfma_f32_32x32x16_bf16 v[64:79], a[116:119], v[100:103], v[64:79]\n"
"s_waitcnt lgkmcnt(3)\n"
"v_mfma_f32_32x32x16_bf16 v[48:63], a[232:235], v[80:83], v[48:63]\n"
"ds_read_b128 v[96:99], v1 offset:34816\n"
"v_mfma_f32_32x32x16_bf16 v[64:79], a[120:123], v[80:83], v[64:79]\n"
"s_waitcnt lgkmcnt(3)\n"
"v_mfma_f32_32x32x16_bf16 v[48:63], a[236:239], v[84:87], v[48:63]\n"
"ds_read_b128 v[100:103], v1 offset:35840\n"
"v_mfma_f32_32x32x16_bf16 v[64:79], a[124:127], v[84:87], v[64:79]\n"
"s_waitcnt lgkmcnt(3)\n"
"v_mfma_f32_32x32x16_bf16 v[48:63], a[240:243], v[88:91], v[48:63]\n"
"v_mfma_f32_32x32x16_bf16 v[64:79], a[128:131], v[88:91], v[64:79]\n"
"s_waitcnt vmcnt(4) lgkmcnt(0)\n"
"s_barrier\n"
"v_mfma_f32_32x32x16_bf16 v[48:63], a[244:247], v[92:95], v[48:63]\n"
"s_sub_u32 s77, 36864, s77\n"
"v_add_u32 v1, s77, v0\n"
"ds_read_b128 v[80:83], v1\n"
"ds_read_b128 v[84:87], v1 offset:1024\n"
"v_mfma_f32_32x32x16_bf16 v[64:79], a[132:135], v[92:95], v[64:79]\n"
"buffer_store_dword v4, v197, s[44:47], 0 offen sc1\n"
"v_mfma_f32_32x32x16_bf16 v[48:63], a[248:251], v[96:99], v[48:63]\n"
"ds_read_b128 v[88:91], v1 offset:2048\n"
"v_mfma_f32_32x32x16_bf16 v[64:79], a[136:139], v[96:99], v[64:79]\n"
"s_sub_u32 s60, s57, 1\n"
"s_mov_b32 s86, s62\n"
"s_add_u32 s57, s57, 1\n"
"s_cmp_eq_u32 s87, 0\n"
"v_mfma_f32_32x32x16_bf16 v[48:63], a[252:255], v[100:103], v[48:63]\n"
"ds_read_b128 v[92:95], v1 offset:3072\n"
"v_mfma_f32_32x32x16_bf16 v[64:79], a[140:143], v[100:103], v[64:79]\n"
"s_cbranch_scc1 LfxApack36_%=\n"
"LPBpack36_%=:\n"
"s_waitcnt lgkmcnt(3)\n"
"v_mfma_f32_32x32x16_bf16 v[16:31], v[128:131], v[80:83], 0\n"
"ds_read_b128 v[96:99], v1 offset:4096\n"
"s_add_u32 s92, s57, 1\n"
"v_readlane_b32 s92, v6, s92\n"
"s_sub_u32 s64, 36864, s77\n"
"v_mfma_f32_32x32x16_bf16 v[32:47], a[0:3], v[80:83], 0\n"
"s_add_u32 s64, s64, s55\n"
"s_mul_i32 s92, s92, s50\n"
"s_add_u32 s65, s92, s49\n"
"s_sub_u32 s90, s57, 1\n"
"s_and_b32 s90, s90, 1\n"
"s_waitcnt lgkmcnt(3)\n"
"v_mfma_f32_32x32x16_bf16 v[16:31], v[132:135], v[84:87], v[16:31]\n"
"ds_read_b128 v[100:103], v1 offset:5120\n"
"s_lshl_b32 s90, s90, 8\n"
"v_add_u32 v194, s90, v212\n"
"ds_read_b32 v195, v194\n"
"v_mfma_f32_32x32x16_bf16 v[32:47], a[4:7], v[84:87], v[32:47]\n"
"s_add_u32 m0, s64, 0\n"
"s_add_u32 s92, s65, 0\n"
"buffer_load_dwordx4 v2, s[36:39], s92 offen sc1 lds\n"
"s_sub_u32 s93, s57, 1\n"
"s_waitcnt lgkmcnt(4)\n"
"v_mfma_f32_32x32x16_bf16 v[16:31], v[136:139], v[88:91], v[16:31]\n"
"ds_read_b128 v[80:83], v1 offset:6144\n"
"v_readlane_b32 s93, v6, s93\n"
"v_mfma_f32_32x32x16_bf16 v[32:47], a[8:11], v[88:91], v[32:47]\n"
"s_add_u32 m0, s64, 4096\n"
"s_add_u32 s92, s65, 4096\n"
"buffer_load_dwordx4 v2, s[36:39], s92 offen sc1 lds\n"
"s_mul_i32 s93, s93, s52\n"
"s_waitcnt lgkmcnt(4)\n"
"v_mfma_f32_32x32x16_bf16 v[16:31], v[140:143], v[92:95], v[16:31]\n"
"ds_read_b128 v[84:87], v1 offset:7168\n"
"s_waitcnt lgkmcnt(2)\n"
"v_cmp_ne_u32_e64 s[80:81], s97, v195\n"
"s_add_u32 s66, s93, s51\n"
"v_readlane_b32 s93, v6, s60\n"
"v_mfma_f32_32x32x16_bf16 v[32:47], a[12:15], v[92:95], v[32:47]\n"
"s_add_u32 m0, s64, 8192\n"
"s_add_u32 s92, s65, 8192\n"
"buffer_load_dwordx4 v2, s[36:39], s92 offen sc1 lds\n"
"s_and_b64 s[80:81], s[80:81], s[88:89]\n"
"v_mfma_f32_32x32x16_bf16 v[16:31], v[144:147], v[96:99], v[16:31]\n"
"ds_read_b128 v[88:91], v1 offset:8192\n"
"s_ff1_i32_b64 s90, s[80:81]\n"
"s_cmp_lt_i32 s90, 0\n"
"s_cselect_b32 s90, 16, s90\n"
"s_mul_i32 s90, s90, 65536\n"
"v_mfma_f32_32x32x16_bf16 v[32:47], a[16:19], v[96:99], v[32:47]\n"
"s_add_u32 m0, s64, 12288\n"
"s_add_u32 s92, s65, 12288\n"
"buffer_load_dwordx4 v2, s[36:39], s92 offen sc1 lds\n"
"s_lshr_b32 s90, s90, 16\n"
"v_mfma_f32_32x32x16_bf16 v[16:31], v[148:151], v[100:103], v[16:31]\n"
"ds_read_b128 v[92:95], v1 offset:9216\n"
"s_sub_u32 s91, s48, s86\n"
"s_min_u32 s90, s90, s91\n"
"s_add_u32 s90, s90, s86\n"
"s_mul_i32 s93, s93, s54\n"
"v_mfma_f32_32x32x16_bf16 v[32:47], a[20:23], v[100:103], v[32:47]\n"
"s_add_u32 m0, s64, 16384\n"
"s_add_u32 s92, s65, 16384\n"
"buffer_load_dwordx4 v2, s[36:39], s92 offen sc1 lds\n"
"s_add_u32 s93, s93, s53\n"
"s_waitcnt lgkmcnt(3)\n"
"v_mfma_f32_32x32x16_bf16 v[16:31], v[152:155], v[80:83], v[16:31]\n"
"ds_read_b128 v[96:99], v1 offset:10240\n"
"s_cmp_lt_i32 s86, 0\n"
"s_cselect_b32 s58, s58, s90\n"
"v_mfma_f32_32x32x16_bf16 v[32:47], a[24:27], v[80:83], v[32:47]\n"
"s_add_u32 m0, s64, 20480\n"
"s_add_u32 s92, s65, 20480\n"
"buffer_load_dwordx4 v2, s[36:39], s92 offen sc1 lds\n"
"s_waitcnt lgkmcnt(3)\n"
"v_mfma_f32_32x32x16_bf16 v[16:31], v[156:159], v[84:87], v[16:31]\n"
"ds_read_b128 v[100:103], v1 offset:11264\n"
"s_cmp_lt_u32 s58, s48\n"
"s_cselect_b32 s62, s58, -1\n"
"s_sub_u32 s90, s58, s57\n"
"v_mfma_f32_32x32x16_bf16 v[32:47], a[28:31], v[84:87], v[32:47]\n"
"s_add_u32 m0, s64, 24576\n"
"s_add_u32 s92, s65, 24576\n"
"buffer_load_dwordx4 v2, s[36:39], s92 offen sc1 lds\n"
"s_waitcnt lgkmcnt(3)\n"
"v_mfma_f32_32x32x16_bf16 v[16:31], a[144:147], v[88:91], v[16:31]\n"
"ds_read_b128 v[80:83], v1 offset:12288\n"
"s_cmp_le_u32 s90, 4\n"
"s_cselect_b32 s62, s62, -1\n"
"s_max_i32 s90, s62, 0\n"
"v_mfma_f32_32x32x16_bf16 v[32:47], a[32:35], v[88:91], v[32:47]\n"
"s_add_u32 m0, s64, 28672\n"
"s_add_u32 s92, s65, 28672\n"
"buffer_load_dwordx4 v2, s[36:39], s92 offen sc1 lds\n"
"s_lshl_b32 s90, s90, 2\n"
"s_waitcnt lgkmcnt(3)\n"
"v_mfma_f32_32x32x16_bf16 v[16:31], a[148:151], v[92:95], v[16:31]\n"
"ds_read_b128 v[84:87], v1 offset:13312\n"
"s_add_u32 s90, s90, 86528\n"
"v_add_u32 v196, s90, v8\n"
"ds_read_b32 v196, v196\n"
"v_mfma_f32_32x32x16_bf16 v[32:47], a[36:39], v[92:95], v[32:47]\n"
"s_add_u32 m0, s64, 32768\n"
"s_add_u32 s92, s65, 32768\n"
"buffer_load_dwordx4 v2, s[36:39], s92 offen sc1 lds\n"
"s_waitcnt lgkmcnt(4)\n"
"v_mfma_f32_32x32x16_bf16 v[16:31], a[152:155], v[96:99], v[16:31]\n"
"ds_read_b128 v[88:91], v1 offset:14336\n"
"s_cmp_ge_i32 s62, 0\n"
"s_cselect_b64 s[94:95], s[78:79], 0\n"
"s_and_b32 s91, s57, 1\n"
"v_mfma_f32_32x32x16_bf16 v[32:47], a[40:43], v[96:99], v[32:47]\n"
"s_lshl_b32 s91, s91, 8\n"
"s_add_u32 s91, s91, 86016\n"
"s_add_u32 s90, s57, 2\n"
"s_cmp_lt_u32 s90, s58\n"
"s_cselect_b32 s87, 1, 0\n"
"s_waitcnt lgkmcnt(4)\n"
"v_mfma_f32_32x32x16_bf16 v[16:31], a[156:159], v[100:103], v[16:31]\n"
"ds_read_b128 v[92:95], v1 offset:15360\n"
"s_cmp_lt_i32 s60, 0\n"
"s_cselect_b32 s93, 0x7ffffff0, s93\n"
"v_add_u32 v197, s93, v3\n"
"v_mfma_f32_32x32x16_bf16 v[32:47], a[44:47], v[100:103], v[32:47]\n"
"v_cvt_pk_bf16_f32 v160, v48, v49\n"
"v_cvt_pk_bf16_f32 v161, v50, v51\n"
"v_cvt_pk_bf16_f32 v162, v52, v53\n"
"v_cvt_pk_bf16_f32 v163, v54, v55\n"
"v_cvt_pk_bf16_f32 v164, v56, v57\n"
"s_waitcnt lgkmcnt(4)\n"
"v_mfma_f32_32x32x16_bf16 v[16:31], a[160:163], v[80:83], v[16:31]\n"
"ds_read_b128 v[96:99], v1 offset:16384\n"
"s_waitcnt lgkmcnt(3)\n"
"v_add_u32 v196, v196, v9\n"
"v_cvt_pk_bf16_f32 v165, v58, v59\n"
"v_cvt_pk_bf16_f32 v166, v60, v61\n"
"v_mfma_f32_32x32x16_bf16 v[32:47], a[48:51], v[80:83], v[32:47]\n"
"s_mov_b32 m0, s91\n"
"s_mov_b64 exec, s[94:95]\n"
"buffer_load_dword v196, s[44:47], 0 offen sc1 lds\n"
"s_mov_b64 exec, -1\n"
"v_cvt_pk_bf16_f32 v167, v62, v63\n"
"v_mfma_f32_32x32x16_bf16 v[16:31], a[164:167], v[84:87], v[16:31]\n"
"ds_read_b128 v[100:103], v1 offset:17408\n"
"v_cvt_pk_bf16_f32 v168, v64, v65\n"
"v_cvt_pk_bf16_f32 v169, v66, v67\n"
"v_cvt_pk_bf16_f32 v170, v68, v69\n"
"v_cvt_pk_bf16_f32 v171, v70, v71\n"
"v_mfma_f32_32x32x16_bf16 v[32:47], a[52:55], v[84:87], v[32:47]\n"
"v_cvt_pk_bf16_f32 v172, v72, v73\n"
"v_cvt_pk_bf16_f32 v173, v74, v75\n"
"v_cvt_pk_bf16_f32 v174, v76, v77\n"
"v_cvt_pk_bf16_f32 v175, v78, v79\n"
"v_permlane32_swap_b32 v160, v162\n"
"s_waitcnt lgkmcnt(3)\n"
"v_mfma_f32_32x32x16_bf16 v[16:31], a[168:171], v[88:91], v[16:31]\n"
"ds_read_b128 v[80:83], v1 offset:18432\n"
"v_permlane32_swap_b32 v161, v163\n"
"v_permlane32_swap_b32 v164, v166\n"
"v_permlane32_swap_b32 v165, v167\n"
"v_mfma_f32_32x32x16_bf16 v[32:47], a[56:59], v[88:91], v[32:47]\n"
"v_permlane32_swap_b32 v168, v170\n"
"v_permlane32_swap_b32 v169, v171\n"
"v_permlane32_swap_b32 v172, v174\n"
"v_permlane32_swap_b32 v173, v175\n"
"s_waitcnt lgkmcnt(3)\n"
"v_mfma_f32_32x32x16_bf16 v[16:31], a[172:175], v[92:95], v[16:31]\n"
"ds_read_b128 v[84:87], v1 offset:19456\n"
"buffer_store_dwordx4 v[160:163], v11, s[40:43], s66 offen sc1\n"
"v_mfma_f32_32x32x16_bf16 v[32:47], a[60:63], v[92:95], v[32:47]\n"
"buffer_store_dwordx4 v[164:167], v12, s[40:43], s66 offen sc1\n"
"s_waitcnt lgkmcnt(3)\n"
"v_mfma_f32_32x32x16_bf16 v[16:31], a[176:179], v[96:99], v[16:31]\n"
"ds_read_b128 v[88:91], v1 offset:20480\n"
"buffer_store_dwordx4 v[168:171], v13, s[40:43], s66 offen sc1\n"
"v_mfma_f32_32x32x16_bf16 v[32:47], a[64:67], v[96:99], v[32:47]\n"
"buffer_store_dwordx4 v[172:175], v14, s[40:43], s66 offen sc1\n"
"s_waitcnt lgkmcnt(3)\n"
"v_mfma_f32_32x32x16_bf16 v[16:31], a[180:183], v[100:103], v[16:31]\n"
"ds_read_b128 v[92:95], v1 offset:21504\n"
"v_mfma_f32_32x32x16_bf16 v[32:47], a[68:71], v[100:103], v[32:47]\n"
"s_waitcnt lgkmcnt(3)\n"
"v_mfma_f32_32x32x16_bf16 v[16:31], a[184:187], v[80:83], v[16:31]\n"
"ds_read_b128 v[96:99], v1 offset:22528\n"
"v_mfma_f32_32x32x16_bf16 v[32:47], a[72:75], v[80:83], v[32:47]\n"
"s_waitcnt lgkmcnt(3)\n"
"v_mfma_f32_32x32x16_bf16 v[16:31], a[188:191], v[84:87], v[16:31]\n"
"ds_read_b128 v[100:103], v1 offset:23552\n"
"v_mfma_f32_32x32x16_bf16 v[32:47], a[76:79], v[84:87], v[32:47]\n"
"s_waitcnt lgkmcnt(3)\n"
"v_mfma_f32_32x32x16_bf16 v[16:31], a[192:195], v[88:91], v[16:31]\n"
"ds_read_b128 v[80:83], v1 offset:24576\n"
"v_mfma_f32_32x32x16_bf16 v[32:47], a[80:83], v[88:91], v[32:47]\n"
"s_waitcnt lgkmcnt(3)\n"
"v_mfma_f32_32x32x16_bf16 v[16:31], a[196:199], v[92:95], v[16:31]\n"
"ds_read_b128 v[84:87], v1 offset:25600\n"
"v_mfma_f32_32x32x16_bf16 v[32:47], a[84:87], v[92:95], v[32:47]\n"
"s_waitcnt lgkmcnt(3)\n"
"v_mfma_f32_32x32x16_bf16 v[16:31], a[200:203], v[96:99], v[16:31]\n"
"ds_read_b128 v[88:91], v1 offset:26624\n"
"v_mfma_f32_32x32x16_bf16 v[32:47], a[88:91], v[96:99], v[32:47]\n"
"s_waitcnt lgkmcnt(3)\n"
"v_mfma_f32_32x32x16_bf16 v[16:31], a[204:207], v[100:103], v[16:31]\n"
"ds_read_b128 v[92:95], v1 offset:27648\n"
"v_mfma_f32_32x32x16_bf16 v[32:47], a[92:95], v[100:103], v[32:47]\n"
"s_waitcnt lgkmcnt(3)\n"
"v_mfma_f32_32x32x16_bf16 v[16:31], a[208:211], v[80:83], v[16:31]\n"
"ds_read_b128 v[96:99], v1 offset:28672\n"
"v_mfma_f32_32x32x16_bf16 v[32:47], a[96:99], v[80:83], v[32:47]\n"
"s_waitcnt lgkmcnt(3)\n"
"v_mfma_f32_32x32x16_bf16 v[16:31], a[212:215], v[84:87], v[16:31]\n"
"ds_read_b128 v[100:103], v1 offset:29696\n"
"v_mfma_f32_32x32x16_bf16 v[32:47], a[100:103], v[84:87], v[32:47]\n"
"s_waitcnt lgkmcnt(3)\n"
"v_mfma_f32_32x32x16_bf16 v[16:31], a[216:219], v[88:91], v[16:31]\n"
"ds_read_b128 v[80:83], v1 offset:30720\n"
"v_mfma_f32_32x32x16_bf16 v[32:47], a[104:107], v[88:91], v[32:47]\n"
"s_waitcnt lgkmcnt(3)\n"
"v_mfma_f32_32x32x16_bf16 v[16:31], a[220:223], v[92:95], v[16:31]\n"
"ds_read_b128 v[84:87], v1 offset:31744\n"
"v_mfma_f32_32x32x16_bf16 v[32:47], a[108:111], v[92:95], v[32:47]\n"
"s_waitcnt lgkmcnt(3)\n"
"v_mfma_f32_32x32x16_bf16 v[16:31], a[224:227], v[96:99], v[16:31]\n"
"ds_read_b128 v[88:91], v1 offset:32768\n"
"v_mfma_f32_32x32x16_bf16 v[32:47], a[112:115], v[96:99], v[32:47]\n"
"s_waitcnt lgkmcnt(3)\n"
"v_mfma_f32_32x32x16_bf16 v[16:31], a[228:231], v[100:103], v[16:31]\n"
"ds_read_b128 v[92:95], v1 offset:33792\n"
"v_mfma_f32_32x32x16_bf16 v[32:47], a[116:119], v[100:103], v[32:47]\n"
"s_waitcnt lgkmcnt(3)\n"
"v_mfma_f32_32x32x16_bf16 v[16:31], a[232:235], v[80:83], v[16:31]\n"
"ds_read_b128 v[96:99], v1 offset:34816\n"
"v_mfma_f32_32x32x16_bf16 v[32:47], a[120:123], v[80:83], v[32:47]\n"
"s_waitcnt lgkmcnt(3)\n"
"v_mfma_f32_32x32x16_bf16 v[16:31], a[236:239], v[84:87], v[16:31]\n"
"ds_read_b128 v[100:103], v1 offset:35840\n"
"v_mfma_f32_32x32x16_bf16 v[32:47], a[124:127], v[84:87], v[32:47]\n"
"s_waitcnt lgkmcnt(3)\n"
"v_mfma_f32_32x32x16_bf16 v[16:31], a[240:243], v[88:91], v[16:31]\n"
"v_mfma_f32_32x32x16_bf16 v[32:47], a[128:131], v[88:91], v[32:47]\n"
"s_waitcnt vmcnt(4) lgkmcnt(0)\n"
"s_barrier\n"
"v_mfma_f32_32x32x16_bf16 v[16:31], a[244:247], v[92:95], v[16:31]\n"
"s_sub_u32 s77, 36864, s77\n"
"v_add_u32 v1, s77, v0\n"
"ds_read_b128 v[80:83], v1\n"
"ds_read_b128 v[84:87], v1 offset:1024\n"
"v_mfma_f32_32x32x16_bf16 v[32:47], a[132:135], v[92:95], v[32:47]\n"
"buffer_store_dword v4, v197, s[44:47], 0 offen sc1\n"
"v_mfma_f32_32x32x16_bf16 v[16:31], a[248:251], v[96:99], v[16:31]\n"
"ds_read_b128 v[88:91], v1 offset:2048\n"
"v_mfma_f32_32x32x16_bf16 v[32:47], a[136:139], v[96:99], v[32:47]\n"
"s_sub_u32 s60, s57, 1\n"
"s_mov_b32 s86, s62\n"
"s_add_u32 s57, s57, 1\n"
"s_cmp_eq_u32 s87, 0\n"
"v_mfma_f32_32x32x16_bf16 v[16:31], a[252:255], v[100:103], v[16:31]\n"
"ds_read_b128 v[92:95], v1 offset:3072\n"
"v_mfma_f32_32x32x16_bf16 v[32:47], a[140:143], v[100:103], v[32:47]\n"
"s_cbranch_scc0 LPApack36_%=\n"
"s_mov_b32 s99, 0\n"
"s_sub_u32 s57, s57, 1\n"
"s_branch Le1pack36_%=\n"
"LfxApack36_%=:\n"
"s_mov_b32 s99, 1\n"
"s_sub_u32 s57, s57, 1\n"
"Le1pack36_%=:\n"
"s_mov_b32 s60, -1\n"
"s_mov_b32 s61, -1\n"
"s_cmp_eq_u32 s59, 0\n"
"s_cbranch_scc1 Le2pack36_%=\n"
"s_sub_u32 s60, s57, 1\n"
"Le2pack36_%=:\n"
"s_cmp_lt_i32 s62, 0\n"
"s_cbranch_scc1 Le3pack36_%=\n"
"s_and_b32 s72, s57, 1\n"
"s_lshl_b32 s72, s72, 8\n"
"s_add_u32 s72, s72, 86016\n"
"s_sub_u32 s67, s72, 0\n"
"v_lshrrev_b32 v192, 2, v0\n"
"v_add_u32 v192, s67, v192\n"
"ds_read_b32 v193, v192\n"
"s_waitcnt lgkmcnt(0)\n"
"v_cmp_eq_u32_e64 s[80:81], s97, v193\n"
"s_nop 1\n"
"s_sub_u32 s68, s48, s62\n"
"s_min_u32 s68, s68, 16\n"
"s_mov_b32 s70, 0\n"
"Levepack36_%=:\n"
"s_cmp_ge_u32 s70, s68\n"
"s_cbranch_scc1 Lexepack36_%=\n"
"s_and_b32 s69, s80, 1\n"
"s_cmp_eq_u32 s69, 1\n"
"s_cbranch_scc0 Lexepack36_%=\n"
"s_lshr_b64 s[80:81], s[80:81], 1\n"
"s_add_u32 s70, s70, 1\n"
"s_branch Levepack36_%=\n"
"Lexepack36_%=:\n"
"s_add_u32 s58, s58, s70\n"
"s_waitcnt lgkmcnt(0)\n"
"Le3pack36_%=:\n"
"s_mov_b32 s59, 0\n"
"s_add_u32 s68, s57, 2\n"
"s_cmp_lt_u32 s68, s58\n"
"s_cbranch_scc0 Le4pack36_%=\n"
"s_cmp_ge_u32 s57, 3\n"
"s_cbranch_scc0 Le4pack36_%=\n"
"s_add_u32 s59, s99, 1\n"
"s_mov_b32 s86, -1\n"
"s_add_u32 s57, s57, 1\n"
"s_branch Ltoppack36_%=\n"
"Le4pack36_%=:\n"
"s_mov_b32 s61, s57\n"
"s_cmp_eq_u32 s99, 0\n"
"s_cbranch_scc0 Le5pack36_%=\n"
"s_nop 3\n"
"v_readlane_b32 s67, v6, s57\n"
"s_nop 0\n"
"s_mul_i32 s67, s67, s52\n"
"s_add_u32 s66, s67, s51\n"
"v_cvt_pk_bf16_f32 v160, v16, v17\n"
"v_cvt_pk_bf16_f32 v161, v18, v19\n"
"v_cvt_pk_bf16_f32 v162, v20, v21\n"
"v_cvt_pk_bf16_f32 v163, v22, v23\n"
"v_cvt_pk_bf16_f32 v164, v24, v25\n"
"v_cvt_pk_bf16_f32 v165, v26, v27\n"
"v_cvt_pk_bf16_f32 v166, v28, v29\n"
"v_cvt_pk_bf16_f32 v167, v30, v31\n"
"v_cvt_pk_bf16_f32 v168, v32, v33\n"
"v_cvt_pk_bf16_f32 v169, v34, v35\n"
"v_cvt_pk_bf16_f32 v170, v36, v37\n"
"v_cvt_pk_bf16_f32 v171, v38, v39\n"
"v_cvt_pk_bf16_f32 v172, v40, v41\n"
"v_cvt_pk_bf16_f32 v173, v42, v43\n"
"v_cvt_pk_bf16_f32 v174, v44, v45\n"
"v_cvt_pk_bf16_f32 v175, v46, v47\n"
"v_permlane32_swap_b32 v160, v162\n"
"v_permlane32_swap_b32 v161, v163\n"
"v_permlane32_swap_b32 v164, v166\n"
"v_permlane32_swap_b32 v165, v167\n"
"v_permlane32_swap_b32 v168, v170\n"
"v_permlane32_swap_b32 v169, v171\n"
"v_permlane32_swap_b32 v172, v174\n"
"v_permlane32_swap_b32 v173, v175\n"
"buffer_store_dwordx4 v[160:163], v11, s[40:43], s66 offen sc1\n"
"buffer_store_dwordx4 v[164:167], v12, s[40:43], s66 offen sc1\n"
"buffer_store_dwordx4 v[168:171], v13, s[40:43], s66 offen sc1\n"
"buffer_store_dwordx4 v[172:175], v14, s[40:43], s66 offen sc1\n"
"s_add_u32 s57, s57, 1\n"
"s_branch Ltoppack36_%=\n"
"Le5pack36_%=:\n"
"s_nop 3\n"
"v_readlane_b32 s67, v6, s57\n"
"s_nop 0\n"
"s_mul_i32 s67, s67, s52\n"
"s_add_u32 s66, s67, s51\n"
"v_cvt_pk_bf16_f32 v160, v48, v49\n"
"v_cvt_pk_bf16_f32 v161, v50, v51\n"
"v_cvt_pk_bf16_f32 v162, v52, v53\n"
"v_cvt_pk_bf16_f32 v163, v54, v55\n"
"v_cvt_pk_bf16_f32 v164, v56, v57\n"
"v_cvt_pk_bf16_f32 v165, v58, v59\n"
"v_cvt_pk_bf16_f32 v166, v60, v61\n"
"v_cvt_pk_bf16_f32 v167, v62, v63\n"
"v_cvt_pk_bf16_f32 v168, v64, v65\n"
"v_cvt_pk_bf16_f32 v169, v66, v67\n"
"v_cvt_pk_bf16_f32 v170, v68, v69\n"
"v_cvt_pk_bf16_f32 v171, v70, v71\n"
"v_cvt_pk_bf16_f32 v172, v72, v73\n"
"v_cvt_pk_bf16_f32 v173, v74, v75\n"
"v_cvt_pk_bf16_f32 v174, v76, v77\n"
"v_cvt_pk_bf16_f32 v175, v78, v79\n"
"v_permlane32_swap_b32 v160, v162\n"
"v_permlane32_swap_b32 v161, v163\n"
"v_permlane32_swap_b32 v164, v166\n"
"v_permlane32_swap_b32 v165, v167\n"
"v_permlane32_swap_b32 v168, v170\n"
"v_permlane32_swap_b32 v169, v171\n"
"v_permlane32_swap_b32 v172, v174\n"
"v_permlane32_swap_b32 v173, v175\n"
"buffer_store_dwordx4 v[160:163], v11, s[40:43], s66 offen sc1\n"
"buffer_store_dwordx4 v[164:167], v12, s[40:43], s66 offen sc1\n"
"buffer_store_dwordx4 v[168:171], v13, s[40:43], s66 offen sc1\n"
"buffer_store_dwordx4 v[172:175], v14, s[40:43], s66 offen sc1\n"
"s_add_u32 s57, s57, 1\n"
"s_branch Ltoppack36_%=\n"
"Lnonpack36_%=:\n"
"s_max_i32 s68, s60, s61\n"
"s_cmp_ge_i32 s68, 0\n"
"s_cselect_b32 s63, 1, 0\n"
"s_waitcnt lgkmcnt(3)\n"
"v_mfma_f32_32x32x16_bf16 v[16:31], v[128:131], v[80:83], 0\n"
"ds_read_b128 v[96:99], v1 offset:4096\n"
"v_mfma_f32_32x32x16_bf16 v[32:47], a[0:3], v[80:83], 0\n"
"s_add_u32 s90, s57, 1\n"
"s_cmp_lt_u32 s90, s48\n"
"s_cselect_b32 s62, s90, -1\n"
"s_lshl_b32 s90, s90, 2\n"
"s_add_u32 s90, s90, 86528\n"
"v_add_u32 v196, s90, v8\n"
"s_waitcnt lgkmcnt(3)\n"
"v_mfma_f32_32x32x16_bf16 v[16:31], v[132:135], v[84:87], v[16:31]\n"
"ds_read_b128 v[100:103], v1 offset:5120\n"
"ds_read_b32 v196, v196\n"
"s_cmp_ge_i32 s62, 0\n"
"s_cselect_b64 s[94:95], s[78:79], 0\n"
"v_mfma_f32_32x32x16_bf16 v[32:47], a[4:7], v[84:87], v[32:47]\n"
"s_and_b32 s94, s94, 3\n"
"s_mov_b32 s95, 0\n"
"s_waitcnt lgkmcnt(4)\n"
"v_mfma_f32_32x32x16_bf16 v[16:31], v[136:139], v[88:91], v[16:31]\n"
"ds_read_b128 v[80:83], v1 offset:6144\n"
"v_mfma_f32_32x32x16_bf16 v[32:47], a[8:11], v[88:91], v[32:47]\n"
"s_waitcnt lgkmcnt(4)\n"
"v_mfma_f32_32x32x16_bf16 v[16:31], v[140:143], v[92:95], v[16:31]\n"
"ds_read_b128 v[84:87], v1 offset:7168\n"
"s_waitcnt lgkmcnt(2)\n"
"v_add_u32 v196, v196, v9\n"
"v_cndmask_b32_e64 v196, v15, v196, s[94:95]\n"
"v_mfma_f32_32x32x16_bf16 v[32:47], a[12:15], v[92:95], v[32:47]\n"
"s_mov_b32 s91, 87296\n"
"s_cmp_eq_u32 %0, 2\n"
"s_cselect_b32 m0, s91, 87040\n"
"s_nop 0\n"
"buffer_load_dword v196, s[44:47], 0 offen sc1 lds\n"
"v_mfma_f32_32x32x16_bf16 v[16:31], v[144:147], v[96:99], v[16:31]\n"
"ds_read_b128 v[88:91], v1 offset:8192\n"
"v_mfma_f32_32x32x16_bf16 v[32:47], a[16:19], v[96:99], v[32:47]\n"
"v_mfma_f32_32x32x16_bf16 v[16:31], v[148:151], v[100:103], v[16:31]\n"
"ds_read_b128 v[92:95], v1 offset:9216\n"
"v_mfma_f32_32x32x16_bf16 v[32:47], a[20:23], v[100:103], v[32:47]\n"
"s_waitcnt lgkmcnt(3)\n"
"v_mfma_f32_32x32x16_bf16 v[16:31], v[152:155], v[80:83], v[16:31]\n"
"ds_read_b128 v[96:99], v1 offset:10240\n"
"v_mfma_f32_32x32x16_bf16 v[32:47], a[24:27], v[80:83], v[32:47]\n"
"s_waitcnt lgkmcnt(3)\n"
"v_mfma_f32_32x32x16_bf16 v[16:31], v[156:159], v[84:87], v[16:31]\n"
"ds_read_b128 v[100:103], v1 offset:11264\n"
"v_mfma_f32_32x32x16_bf16 v[32:47], a[28:31], v[84:87], v[32:47]\n"
"s_waitcnt lgkmcnt(3)\n"
"v_mfma_f32_32x32x16_bf16 v[16:31], a[144:147], v[88:91], v[16:31]\n"
"ds_read_b128 v[80:83], v1 offset:12288\n"
"v_mfma_f32_32x32x16_bf16 v[32:47], a[32:35], v[88:91], v[32:47]\n"
"s_waitcnt lgkmcnt(3)\n"
"v_mfma_f32_32x32x16_bf16 v[16:31], a[148:151], v[92:95], v[16:31]\n"
"ds_read_b128 v[84:87], v1 offset:13312\n"
"v_mfma_f32_32x32x16_bf16 v[32:47], a[36:39], v[92:95], v[32:47]\n"
"s_waitcnt lgkmcnt(3)\n"
"v_mfma_f32_32x32x16_bf16 v[16:31], a[152:155], v[96:99], v[16:31]\n"
"ds_read_b128 v[88:91], v1 offset:14336\n"
"v_mfma_f32_32x32x16_bf16 v[32:47], a[40:43], v[96:99], v[32:47]\n"
"s_cmp_eq_u32 s63, 0\n"
"s_cbranch_scc1 LnmLpack36_%=\n"
"s_waitcnt vmcnt(1)\n"
"s_barrier\n"
"v_readlane_b32 s93, v6, s60\n"
"s_nop 0\n"
"s_mul_i32 s93, s93, s54\n"
"s_add_u32 s93, s93, s53\n"
"s_cmp_lt_i32 s60, 0\n"
"s_cselect_b32 s93, 0x7ffffff0, s93\n"
"v_add_u32 v197, s93, v3\n"
"buffer_store_dword v4, v197, s[44:47], 0 offen sc1\n"
"v_readlane_b32 s93, v6, s61\n"
"s_nop 0\n"
"s_mul_i32 s93, s93, s54\n"
"s_add_u32 s93, s93, s53\n"
"s_cmp_lt_i32 s61, 0\n"
"s_cselect_b32 s93, 0x7ffffff0, s93\n"
"v_add_u32 v199, s93, v3\n"
"buffer_store_dword v4, v199, s[44:47], 0 offen sc1\n"
"LnmLpack36_%=:\n"
"s_waitcnt lgkmcnt(3)\n"
"v_mfma_f32_32x32x16_bf16 v[16:31], a[156:159], v[100:103], v[16:31]\n"
"ds_read_b128 v[92:95], v1 offset:15360\n"
"v_mfma_f32_32x32x16_bf16 v[32:47], a[44:47], v[100:103], v[32:47]\n"
"s_waitcnt lgkmcnt(3)\n"
"v_mfma_f32_32x32x16_bf16 v[16:31], a[160:163], v[80:83], v[16:31]\n"
"ds_read_b128 v[96:99], v1 offset:16384\n"
"v_mfma_f32_32x32x16_bf16 v[32:47], a[48:51], v[80:83], v[32:47]\n"
"s_waitcnt lgkmcnt(3)\n"
"v_mfma_f32_32x32x16_bf16 v[16:31], a[164:167], v[84:87], v[16:31]\n"
"ds_read_b128 v[100:103], v1 offset:17408\n"
"v_mfma_f32_32x32x16_bf16 v[32:47], a[52:55], v[84:87], v[32:47]\n"
"s_waitcnt lgkmcnt(3)\n"
"v_mfma_f32_32x32x16_bf16 v[16:31], a[168:171], v[88:91], v[16:31]\n"
"ds_read_b128 v[80:83], v1 offset:18432\n"
"v_mfma_f32_32x32x16_bf16 v[32:47], a[56:59], v[88:91], v[32:47]\n"
"s_waitcnt lgkmcnt(3)\n"
"v_mfma_f32_32x32x16_bf16 v[16:31], a[172:175], v[92:95], v[16:31]\n"
"ds_read_b128 v[84:87], v1 offset:19456\n"
"v_mfma_f32_32x32x16_bf16 v[32:47], a[60:63], v[92:95], v[32:47]\n"
"s_mov_b32 s91, 87552\n"
"s_cmp_eq_u32 %0, 2\n"
"s_cselect_b32 m0, s91, 87040\n"
"s_nop 0\n"
"buffer_load_dword v196, s[44:47], 0 offen sc1 lds\n"
"s_waitcnt lgkmcnt(3)\n"
"v_mfma_f32_32x32x16_bf16 v[16:31], a[176:179], v[96:99], v[16:31]\n"
"ds_read_b128 v[88:91], v1 offset:20480\n"
"v_mfma_f32_32x32x16_bf16 v[32:47], a[64:67], v[96:99], v[32:47]\n"
"s_waitcnt lgkmcnt(3)\n"
"v_mfma_f32_32x32x16_bf16 v[16:31], a[180:183], v[100:103], v[16:31]\n"
"ds_read_b128 v[92:95], v1 offset:21504\n"
"v_mfma_f32_32x32x16_bf16 v[32:47], a[68:71], v[100:103], v[32:47]\n"
"s_waitcnt lgkmcnt(3)\n"
"v_mfma_f32_32x32x16_bf16 v[16:31], a[184:187], v[80:83], v[16:31]\n"
"ds_read_b128 v[96:99], v1 offset:22528\n"
"v_mfma_f32_32x32x16_bf16 v[32:47], a[72:75], v[80:83], v[32:47]\n"
"s_waitcnt lgkmcnt(3)\n"
"v_mfma_f32_32x32x16_bf16 v[16:31], a[188:191], v[84:87], v[16:31]\n"
"ds_read_b128 v[100:103], v1 offset:23552\n"
"v_mfma_f32_32x32x16_bf16 v[32:47], a[76:79], v[84:87], v[32:47]\n"
"s_waitcnt lgkmcnt(3)\n"
"v_mfma_f32_32x32x16_bf16 v[16:31], a[192:195], v[88:91], v[16:31]\n"
"ds_read_b128 v[80:83], v1 offset:24576\n"
"v_mfma_f32_32x32x16_bf16 v[32:47], a[80:83], v[88:91], v[32:47]\n"
"s_waitcnt lgkmcnt(3)\n"
"v_mfma_f32_32x32x16_bf16 v[16:31], a[196:199], v[92:95], v[16:31]\n"
"ds_read_b128 v[84:87], v1 offset:25600\n"
"v_mfma_f32_32x32x16_bf16 v[32:47], a[84:87], v[92:95], v[32:47]\n"
"s_waitcnt lgkmcnt(3)\n"
"v_mfma_f32_32x32x16_bf16 v[16:31], a[200:203], v[96:99], v[16:31]\n"
"ds_read_b128 v[88:91], v1 offset:26624\n"
"v_mfma_f32_32x32x16_bf16 v[32:47], a[88:91], v[96:99], v[32:47]\n"
"s_waitcnt lgkmcnt(3)\n"
"v_mfma_f32_32x32x16_bf16 v[16:31], a[204:207], v[100:103], v[16:31]\n"
"ds_read_b128 v[92:95], v1 offset:27648\n"
"v_mfma_f32_32x32x16_bf16 v[32:47], a[92:95], v[100:103], v[32:47]\n"
"s_cmp_eq_u32 s63, 0\n"
"s_cbranch_scc1 LpwLpack36_%=\n"
"s_waitcnt vmcnt(3)\n"
"s_branch LpxLpack36_%=\n"
"LpwLpack36_%=:\n"
"s_waitcnt vmcnt(1)\n"
"LpxLpack36_%=:\n"
"s_barrier\n"
"s_waitcnt lgkmcnt(3)\n"
"v_mfma_f32_32x32x16_bf16 v[16:31], a[208:211], v[80:83], v[16:31]\n"
"ds_read_b128 v[96:99], v1 offset:28672\n"
"ds_read_b32 v195, v212 offset:1280\n"
"s_add_u32 s92, s57, 1\n"
"v_readlane_b32 s92, v6, s92\n"
"s_sub_u32 s64, 36864, s77\n"
"v_mfma_f32_32x32x16_bf16 v[32:47], a[96:99], v[80:83], v[32:47]\n"
"s_add_u32 s64, s64, s55\n"
"s_mul_i32 s92, s92, s50\n"
"s_add_u32 s65, s92, s49\n"
"s_waitcnt lgkmcnt(4)\n"
"v_mfma_f32_32x32x16_bf16 v[16:31], a[212:215], v[84:87], v[16:31]\n"
"ds_read_b128 v[100:103], v1 offset:29696\n"
"v_mfma_f32_32x32x16_bf16 v[32:47], a[100:103], v[84:87], v[32:47]\n"
"s_waitcnt lgkmcnt(1)\n"
"v_cmp_ne_u32_e64 s[80:81], s97, v195\n"
"v_mfma_f32_32x32x16_bf16 v[16:31], a[216:219], v[88:91], v[16:31]\n"
"ds_read_b128 v[80:83], v1 offset:30720\n"
"s_and_b32 s80, s80, 3\n"
"s_mov_b32 s81, 0\n"
"s_ff1_i32_b64 s90, s[80:81]\n"
"s_cmp_lt_i32 s90, 0\n"
"s_cselect_b32 s90, 2, s90\n"
"v_mfma_f32_32x32x16_bf16 v[32:47], a[104:107], v[88:91], v[32:47]\n"
"s_mul_i32 s90, s90, 65536\n"
"s_lshr_b32 s90, s90, 16\n"
"s_sub_u32 s91, s48, s62\n"
"s_min_u32 s90, s90, s91\n"
"s_add_u32 s90, s90, s62\n"
"v_mfma_f32_32x32x16_bf16 v[16:31], a[220:223], v[92:95], v[16:31]\n"
"ds_read_b128 v[84:87], v1 offset:31744\n"
"s_cmp_lt_i32 s62, 0\n"
"s_cselect_b32 s58, s58, s90\n"
"s_add_u32 s90, s57, 1\n"
"v_mfma_f32_32x32x16_bf16 v[32:47], a[108:111], v[92:95], v[32:47]\n"
"s_cmp_lt_u32 s90, s58\n"
"s_cselect_b64 s[94:95], -1, 0\n"
"s_cselect_b32 s87, 1, 0\n"
"v_cndmask_b32_e64 v198, v15, v2, s[94:95]\n"
"v_mfma_f32_32x32x16_bf16 v[16:31], a[224:227], v[96:99], v[16:31]\n"
"ds_read_b128 v[88:91], v1 offset:32768\n"
"s_add_u32 m0, s64, 0\n"
"s_add_u32 s92, s65, 0\n"
"buffer_load_dwordx4 v198, s[36:39], s92 offen sc1 lds\n"
"v_mfma_f32_32x32x16_bf16 v[32:47], a[112:115], v[96:99], v[32:47]\n"
"s_add_u32 m0, s64, 4096\n"
"s_add_u32 s92, s65, 4096\n"
"buffer_load_dwordx4 v198, s[36:39], s92 offen sc1 lds\n"
"s_waitcnt lgkmcnt(3)\n"
"v_mfma_f32_32x32x16_bf16 v[16:31], a[228:231], v[100:103], v[16:31]\n"
"ds_read_b128 v[92:95], v1 offset:33792\n"
"s_add_u32 m0, s64, 8192\n"
"s_add_u32 s92, s65, 8192\n"
"buffer_load_dwordx4 v198, s[36:39], s92 offen sc1 lds\n"
"v_mfma_f32_32x32x16_bf16 v[32:47], a[116:119], v[100:103], v[32:47]\n"
"s_add_u32 m0, s64, 12288\n"
"s_add_u32 s92, s65, 12288\n"
"buffer_load_dwordx4 v198, s[36:39], s92 offen sc1 lds\n"
"s_waitcnt lgkmcnt(3)\n"
"v_mfma_f32_32x32x16_bf16 v[16:31], a[232:235], v[80:83], v[16:31]\n"
"ds_read_b128 v[96:99], v1 offset:34816\n"
"s_add_u32 m0, s64, 16384\n"
"s_add_u32 s92, s65, 16384\n"
"buffer_load_dwordx4 v198, s[36:39], s92 offen sc1 lds\n"
"v_mfma_f32_32x32x16_bf16 v[32:47], a[120:123], v[80:83], v[32:47]\n"
"s_add_u32 m0, s64, 20480\n"
"s_add_u32 s92, s65, 20480\n"
"buffer_load_dwordx4 v198, s[36:39], s92 offen sc1 lds\n"
"s_waitcnt lgkmcnt(3)\n"
"v_mfma_f32_32x32x16_bf16 v[16:31], a[236:239], v[84:87], v[16:31]\n"
"ds_read_b128 v[100:103], v1 offset:35840\n"
"s_add_u32 m0, s64, 24576\n"
"s_add_u32 s92, s65, 24576\n"
"buffer_load_dwordx4 v198, s[36:39], s92 offen sc1 lds\n"
"v_mfma_f32_32x32x16_bf16 v[32:47], a[124:127], v[84:87], v[32:47]\n"
"s_add_u32 m0, s64, 28672\n"
"s_add_u32 s92, s65, 28672\n"
"buffer_load_dwordx4 v198, s[36:39], s92 offen sc1 lds\n"
"s_waitcnt lgkmcnt(3)\n"
"v_mfma_f32_32x32x16_bf16 v[16:31], a[240:243], v[88:91], v[16:31]\n"
"s_add_u32 m0, s64, 32768\n"
"s_add_u32 s92, s65, 32768\n"
"buffer_load_dwordx4 v198, s[36:39], s92 offen sc1 lds\n"
"v_mfma_f32_32x32x16_bf16 v[32:47], a[128:131], v[88:91], v[32:47]\n"
"s_waitcnt lgkmcnt(2)\n"
"v_mfma_f32_32x32x16_bf16 v[16:31], a[244:247], v[92:95], v[16:31]\n"
"v_mfma_f32_32x32x16_bf16 v[32:47], a[132:135], v[92:95], v[32:47]\n"
"s_waitcnt lgkmcnt(1)\n"
"v_mfma_f32_32x32x16_bf16 v[16:31], a[248:251], v[96:99], v[16:31]\n"
"v_mfma_f32_32x32x16_bf16 v[32:47], a[136:139], v[96:99], v[32:47]\n"
"s_waitcnt lgkmcnt(0)\n"
"v_mfma_f32_32x32x16_bf16 v[16:31], a[252:255], v[100:103], v[16:31]\n"
"v_mfma_f32_32x32x16_bf16 v[32:47], a[140:143], v[100:103], v[32:47]\n"
"s_mov_b32 s60, -1\n"
"s_mov_b32 s61, -1\n"
"s_cmp_eq_u32 s87, 0\n"
"s_cbranch_scc0 Lwaypack36_%=\n"
"s_cmp_lt_i32 s62, 0\n"
"s_cbranch_scc1 Lnoldpack36_%=\n"
"s_waitcnt vmcnt(9) lgkmcnt(0)\n"
"s_barrier\n"
"s_sub_u32 s67, 87552, 0\n"
"v_lshrrev_b32 v192, 2, v0\n"
"v_add_u32 v192, s67, v192\n"
"ds_read_b32 v193, v192\n"
"s_waitcnt lgkmcnt(0)\n"
"v_cmp_eq_u32_e64 s[80:81], s97, v193\n"
"s_nop 1\n"
"s_sub_u32 s68, s48, s62\n"
"s_min_u32 s68, s68, 2\n"
"s_mov_b32 s70, 0\n"
"Levs2pack36_%=:\n"
"s_cmp_ge_u32 s70, s68\n"
"s_cbranch_scc1 Lexs2pack36_%=\n"
"s_and_b32 s69, s80, 1\n"
"s_cmp_eq_u32 s69, 1\n"
"s_cbranch_scc0 Lexs2pack36_%=\n"
"s_lshr_b64 s[80:81], s[80:81], 1\n"
"s_add_u32 s70, s70, 1\n"
"s_branch Levs2pack36_%=\n"
"Lexs2pack36_%=:\n"
"s_add_u32 s58, s62, s70\n"
"s_cmp_lt_u32 s62, s58\n"
"s_cbranch_scc0 Lnoldpack36_%=\n"
"v_readlane_b32 s67, v6, s62\n"
"s_nop 0\n"
"s_mul_i32 s67, s67, s50\n"
"s_add_u32 s65, s67, s49\n"
"s_sub_u32 s64, 36864, s77\n"
"s_lshl_b32 s68, %0, 10\n"
"s_add_u32 s64, s64, s68\n"
"s_add_u32 m0, s64, 0\n"
"s_add_u32 s70, s65, 0\n"
"buffer_load_dwordx4 v2, s[36:39], s70 offen sc1 lds\n"
"s_add_u32 m0, s64, 4096\n"
"s_add_u32 s70, s65, 4096\n"
"buffer_load_dwordx4 v2, s[36:39], s70 offen sc1 lds\n"
"s_add_u32 m0, s64, 8192\n"
"s_add_u32 s70, s65, 8192\n"
"buffer_load_dwordx4 v2, s[36:39], s70 offen sc1 lds\n"
"s_add_u32 m0, s64, 12288\n"
"s_add_u32 s70, s65, 12288\n"
"buffer_load_dwordx4 v2, s[36:39], s70 offen sc1 lds\n"
"s_add_u32 m0, s64, 16384\n"
"s_add_u32 s70, s65, 16384\n"
"buffer_load_dwordx4 v2, s[36:39], s70 offen sc1 lds\n"
"s_add_u32 m0, s64, 20480\n"
"s_add_u32 s70, s65, 20480\n"
"buffer_load_dwordx4 v2, s[36:39], s70 offen sc1 lds\n"
"s_add_u32 m0, s64, 24576\n"
"s_add_u32 s70, s65, 24576\n"
"buffer_load_dwordx4 v2, s[36:39], s70 offen sc1 lds\n"
"s_add_u32 m0, s64, 28672\n"
"s_add_u32 s70, s65, 28672\n"
"buffer_load_dwordx4 v2, s[36:39], s70 offen sc1 lds\n"
"s_add_u32 m0, s64, 32768\n"
"s_add_u32 s70, s65, 32768\n"
"buffer_load_dwordx4 v2, s[36:39], s70 offen sc1 lds\n"
"Lwaypack36_%=:\n"
"s_waitcnt lgkmcnt(0)\n"
"s_nop 7\n"
"v_readlane_b32 s67, v6, s57\n"
"s_nop 0\n"
"s_mul_i32 s67, s67, s52\n"
"s_add_u32 s66, s67, s51\n"
"v_cvt_pk_bf16_f32 v160, v16, v17\n"
"v_cvt_pk_bf16_f32 v161, v18, v19\n"
"v_cvt_pk_bf16_f32 v162, v20, v21\n"
"v_cvt_pk_bf16_f32 v163, v22, v23\n"
"v_cvt_pk_bf16_f32 v164, v24, v25\n"
"v_cvt_pk_bf16_f32 v165, v26, v27\n"
"v_cvt_pk_bf16_f32 v166, v28, v29\n"
"v_cvt_pk_bf16_f32 v167, v30, v31\n"
"v_cvt_pk_bf16_f32 v168, v32, v33\n"
"v_cvt_pk_bf16_f32 v169, v34, v35\n"
"v_cvt_pk_bf16_f32 v170, v36, v37\n"
"v_cvt_pk_bf16_f32 v171, v38, v39\n"
"v_cvt_pk_bf16_f32 v172, v40, v41\n"
"v_cvt_pk_bf16_f32 v173, v42, v43\n"
"v_cvt_pk_bf16_f32 v174, v44, v45\n"
"v_cvt_pk_bf16_f32 v175, v46, v47\n"
"v_permlane32_swap_b32 v160, v162\n"
"v_permlane32_swap_b32 v161, v163\n"
"v_permlane32_swap_b32 v164, v166\n"
"v_permlane32_swap_b32 v165, v167\n"
"v_permlane32_swap_b32 v168, v170\n"
"v_permlane32_swap_b32 v169, v171\n"
"v_permlane32_swap_b32 v172, v174\n"
"v_permlane32_swap_b32 v173, v175\n"
"buffer_store_dwordx4 v[160:163], v11, s[40:43], s66 offen sc1\n"
"buffer_store_dwordx4 v[164:167], v12, s[40:43], s66 offen sc1\n"
"buffer_store_dwordx4 v[168:171], v13, s[40:43], s66 offen sc1\n"
"buffer_store_dwordx4 v[172:175], v14, s[40:43], s66 offen sc1\n"
"s_waitcnt vmcnt(4) lgkmcnt(0)\n"
"s_barrier\n"
"s_mov_b32 s61, s57\n"
"s_add_u32 s62, s57, 1\n"
"s_sub_u32 s77, 36864, s77\n"
"v_add_u32 v1, s77, v0\n"
"ds_read_b128 v[80:83], v1\n"
"ds_read_b128 v[84:87], v1 offset:1024\n"
"ds_read_b128 v[88:91], v1 offset:2048\n"
"ds_read_b128 v[92:95], v1 offset:3072\n"
"s_add_u32 s57, s57, 1\n"
"s_branch Ltoppack36_%=\n"
"Lnoldpack36_%=:\n"
"s_add_u32 s62, s57, 1\n"
"s_waitcnt lgkmcnt(0)\n"
"s_cmp_lt_u32 s62, s48\n"
"s_cbranch_scc0 Ln1pack36_%=\n"
"s_lshl_b32 s67, s62, 2\n"
"s_add_u32 s67, s67, 86528\n"
"v_add_u32 v192, s67, v8\n"
"ds_read_b32 v192, v192\n"
"s_waitcnt lgkmcnt(0)\n"
"v_add_u32 v192, v192, v9\n"
"s_mov_b32 m0, 87808\n"
"s_and_b32 exec_lo, s78, 3\n"
"s_mov_b32 exec_hi, 0\n"
"buffer_load_dword v192, s[44:47], 0 offen sc1 lds\n"
"s_mov_b64 exec, -1\n"
"s_waitcnt lgkmcnt(0)\n"
"Ln1pack36_%=:\n"
"s_nop 7\n"
"v_readlane_b32 s67, v6, s57\n"
"s_nop 0\n"
"s_mul_i32 s67, s67, s52\n"
"s_add_u32 s66, s67, s51\n"
"v_cvt_pk_bf16_f32 v160, v16, v17\n"
"v_cvt_pk_bf16_f32 v161, v18, v19\n"
"v_cvt_pk_bf16_f32 v162, v20, v21\n"
"v_cvt_pk_bf16_f32 v163, v22, v23\n"
"v_cvt_pk_bf16_f32 v164, v24, v25\n"
"v_cvt_pk_bf16_f32 v165, v26, v27\n"
"v_cvt_pk_bf16_f32 v166, v28, v29\n"
"v_cvt_pk_bf16_f32 v167, v30, v31\n"
"v_cvt_pk_bf16_f32 v168, v32, v33\n"
"v_cvt_pk_bf16_f32 v169, v34, v35\n"
"v_cvt_pk_bf16_f32 v170, v36, v37\n"
"v_cvt_pk_bf16_f32 v171, v38, v39\n"
"v_cvt_pk_bf16_f32 v172, v40, v41\n"
"v_cvt_pk_bf16_f32 v173, v42, v43\n"
"v_cvt_pk_bf16_f32 v174, v44, v45\n"
"v_cvt_pk_bf16_f32 v175, v46, v47\n"
"v_permlane32_swap_b32 v160, v162\n"
"v_permlane32_swap_b32 v161, v163\n"
"v_permlane32_swap_b32 v164, v166\n"
"v_permlane32_swap_b32 v165, v167\n"
"v_permlane32_swap_b32 v168, v170\n"
"v_permlane32_swap_b32 v169, v171\n"
"v_permlane32_swap_b32 v172, v174\n"
"v_permlane32_swap_b32 v173, v175\n"
"buffer_store_dwordx4 v[160:163], v11, s[40:43], s66 offen sc1\n"
"buffer_store_dwordx4 v[164:167], v12, s[40:43], s66 offen sc1\n"
"buffer_store_dwordx4 v[168:171], v13, s[40:43], s66 offen sc1\n"
"buffer_store_dwordx4 v[172:175], v14, s[40:43], s66 offen sc1\n"
"s_waitcnt vmcnt(4) lgkmcnt(0)\n"
"s_barrier\n"
"s_cmp_lt_u32 s62, s48\n"
"s_cbranch_scc0 Ln3pack36_%=\n"
"s_sub_u32 s67, 87808, 0\n"
"v_lshrrev_b32 v192, 2, v0\n"
"v_add_u32 v192, s67, v192\n"
"ds_read_b32 v193, v192\n"
"s_waitcnt lgkmcnt(0)\n"
"v_cmp_eq_u32_e64 s[80:81], s97, v193\n"
"s_nop 1\n"
"s_sub_u32 s68, s48, s62\n"
"s_min_u32 s68, s68, 2\n"
"s_mov_b32 s70, 0\n"
"Levnpack36_%=:\n"
"s_cmp_ge_u32 s70, s68\n"
"s_cbranch_scc1 Lexnpack36_%=\n"
"s_and_b32 s69, s80, 1\n"
"s_cmp_eq_u32 s69, 1\n"
"s_cbranch_scc0 Lexnpack36_%=\n"
"s_lshr_b64 s[80:81], s[80:81], 1\n"
"s_add_u32 s70, s70, 1\n"
"s_branch Levnpack36_%=\n"
"Lexnpack36_%=:\n"
"s_add_u32 s58, s62, s70\n"
"s_cmp_lt_u32 s62, s58\n"
"s_cbranch_scc0 Ln2pack36_%=\n"
"v_readlane_b32 s67, v6, s62\n"
"s_nop 0\n"
"s_mul_i32 s67, s67, s50\n"
"s_add_u32 s65, s67, s49\n"
"s_sub_u32 s64, 36864, s77\n"
"s_lshl_b32 s68, %0, 10\n"
"s_add_u32 s64, s64, s68\n"
"s_add_u32 m0, s64, 0\n"
"s_add_u32 s70, s65, 0\n"
"buffer_load_dwordx4 v2, s[36:39], s70 offen sc1 lds\n"
"s_add_u32 m0, s64, 4096\n"
"s_add_u32 s70, s65, 4096\n"
"buffer_load_dwordx4 v2, s[36:39], s70 offen sc1 lds\n"
"s_add_u32 m0, s64, 8192\n"
"s_add_u32 s70, s65, 8192\n"
"buffer_load_dwordx4 v2, s[36:39], s70 offen sc1 lds\n"
"s_add_u32 m0, s64, 12288\n"
"s_add_u32 s70, s65, 12288\n"
"buffer_load_dwordx4 v2, s[36:39], s70 offen sc1 lds\n"
"s_add_u32 m0, s64, 16384\n"
"s_add_u32 s70, s65, 16384\n"
"buffer_load_dwordx4 v2, s[36:39], s70 offen sc1 lds\n"
"s_add_u32 m0, s64, 20480\n"
"s_add_u32 s70, s65, 20480\n"
"buffer_load_dwordx4 v2, s[36:39], s70 offen sc1 lds\n"
"s_add_u32 m0, s64, 24576\n"
"s_add_u32 s70, s65, 24576\n"
"buffer_load_dwordx4 v2, s[36:39], s70 offen sc1 lds\n"
"s_add_u32 m0, s64, 28672\n"
"s_add_u32 s70, s65, 28672\n"
"buffer_load_dwordx4 v2, s[36:39], s70 offen sc1 lds\n"
"s_add_u32 m0, s64, 32768\n"
"s_add_u32 s70, s65, 32768\n"
"buffer_load_dwordx4 v2, s[36:39], s70 offen sc1 lds\n"
"s_waitcnt vmcnt(9) lgkmcnt(0)\n"
"s_barrier\n"
"s_cmp_lt_i32 s57, 0\n"
"s_cbranch_scc1 Lnsn1pack36_%=\n"
"v_readlane_b32 s67, v6, s57\n"
"s_nop 0\n"
"s_mul_i32 s67, s67, s54\n"
"s_add_u32 s67, s67, s53\n"
"buffer_store_dword v4, v3, s[44:47], s67 offen sc1\n"
"Lnsn1pack36_%=:\n"
"s_waitcnt vmcnt(1) lgkmcnt(0)\n"
"s_barrier\n"
"s_branch Ln4pack36_%=\n"
"Ln2pack36_%=:\n"
"s_lshl_b32 s67, s62, 2\n"
"s_add_u32 s67, s67, 86528\n"
"v_add_u32 v192, s67, v8\n"
"ds_read_b32 v192, v192\n"
"s_waitcnt lgkmcnt(0)\n"
"v_add_u32 v192, v192, v9\n"
"s_mov_b32 m0, 87296\n"
"s_and_b32 exec_lo, s78, 3\n"
"s_mov_b32 exec_hi, 0\n"
"buffer_load_dword v192, s[44:47], 0 offen sc1 lds\n"
"s_mov_b64 exec, -1\n"
"s_waitcnt vmcnt(0) lgkmcnt(0)\n"
"s_barrier\n"
"s_cmp_lt_i32 s57, 0\n"
"s_cbranch_scc1 Lnsn2pack36_%=\n"
"v_readlane_b32 s67, v6, s57\n"
"s_nop 0\n"
"s_mul_i32 s67, s67, s54\n"
"s_add_u32 s67, s67, s53\n"
"buffer_store_dword v4, v3, s[44:47], s67 offen sc1\n"
"Lnsn2pack36_%=:\n"
"s_sub_u32 s67, 87296, 0\n"
"v_lshrrev_b32 v192, 2, v0\n"
"v_add_u32 v192, s67, v192\n"
"ds_read_b32 v193, v192\n"
"s_waitcnt lgkmcnt(0)\n"
"v_cmp_eq_u32_e64 s[80:81], s97, v193\n"
"s_nop 1\n"
"s_sub_u32 s68, s48, s62\n"
"s_min_u32 s68, s68, 2\n"
"s_mov_b32 s70, 0\n"
"Levn2pack36_%=:\n"
"s_cmp_ge_u32 s70, s68\n"
"s_cbranch_scc1 Lexn2pack36_%=\n"
"s_and_b32 s69, s80, 1\n"
"s_cmp_eq_u32 s69, 1\n"
"s_cbranch_scc0 Lexn2pack36_%=\n"
"s_lshr_b64 s[80:81], s[80:81], 1\n"
"s_add_u32 s70, s70, 1\n"
"s_branch Levn2pack36_%=\n"
"Lexn2pack36_%=:\n"
"s_add_u32 s58, s62, s70\n"
"s_cmp_lt_u32 s62, s58\n"
"s_cbranch_scc0 Ln5pack36_%=\n"
"v_readlane_b32 s67, v6, s62\n"
"s_nop 0\n"
"s_mul_i32 s67, s67, s50\n"
"s_add_u32 s65, s67, s49\n"
"s_sub_u32 s64, 36864, s77\n"
"s_lshl_b32 s68, %0, 10\n"
"s_add_u32 s64, s64, s68\n"
"s_add_u32 m0, s64, 0\n"
"s_add_u32 s70, s65, 0\n"
"buffer_load_dwordx4 v2, s[36:39], s70 offen sc1 lds\n"
"s_add_u32 m0, s64, 4096\n"
"s_add_u32 s70, s65, 4096\n"
"buffer_load_dwordx4 v2, s[36:39], s70 offen sc1 lds\n"
"s_add_u32 m0, s64, 8192\n"
"s_add_u32 s70, s65, 8192\n"
"buffer_load_dwordx4 v2, s[36:39], s70 offen sc1 lds\n"
"s_add_u32 m0, s64, 12288\n"
"s_add_u32 s70, s65, 12288\n"
"buffer_load_dwordx4 v2, s[36:39], s70 offen sc1 lds\n"
"s_add_u32 m0, s64, 16384\n"
"s_add_u32 s70, s65, 16384\n"
"buffer_load_dwordx4 v2, s[36:39], s70 offen sc1 lds\n"
"s_add_u32 m0, s64, 20480\n"
"s_add_u32 s70, s65, 20480\n"
"buffer_load_dwordx4 v2, s[36:39], s70 offen sc1 lds\n"
"s_add_u32 m0, s64, 24576\n"
"s_add_u32 s70, s65, 24576\n"
"buffer_load_dwordx4 v2, s[36:39], s70 offen sc1 lds\n"
"s_add_u32 m0, s64, 28672\n"
"s_add_u32 s70, s65, 28672\n"
"buffer_load_dwordx4 v2, s[36:39], s70 offen sc1 lds\n"
"s_add_u32 m0, s64, 32768\n"
"s_add_u32 s70, s65, 32768\n"
"buffer_load_dwordx4 v2, s[36:39], s70 offen sc1 lds\n"
"s_waitcnt vmcnt(0) lgkmcnt(0)\n"
"s_barrier\n"
"s_branch Ln4pack36_%=\n"
"Ln5pack36_%=:\n"
"s_cmp_eq_u32 %0, 0\n"
"s_cbranch_scc0 Lbwnpack36_%=\n"
"s_cmp_lg_u32 s75, 0\n"
"s_cbranch_scc1 Lbwnpack36_%=\n"
"v_readlane_b32 s72, v7, s62\n"
"s_nop 3\n"
"s_mov_b32 s71, 0\n"
"s_cmp_lt_i32 s74, 0\n"
"s_cbranch_scc1 Lplnpack36_%=\n"
"s_sub_u32 s67, s62, s74\n"
"s_cmp_gt_u32 s67, 3\n"
"s_cbranch_scc1 Lplnpack36_%=\n"
"s_add_u32 s68, s62, 2\n"
"s_cmp_ge_u32 s68, s48\n"
"s_cbranch_scc1 Lplnpack36_%=\n"
"s_mul_i32 s67, s67, 240\n"
"s_add_u32 s67, s67, s73\n"
"s_sub_u32 s67, s67, 40\n"
"s_mov_b32 s76, 0\n"
"Lslnpack36_%=:\n"
"s_memrealtime s[84:85]\n"
"s_waitcnt lgkmcnt(0)\n"
"s_sub_u32 s68, s84, s67\n"
"s_cmp_ge_i32 s68, 0\n"
"s_cbranch_scc1 Lplnpack36_%=\n"
"s_sleep 2\n"
"s_mov_b32 s71, 1\n"
"s_add_u32 s76, s76, 1\n"
"s_cmp_lt_u32 s76, 32768\n"
"s_cbranch_scc1 Lslnpack36_%=\n"
"Lplnpack36_%=:\n"
"s_mov_b32 s76, 0\n"
"Lppnpack36_%=:\n"
"buffer_load_dword v192, v5, s[44:47], s72 offen sc1\n"
"s_waitcnt vmcnt(0)\n"
"v_cmp_ne_u32_e64 s[80:81], s97, v192\n"
"s_nop 1\n"
"s_and_b64 s[80:81], s[80:81], s[82:83]\n"
"s_cbranch_scc0 Lrdnpack36_%=\n"
"s_sleep 3\n"
"s_add_u32 s76, s76, 1\n"
"s_cmp_lt_u32 s76, 32768\n"
"s_cbranch_scc1 Lppnpack36_%=\n"
"s_mov_b32 s75, 1\n"
"v_mov_b32 v192, 16384\n"
"v_mov_b32 v193, 7000\n"
"buffer_store_dword v193, v192, s[44:47], 0 offen sc1\n"
"Lrdnpack36_%=:\n"
"s_mov_b32 s74, -1\n"
"s_cmp_eq_u32 s76, 0\n"
"s_cbranch_scc1 Lbwnpack36_%=\n"
"s_memrealtime s[84:85]\n"
"s_waitcnt lgkmcnt(0)\n"
"s_mov_b32 s73, s84\n"
"s_mov_b32 s74, s62\n"
"Lbwnpack36_%=:\n"
"s_waitcnt lgkmcnt(0)\n"
"s_barrier\n"
"v_readlane_b32 s67, v6, s62\n"
"s_nop 0\n"
"s_mul_i32 s67, s67, s50\n"
"s_add_u32 s65, s67, s49\n"
"s_sub_u32 s64, 36864, s77\n"
"s_lshl_b32 s68, %0, 10\n"
"s_add_u32 s64, s64, s68\n"
"s_add_u32 m0, s64, 0\n"
"s_add_u32 s70, s65, 0\n"
"buffer_load_dwordx4 v2, s[36:39], s70 offen sc1 lds\n"
"s_add_u32 m0, s64, 4096\n"
"s_add_u32 s70, s65, 4096\n"
"buffer_load_dwordx4 v2, s[36:39], s70 offen sc1 lds\n"
"s_add_u32 m0, s64, 8192\n"
"s_add_u32 s70, s65, 8192\n"
"buffer_load_dwordx4 v2, s[36:39], s70 offen sc1 lds\n"
"s_add_u32 m0, s64, 12288\n"
"s_add_u32 s70, s65, 12288\n"
"buffer_load_dwordx4 v2, s[36:39], s70 offen sc1 lds\n"
"s_add_u32 m0, s64, 16384\n"
"s_add_u32 s70, s65, 16384\n"
"buffer_load_dwordx4 v2, s[36:39], s70 offen sc1 lds\n"
"s_add_u32 m0, s64, 20480\n"
"s_add_u32 s70, s65, 20480\n"
"buffer_load_dwordx4 v2, s[36:39], s70 offen sc1 lds\n"
"s_add_u32 m0, s64, 24576\n"
"s_add_u32 s70, s65, 24576\n"
"buffer_load_dwordx4 v2, s[36:39], s70 offen sc1 lds\n"
"s_add_u32 m0, s64, 28672\n"
"s_add_u32 s70, s65, 28672\n"
"buffer_load_dwordx4 v2, s[36:39], s70 offen sc1 lds\n"
"s_add_u32 m0, s64, 32768\n"
"s_add_u32 s70, s65, 32768\n"
"buffer_load_dwordx4 v2, s[36:39], s70 offen sc1 lds\n"
"s_waitcnt vmcnt(0) lgkmcnt(0)\n"
"s_barrier\n"
"s_add_u32 s58, s62, 1\n"
"Ln4pack36_%=:\n"
"s_sub_u32 s77, 36864, s77\n"
"v_add_u32 v1, s77, v0\n"
"ds_read_b128 v[80:83], v1\n"
"ds_read_b128 v[84:87], v1 offset:1024\n"
"ds_read_b128 v[88:91], v1 offset:2048\n"
"ds_read_b128 v[92:95], v1 offset:3072\n"
"s_add_u32 s57, s57, 1\n"
"s_branch Ltoppack36_%=\n"
"Ln3pack36_%=:\n"
"s_waitcnt vmcnt(0) lgkmcnt(0)\n"
"s_barrier\n"
"s_cmp_lt_i32 s57, 0\n"
"s_cbranch_scc1 Lnsn3pack36_%=\n"
"v_readlane_b32 s67, v6, s57\n"
"s_nop 0\n"
"s_mul_i32 s67, s67, s54\n"
"s_add_u32 s67, s67, s53\n"
"buffer_store_dword v4, v3, s[44:47], s67 offen sc1\n"
"Lnsn3pack36_%=:\n"
"s_waitcnt vmcnt(0) lgkmcnt(0)\n"
"s_barrier\n"
    GEMM_ASM_END
"v0","v1","v2","v3","v4","v5","v6","v7","v8","v9","v10","v11","v12","v13","v14","v15","v16","v17","v18","v19","v20","v21","v22","v23","v24","v25","v26","v27","v28","v29","v30","v31","v32","v33","v34","v35","v36","v37","v38","v39","v40","v41","v42","v43","v44","v45","v46","v47","v48","v49","v50","v51","v52","v53","v54","v55","v56","v57","v58","v59","v60","v61","v62","v63","v64","v65","v66","v67","v68","v69","v70","v71","v72","v73","v74","v75","v76","v77","v78","v79","v80","v81","v82","v83","v84","v85","v86","v87","v88","v89","v90","v91","v92","v93","v94","v95","v96","v97","v98","v99","v100","v101","v102","v103","v104","v105","v106","v107","v108","v109","v110","v111","v112","v113","v114","v115","v116","v117","v118","v119","v120","v121","v122","v123","v124","v125","v126","v128","v129","v130","v131","v132","v133","v134","v135","v136","v137","v138","v139","v140","v141","v142","v143","v144","v145","v146","v147","v148","v149","v150","v151","v152","v153","v154","v155","v156","v157","v158","v159","v160","v161","v162","v163","v164","v165","v166","v167","v168","v169","v170","v171","v172","v173","v174","v175","v176","v177","v178","v179","v180","v181","v182","v183","v184","v185","v186","v187","v188","v189","v190","v191","v192","v193","v194","v195","v196","v197","v198","v199","v200","v201","v202","v203","v204","v205","v206","v207","v208","v209","v210","v211","v212","v213","v214","v215","v216","v217","v218","v219","v220","v221","v222","v223","v224","v225","v226","v227","v228","v229","v230","v231","v232","v233","v234","v235","v236","v237","v238","v239","v240","v241","v242","v243","v244","v245","v246","v247","v248","v249","v250","v251","v252","v253","v254","v255","a0","a1","a2","a3","a4","a5","a6","a7","a8","a9","a10","a11","a12","a13","a14","a15","a16","a17","a18","a19","a20","a21","a22","a23","a24","a25","a26","a27","a28","a29","a30","a31","a32","a33","a34","a35","a36","a37","a38","a39","a40","a41","a42","a43","a44","a45","a46","a47","a48","a49","a50","a51","a52","a53","a54","a55","a56","a57","a58","a59","a60","a61","a62","a63","a64","a65","a66","a67","a68","a69","a70","a71","a72","a73","a74","a75","a76","a77","a78","a79","a80","a81","a82","a83","a84","a85","a86","a87","a88","a89","a90","a91","a92","a93","a94","a95","a96","a97","a98","a99","a100","a101","a102","a103","a104","a105","a106","a107","a108","a109","a110","a111","a112","a113","a114","a115","a116","a117","a118","a119","a120","a121","a122","a123","a124","a125","a126","a127","a128","a129","a130","a131","a132","a133","a134","a135","a136","a137","a138","a139","a140","a141","a142","a143","a144","a145","a146","a147","a148","a149","a150","a151","a152","a153","a154","a155","a156","a157","a158","a159","a160","a161","a162","a163","a164","a165","a166","a167","a168","a169","a170","a171","a172","a173","a174","a175","a176","a177","a178","a179","a180","a181","a182","a183","a184","a185","a186","a187","a188","a189","a190","a191","a192","a193","a194","a195","a196","a197","a198","a199","a200","a201","a202","a203","a204","a205","a206","a207","a208","a209","a210","a211","a212","a213","a214","a215","a216","a217","a218","a219","a220","a221","a222","a223","a224","a225","a226","a227","a228","a229","a230","a231","a232","a233","a234","a235","a236","a237","a238","a239","a240","a241","a242","a243","a244","a245","a246","a247","a248","a249","a250","a251","a252","a253","a254","a255","s36","s37","s38","s39","s40","s41","s42","s43","s44","s45","s46","s47","s48","s49","s50","s51","s52","s53","s54","s55","s56","s57","s58","s59","s60","s61","s62","s63","s64","s65","s66","s67","s68","s69","s70","s71","s72","s73","s74","s75","s76","s77","s78","s79","s80","s81","s82","s83","s84","s85","s86","s87","s88","s89","s90","s91","s92","s93","s94","s95","s96","s97","s98","s99","vcc","scc","memory"
    ); }
  else { GEMM_ASM_BEGIN
"v_mbcnt_lo_u32_b32 v10, -1, 0\n"
"v_mbcnt_hi_u32_b32 v10, -1, v10\n"
"v_lshlrev_b32 v0, 4, v10\n"
"v_lshlrev_b32 v192, 2, v10\n"
"v_add_u32 v192, 87040, v192\n"
"ds_read_b32 v193, v192\n"
"s_lshl_b32 s67, %0, 12\n"
"s_add_u32 s67, s67, 90112\n"
"v_lshlrev_b32 v194, 6, v10\n"
"v_add_u32 v194, s67, v194\n"
"ds_read_b128 v[160:163], v194\n"
"ds_read_b128 v[164:167], v194 offset:16\n"
"ds_read_b128 v[168:171], v194 offset:32\n"
"ds_read_b128 v[172:175], v194 offset:48\n"
"s_waitcnt lgkmcnt(0)\n"
"v_readlane_b32 s36, v193, 0\n"
"v_readlane_b32 s37, v193, 1\n"
"v_readlane_b32 s38, v193, 2\n"
"v_readlane_b32 s40, v193, 3\n"
"v_readlane_b32 s41, v193, 4\n"
"v_readlane_b32 s42, v193, 5\n"
"v_readlane_b32 s44, v193, 6\n"
"v_readlane_b32 s45, v193, 7\n"
"v_readlane_b32 s46, v193, 8\n"
"v_readlane_b32 s48, v193, 9\n"
"v_readlane_b32 s49, v193, 10\n"
"v_readlane_b32 s50, v193, 11\n"
"v_readlane_b32 s51, v193, 12\n"
"v_readlane_b32 s52, v193, 13\n"
"v_readlane_b32 s53, v193, 14\n"
"v_readlane_b32 s54, v193, 15\n"
"v_readlane_b32 s55, v193, 16\n"
"v_readlane_b32 s56, v193, 17\n"
"v_readlane_b32 s97, v193, 18\n"
"v_readlane_b32 s98, v193, 19\n"
"s_nop 1\n"
"s_and_b32 s37, s37, 0xffff\n"
"s_mov_b32 s39, 0x20000\n"
"s_and_b32 s41, s41, 0xffff\n"
"s_mov_b32 s43, 0x20000\n"
"s_and_b32 s45, s45, 0xffff\n"
"s_mov_b32 s47, 0x20000\n"
"v_mov_b32 v176, v160\n"
"v_mov_b32 v177, v161\n"
"v_mov_b32 v178, v162\n"
"v_mov_b32 v179, v163\n"
"v_mov_b32 v2, v164\n"
"v_mov_b32 v11, v165\n"
"v_mov_b32 v12, v166\n"
"v_mov_b32 v13, v167\n"
"v_mov_b32 v14, v168\n"
"v_mov_b32 v3, v169\n"
"v_mov_b32 v8, v170\n"
"v_mov_b32 v9, v171\n"
"v_mov_b32 v180, v172\n"
"v_mov_b32 v181, v173\n"
"v_mov_b32 v6, v174\n"
"v_mov_b32 v7, v175\n"
"v_mov_b32 v4, 1\n"
"v_mov_b32 v15, 0x7ffffff0\n"
"v_lshlrev_b32 v5, 7, v10\n"
"v_cmp_gt_u32_e32 vcc, 1, v10\n"
"s_nop 1\n"
"v_cndmask_b32_e32 v5, v15, v5, vcc\n"
"s_mov_b64 s[82:83], 1\n"
"s_mov_b64 s[78:79], 0\n"
"s_cmp_eq_u32 %0, 2\n"
"s_cbranch_scc0 Lnxvord36_%=\n"
"s_mov_b32 s78, 0xffff\n"
"s_mov_b32 s79, 0x0\n"
"Lnxvord36_%=:\n"
"s_mov_b32 s75, 0\n"
"s_mov_b32 s74, -1\n"
"s_mov_b32 s73, 0\n"
"s_barrier\n"
"v_mov_b32 v200, 20480\n"
"buffer_load_dword v200, v200, s[44:47], 0 offen sc1\n"
"v_add_u32 v182, 0x10000, v180\n"
"v_add_u32 v183, 0x10000, v181\n"
"v_add_u32 v184, 0x20000, v180\n"
"v_add_u32 v185, 0x20000, v181\n"
"s_lshl_b32 s69, %0, 10\n"
"s_mov_b32 s96, 0\n"
"s_add_u32 m0, s69, 0\n"
"s_nop 0\n"
"global_load_lds_dwordx4 v[176:177], off\n"
"v_add_co_u32_e32 v176, vcc, s55, v176\n"
"v_addc_co_u32_e32 v177, vcc, 0, v177, vcc\n"
"s_add_u32 m0, s69, 4096\n"
"s_nop 0\n"
"global_load_lds_dwordx4 v[176:177], off\n"
"v_add_co_u32_e32 v176, vcc, s55, v176\n"
"v_addc_co_u32_e32 v177, vcc, 0, v177, vcc\n"
"s_add_u32 m0, s69, 8192\n"
"s_nop 0\n"
"global_load_lds_dwordx4 v[176:177], off\n"
"v_add_co_u32_e32 v176, vcc, s55, v176\n"
"v_addc_co_u32_e32 v177, vcc, 0, v177, vcc\n"
"s_add_u32 m0, s69, 12288\n"
"s_nop 0\n"
"global_load_lds_dwordx4 v[176:177], off\n"
"v_add_co_u32_e32 v176, vcc, s55, v176\n"
"v_addc_co_u32_e32 v177, vcc, 0, v177, vcc\n"
"s_add_u32 m0, s69, 16384\n"
"s_nop 0\n"
"global_load_lds_dwordx4 v[176:177], off\n"
"v_add_co_u32_e32 v176, vcc, s55, v176\n"
"v_addc_co_u32_e32 v177, vcc, 0, v177, vcc\n"
"s_add_u32 m0, s69, 20480\n"
"s_nop 0\n"
"global_load_lds_dwordx4 v[176:177], off\n"
"v_add_co_u32_e32 v176, vcc, s55, v176\n"
"v_addc_co_u32_e32 v177, vcc, 0, v177, vcc\n"
"s_add_u32 m0, s69, 24576\n"
"s_nop 0\n"
"global_load_lds_dwordx4 v[176:177], off\n"
"v_add_co_u32_e32 v176, vcc, s55, v176\n"
"v_addc_co_u32_e32 v177, vcc, 0, v177, vcc\n"
"s_add_u32 m0, s69, 28672\n"
"s_nop 0\n"
"global_load_lds_dwordx4 v[176:177], off\n"
"v_add_co_u32_e32 v176, vcc, s55, v176\n"
"v_addc_co_u32_e32 v177, vcc, 0, v177, vcc\n"
"s_add_u32 m0, s69, 32768\n"
"s_nop 0\n"
"global_load_lds_dwordx4 v[176:177], off\n"
"v_add_co_u32_e32 v176, vcc, s55, v176\n"
"v_addc_co_u32_e32 v177, vcc, 0, v177, vcc\n"
"s_add_u32 m0, s69, 36864\n"
"s_nop 0\n"
"global_load_lds_dwordx4 v[176:177], off\n"
"v_add_co_u32_e32 v176, vcc, s55, v176\n"
"v_addc_co_u32_e32 v177, vcc, 0, v177, vcc\n"
"s_add_u32 m0, s69, 40960\n"
"s_nop 0\n"
"global_load_lds_dwordx4 v[176:177], off\n"
"v_add_co_u32_e32 v176, vcc, s55, v176\n"
"v_addc_co_u32_e32 v177, vcc, 0, v177, vcc\n"
"s_add_u32 m0, s69, 45056\n"
"s_nop 0\n"
"global_load_lds_dwordx4 v[176:177], off\n"
"v_add_co_u32_e32 v176, vcc, s55, v176\n"
"v_addc_co_u32_e32 v177, vcc, 0, v177, vcc\n"
"s_add_u32 m0, s69, 49152\n"
"s_nop 0\n"
"global_load_lds_dwordx4 v[176:177], off\n"
"v_add_co_u32_e32 v176, vcc, s55, v176\n"
"v_addc_co_u32_e32 v177, vcc, 0, v177, vcc\n"
"s_add_u32 m0, s69, 53248\n"
"s_nop 0\n"
"global_load_lds_dwordx4 v[176:177], off\n"
"v_add_co_u32_e32 v176, vcc, s55, v176\n"
"v_addc_co_u32_e32 v177, vcc, 0, v177, vcc\n"
"s_add_u32 m0, s69, 57344\n"
"s_nop 0\n"
"global_load_lds_dwordx4 v[176:177], off\n"
"v_add_co_u32_e32 v176, vcc, s55, v176\n"
"v_addc_co_u32_e32 v177, vcc, 0, v177, vcc\n"
"s_add_u32 m0, s69, 61440\n"
"s_nop 0\n"
"global_load_lds_dwordx4 v[176:177], off\n"
"v_add_co_u32_e32 v176, vcc, s55, v176\n"
"v_addc_co_u32_e32 v177, vcc, 0, v177, vcc\n"
"s_add_u32 m0, s69, 65536\n"
"s_nop 0\n"
"global_load_lds_dwordx4 v[176:177], off\n"
"v_add_co_u32_e32 v176, vcc, s55, v176\n"
"v_addc_co_u32_e32 v177, vcc, 0, v177, vcc\n"
"s_add_u32 m0, s69, 69632\n"
"s_nop 0\n"
"global_load_lds_dwordx4 v[176:177], off\n"
"v_add_co_u32_e32 v176, vcc, s55, v176\n"
"v_addc_co_u32_e32 v177, vcc, 0, v177, vcc\n"
"s_add_u32 m0, s69, 73728\n"
"s_nop 0\n"
"global_load_lds_dwordx4 v[176:177], off\n"
"v_add_co_u32_e32 v176, vcc, s55, v176\n"
"v_addc_co_u32_e32 v177, vcc, 0, v177, vcc\n"
"s_add_u32 m0, s69, 77824\n"
"s_nop 0\n"
"global_load_lds_dwordx4 v[176:177], off\n"
"v_add_co_u32_e32 v176, vcc, s55, v176\n"
"v_addc_co_u32_e32 v177, vcc, 0, v177, vcc\n"
"s_add_u32 m0, s69, 81920\n"
"s_nop 0\n"
"global_load_lds_dwordx4 v[176:177], off\n"
"v_add_co_u32_e32 v176, vcc, s55, v176\n"
"v_addc_co_u32_e32 v177, vcc, 0, v177, vcc\n"
"s_add_u32 m0, s69, 86016\n"
"s_nop 0\n"
"global_load_lds_dwordx4 v[176:177], off\n"
"v_add_co_u32_e32 v176, vcc, s55, v176\n"
"v_addc_co_u32_e32 v177, vcc, 0, v177, vcc\n"
"s_add_u32 m0, s69, 90112\n"
"s_nop 0\n"
"global_load_lds_dwordx4 v[176:177], off\n"
"v_add_co_u32_e32 v176, vcc, s55, v176\n"
"v_addc_co_u32_e32 v177, vcc, 0, v177, vcc\n"
"s_add_u32 m0, s69, 94208\n"
"s_nop 0\n"
"global_load_lds_dwordx4 v[176:177], off\n"
"v_add_co_u32_e32 v176, vcc, s55, v176\n"
"v_addc_co_u32_e32 v177, vcc, 0, v177, vcc\n"
"s_add_u32 m0, s69, 98304\n"
"s_nop 0\n"
"global_load_lds_dwordx4 v[176:177], off\n"
"v_add_co_u32_e32 v176, vcc, s55, v176\n"
"v_addc_co_u32_e32 v177, vcc, 0, v177, vcc\n"
"s_add_u32 m0, s69, 102400\n"
"s_nop 0\n"
"global_load_lds_dwordx4 v[176:177], off\n"
"v_add_co_u32_e32 v176, vcc, s55, v176\n"
"v_addc_co_u32_e32 v177, vcc, 0, v177, vcc\n"
"s_add_u32 m0, s69, 106496\n"
"s_nop 0\n"
"global_load_lds_dwordx4 v[176:177], off\n"
"v_add_co_u32_e32 v176, vcc, s55, v176\n"
"v_addc_co_u32_e32 v177, vcc, 0, v177, vcc\n"
"s_add_u32 m0, s69, 110592\n"
"s_nop 0\n"
"global_load_lds_dwordx4 v[176:177], off\n"
"v_add_co_u32_e32 v176, vcc, s55, v176\n"
"v_addc_co_u32_e32 v177, vcc, 0, v177, vcc\n"
"s_add_u32 m0, s69, 114688\n"
"s_nop 0\n"
"global_load_lds_dwordx4 v[176:177], off\n"
"v_add_co_u32_e32 v176, vcc, s55, v176\n"
"v_addc_co_u32_e32 v177, vcc, 0, v177, vcc\n"
"s_add_u32 m0, s69, 118784\n"
"s_nop 0\n"
"global_load_lds_dwordx4 v[176:177], off\n"
"v_add_co_u32_e32 v176, vcc, s55, v176\n"
"v_addc_co_u32_e32 v177, vcc, 0, v177, vcc\n"
"s_add_u32 m0, s69, 122880\n"
"s_nop 0\n"
"global_load_lds_dwordx4 v[176:177], off\n"
"v_add_co_u32_e32 v176, vcc, s55, v176\n"
"v_addc_co_u32_e32 v177, vcc, 0, v177, vcc\n"
"s_add_u32 m0, s69, 126976\n"
"s_nop 0\n"
"global_load_lds_dwordx4 v[176:177], off\n"
"v_add_co_u32_e32 v176, vcc, s55, v176\n"
"v_addc_co_u32_e32 v177, vcc, 0, v177, vcc\n"
"s_add_u32 m0, s69, 131072\n"
"s_nop 0\n"
"global_load_lds_dwordx4 v[176:177], off\n"
"v_add_co_u32_e32 v176, vcc, s55, v176\n"
"v_addc_co_u32_e32 v177, vcc, 0, v177, vcc\n"
"s_add_u32 m0, s69, 135168\n"
"s_nop 0\n"
"global_load_lds_dwordx4 v[176:177], off\n"
"v_add_co_u32_e32 v176, vcc, s55, v176\n"
"v_addc_co_u32_e32 v177, vcc, 0, v177, vcc\n"
"s_add_u32 m0, s69, 139264\n"
"s_nop 0\n"
"global_load_lds_dwordx4 v[176:177], off\n"
"v_add_co_u32_e32 v176, vcc, s55, v176\n"
"v_addc_co_u32_e32 v177, vcc, 0, v177, vcc\n"
"s_add_u32 m0, s69, 143360\n"
"s_nop 0\n"
"global_load_lds_dwordx4 v[176:177], off\n"
"s_waitcnt vmcnt(27)\n"
"s_barrier\n"
"v_readfirstlane_b32 s90, v200\n"
"s_nop 3\n"
"s_add_u32 s90, s90, 1\n"
"s_cmp_eq_u32 s98, 1\n"
"s_cselect_b32 s97, s90, s97\n"
"v_mov_b32 v4, s97\n"
"v_mov_b32 v201, 163836\n"
"ds_write_b32 v201, v4\n"
"ds_read_b64_tr_b16 v[128:129], v180\n"
"ds_read_b64_tr_b16 v[130:131], v181\n"
"ds_read_b64_tr_b16 v[132:133], v180 offset:4096\n"
"ds_read_b64_tr_b16 v[134:135], v181 offset:4096\n"
"ds_read_b64_tr_b16 v[136:137], v180 offset:8192\n"
"ds_read_b64_tr_b16 v[138:139], v181 offset:8192\n"
"ds_read_b64_tr_b16 v[140:141], v180 offset:12288\n"
"ds_read_b64_tr_b16 v[142:143], v181 offset:12288\n"
"ds_read_b64_tr_b16 v[144:145], v180 offset:16384\n"
"ds_read_b64_tr_b16 v[146:147], v181 offset:16384\n"
"ds_read_b64_tr_b16 v[148:149], v180 offset:20480\n"
"ds_read_b64_tr_b16 v[150:151], v181 offset:20480\n"
"ds_read_b64_tr_b16 v[152:153], v180 offset:24576\n"
"ds_read_b64_tr_b16 v[154:155], v181 offset:24576\n"
"s_waitcnt lgkmcnt(8)\n"
"ds_read_b64_tr_b16 v[156:157], v180 offset:28672\n"
"ds_read_b64_tr_b16 v[158:159], v181 offset:28672\n"
"ds_read_b64_tr_b16 a[144:145], v180 offset:32768\n"
"ds_read_b64_tr_b16 a[146:147], v181 offset:32768\n"
"s_waitcnt lgkmcnt(0)\n"
"s_barrier\n"
"s_add_u32 m0, s69, 0\n"
"s_nop 0\n"
"global_load_lds_dwordx4 v[178:179], off\n"
"v_add_co_u32_e32 v178, vcc, s55, v178\n"
"v_addc_co_u32_e32 v179, vcc, 0, v179, vcc\n"
"s_add_u32 m0, s69, 4096\n"
"s_nop 0\n"
"global_load_lds_dwordx4 v[178:179], off\n"
"v_add_co_u32_e32 v178, vcc, s55, v178\n"
"v_addc_co_u32_e32 v179, vcc, 0, v179, vcc\n"
"s_add_u32 m0, s69, 8192\n"
"s_nop 0\n"
"global_load_lds_dwordx4 v[178:179], off\n"
"v_add_co_u32_e32 v178, vcc, s55, v178\n"
"v_addc_co_u32_e32 v179, vcc, 0, v179, vcc\n"
"s_add_u32 m0, s69, 12288\n"
"s_nop 0\n"
"global_load_lds_dwordx4 v[178:179], off\n"
"v_add_co_u32_e32 v178, vcc, s55, v178\n"
"v_addc_co_u32_e32 v179, vcc, 0, v179, vcc\n"
"s_add_u32 m0, s69, 16384\n"
"s_nop 0\n"
"global_load_lds_dwordx4 v[178:179], off\n"
"v_add_co_u32_e32 v178, vcc, s55, v178\n"
"v_addc_co_u32_e32 v179, vcc, 0, v179, vcc\n"
"s_add_u32 m0, s69, 20480\n"
"s_nop 0\n"
"global_load_lds_dwordx4 v[178:179], off\n"
"v_add_co_u32_e32 v178, vcc, s55, v178\n"
"v_addc_co_u32_e32 v179, vcc, 0, v179, vcc\n"
"s_add_u32 m0, s69, 24576\n"
"s_nop 0\n"
"global_load_lds_dwordx4 v[178:179], off\n"
"v_add_co_u32_e32 v178, vcc, s55, v178\n"
"v_addc_co_u32_e32 v179, vcc, 0, v179, vcc\n"
"s_add_u32 m0, s69, 28672\n"
"s_nop 0\n"
"global_load_lds_dwordx4 v[178:179], off\n"
"v_add_co_u32_e32 v178, vcc, s55, v178\n"
"v_addc_co_u32_e32 v179, vcc, 0, v179, vcc\n"
"s_add_u32 m0, s69, 32768\n"
"s_nop 0\n"
"global_load_lds_dwordx4 v[178:179], off\n"
"v_add_co_u32_e32 v178, vcc, s55, v178\n"
"v_addc_co_u32_e32 v179, vcc, 0, v179, vcc\n"
"s_waitcnt vmcnt(27)\n"
"s_barrier\n"
"ds_read_b64_tr_b16 a[148:149], v180 offset:36864\n"
"ds_read_b64_tr_b16 a[150:151], v181 offset:36864\n"
"ds_read_b64_tr_b16 a[152:153], v180 offset:40960\n"
"ds_read_b64_tr_b16 a[154:155], v181 offset:40960\n"
"ds_read_b64_tr_b16 a[156:157], v180 offset:45056\n"
"ds_read_b64_tr_b16 a[158:159], v181 offset:45056\n"
"ds_read_b64_tr_b16 a[160:161], v180 offset:49152\n"
"ds_read_b64_tr_b16 a[162:163], v181 offset:49152\n"
"ds_read_b64_tr_b16 a[164:165], v180 offset:53248\n"
"ds_read_b64_tr_b16 a[166:167], v181 offset:53248\n"
"ds_read_b64_tr_b16 a[168:169], v180 offset:57344\n"
"ds_read_b64_tr_b16 a[170:171], v181 offset:57344\n"
"ds_read_b64_tr_b16 a[172:173], v180 offset:61440\n"
"ds_read_b64_tr_b16 a[174:175], v181 offset:61440\n"
"ds_read_b64_tr_b16 a[176:177], v182\n"
"s_waitcnt lgkmcnt(8)\n"
"ds_read_b64_tr_b16 a[178:179], v183\n"
"ds_read_b64_tr_b16 a[180:181], v182 offset:4096\n"
"ds_read_b64_tr_b16 a[182:183], v183 offset:4096\n"
"s_waitcnt lgkmcnt(0)\n"
"s_barrier\n"
"s_add_u32 m0, s69, 36864\n"
"s_nop 0\n"
"global_load_lds_dwordx4 v[178:179], off\n"
"v_add_co_u32_e32 v178, vcc, s55, v178\n"
"v_addc_co_u32_e32 v179, vcc, 0, v179, vcc\n"
"s_add_u32 m0, s69, 40960\n"
"s_nop 0\n"
"global_load_lds_dwordx4 v[178:179], off\n"
"v_add_co_u32_e32 v178, vcc, s55, v178\n"
"v_addc_co_u32_e32 v179, vcc, 0, v179, vcc\n"
"s_add_u32 m0, s69, 45056\n"
"s_nop 0\n"
"global_load_lds_dwordx4 v[178:179], off\n"
"v_add_co_u32_e32 v178, vcc, s55, v178\n"
"v_addc_co_u32_e32 v179, vcc, 0, v179, vcc\n"
"s_add_u32 m0, s69, 49152\n"
"s_nop 0\n"
"global_load_lds_dwordx4 v[178:179], off\n"
"v_add_co_u32_e32 v178, vcc, s55, v178\n"
"v_addc_co_u32_e32 v179, vcc, 0, v179, vcc\n"
"s_add_u32 m0, s69, 53248\n"
"s_nop 0\n"
"global_load_lds_dwordx4 v[178:179], off\n"
"v_add_co_u32_e32 v178, vcc, s55, v178\n"
"v_addc_co_u32_e32 v179, vcc, 0, v179, vcc\n"
"s_add_u32 m0, s69, 57344\n"
"s_nop 0\n"
"global_load_lds_dwordx4 v[178:179], off\n"
"v_add_co_u32_e32 v178, vcc, s55, v178\n"
"v_addc_co_u32_e32 v179, vcc, 0, v179, vcc\n"
"s_add_u32 m0, s69, 61440\n"
"s_nop 0\n"
"global_load_lds_dwordx4 v[178:179], off\n"
"v_add_co_u32_e32 v178, vcc, s55, v178\n"
"v_addc_co_u32_e32 v179, vcc, 0, v179, vcc\n"
"s_add_u32 m0, s69, 65536\n"
"s_nop 0\n"
"global_load_lds_dwordx4 v[178:179], off\n"
"v_add_co_u32_e32 v178, vcc, s55, v178\n"
"v_addc_co_u32_e32 v179, vcc, 0, v179, vcc\n"
"s_add_u32 m0, s69, 69632\n"
"s_nop 0\n"
"global_load_lds_dwordx4 v[178:179], off\n"
"v_add_co_u32_e32 v178, vcc, s55, v178\n"
"v_addc_co_u32_e32 v179, vcc, 0, v179, vcc\n"
"s_waitcnt vmcnt(27)\n"
"s_barrier\n"
"ds_read_b64_tr_b16 a[184:185], v182 offset:8192\n"
"ds_read_b64_tr_b16 a[186:187], v183 offset:8192\n"
"ds_read_b64_tr_b16 a[188:189], v182 offset:12288\n"
"ds_read_b64_tr_b16 a[190:191], v183 offset:12288\n"
"ds_read_b64_tr_b16 a[192:193], v182 offset:16384\n"
"ds_read_b64_tr_b16 a[194:195], v183 offset:16384\n"
"ds_read_b64_tr_b16 a[196:197], v182 offset:20480\n"
"ds_read_b64_tr_b16 a[198:199], v183 offset:20480\n"
"ds_read_b64_tr_b16 a[200:201], v182 offset:24576\n"
"ds_read_b64_tr_b16 a[202:203], v183 offset:24576\n"
"ds_read_b64_tr_b16 a[204:205], v182 offset:28672\n"
"ds_read_b64_tr_b16 a[206:207], v183 offset:28672\n"
"ds_read_b64_tr_b16 a[208:209], v182 offset:32768\n"
"ds_read_b64_tr_b16 a[210:211], v183 offset:32768\n"
"ds_read_b64_tr_b16 a[212:213], v182 offset:36864\n"
"s_waitcnt lgkmcnt(8)\n"
"ds_read_b64_tr_b16 a[214:215], v183 offset:36864\n"
"ds_read_b64_tr_b16 a[216:217], v182 offset:40960\n"
"ds_read_b64_tr_b16 a[218:219], v183 offset:40960\n"
"s_waitcnt lgkmcnt(0)\n"
"s_barrier\n"
"s_add_u32 m0, s69, 73728\n"
"s_nop 0\n"
"global_load_lds_dwordx4 v[178:179], off\n"
"v_add_co_u32_e32 v178, vcc, s55, v178\n"
"v_addc_co_u32_e32 v179, vcc, 0, v179, vcc\n"
"s_add_u32 m0, s69, 77824\n"
"s_nop 0\n"
"global_load_lds_dwordx4 v[178:179], off\n"
"v_add_co_u32_e32 v178, vcc, s55, v178\n"
"v_addc_co_u32_e32 v179, vcc, 0, v179, vcc\n"
"s_add_u32 m0, s69, 81920\n"
"s_nop 0\n"
"global_load_lds_dwordx4 v[178:179], off\n"
"v_add_co_u32_e32 v178, vcc, s55, v178\n"
"v_addc_co_u32_e32 v179, vcc, 0, v179, vcc\n"
"s_add_u32 m0, s69, 86016\n"
"s_nop 0\n"
"global_load_lds_dwordx4 v[178:179], off\n"
"v_add_co_u32_e32 v178, vcc, s55, v178\n"
"v_addc_co_u32_e32 v179, vcc, 0, v179, vcc\n"
"s_add_u32 m0, s69, 90112\n"
"s_nop 0\n"
"global_load_lds_dwordx4 v[178:179], off\n"
"v_add_co_u32_e32 v178, vcc, s55, v178\n"
"v_addc_co_u32_e32 v179, vcc, 0, v179, vcc\n"
"s_add_u32 m0, s69, 94208\n"
"s_nop 0\n"
"global_load_lds_dwordx4 v[178:179], off\n"
"v_add_co_u32_e32 v178, vcc, s55, v178\n"
"v_addc_co_u32_e32 v179, vcc, 0, v179, vcc\n"
"s_add_u32 m0, s69, 98304\n"
"s_nop 0\n"
"global_load_lds_dwordx4 v[178:179], off\n"
"v_add_co_u32_e32 v178, vcc, s55, v178\n"
"v_addc_co_u32_e32 v179, vcc, 0, v179, vcc\n"
"s_add_u32 m0, s69, 102400\n"
"s_nop 0\n"
"global_load_lds_dwordx4 v[178:179], off\n"
"v_add_co_u32_e32 v178, vcc, s55, v178\n"
"v_addc_co_u32_e32 v179, vcc, 0, v179, vcc\n"
"s_add_u32 m0, s69, 106496\n"
"s_nop 0\n"
"global_load_lds_dwordx4 v[178:179], off\n"
"v_add_co_u32_e32 v178, vcc, s55, v178\n"
"v_addc_co_u32_e32 v179, vcc, 0, v179, vcc\n"
"s_waitcnt vmcnt(27)\n"
"s_barrier\n"
"ds_read_b64_tr_b16 a[220:221], v182 offset:45056\n"
"ds_read_b64_tr_b16 a[222:223], v183 offset:45056\n"
"ds_read_b64_tr_b16 a[224:225], v182 offset:49152\n"
"ds_read_b64_tr_b16 a[226:227], v183 offset:49152\n"
"ds_read_b64_tr_b16 a[228:229], v182 offset:53248\n"
"ds_read_b64_tr_b16 a[230:231], v183 offset:53248\n"
"ds_read_b64_tr_b16 a[232:233], v182 offset:57344\n"
"ds_read_b64_tr_b16 a[234:235], v183 offset:57344\n"
"ds_read_b64_tr_b16 a[236:237], v182 offset:61440\n"
"ds_read_b64_tr_b16 a[238:239], v183 offset:61440\n"
"ds_read_b64_tr_b16 a[240:241], v184\n"
"ds_read_b64_tr_b16 a[242:243], v185\n"
"ds_read_b64_tr_b16 a[244:245], v184 offset:4096\n"
"ds_read_b64_tr_b16 a[246:247], v185 offset:4096\n"
"ds_read_b64_tr_b16 a[248:249], v184 offset:8192\n"
"s_waitcnt lgkmcnt(8)\n"
"ds_read_b64_tr_b16 a[250:251], v185 offset:8192\n"
"ds_read_b64_tr_b16 a[252:253], v184 offset:12288\n"
"ds_read_b64_tr_b16 a[254:255], v185 offset:12288\n"
"s_waitcnt lgkmcnt(0)\n"
"s_barrier\n"
"s_waitcnt vmcnt(18)\n"
"s_barrier\n"
"ds_read_b64_tr_b16 a[0:1], v180\n"
"ds_read_b64_tr_b16 a[2:3], v181\n"
"ds_read_b64_tr_b16 a[4:5], v180 offset:4096\n"
"ds_read_b64_tr_b16 a[6:7], v181 offset:4096\n"
"ds_read_b64_tr_b16 a[8:9], v180 offset:8192\n"
"ds_read_b64_tr_b16 a[10:11], v181 offset:8192\n"
"ds_read_b64_tr_b16 a[12:13], v180 offset:12288\n"
"ds_read_b64_tr_b16 a[14:15], v181 offset:12288\n"
"ds_read_b64_tr_b16 a[16:17], v180 offset:16384\n"
"ds_read_b64_tr_b16 a[18:19], v181 offset:16384\n"
"ds_read_b64_tr_b16 a[20:21], v180 offset:20480\n"
"ds_read_b64_tr_b16 a[22:23], v181 offset:20480\n"
"ds_read_b64_tr_b16 a[24:25], v180 offset:24576\n"
"ds_read_b64_tr_b16 a[26:27], v181 offset:24576\n"
"ds_read_b64_tr_b16 a[28:29], v180 offset:28672\n"
"s_waitcnt lgkmcnt(8)\n"
"ds_read_b64_tr_b16 a[30:31], v181 offset:28672\n"
"ds_read_b64_tr_b16 a[32:33], v180 offset:32768\n"
"ds_read_b64_tr_b16 a[34:35], v181 offset:32768\n"
"s_waitcnt lgkmcnt(0)\n"
"s_barrier\n"
"s_waitcnt vmcnt(9)\n"
"s_barrier\n"
"ds_read_b64_tr_b16 a[36:37], v180 offset:36864\n"
"ds_read_b64_tr_b16 a[38:39], v181 offset:36864\n"
"ds_read_b64_tr_b16 a[40:41], v180 offset:40960\n"
"ds_read_b64_tr_b16 a[42:43], v181 offset:40960\n"
"ds_read_b64_tr_b16 a[44:45], v180 offset:45056\n"
"ds_read_b64_tr_b16 a[46:47], v181 offset:45056\n"
"ds_read_b64_tr_b16 a[48:49], v180 offset:49152\n"
"ds_read_b64_tr_b16 a[50:51], v181 offset:49152\n"
"ds_read_b64_tr_b16 a[52:53], v180 offset:53248\n"
"ds_read_b64_tr_b16 a[54:55], v181 offset:53248\n"
"ds_read_b64_tr_b16 a[56:57], v180 offset:57344\n"
"ds_read_b64_tr_b16 a[58:59], v181 offset:57344\n"
"ds_read_b64_tr_b16 a[60:61], v180 offset:61440\n"
"ds_read_b64_tr_b16 a[62:63], v181 offset:61440\n"
"ds_read_b64_tr_b16 a[64:65], v182\n"
"s_waitcnt lgkmcnt(8)\n"
"ds_read_b64_tr_b16 a[66:67], v183\n"
"ds_read_b64_tr_b16 a[68:69], v182 offset:4096\n"
"ds_read_b64_tr_b16 a[70:71], v183 offset:4096\n"
"s_waitcnt lgkmcnt(0)\n"
"s_barrier\n"
"v_readlane_b32 s72, v7, 0\n"
"s_nop 4\n"
"buffer_load_dword v198, v5, s[44:47], s72 offen sc1\n"
"s_add_u32 m0, s69, 110592\n"
"s_nop 0\n"
"global_load_lds_dwordx4 v[178:179], off\n"
"v_add_co_u32_e32 v178, vcc, s55, v178\n"
"v_addc_co_u32_e32 v179, vcc, 0, v179, vcc\n"
"s_add_u32 m0, s69, 114688\n"
"s_nop 0\n"
"global_load_lds_dwordx4 v[178:179], off\n"
"v_add_co_u32_e32 v178, vcc, s55, v178\n"
"v_addc_co_u32_e32 v179, vcc, 0, v179, vcc\n"
"s_add_u32 m0, s69, 118784\n"
"s_nop 0\n"
"global_load_lds_dwordx4 v[178:179], off\n"
"v_add_co_u32_e32 v178, vcc, s55, v178\n"
"v_addc_co_u32_e32 v179, vcc, 0, v179, vcc\n"
"s_add_u32 m0, s69, 122880\n"
"s_nop 0\n"
"global_load_lds_dwordx4 v[178:179], off\n"
"v_add_co_u32_e32 v178, vcc, s55, v178\n"
"v_addc_co_u32_e32 v179, vcc, 0, v179, vcc\n"
"s_add_u32 m0, s69, 126976\n"
"s_nop 0\n"
"global_load_lds_dwordx4 v[178:179], off\n"
"v_add_co_u32_e32 v178, vcc, s55, v178\n"
"v_addc_co_u32_e32 v179, vcc, 0, v179, vcc\n"
"s_add_u32 m0, s69, 131072\n"
"s_nop 0\n"
"global_load_lds_dwordx4 v[178:179], off\n"
"v_add_co_u32_e32 v178, vcc, s55, v178\n"
"v_addc_co_u32_e32 v179, vcc, 0, v179, vcc\n"
"s_add_u32 m0, s69, 135168\n"
"s_nop 0\n"
"global_load_lds_dwordx4 v[178:179], off\n"
"v_add_co_u32_e32 v178, vcc, s55, v178\n"
"v_addc_co_u32_e32 v179, vcc, 0, v179, vcc\n"
"s_add_u32 m0, s69, 139264\n"
"s_nop 0\n"
"global_load_lds_dwordx4 v[178:179], off\n"
"v_add_co_u32_e32 v178, vcc, s55, v178\n"
"v_addc_co_u32_e32 v179, vcc, 0, v179, vcc\n"
"s_add_u32 m0, s69, 143360\n"
"s_nop 0\n"
"global_load_lds_dwordx4 v[178:179], off\n"
"s_waitcnt vmcnt(9)\n"
"s_barrier\n"
"ds_read_b64_tr_b16 a[72:73], v182 offset:8192\n"
"ds_read_b64_tr_b16 a[74:75], v183 offset:8192\n"
"ds_read_b64_tr_b16 a[76:77], v182 offset:12288\n"
"ds_read_b64_tr_b16 a[78:79], v183 offset:12288\n"
"ds_read_b64_tr_b16 a[80:81], v182 offset:16384\n"
"ds_read_b64_tr_b16 a[82:83], v183 offset:16384\n"
"ds_read_b64_tr_b16 a[84:85], v182 offset:20480\n"
"ds_read_b64_tr_b16 a[86:87], v183 offset:20480\n"
"ds_read_b64_tr_b16 a[88:89], v182 offset:24576\n"
"ds_read_b64_tr_b16 a[90:91], v183 offset:24576\n"
"ds_read_b64_tr_b16 a[92:93], v182 offset:28672\n"
"ds_read_b64_tr_b16 a[94:95], v183 offset:28672\n"
"ds_read_b64_tr_b16 a[96:97], v182 offset:32768\n"
"ds_read_b64_tr_b16 a[98:99], v183 offset:32768\n"
"ds_read_b64_tr_b16 a[100:101], v182 offset:36864\n"
"s_waitcnt lgkmcnt(8)\n"
"ds_read_b64_tr_b16 a[102:103], v183 offset:36864\n"
"ds_read_b64_tr_b16 a[104:105], v182 offset:40960\n"
"ds_read_b64_tr_b16 a[106:107], v183 offset:40960\n"
"s_waitcnt lgkmcnt(0)\n"
"s_barrier\n"
"v_cmp_ne_u32_e64 s[80:81], s97, v198\n"
"s_nop 1\n"
"s_and_b64 s[80:81], s[80:81], s[82:83]\n"
"s_cbranch_scc1 Lx0nvord36_%=\n"
"s_mov_b32 s96, 1\n"
"s_mov_b32 s57, 0\n"
"s_mov_b32 s77, 36864\n"
"v_readlane_b32 s67, v6, s57\n"
"s_nop 0\n"
"s_mul_i32 s67, s67, s50\n"
"s_add_u32 s65, s67, s49\n"
"s_sub_u32 s64, 36864, s77\n"
"s_lshl_b32 s68, %0, 10\n"
"s_add_u32 s64, s64, s68\n"
"s_add_u32 m0, s64, 0\n"
"s_add_u32 s70, s65, 0\n"
"buffer_load_dwordx4 v2, s[36:39], s70 offen sc1 lds\n"
"s_add_u32 m0, s64, 4096\n"
"s_add_u32 s70, s65, 4096\n"
"buffer_load_dwordx4 v2, s[36:39], s70 offen sc1 lds\n"
"s_add_u32 m0, s64, 8192\n"
"s_add_u32 s70, s65, 8192\n"
"buffer_load_dwordx4 v2, s[36:39], s70 offen sc1 lds\n"
"s_add_u32 m0, s64, 12288\n"
"s_add_u32 s70, s65, 12288\n"
"buffer_load_dwordx4 v2, s[36:39], s70 offen sc1 lds\n"
"s_add_u32 m0, s64, 16384\n"
"s_add_u32 s70, s65, 16384\n"
"buffer_load_dwordx4 v2, s[36:39], s70 offen sc1 lds\n"
"s_add_u32 m0, s64, 20480\n"
"s_add_u32 s70, s65, 20480\n"
"buffer_load_dwordx4 v2, s[36:39], s70 offen sc1 lds\n"
"s_add_u32 m0, s64, 24576\n"
"s_add_u32 s70, s65, 24576\n"
"buffer_load_dwordx4 v2, s[36:39], s70 offen sc1 lds\n"
"s_add_u32 m0, s64, 28672\n"
"s_add_u32 s70, s65, 28672\n"
"buffer_load_dwordx4 v2, s[36:39], s70 offen sc1 lds\n"
"s_add_u32 m0, s64, 32768\n"
"s_add_u32 s70, s65, 32768\n"
"buffer_load_dwordx4 v2, s[36:39], s70 offen sc1 lds\n"
"Lx0nvord36_%=:\n"
"s_cmp_eq_u32 s96, 0\n"
"s_cbranch_scc1 Lw7avord36_%=\n"
"s_waitcnt vmcnt(9)\n"
"s_branch Lw7bvord36_%=\n"
"Lw7avord36_%=:\n"
"s_waitcnt vmcnt(0)\n"
"Lw7bvord36_%=:\n"
"s_barrier\n"
"ds_read_b64_tr_b16 a[108:109], v182 offset:45056\n"
"ds_read_b64_tr_b16 a[110:111], v183 offset:45056\n"
"ds_read_b64_tr_b16 a[112:113], v182 offset:49152\n"
"ds_read_b64_tr_b16 a[114:115], v183 offset:49152\n"
"ds_read_b64_tr_b16 a[116:117], v182 offset:53248\n"
"ds_read_b64_tr_b16 a[118:119], v183 offset:53248\n"
"ds_read_b64_tr_b16 a[120:121], v182 offset:57344\n"
"ds_read_b64_tr_b16 a[122:123], v183 offset:57344\n"
"ds_read_b64_tr_b16 a[124:125], v182 offset:61440\n"
"ds_read_b64_tr_b16 a[126:127], v183 offset:61440\n"
"ds_read_b64_tr_b16 a[128:129], v184\n"
"ds_read_b64_tr_b16 a[130:131], v185\n"
"ds_read_b64_tr_b16 a[132:133], v184 offset:4096\n"
"ds_read_b64_tr_b16 a[134:135], v185 offset:4096\n"
"ds_read_b64_tr_b16 a[136:137], v184 offset:8192\n"
"s_waitcnt lgkmcnt(8)\n"
"ds_read_b64_tr_b16 a[138:139], v185 offset:8192\n"
"ds_read_b64_tr_b16 a[140:141], v184 offset:12288\n"
"ds_read_b64_tr_b16 a[142:143], v185 offset:12288\n"
"s_waitcnt lgkmcnt(0)\n"
"s_barrier\n"
"s_lshl_b32 s55, %0, 10\n"
"s_mov_b32 s88, 0xffff\n"
"s_mov_b32 s89, 0x0\n"
"v_lshrrev_b32 v212, 2, v0\n"
"v_add_u32 v212, 86016, v212\n"
"v_lshlrev_b32 v192, 2, v10\n"
"v_add_u32 v192, 86528, v192\n"
"ds_write_b32 v192, v7\n"
"ds_write_b32 v192, v15 offset:256\n"
"s_mov_b32 s57, 0\n"
"s_mov_b32 s59, 0\n"
"s_mov_b32 s60, -1\n"
"s_mov_b32 s61, -1\n"
"s_mov_b32 s77, 36864\n"
"s_waitcnt lgkmcnt(0)\n"
"s_cmp_lg_u32 s96, 0\n"
"s_cbranch_scc1 Lbwivord36_%=\n"
"s_cmp_eq_u32 %0, 0\n"
"s_cbranch_scc0 Lbwivord36_%=\n"
"s_cmp_lg_u32 s75, 0\n"
"s_cbranch_scc1 Lbwivord36_%=\n"
"v_readlane_b32 s72, v7, s57\n"
"s_nop 3\n"
"s_mov_b32 s71, 0\n"
"s_cmp_lt_i32 s74, 0\n"
"s_cbranch_scc1 Lplivord36_%=\n"
"s_sub_u32 s67, s57, s74\n"
"s_cmp_gt_u32 s67, 3\n"
"s_cbranch_scc1 Lplivord36_%=\n"
"s_add_u32 s68, s57, 2\n"
"s_cmp_ge_u32 s68, s48\n"
"s_cbranch_scc1 Lplivord36_%=\n"
"s_mul_i32 s67, s67, 240\n"
"s_add_u32 s67, s67, s73\n"
"s_sub_u32 s67, s67, 40\n"
"s_mov_b32 s76, 0\n"
"Lslivord36_%=:\n"
"s_memrealtime s[84:85]\n"
"s_waitcnt lgkmcnt(0)\n"
"s_sub_u32 s68, s84, s67\n"
"s_cmp_ge_i32 s68, 0\n"
"s_cbranch_scc1 Lplivord36_%=\n"
"s_sleep 2\n"
"s_mov_b32 s71, 1\n"
"s_add_u32 s76, s76, 1\n"
"s_cmp_lt_u32 s76, 32768\n"
"s_cbranch_scc1 Lslivord36_%=\n"
"Lplivord36_%=:\n"
"s_mov_b32 s76, 0\n"
"Lppivord36_%=:\n"
"buffer_load_dword v192, v5, s[44:47], s72 offen sc1\n"
"s_waitcnt vmcnt(0)\n"
"v_cmp_ne_u32_e64 s[80:81], s97, v192\n"
"s_nop 1\n"
"s_and_b64 s[80:81], s[80:81], s[82:83]\n"
"s_cbranch_scc0 Lrdivord36_%=\n"
"s_sleep 3\n"
"s_add_u32 s76, s76, 1\n"
"s_cmp_lt_u32 s76, 32768\n"
"s_cbranch_scc1 Lppivord36_%=\n"
"s_mov_b32 s75, 1\n"
"v_mov_b32 v192, 16384\n"
"v_mov_b32 v193, 7000\n"
"buffer_store_dword v193, v192, s[44:47], 0 offen sc1\n"
"Lrdivord36_%=:\n"
"s_mov_b32 s74, -1\n"
"s_cmp_eq_u32 s76, 0\n"
"s_cbranch_scc1 Lbwivord36_%=\n"
"s_memrealtime s[84:85]\n"
"s_waitcnt lgkmcnt(0)\n"
"s_mov_b32 s73, s84\n"
"s_mov_b32 s74, s57\n"
"Lbwivord36_%=:\n"
"s_barrier\n"
"s_cmp_lg_u32 s96, 0\n"
"s_cbranch_scc1 Lx0svord36_%=\n"
"v_readlane_b32 s67, v6, s57\n"
"s_nop 0\n"
"s_mul_i32 s67, s67, s50\n"
"s_add_u32 s65, s67, s49\n"
"s_sub_u32 s64, 36864, s77\n"
"s_lshl_b32 s68, %0, 10\n"
"s_add_u32 s64, s64, s68\n"
"s_add_u32 m0, s64, 0\n"
"s_add_u32 s70, s65, 0\n"
"buffer_load_dwordx4 v2, s[36:39], s70 offen sc1 lds\n"
"s_add_u32 m0, s64, 4096\n"
"s_add_u32 s70, s65, 4096\n"
"buffer_load_dwordx4 v2, s[36:39], s70 offen sc1 lds\n"
"s_add_u32 m0, s64, 8192\n"
"s_add_u32 s70, s65, 8192\n"
"buffer_load_dwordx4 v2, s[36:39], s70 offen sc1 lds\n"
"s_add_u32 m0, s64, 12288\n"
"s_add_u32 s70, s65, 12288\n"
"buffer_load_dwordx4 v2, s[36:39], s70 offen sc1 lds\n"
"s_add_u32 m0, s64, 16384\n"
"s_add_u32 s70, s65, 16384\n"
"buffer_load_dwordx4 v2, s[36:39], s70 offen sc1 lds\n"
"s_add_u32 m0, s64, 20480\n"
"s_add_u32 s70, s65, 20480\n"
"buffer_load_dwordx4 v2, s[36:39], s70 offen sc1 lds\n"
"s_add_u32 m0, s64, 24576\n"
"s_add_u32 s70, s65, 24576\n"
"buffer_load_dwordx4 v2, s[36:39], s70 offen sc1 lds\n"
"s_add_u32 m0, s64, 28672\n"
"s_add_u32 s70, s65, 28672\n"
"buffer_load_dwordx4 v2, s[36:39], s70 offen sc1 lds\n"
"s_add_u32 m0, s64, 32768\n"
"s_add_u32 s70, s65, 32768\n"
"buffer_load_dwordx4 v2, s[36:39], s70 offen sc1 lds\n"
"Lx0svord36_%=:\n"
"s_waitcnt vmcnt(0) lgkmcnt(0)\n"
"s_barrier\n"
"s_sub_u32 s77, 36864, s77\n"
"v_add_u32 v1, s77, v0\n"
"ds_read_b128 v[80:83], v1\n"
"ds_read_b128 v[84:87], v1 offset:1024\n"
"ds_read_b128 v[88:91], v1 offset:2048\n"
"ds_read_b128 v[92:95], v1 offset:3072\n"
"s_mov_b32 s58, 1\n"
"Ltopvord36_%=:\n"
"s_cmp_eq_u32 s59, 1\n"
"s_cbranch_scc1 LPAvord36_%=\n"
"s_cmp_eq_u32 s59, 2\n"
"s_cbranch_scc1 LPBvord36_%=\n"
"s_add_u32 s67, s57, 1\n"
"s_cmp_lt_u32 s67, s58\n"
"s_cbranch_scc0 Lnonvord36_%=\n"
"v_readlane_b32 s67, v6, s67\n"
"s_nop 0\n"
"s_mul_i32 s67, s67, s50\n"
"s_add_u32 s65, s67, s49\n"
"s_sub_u32 s64, 36864, s77\n"
"s_lshl_b32 s68, %0, 10\n"
"s_add_u32 s64, s64, s68\n"
"s_mov_b32 s62, -1\n"
"s_cmp_ge_u32 s58, s48\n"
"s_cbranch_scc1 Lq1vord36_%=\n"
"s_sub_u32 s68, s58, s57\n"
"s_cmp_le_u32 s68, 4\n"
"s_cselect_b32 s62, s58, -1\n"
"Lq1vord36_%=:\n"
"s_max_i32 s68, s60, s61\n"
"s_mov_b32 s63, 0\n"
"s_cmp_lt_i32 s68, 0\n"
"s_cbranch_scc1 Lq2vord36_%=\n"
"s_mov_b32 s63, 1\n"
"Lq2vord36_%=:\n"
"s_waitcnt lgkmcnt(3)\n"
"v_mfma_f32_32x32x16_bf16 v[16:31], v[80:83], v[128:131], 0\n"
"ds_read_b128 v[96:99], v1 offset:4096\n"
"v_mfma_f32_32x32x16_bf16 v[32:47], v[80:83], a[0:3], 0\n"
"s_waitcnt lgkmcnt(3)\n"
"v_mfma_f32_32x32x16_bf16 v[16:31], v[84:87], v[132:135], v[16:31]\n"
"ds_read_b128 v[100:103], v1 offset:5120\n"
"v_mfma_f32_32x32x16_bf16 v[32:47], v[84:87], a[4:7], v[32:47]\n"
"s_add_u32 m0, s64, 0\n"
"s_add_u32 s70, s65, 0\n"
"buffer_load_dwordx4 v2, s[36:39], s70 offen sc1 lds\n"
"s_waitcnt lgkmcnt(3)\n"
"v_mfma_f32_32x32x16_bf16 v[16:31], v[88:91], v[136:139], v[16:31]\n"
"ds_read_b128 v[80:83], v1 offset:6144\n"
"v_mfma_f32_32x32x16_bf16 v[32:47], v[88:91], a[8:11], v[32:47]\n"
"s_waitcnt lgkmcnt(3)\n"
"v_mfma_f32_32x32x16_bf16 v[16:31], v[92:95], v[140:143], v[16:31]\n"
"ds_read_b128 v[84:87], v1 offset:7168\n"
"v_mfma_f32_32x32x16_bf16 v[32:47], v[92:95], a[12:15], v[32:47]\n"
"s_add_u32 m0, s64, 4096\n"
"s_add_u32 s70, s65, 4096\n"
"buffer_load_dwordx4 v2, s[36:39], s70 offen sc1 lds\n"
"s_waitcnt lgkmcnt(3)\n"
"v_mfma_f32_32x32x16_bf16 v[16:31], v[96:99], v[144:147], v[16:31]\n"
"ds_read_b128 v[88:91], v1 offset:8192\n"
"v_mfma_f32_32x32x16_bf16 v[32:47], v[96:99], a[16:19], v[32:47]\n"
"s_waitcnt lgkmcnt(3)\n"
"v_mfma_f32_32x32x16_bf16 v[16:31], v[100:103], v[148:151], v[16:31]\n"
"ds_read_b128 v[92:95], v1 offset:9216\n"
"v_mfma_f32_32x32x16_bf16 v[32:47], v[100:103], a[20:23], v[32:47]\n"
"s_add_u32 m0, s64, 8192\n"
"s_add_u32 s70, s65, 8192\n"
"buffer_load_dwordx4 v2, s[36:39], s70 offen sc1 lds\n"
"s_waitcnt lgkmcnt(3)\n"
"v_mfma_f32_32x32x16_bf16 v[16:31], v[80:83], v[152:155], v[16:31]\n"
"ds_read_b128 v[96:99], v1 offset:10240\n"
"v_mfma_f32_32x32x16_bf16 v[32:47], v[80:83], a[24:27], v[32:47]\n"
"s_waitcnt lgkmcnt(3)\n"
"v_mfma_f32_32x32x16_bf16 v[16:31], v[84:87], v[156:159], v[16:31]\n"
"ds_read_b128 v[100:103], v1 offset:11264\n"
"v_mfma_f32_32x32x16_bf16 v[32:47], v[84:87], a[28:31], v[32:47]\n"
"s_add_u32 m0, s64, 12288\n"
"s_add_u32 s70, s65, 12288\n"
"buffer_load_dwordx4 v2, s[36:39], s70 offen sc1 lds\n"
"s_waitcnt lgkmcnt(3)\n"
"v_mfma_f32_32x32x16_bf16 v[16:31], v[88:91], a[144:147], v[16:31]\n"
"ds_read_b128 v[80:83], v1 offset:12288\n"
"v_mfma_f32_32x32x16_bf16 v[32:47], v[88:91], a[32:35], v[32:47]\n"
"s_waitcnt lgkmcnt(3)\n"
"v_mfma_f32_32x32x16_bf16 v[16:31], v[92:95], a[148:151], v[16:31]\n"
"ds_read_b128 v[84:87], v1 offset:13312\n"
"v_mfma_f32_32x32x16_bf16 v[32:47], v[92:95], a[36:39], v[32:47]\n"
"s_add_u32 m0, s64, 16384\n"
"s_add_u32 s70, s65, 16384\n"
"buffer_load_dwordx4 v2, s[36:39], s70 offen sc1 lds\n"
"s_waitcnt lgkmcnt(3)\n"
"v_mfma_f32_32x32x16_bf16 v[16:31], v[96:99], a[152:155], v[16:31]\n"
"ds_read_b128 v[88:91], v1 offset:14336\n"
"v_mfma_f32_32x32x16_bf16 v[32:47], v[96:99], a[40:43], v[32:47]\n"
"s_waitcnt lgkmcnt(3)\n"
"v_mfma_f32_32x32x16_bf16 v[16:31], v[100:103], a[156:159], v[16:31]\n"
"ds_read_b128 v[92:95], v1 offset:15360\n"
"v_mfma_f32_32x32x16_bf16 v[32:47], v[100:103], a[44:47], v[32:47]\n"
"s_add_u32 m0, s64, 20480\n"
"s_add_u32 s70, s65, 20480\n"
"buffer_load_dwordx4 v2, s[36:39], s70 offen sc1 lds\n"
"s_waitcnt lgkmcnt(3)\n"
"v_mfma_f32_32x32x16_bf16 v[16:31], v[80:83], a[160:163], v[16:31]\n"
"ds_read_b128 v[96:99], v1 offset:16384\n"
"v_mfma_f32_32x32x16_bf16 v[32:47], v[80:83], a[48:51], v[32:47]\n"
"s_waitcnt lgkmcnt(3)\n"
"v_mfma_f32_32x32x16_bf16 v[16:31], v[84:87], a[164:167], v[16:31]\n"
"ds_read_b128 v[100:103], v1 offset:17408\n"
"v_mfma_f32_32x32x16_bf16 v[32:47], v[84:87], a[52:55], v[32:47]\n"
"s_add_u32 m0, s64, 24576\n"
"s_add_u32 s70, s65, 24576\n"
"buffer_load_dwordx4 v2, s[36:39], s70 offen sc1 lds\n"
"s_waitcnt lgkmcnt(3)\n"
"v_mfma_f32_32x32x16_bf16 v[16:31], v[88:91], a[168:171], v[16:31]\n"
"ds_read_b128 v[80:83], v1 offset:18432\n"
"v_mfma_f32_32x32x16_bf16 v[32:47], v[88:91], a[56:59], v[32:47]\n"
"s_cmp_eq_u32 s63, 0\n"
"s_cbranch_scc1 LnmDvord36_%=\n"
"s_waitcnt vmcnt(7)\n"
"s_barrier\n"
"s_cmp_lt_i32 s60, 0\n"
"s_cbranch_scc1 LnsmaDvord36_%=\n"
"v_readlane_b32 s67, v6, s60\n"
"s_nop 0\n"
"s_mul_i32 s67, s67, s54\n"
"s_add_u32 s67, s67, s53\n"
"buffer_store_dword v4, v3, s[44:47], s67 offen sc1\n"
"LnsmaDvord36_%=:\n"
"s_cmp_lt_i32 s61, 0\n"
"s_cbranch_scc1 LnsmbDvord36_%=\n"
"v_readlane_b32 s67, v6, s61\n"
"s_nop 0\n"
"s_mul_i32 s67, s67, s54\n"
"s_add_u32 s67, s67, s53\n"
"buffer_store_dword v4, v3, s[44:47], s67 offen sc1\n"
"LnsmbDvord36_%=:\n"
"LnmDvord36_%=:\n"
"s_waitcnt lgkmcnt(3)\n"
"v_mfma_f32_32x32x16_bf16 v[16:31], v[92:95], a[172:175], v[16:31]\n"
"ds_read_b128 v[84:87], v1 offset:19456\n"
"v_mfma_f32_32x32x16_bf16 v[32:47], v[92:95], a[60:63], v[32:47]\n"
"s_add_u32 m0, s64, 28672\n"
"s_add_u32 s70, s65, 28672\n"
"buffer_load_dwordx4 v2, s[36:39], s70 offen sc1 lds\n"
"s_waitcnt lgkmcnt(3)\n"
"v_mfma_f32_32x32x16_bf16 v[16:31], v[96:99], a[176:179], v[16:31]\n"
"ds_read_b128 v[88:91], v1 offset:20480\n"
"v_mfma_f32_32x32x16_bf16 v[32:47], v[96:99], a[64:67], v[32:47]\n"
"s_waitcnt lgkmcnt(3)\n"
"v_mfma_f32_32x32x16_bf16 v[16:31], v[100:103], a[180:183], v[16:31]\n"
"ds_read_b128 v[92:95], v1 offset:21504\n"
"v_mfma_f32_32x32x16_bf16 v[32:47], v[100:103], a[68:71], v[32:47]\n"
"s_add_u32 m0, s64, 32768\n"
"s_add_u32 s70, s65, 32768\n"
"buffer_load_dwordx4 v2, s[36:39], s70 offen sc1 lds\n"
"s_waitcnt lgkmcnt(3)\n"
"v_mfma_f32_32x32x16_bf16 v[16:31], v[80:83], a[184:187], v[16:31]\n"
"ds_read_b128 v[96:99], v1 offset:22528\n"
"v_mfma_f32_32x32x16_bf16 v[32:47], v[80:83], a[72:75], v[32:47]\n"
"s_cmp_lt_i32 s62, 0\n"
"s_cbranch_scc1 LnpDvord36_%=\n"
"s_and_b32 s72, s57, 1\n"
"s_lshl_b32 s72, s72, 8\n"
"s_add_u32 s72, s72, 86016\n"
"s_waitcnt lgkmcnt(0)\n"
"s_lshl_b32 s67, s62, 2\n"
"s_add_u32 s67, s67, 86528\n"
"v_add_u32 v192, s67, v8\n"
"ds_read_b32 v192, v192\n"
"s_waitcnt lgkmcnt(0)\n"
"v_add_u32 v192, v192, v9\n"
"s_mov_b32 m0, s72\n"
"s_mov_b64 exec, s[78:79]\n"
"buffer_load_dword v192, s[44:47], 0 offen sc1 lds\n"
"s_mov_b64 exec, -1\n"
"s_waitcnt lgkmcnt(0)\n"
"LnpDvord36_%=:\n"
"s_waitcnt lgkmcnt(3)\n"
"v_mfma_f32_32x32x16_bf16 v[16:31], v[84:87], a[188:191], v[16:31]\n"
"ds_read_b128 v[100:103], v1 offset:23552\n"
"v_mfma_f32_32x32x16_bf16 v[32:47], v[84:87], a[76:79], v[32:47]\n"
"s_waitcnt lgkmcnt(3)\n"
"v_mfma_f32_32x32x16_bf16 v[16:31], v[88:91], a[192:195], v[16:31]\n"
"ds_read_b128 v[80:83], v1 offset:24576\n"
"v_mfma_f32_32x32x16_bf16 v[32:47], v[88:91], a[80:83], v[32:47]\n"
"s_waitcnt lgkmcnt(3)\n"
"v_mfma_f32_32x32x16_bf16 v[16:31], v[92:95], a[196:199], v[16:31]\n"
"ds_read_b128 v[84:87], v1 offset:25600\n"
"v_mfma_f32_32x32x16_bf16 v[32:47], v[92:95], a[84:87], v[32:47]\n"
"s_waitcnt lgkmcnt(3)\n"
"v_mfma_f32_32x32x16_bf16 v[16:31], v[96:99], a[200:203], v[16:31]\n"
"ds_read_b128 v[88:91], v1 offset:26624\n"
"v_mfma_f32_32x32x16_bf16 v[32:47], v[96:99], a[88:91], v[32:47]\n"
"s_waitcnt lgkmcnt(3)\n"
"v_mfma_f32_32x32x16_bf16 v[16:31], v[100:103], a[204:207], v[16:31]\n"
"ds_read_b128 v[92:95], v1 offset:27648\n"
"v_mfma_f32_32x32x16_bf16 v[32:47], v[100:103], a[92:95], v[32:47]\n"
"s_waitcnt lgkmcnt(3)\n"
"v_mfma_f32_32x32x16_bf16 v[16:31], v[80:83], a[208:211], v[16:31]\n"
"ds_read_b128 v[96:99], v1 offset:28672\n"
"v_mfma_f32_32x32x16_bf16 v[32:47], v[80:83], a[96:99], v[32:47]\n"
"s_waitcnt lgkmcnt(3)\n"
"v_mfma_f32_32x32x16_bf16 v[16:31], v[84:87], a[212:215], v[16:31]\n"
"ds_read_b128 v[100:103], v1 offset:29696\n"
"v_mfma_f32_32x32x16_bf16 v[32:47], v[84:87], a[100:103], v[32:47]\n"
"s_waitcnt lgkmcnt(3)\n"
"v_mfma_f32_32x32x16_bf16 v[16:31], v[88:91], a[216:219], v[16:31]\n"
"ds_read_b128 v[80:83], v1 offset:30720\n"
"v_mfma_f32_32x32x16_bf16 v[32:47], v[88:91], a[104:107], v[32:47]\n"
"s_waitcnt lgkmcnt(3)\n"
"v_mfma_f32_32x32x16_bf16 v[16:31], v[92:95], a[220:223], v[16:31]\n"
"ds_read_b128 v[84:87], v1 offset:31744\n"
"v_mfma_f32_32x32x16_bf16 v[32:47], v[92:95], a[108:111], v[32:47]\n"
"s_waitcnt lgkmcnt(3)\n"
"v_mfma_f32_32x32x16_bf16 v[16:31], v[96:99], a[224:227], v[16:31]\n"
"ds_read_b128 v[88:91], v1 offset:32768\n"
"v_mfma_f32_32x32x16_bf16 v[32:47], v[96:99], a[112:115], v[32:47]\n"
"s_waitcnt lgkmcnt(3)\n"
"v_mfma_f32_32x32x16_bf16 v[16:31], v[100:103], a[228:231], v[16:31]\n"
"ds_read_b128 v[92:95], v1 offset:33792\n"
"v_mfma_f32_32x32x16_bf16 v[32:47], v[100:103], a[116:119], v[32:47]\n"
"s_waitcnt lgkmcnt(3)\n"
"v_mfma_f32_32x32x16_bf16 v[16:31], v[80:83], a[232:235], v[16:31]\n"
"ds_read_b128 v[96:99], v1 offset:34816\n"
"v_mfma_f32_32x32x16_bf16 v[32:47], v[80:83], a[120:123], v[32:47]\n"
"s_waitcnt lgkmcnt(3)\n"
"v_mfma_f32_32x32x16_bf16 v[16:31], v[84:87], a[236:239], v[16:31]\n"
"ds_read_b128 v[100:103], v1 offset:35840\n"
"v_mfma_f32_32x32x16_bf16 v[32:47], v[84:87], a[124:127], v[32:47]\n"
"s_waitcnt lgkmcnt(3)\n"
"v_mfma_f32_32x32x16_bf16 v[16:31], v[88:91], a[240:243], v[16:31]\n"
"v_mfma_f32_32x32x16_bf16 v[32:47], v[88:91], a[128:131], v[32:47]\n"
"s_waitcnt lgkmcnt(2)\n"
"v_mfma_f32_32x32x16_bf16 v[16:31], v[92:95], a[244:247], v[16:31]\n"
"v_mfma_f32_32x32x16_bf16 v[32:47], v[92:95], a[132:135], v[32:47]\n"
"s_waitcnt lgkmcnt(1)\n"
"v_mfma_f32_32x32x16_bf16 v[16:31], v[96:99], a[248:251], v[16:31]\n"
"v_mfma_f32_32x32x16_bf16 v[32:47], v[96:99], a[136:139], v[32:47]\n"
"s_waitcnt lgkmcnt(0)\n"
"v_mfma_f32_32x32x16_bf16 v[16:31], v[100:103], a[252:255], v[16:31]\n"
"v_mfma_f32_32x32x16_bf16 v[32:47], v[100:103], a[140:143], v[32:47]\n"
"s_waitcnt vmcnt(0) lgkmcnt(0)\n"
"s_barrier\n"
"s_mov_b32 s99, 0\n"
"s_sub_u32 s77, 36864, s77\n"
"v_add_u32 v1, s77, v0\n"
"ds_read_b128 v[80:83], v1\n"
"ds_read_b128 v[84:87], v1 offset:1024\n"
"ds_read_b128 v[88:91], v1 offset:2048\n"
"ds_read_b128 v[92:95], v1 offset:3072\n"
"s_branch Le1vord36_%=\n"
"LPAvord36_%=:\n"
"s_waitcnt lgkmcnt(3)\n"
"v_mfma_f32_32x32x16_bf16 v[48:63], v[80:83], v[128:131], 0\n"
"ds_read_b128 v[96:99], v1 offset:4096\n"
"s_add_u32 s92, s57, 1\n"
"v_readlane_b32 s92, v6, s92\n"
"s_sub_u32 s64, 36864, s77\n"
"v_mfma_f32_32x32x16_bf16 v[64:79], v[80:83], a[0:3], 0\n"
"s_add_u32 s64, s64, s55\n"
"s_mul_i32 s92, s92, s50\n"
"s_add_u32 s65, s92, s49\n"
"s_sub_u32 s90, s57, 1\n"
"s_and_b32 s90, s90, 1\n"
"s_waitcnt lgkmcnt(3)\n"
"v_mfma_f32_32x32x16_bf16 v[48:63], v[84:87], v[132:135], v[48:63]\n"
"ds_read_b128 v[100:103], v1 offset:5120\n"
"s_lshl_b32 s90, s90, 8\n"
"v_add_u32 v194, s90, v212\n"
"ds_read_b32 v195, v194\n"
"v_mfma_f32_32x32x16_bf16 v[64:79], v[84:87], a[4:7], v[64:79]\n"
"s_add_u32 m0, s64, 0\n"
"s_add_u32 s92, s65, 0\n"
"buffer_load_dwordx4 v2, s[36:39], s92 offen sc1 lds\n"
"s_sub_u32 s93, s57, 1\n"
"s_waitcnt lgkmcnt(4)\n"
"v_mfma_f32_32x32x16_bf16 v[48:63], v[88:91], v[136:139], v[48:63]\n"
"ds_read_b128 v[80:83], v1 offset:6144\n"
"v_readlane_b32 s93, v6, s93\n"
"v_mfma_f32_32x32x16_bf16 v[64:79], v[88:91], a[8:11], v[64:79]\n"
"s_add_u32 m0, s64, 4096\n"
"s_add_u32 s92, s65, 4096\n"
"buffer_load_dwordx4 v2, s[36:39], s92 offen sc1 lds\n"
"s_mul_i32 s93, s93, s52\n"
"s_waitcnt lgkmcnt(4)\n"
"v_mfma_f32_32x32x16_bf16 v[48:63], v[92:95], v[140:143], v[48:63]\n"
"ds_read_b128 v[84:87], v1 offset:7168\n"
"s_waitcnt lgkmcnt(2)\n"
"v_cmp_ne_u32_e64 s[80:81], s97, v195\n"
"s_add_u32 s66, s93, s51\n"
"v_readlane_b32 s93, v6, s60\n"
"v_mfma_f32_32x32x16_bf16 v[64:79], v[92:95], a[12:15], v[64:79]\n"
"s_add_u32 m0, s64, 8192\n"
"s_add_u32 s92, s65, 8192\n"
"buffer_load_dwordx4 v2, s[36:39], s92 offen sc1 lds\n"
"s_and_b64 s[80:81], s[80:81], s[88:89]\n"
"v_mfma_f32_32x32x16_bf16 v[48:63], v[96:99], v[144:147], v[48:63]\n"
"ds_read_b128 v[88:91], v1 offset:8192\n"
"s_ff1_i32_b64 s90, s[80:81]\n"
"s_cmp_lt_i32 s90, 0\n"
"s_cselect_b32 s90, 16, s90\n"
"s_mul_i32 s90, s90, 65536\n"
"v_mfma_f32_32x32x16_bf16 v[64:79], v[96:99], a[16:19], v[64:79]\n"
"s_add_u32 m0, s64, 12288\n"
"s_add_u32 s92, s65, 12288\n"
"buffer_load_dwordx4 v2, s[36:39], s92 offen sc1 lds\n"
"s_lshr_b32 s90, s90, 16\n"
"v_mfma_f32_32x32x16_bf16 v[48:63], v[100:103], v[148:151], v[48:63]\n"
"ds_read_b128 v[92:95], v1 offset:9216\n"
"s_sub_u32 s91, s48, s86\n"
"s_min_u32 s90, s90, s91\n"
"s_add_u32 s90, s90, s86\n"
"s_mul_i32 s93, s93, s54\n"
"v_mfma_f32_32x32x16_bf16 v[64:79], v[100:103], a[20:23], v[64:79]\n"
"s_add_u32 m0, s64, 16384\n"
"s_add_u32 s92, s65, 16384\n"
"buffer_load_dwordx4 v2, s[36:39], s92 offen sc1 lds\n"
"s_add_u32 s93, s93, s53\n"
"s_waitcnt lgkmcnt(3)\n"
"v_mfma_f32_32x32x16_bf16 v[48:63], v[80:83], v[152:155], v[48:63]\n"
"ds_read_b128 v[96:99], v1 offset:10240\n"
"s_cmp_lt_i32 s86, 0\n"
"s_cselect_b32 s58, s58, s90\n"
"v_mfma_f32_32x32x16_bf16 v[64:79], v[80:83], a[24:27], v[64:79]\n"
"s_add_u32 m0, s64, 20480\n"
"s_add_u32 s92, s65, 20480\n"
"buffer_load_dwordx4 v2, s[36:39], s92 offen sc1 lds\n"
"s_waitcnt lgkmcnt(3)\n"
"v_mfma_f32_32x32x16_bf16 v[48:63], v[84:87], v[156:159], v[48:63]\n"
"ds_read_b128 v[100:103], v1 offset:11264\n"
"s_cmp_lt_u32 s58, s48\n"
"s_cselect_b32 s62, s58, -1\n"
"s_sub_u32 s90, s58, s57\n"
"v_mfma_f32_32x32x16_bf16 v[64:79], v[84:87], a[28:31], v[64:79]\n"
"s_add_u32 m0, s64, 24576\n"
"s_add_u32 s92, s65, 24576\n"
"buffer_load_dwordx4 v2, s[36:39], s92 offen sc1 lds\n"
"s_waitcnt lgkmcnt(3)\n"
"v_mfma_f32_32x32x16_bf16 v[48:63], v[88:91], a[144:147], v[48:63]\n"
"ds_read_b128 v[80:83], v1 offset:12288\n"
"s_cmp_le_u32 s90, 4\n"
"s_cselect_b32 s62, s62, -1\n"
"s_max_i32 s90, s62, 0\n"
"v_mfma_f32_32x32x16_bf16 v[64:79], v[88:91], a[32:35], v[64:79]\n"
"s_add_u32 m0, s64, 28672\n"
"s_add_u32 s92, s65, 28672\n"
"buffer_load_dwordx4 v2, s[36:39], s92 offen sc1 lds\n"
"s_lshl_b32 s90, s90, 2\n"
"s_waitcnt lgkmcnt(3)\n"
"v_mfma_f32_32x32x16_bf16 v[48:63], v[92:95], a[148:151], v[48:63]\n"
"ds_read_b128 v[84:87], v1 offset:13312\n"
"s_add_u32 s90, s90, 86528\n"
"v_add_u32 v196, s90, v8\n"
"ds_read_b32 v196, v196\n"
"v_mfma_f32_32x32x16_bf16 v[64:79], v[92:95], a[36:39], v[64:79]\n"
"s_add_u32 m0, s64, 32768\n"
"s_add_u32 s92, s65, 32768\n"
"buffer_load_dwordx4 v2, s[36:39], s92 offen sc1 lds\n"
"s_waitcnt lgkmcnt(4)\n"
"v_mfma_f32_32x32x16_bf16 v[48:63], v[96:99], a[152:155], v[48:63]\n"
"ds_read_b128 v[88:91], v1 offset:14336\n"
"s_cmp_ge_i32 s62, 0\n"
"s_cselect_b64 s[94:95], s[78:79], 0\n"
"s_and_b32 s91, s57, 1\n"
"v_mfma_f32_32x32x16_bf16 v[64:79], v[96:99], a[40:43], v[64:79]\n"
"s_lshl_b32 s91, s91, 8\n"
"s_add_u32 s91, s91, 86016\n"
"s_add_u32 s90, s57, 2\n"
"s_cmp_lt_u32 s90, s58\n"
"s_cselect_b32 s87, 1, 0\n"
"s_waitcnt lgkmcnt(4)\n"
"v_mfma_f32_32x32x16_bf16 v[48:63], v[100:103], a[156:159], v[48:63]\n"
"ds_read_b128 v[92:95], v1 offset:15360\n"
"s_cmp_lt_i32 s60, 0\n"
"s_cselect_b32 s93, 0x7ffffff0, s93\n"
"v_add_u32 v197, s93, v3\n"
"v_mfma_f32_32x32x16_bf16 v[64:79], v[100:103], a[44:47], v[64:79]\n"
"v_cvt_pk_bf16_f32 v160, v16, v17\n"
"v_cvt_pk_bf16_f32 v161, v18, v19\n"
"v_cvt_pk_bf16_f32 v164, v20, v21\n"
"v_cvt_pk_bf16_f32 v165, v22, v23\n"
"v_cvt_pk_bf16_f32 v162, v24, v25\n"
"s_waitcnt lgkmcnt(4)\n"
"v_mfma_f32_32x32x16_bf16 v[48:63], v[80:83], a[160:163], v[48:63]\n"
"ds_read_b128 v[96:99], v1 offset:16384\n"
"s_waitcnt lgkmcnt(3)\n"
"v_add_u32 v196, v196, v9\n"
"v_cvt_pk_bf16_f32 v163, v26, v27\n"
"v_cvt_pk_bf16_f32 v166, v28, v29\n"
"v_mfma_f32_32x32x16_bf16 v[64:79], v[80:83], a[48:51], v[64:79]\n"
"s_mov_b32 m0, s91\n"
"s_mov_b64 exec, s[94:95]\n"
"buffer_load_dword v196, s[44:47], 0 offen sc1 lds\n"
"s_mov_b64 exec, -1\n"
"v_cvt_pk_bf16_f32 v167, v30, v31\n"
"v_mfma_f32_32x32x16_bf16 v[48:63], v[84:87], a[164:167], v[48:63]\n"
"ds_read_b128 v[100:103], v1 offset:17408\n"
"v_cvt_pk_bf16_f32 v168, v32, v33\n"
"v_cvt_pk_bf16_f32 v169, v34, v35\n"
"v_cvt_pk_bf16_f32 v172, v36, v37\n"
"v_cvt_pk_bf16_f32 v173, v38, v39\n"
"v_mfma_f32_32x32x16_bf16 v[64:79], v[84:87], a[52:55], v[64:79]\n"
"v_cvt_pk_bf16_f32 v170, v40, v41\n"
"v_cvt_pk_bf16_f32 v171, v42, v43\n"
"v_cvt_pk_bf16_f32 v174, v44, v45\n"
"v_cvt_pk_bf16_f32 v175, v46, v47\n"
"s_waitcnt lgkmcnt(3)\n"
"v_mfma_f32_32x32x16_bf16 v[48:63], v[88:91], a[168:171], v[48:63]\n"
"ds_read_b128 v[80:83], v1 offset:18432\n"
"buffer_store_dwordx4 v[160:163], v11, s[40:43], s66 offen sc1\n"
"v_mfma_f32_32x32x16_bf16 v[64:79], v[88:91], a[56:59], v[64:79]\n"
"buffer_store_dwordx4 v[164:167], v12, s[40:43], s66 offen sc1\n"
"s_waitcnt lgkmcnt(3)\n"
"v_mfma_f32_32x32x16_bf16 v[48:63], v[92:95], a[172:175], v[48:63]\n"
"ds_read_b128 v[84:87], v1 offset:19456\n"
"buffer_store_dwordx4 v[168:171], v13, s[40:43], s66 offen sc1\n"
"v_mfma_f32_32x32x16_bf16 v[64:79], v[92:95], a[60:63], v[64:79]\n"
"buffer_store_dwordx4 v[172:175], v14, s[40:43], s66 offen sc1\n"
"s_waitcnt lgkmcnt(3)\n"
"v_mfma_f32_32x32x16_bf16 v[48:63], v[96:99], a[176:179], v[48:63]\n"
"ds_read_b128 v[88:91], v1 offset:20480\n"
"v_mfma_f32_32x32x16_bf16 v[64:79], v[96:99], a[64:67], v[64:79]\n"
"s_waitcnt lgkmcnt(3)\n"
"v_mfma_f32_32x32x16_bf16 v[48:63], v[100:103], a[180:183], v[48:63]\n"
"ds_read_b128 v[92:95], v1 offset:21504\n"
"v_mfma_f32_32x32x16_bf16 v[64:79], v[100:103], a[68:71], v[64:79]\n"
"s_waitcnt lgkmcnt(3)\n"
"v_mfma_f32_32x32x16_bf16 v[48:63], v[80:83], a[184:187], v[48:63]\n"
"ds_read_b128 v[96:99], v1 offset:22528\n"
"v_mfma_f32_32x32x16_bf16 v[64:79], v[80:83], a[72:75], v[64:79]\n"
"s_waitcnt lgkmcnt(3)\n"
"v_mfma_f32_32x32x16_bf16 v[48:63], v[84:87], a[188:191], v[48:63]\n"
"ds_read_b128 v[100:103], v1 offset:23552\n"
"v_mfma_f32_32x32x16_bf16 v[64:79], v[84:87], a[76:79], v[64:79]\n"
"s_waitcnt lgkmcnt(3)\n"
"v_mfma_f32_32x32x16_bf16 v[48:63], v[88:91], a[192:195], v[48:63]\n"
"ds_read_b128 v[80:83], v1 offset:24576\n"
"v_mfma_f32_32x32x16_bf16 v[64:79], v[88:91], a[80:83], v[64:79]\n"
"s_waitcnt lgkmcnt(3)\n"
"v_mfma_f32_32x32x16_bf16 v[48:63], v[92:95], a[196:199], v[48:63]\n"
"ds_read_b128 v[84:87], v1 offset:25600\n"
"v_mfma_f32_32x32x16_bf16 v[64:79], v[92:95], a[84:87], v[64:79]\n"
"s_waitcnt lgkmcnt(3)\n"
"v_mfma_f32_32x32x16_bf16 v[48:63], v[96:99], a[200:203], v[48:63]\n"
"ds_read_b128 v[88:91], v1 offset:26624\n"
"v_mfma_f32_32x32x16_bf16 v[64:79], v[96:99], a[88:91], v[64:79]\n"
"s_waitcnt lgkmcnt(3)\n"
"v_mfma_f32_32x32x16_bf16 v[48:63], v[100:103], a[204:207], v[48:63]\n"
"ds_read_b128 v[92:95], v1 offset:27648\n"
"v_mfma_f32_32x32x16_bf16 v[64:79], v[100:103], a[92:95], v[64:79]\n"
"s_waitcnt lgkmcnt(3)\n"
"v_mfma_f32_32x32x16_bf16 v[48:63], v[80:83], a[208:211], v[48:63]\n"
"ds_read_b128 v[96:99], v1 offset:28672\n"
"v_mfma_f32_32x32x16_bf16 v[64:79], v[80:83], a[96:99], v[64:79]\n"
"s_waitcnt lgkmcnt(3)\n"
"v_mfma_f32_32x32x16_bf16 v[48:63], v[84:87], a[212:215], v[48:63]\n"
"ds_read_b128 v[100:103], v1 offset:29696\n"
"v_mfma_f32_32x32x16_bf16 v[64:79], v[84:87], a[100:103], v[64:79]\n"
"s_waitcnt lgkmcnt(3)\n"
"v_mfma_f32_32x32x16_bf16 v[48:63], v[88:91], a[216:219], v[48:63]\n"
"ds_read_b128 v[80:83], v1 offset:30720\n"
"v_mfma_f32_32x32x16_bf16 v[64:79], v[88:91], a[104:107], v[64:79]\n"
"s_waitcnt lgkmcnt(3)\n"
"v_mfma_f32_32x32x16_bf16 v[48:63], v[92:95], a[220:223], v[48:63]\n"
"ds_read_b128 v[84:87], v1 offset:31744\n"
"v_mfma_f32_32x32x16_bf16 v[64:79], v[92:95], a[108:111], v[64:79]\n"
"s_waitcnt lgkmcnt(3)\n"
"v_mfma_f32_32x32x16_bf16 v[48:63], v[96:99], a[224:227], v[48:63]\n"
"ds_read_b128 v[88:91], v1 offset:32768\n"
"v_mfma_f32_32x32x16_bf16 v[64:79], v[96:99], a[112:115], v[64:79]\n"
"s_waitcnt lgkmcnt(3)\n"
"v_mfma_f32_32x32x16_bf16 v[48:63], v[100:103], a[228:231], v[48:63]\n"
"ds_read_b128 v[92:95], v1 offset:33792\n"
"v_mfma_f32_32x32x16_bf16 v[64:79], v[100:103], a[116:119], v[64:79]\n"
"s_waitcnt lgkmcnt(3)\n"
"v_mfma_f32_32x32x16_bf16 v[48:63], v[80:83], a[232:235], v[48:63]\n"
"ds_read_b128 v[96:99], v1 offset:34816\n"
"v_mfma_f32_32x32x16_bf16 v[64:79], v[80:83], a[120:123], v[64:79]\n"
"s_waitcnt lgkmcnt(3)\n"
"v_mfma_f32_32x32x16_bf16 v[48:63], v[84:87], a[236:239], v[48:63]\n"
"ds_read_b128 v[100:103], v1 offset:35840\n"
"v_mfma_f32_32x32x16_bf16 v[64:79], v[84:87], a[124:127], v[64:79]\n"
"s_waitcnt lgkmcnt(3)\n"
"v_mfma_f32_32x32x16_bf16 v[48:63], v[88:91], a[240:243], v[48:63]\n"
"v_mfma_f32_32x32x16_bf16 v[64:79], v[88:91], a[128:131], v[64:79]\n"
"s_waitcnt vmcnt(4) lgkmcnt(0)\n"
"s_barrier\n"
"v_mfma_f32_32x32x16_bf16 v[48:63], v[92:95], a[244:247], v[48:63]\n"
"s_sub_u32 s77, 36864, s77\n"
"v_add_u32 v1, s77, v0\n"
"ds_read_b128 v[80:83], v1\n"
"ds_read_b128 v[84:87], v1 offset:1024\n"
"v_mfma_f32_32x32x16_bf16 v[64:79], v[92:95], a[132:135], v[64:79]\n"
"buffer_store_dword v4, v197, s[44:47], 0 offen sc1\n"
"v_mfma_f32_32x32x16_bf16 v[48:63], v[96:99], a[248:251], v[48:63]\n"
"ds_read_b128 v[88:91], v1 offset:2048\n"
"v_mfma_f32_32x32x16_bf16 v[64:79], v[96:99], a[136:139], v[64:79]\n"
"s_sub_u32 s60, s57, 1\n"
"s_mov_b32 s86, s62\n"
"s_add_u32 s57, s57, 1\n"
"s_cmp_eq_u32 s87, 0\n"
"v_mfma_f32_32x32x16_bf16 v[48:63], v[100:103], a[252:255], v[48:63]\n"
"ds_read_b128 v[92:95], v1 offset:3072\n"
"v_mfma_f32_32x32x16_bf16 v[64:79], v[100:103], a[140:143], v[64:79]\n"
"s_cbranch_scc1 LfxAvord36_%=\n"
"LPBvord36_%=:\n"
"s_waitcnt lgkmcnt(3)\n"
"v_mfma_f32_32x32x16_bf16 v[16:31], v[80:83], v[128:131], 0\n"
"ds_read_b128 v[96:99], v1 offset:4096\n"
"s_add_u32 s92, s57, 1\n"
"v_readlane_b32 s92, v6, s92\n"
"s_sub_u32 s64, 36864, s77\n"
"v_mfma_f32_32x32x16_bf16 v[32:47], v[80:83], a[0:3], 0\n"
"s_add_u32 s64, s64, s55\n"
"s_mul_i32 s92, s92, s50\n"
"s_add_u32 s65, s92, s49\n"
"s_sub_u32 s90, s57, 1\n"
"s_and_b32 s90, s90, 1\n"
"s_waitcnt lgkmcnt(3)\n"
"v_mfma_f32_32x32x16_bf16 v[16:31], v[84:87], v[132:135], v[16:31]\n"
"ds_read_b128 v[100:103], v1 offset:5120\n"
"s_lshl_b32 s90, s90, 8\n"
"v_add_u32 v194, s90, v212\n"
"ds_read_b32 v195, v194\n"
"v_mfma_f32_32x32x16_bf16 v[32:47], v[84:87], a[4:7], v[32:47]\n"
"s_add_u32 m0, s64, 0\n"
"s_add_u32 s92, s65, 0\n"
"buffer_load_dwordx4 v2, s[36:39], s92 offen sc1 lds\n"
"s_sub_u32 s93, s57, 1\n"
"s_waitcnt lgkmcnt(4)\n"
"v_mfma_f32_32x32x16_bf16 v[16:31], v[88:91], v[136:139], v[16:31]\n"
"ds_read_b128 v[80:83], v1 offset:6144\n"
"v_readlane_b32 s93, v6, s93\n"
"v_mfma_f32_32x32x16_bf16 v[32:47], v[88:91], a[8:11], v[32:47]\n"
"s_add_u32 m0, s64, 4096\n"
"s_add_u32 s92, s65, 4096\n"
"buffer_load_dwordx4 v2, s[36:39], s92 offen sc1 lds\n"
"s_mul_i32 s93, s93, s52\n"
"s_waitcnt lgkmcnt(4)\n"
"v_mfma_f32_32x32x16_bf16 v[16:31], v[92:95], v[140:143], v[16:31]\n"
"ds_read_b128 v[84:87], v1 offset:7168\n"
"s_waitcnt lgkmcnt(2)\n"
"v_cmp_ne_u32_e64 s[80:81], s97, v195\n"
"s_add_u32 s66, s93, s51\n"
"v_readlane_b32 s93, v6, s60\n"
"v_mfma_f32_32x32x16_bf16 v[32:47], v[92:95], a[12:15], v[32:47]\n"
"s_add_u32 m0, s64, 8192\n"
"s_add_u32 s92, s65, 8192\n"
"buffer_load_dwordx4 v2, s[36:39], s92 offen sc1 lds\n"
"s_and_b64 s[80:81], s[80:81], s[88:89]\n"
"v_mfma_f32_32x32x16_bf16 v[16:31], v[96:99], v[144:147], v[16:31]\n"
"ds_read_b128 v[88:91], v1 offset:8192\n"
"s_ff1_i32_b64 s90, s[80:81]\n"
"s_cmp_lt_i32 s90, 0\n"
"s_cselect_b32 s90, 16, s90\n"
"s_mul_i32 s90, s90, 65536\n"
"v_mfma_f32_32x32x16_bf16 v[32:47], v[96:99], a[16:19], v[32:47]\n"
"s_add_u32 m0, s64, 12288\n"
"s_add_u32 s92, s65, 12288\n"
"buffer_load_dwordx4 v2, s[36:39], s92 offen sc1 lds\n"
"s_lshr_b32 s90, s90, 16\n"
"v_mfma_f32_32x32x16_bf16 v[16:31], v[100:103], v[148:151], v[16:31]\n"
"ds_read_b128 v[92:95], v1 offset:9216\n"
"s_sub_u32 s91, s48, s86\n"
"s_min_u32 s90, s90, s91\n"
"s_add_u32 s90, s90, s86\n"
"s_mul_i32 s93, s93, s54\n"
"v_mfma_f32_32x32x16_bf16 v[32:47], v[100:103], a[20:23], v[32:47]\n"
"s_add_u32 m0, s64, 16384\n"
"s_add_u32 s92, s65, 16384\n"
"buffer_load_dwordx4 v2, s[36:39], s92 offen sc1 lds\n"
"s_add_u32 s93, s93, s53\n"
"s_waitcnt lgkmcnt(3)\n"
"v_mfma_f32_32x32x16_bf16 v[16:31], v[80:83], v[152:155], v[16:31]\n"
"ds_read_b128 v[96:99], v1 offset:10240\n"
"s_cmp_lt_i32 s86, 0\n"
"s_cselect_b32 s58, s58, s90\n"
"v_mfma_f32_32x32x16_bf16 v[32:47], v[80:83], a[24:27], v[32:47]\n"
"s_add_u32 m0, s64, 20480\n"
"s_add_u32 s92, s65, 20480\n"
"buffer_load_dwordx4 v2, s[36:39], s92 offen sc1 lds\n"
"s_waitcnt lgkmcnt(3)\n"
"v_mfma_f32_32x32x16_bf16 v[16:31], v[84:87], v[156:159], v[16:31]\n"
"ds_read_b128 v[100:103], v1 offset:11264\n"
"s_cmp_lt_u32 s58, s48\n"
"s_cselect_b32 s62, s58, -1\n"
"s_sub_u32 s90, s58, s57\n"
"v_mfma_f32_32x32x16_bf16 v[32:47], v[84:87], a[28:31], v[32:47]\n"
"s_add_u32 m0, s64, 24576\n"
"s_add_u32 s92, s65, 24576\n"
"buffer_load_dwordx4 v2, s[36:39], s92 offen sc1 lds\n"
"s_waitcnt lgkmcnt(3)\n"
"v_mfma_f32_32x32x16_bf16 v[16:31], v[88:91], a[144:147], v[16:31]\n"
"ds_read_b128 v[80:83], v1 offset:12288\n"
"s_cmp_le_u32 s90, 4\n"
"s_cselect_b32 s62, s62, -1\n"
"s_max_i32 s90, s62, 0\n"
"v_mfma_f32_32x32x16_bf16 v[32:47], v[88:91], a[32:35], v[32:47]\n"
"s_add_u32 m0, s64, 28672\n"
"s_add_u32 s92, s65, 28672\n"
"buffer_load_dwordx4 v2, s[36:39], s92 offen sc1 lds\n"
"s_lshl_b32 s90, s90, 2\n"
"s_waitcnt lgkmcnt(3)\n"
"v_mfma_f32_32x32x16_bf16 v[16:31], v[92:95], a[148:151], v[16:31]\n"
"ds_read_b128 v[84:87], v1 offset:13312\n"
"s_add_u32 s90, s90, 86528\n"
"v_add_u32 v196, s90, v8\n"
"ds_read_b32 v196, v196\n"
"v_mfma_f32_32x32x16_bf16 v[32:47], v[92:95], a[36:39], v[32:47]\n"
"s_add_u32 m0, s64, 32768\n"
"s_add_u32 s92, s65, 32768\n"
"buffer_load_dwordx4 v2, s[36:39], s92 offen sc1 lds\n"
"s_waitcnt lgkmcnt(4)\n"
"v_mfma_f32_32x32x16_bf16 v[16:31], v[96:99], a[152:155], v[16:31]\n"
"ds_read_b128 v[88:91], v1 offset:14336\n"
"s_cmp_ge_i32 s62, 0\n"
"s_cselect_b64 s[94:95], s[78:79], 0\n"
"s_and_b32 s91, s57, 1\n"
"v_mfma_f32_32x32x16_bf16 v[32:47], v[96:99], a[40:43], v[32:47]\n"
"s_lshl_b32 s91, s91, 8\n"
"s_add_u32 s91, s91, 86016\n"
"s_add_u32 s90, s57, 2\n"
"s_cmp_lt_u32 s90, s58\n"
"s_cselect_b32 s87, 1, 0\n"
"s_waitcnt lgkmcnt(4)\n"
"v_mfma_f32_32x32x16_bf16 v[16:31], v[100:103], a[156:159], v[16:31]\n"
"ds_read_b128 v[92:95], v1 offset:15360\n"
"s_cmp_lt_i32 s60, 0\n"
"s_cselect_b32 s93, 0x7ffffff0, s93\n"
"v_add_u32 v197, s93, v3\n"
"v_mfma_f32_32x32x16_bf16 v[32:47], v[100:103], a[44:47], v[32:47]\n"
"v_cvt_pk_bf16_f32 v160, v48, v49\n"
"v_cvt_pk_bf16_f32 v161, v50, v51\n"
"v_cvt_pk_bf16_f32 v164, v52, v53\n"
"v_cvt_pk_bf16_f32 v165, v54, v55\n"
"v_cvt_pk_bf16_f32 v162, v56, v57\n"
"s_waitcnt lgkmcnt(4)\n"
"v_mfma_f32_32x32x16_bf16 v[16:31], v[80:83], a[160:163], v[16:31]\n"
"ds_read_b128 v[96:99], v1 offset:16384\n"
"s_waitcnt lgkmcnt(3)\n"
"v_add_u32 v196, v196, v9\n"
"v_cvt_pk_bf16_f32 v163, v58, v59\n"
"v_cvt_pk_bf16_f32 v166, v60, v61\n"
"v_mfma_f32_32x32x16_bf16 v[32:47], v[80:83], a[48:51], v[32:47]\n"
"s_mov_b32 m0, s91\n"
"s_mov_b64 exec, s[94:95]\n"
"buffer_load_dword v196, s[44:47], 0 offen sc1 lds\n"
"s_mov_b64 exec, -1\n"
"v_cvt_pk_bf16_f32 v167, v62, v63\n"
"v_mfma_f32_32x32x16_bf16 v[16:31], v[84:87], a[164:167], v[16:31]\n"
"ds_read_b128 v[100:103], v1 offset:17408\n"
"v_cvt_pk_bf16_f32 v168, v64, v65\n"
"v_cvt_pk_bf16_f32 v169, v66, v67\n"
"v_cvt_pk_bf16_f32 v172, v68, v69\n"
"v_cvt_pk_bf16_f32 v173, v70, v71\n"
"v_mfma_f32_32x32x16_bf16 v[32:47], v[84:87], a[52:55], v[32:47]\n"
"v_cvt_pk_bf16_f32 v170, v72, v73\n"
"v_cvt_pk_bf16_f32 v171, v74, v75\n"
"v_cvt_pk_bf16_f32 v174, v76, v77\n"
"v_cvt_pk_bf16_f32 v175, v78, v79\n"
"s_waitcnt lgkmcnt(3)\n"
"v_mfma_f32_32x32x16_bf16 v[16:31], v[88:91], a[168:171], v[16:31]\n"
"ds_read_b128 v[80:83], v1 offset:18432\n"
"buffer_store_dwordx4 v[160:163], v11, s[40:43], s66 offen sc1\n"
"v_mfma_f32_32x32x16_bf16 v[32:47], v[88:91], a[56:59], v[32:47]\n"
"buffer_store_dwordx4 v[164:167], v12, s[40:43], s66 offen sc1\n"
"s_waitcnt lgkmcnt(3)\n"
"v_mfma_f32_32x32x16_bf16 v[16:31], v[92:95], a[172:175], v[16:31]\n"
"ds_read_b128 v[84:87], v1 offset:19456\n"
"buffer_store_dwordx4 v[168:171], v13, s[40:43], s66 offen sc1\n"
"v_mfma_f32_32x32x16_bf16 v[32:47], v[92:95], a[60:63], v[32:47]\n"
"buffer_store_dwordx4 v[172:175], v14, s[40:43], s66 offen sc1\n"
"s_waitcnt lgkmcnt(3)\n"
"v_mfma_f32_32x32x16_bf16 v[16:31], v[96:99], a[176:179], v[16:31]\n"
"ds_read_b128 v[88:91], v1 offset:20480\n"
"v_mfma_f32_32x32x16_bf16 v[32:47], v[96:99], a[64:67], v[32:47]\n"
"s_waitcnt lgkmcnt(3)\n"
"v_mfma_f32_32x32x16_bf16 v[16:31], v[100:103], a[180:183], v[16:31]\n"
"ds_read_b128 v[92:95], v1 offset:21504\n"
"v_mfma_f32_32x32x16_bf16 v[32:47], v[100:103], a[68:71], v[32:47]\n"
"s_waitcnt lgkmcnt(3)\n"
"v_mfma_f32_32x32x16_bf16 v[16:31], v[80:83], a[184:187], v[16:31]\n"
"ds_read_b128 v[96:99], v1 offset:22528\n"
"v_mfma_f32_32x32x16_bf16 v[32:47], v[80:83], a[72:75], v[32:47]\n"
"s_waitcnt lgkmcnt(3)\n"
"v_mfma_f32_32x32x16_bf16 v[16:31], v[84:87], a[188:191], v[16:31]\n"
"ds_read_b128 v[100:103], v1 offset:23552\n"
"v_mfma_f32_32x32x16_bf16 v[32:47], v[84:87], a[76:79], v[32:47]\n"
"s_waitcnt lgkmcnt(3)\n"
"v_mfma_f32_32x32x16_bf16 v[16:31], v[88:91], a[192:195], v[16:31]\n"
"ds_read_b128 v[80:83], v1 offset:24576\n"
"v_mfma_f32_32x32x16_bf16 v[32:47], v[88:91], a[80:83], v[32:47]\n"
"s_waitcnt lgkmcnt(3)\n"
"v_mfma_f32_32x32x16_bf16 v[16:31], v[92:95], a[196:199], v[16:31]\n"
"ds_read_b128 v[84:87], v1 offset:25600\n"
"v_mfma_f32_32x32x16_bf16 v[32:47], v[92:95], a[84:87], v[32:47]\n"
"s_waitcnt lgkmcnt(3)\n"
"v_mfma_f32_32x32x16_bf16 v[16:31], v[96:99], a[200:203], v[16:31]\n"
"ds_read_b128 v[88:91], v1 offset:26624\n"
"v_mfma_f32_32x32x16_bf16 v[32:47], v[96:99], a[88:91], v[32:47]\n"
"s_waitcnt lgkmcnt(3)\n"
"v_mfma_f32_32x32x16_bf16 v[16:31], v[100:103], a[204:207], v[16:31]\n"
"ds_read_b128 v[92:95], v1 offset:27648\n"
"v_mfma_f32_32x32x16_bf16 v[32:47], v[100:103], a[92:95], v[32:47]\n"
"s_waitcnt lgkmcnt(3)\n"
"v_mfma_f32_32x32x16_bf16 v[16:31], v[80:83], a[208:211], v[16:31]\n"
"ds_read_b128 v[96:99], v1 offset:28672\n"
"v_mfma_f32_32x32x16_bf16 v[32:47], v[80:83], a[96:99], v[32:47]\n"
"s_waitcnt lgkmcnt(3)\n"
"v_mfma_f32_32x32x16_bf16 v[16:31], v[84:87], a[212:215], v[16:31]\n"
"ds_read_b128 v[100:103], v1 offset:29696\n"
"v_mfma_f32_32x32x16_bf16 v[32:47], v[84:87], a[100:103], v[32:47]\n"
"s_waitcnt lgkmcnt(3)\n"
"v_mfma_f32_32x32x16_bf16 v[16:31], v[88:91], a[216:219], v[16:31]\n"
"ds_read_b128 v[80:83], v1 offset:30720\n"
"v_mfma_f32_32x32x16_bf16 v[32:47], v[88:91], a[104:107], v[32:47]\n"
"s_waitcnt lgkmcnt(3)\n"
"v_mfma_f32_32x32x16_bf16 v[16:31], v[92:95], a[220:223], v[16:31]\n"
"ds_read_b128 v[84:87], v1 offset:31744\n"
"v_mfma_f32_32x32x16_bf16 v[32:47], v[92:95], a[108:111], v[32:47]\n"
"s_waitcnt lgkmcnt(3)\n"
"v_mfma_f32_32x32x16_bf16 v[16:31], v[96:99], a[224:227], v[16:31]\n"
"ds_read_b128 v[88:91], v1 offset:32768\n"
"v_mfma_f32_32x32x16_bf16 v[32:47], v[96:99], a[112:115], v[32:47]\n"
"s_waitcnt lgkmcnt(3)\n"
"v_mfma_f32_32x32x16_bf16 v[16:31], v[100:103], a[228:231], v[16:31]\n"
"ds_read_b128 v[92:95], v1 offset:33792\n"
"v_mfma_f32_32x32x16_bf16 v[32:47], v[100:103], a[116:119], v[32:47]\n"
"s_waitcnt lgkmcnt(3)\n"
"v_mfma_f32_32x32x16_bf16 v[16:31], v[80:83], a[232:235], v[16:31]\n"
"ds_read_b128 v[96:99], v1 offset:34816\n"
"v_mfma_f32_32x32x16_bf16 v[32:47], v[80:83], a[120:123], v[32:47]\n"
"s_waitcnt lgkmcnt(3)\n"
"v_mfma_f32_32x32x16_bf16 v[16:31], v[84:87], a[236:239], v[16:31]\n"
"ds_read_b128 v[100:103], v1 offset:35840\n"
"v_mfma_f32_32x32x16_bf16 v[32:47], v[84:87], a[124:127], v[32:47]\n"
"s_waitcnt lgkmcnt(3)\n"
"v_mfma_f32_32x32x16_bf16 v[16:31], v[88:91], a[240:243], v[16:31]\n"
"v_mfma_f32_32x32x16_bf16 v[32:47], v[88:91], a[128:131], v[32:47]\n"
"s_waitcnt vmcnt(4) lgkmcnt(0)\n"
"s_barrier\n"
"v_mfma_f32_32x32x16_bf16 v[16:31], v[92:95], a[244:247], v[16:31]\n"
"s_sub_u32 s77, 36864, s77\n"
"v_add_u32 v1, s77, v0\n"
"ds_read_b128 v[80:83], v1\n"
"ds_read_b128 v[84:87], v1 offset:1024\n"
"v_mfma_f32_32x32x16_bf16 v[32:47], v[92:95], a[132:135], v[32:47]\n"
"buffer_store_dword v4, v197, s[44:47], 0 offen sc1\n"
"v_mfma_f32_32x32x16_bf16 v[16:31], v[96:99], a[248:251], v[16:31]\n"
"ds_read_b128 v[88:91], v1 offset:2048\n"
"v_mfma_f32_32x32x16_bf16 v[32:47], v[96:99], a[136:139], v[32:47]\n"
"s_sub_u32 s60, s57, 1\n"
"s_mov_b32 s86, s62\n"
"s_add_u32 s57, s57, 1\n"
"s_cmp_eq_u32 s87, 0\n"
"v_mfma_f32_32x32x16_bf16 v[16:31], v[100:103], a[252:255], v[16:31]\n"
"ds_read_b128 v[92:95], v1 offset:3072\n"
"v_mfma_f32_32x32x16_bf16 v[32:47], v[100:103], a[140:143], v[32:47]\n"
"s_cbranch_scc0 LPAvord36_%=\n"
"s_mov_b32 s99, 0\n"
"s_sub_u32 s57, s57, 1\n"
"s_branch Le1vord36_%=\n"
"LfxAvord36_%=:\n"
"s_mov_b32 s99, 1\n"
"s_sub_u32 s57, s57, 1\n"
"Le1vord36_%=:\n"
"s_mov_b32 s60, -1\n"
"s_mov_b32 s61, -1\n"
"s_cmp_eq_u32 s59, 0\n"
"s_cbranch_scc1 Le2vord36_%=\n"
"s_sub_u32 s60, s57, 1\n"
"Le2vord36_%=:\n"
"s_cmp_lt_i32 s62, 0\n"
"s_cbranch_scc1 Le3vord36_%=\n"
"s_and_b32 s72, s57, 1\n"
"s_lshl_b32 s72, s72, 8\n"
"s_add_u32 s72, s72, 86016\n"
"s_sub_u32 s67, s72, 0\n"
"v_lshrrev_b32 v192, 2, v0\n"
"v_add_u32 v192, s67, v192\n"
"ds_read_b32 v193, v192\n"
"s_waitcnt lgkmcnt(0)\n"
"v_cmp_eq_u32_e64 s[80:81], s97, v193\n"
"s_nop 1\n"
"s_sub_u32 s68, s48, s62\n"
"s_min_u32 s68, s68, 16\n"
"s_mov_b32 s70, 0\n"
"Levevord36_%=:\n"
"s_cmp_ge_u32 s70, s68\n"
"s_cbranch_scc1 Lexevord36_%=\n"
"s_and_b32 s69, s80, 1\n"
"s_cmp_eq_u32 s69, 1\n"
"s_cbranch_scc0 Lexevord36_%=\n"
"s_lshr_b64 s[80:81], s[80:81], 1\n"
"s_add_u32 s70, s70, 1\n"
"s_branch Levevord36_%=\n"
"Lexevord36_%=:\n"
"s_add_u32 s58, s58, s70\n"
"s_waitcnt lgkmcnt(0)\n"
"Le3vord36_%=:\n"
"s_mov_b32 s59, 0\n"
"s_add_u32 s68, s57, 2\n"
"s_cmp_lt_u32 s68, s58\n"
"s_cbranch_scc0 Le4vord36_%=\n"
"s_cmp_ge_u32 s57, 3\n"
"s_cbranch_scc0 Le4vord36_%=\n"
"s_add_u32 s59, s99, 1\n"
"s_mov_b32 s86, -1\n"
"s_add_u32 s57, s57, 1\n"
"s_branch Ltopvord36_%=\n"
"Le4vord36_%=:\n"
"s_mov_b32 s61, s57\n"
"s_cmp_eq_u32 s99, 0\n"
"s_cbranch_scc0 Le5vord36_%=\n"
"s_nop 3\n"
"v_readlane_b32 s67, v6, s57\n"
"s_nop 0\n"
"s_mul_i32 s67, s67, s52\n"
"s_add_u32 s66, s67, s51\n"
"v_cvt_pk_bf16_f32 v160, v16, v17\n"
"v_cvt_pk_bf16_f32 v161, v18, v19\n"
"v_cvt_pk_bf16_f32 v164, v20, v21\n"
"v_cvt_pk_bf16_f32 v165, v22, v23\n"
"v_cvt_pk_bf16_f32 v162, v24, v25\n"
"v_cvt_pk_bf16_f32 v163, v26, v27\n"
"v_cvt_pk_bf16_f32 v166, v28, v29\n"
"v_cvt_pk_bf16_f32 v167, v30, v31\n"
"v_cvt_pk_bf16_f32 v168, v32, v33\n"
"v_cvt_pk_bf16_f32 v169, v34, v35\n"
"v_cvt_pk_bf16_f32 v172, v36, v37\n"
"v_cvt_pk_bf16_f32 v173, v38, v39\n"
"v_cvt_pk_bf16_f32 v170, v40, v41\n"
"v_cvt_pk_bf16_f32 v171, v42, v43\n"
"v_cvt_pk_bf16_f32 v174, v44, v45\n"
"v_cvt_pk_bf16_f32 v175, v46, v47\n"
"buffer_store_dwordx4 v[160:163], v11, s[40:43], s66 offen sc1\n"
"buffer_store_dwordx4 v[164:167], v12, s[40:43], s66 offen sc1\n"
"buffer_store_dwordx4 v[168:171], v13, s[40:43], s66 offen sc1\n"
"buffer_store_dwordx4 v[172:175], v14, s[40:43], s66 offen sc1\n"
"s_add_u32 s57, s57, 1\n"
"s_branch Ltopvord36_%=\n"
"Le5vord36_%=:\n"
"s_nop 3\n"
"v_readlane_b32 s67, v6, s57\n"
"s_nop 0\n"
"s_mul_i32 s67, s67, s52\n"
"s_add_u32 s66, s67, s51\n"
"v_cvt_pk_bf16_f32 v160, v48, v49\n"
"v_cvt_pk_bf16_f32 v161, v50, v51\n"
"v_cvt_pk_bf16_f32 v164, v52, v53\n"
"v_cvt_pk_bf16_f32 v165, v54, v55\n"
"v_cvt_pk_bf16_f32 v162, v56, v57\n"
"v_cvt_pk_bf16_f32 v163, v58, v59\n"
"v_cvt_pk_bf16_f32 v166, v60, v61\n"
"v_cvt_pk_bf16_f32 v167, v62, v63\n"
"v_cvt_pk_bf16_f32 v168, v64, v65\n"
"v_cvt_pk_bf16_f32 v169, v66, v67\n"
"v_cvt_pk_bf16_f32 v172, v68, v69\n"
"v_cvt_pk_bf16_f32 v173, v70, v71\n"
"v_cvt_pk_bf16_f32 v170, v72, v73\n"
"v_cvt_pk_bf16_f32 v171, v74, v75\n"
"v_cvt_pk_bf16_f32 v174, v76, v77\n"
"v_cvt_pk_bf16_f32 v175, v78, v79\n"
"buffer_store_dwordx4 v[160:163], v11, s[40:43], s66 offen sc1\n"
"buffer_store_dwordx4 v[164:167], v12, s[40:43], s66 offen sc1\n"
"buffer_store_dwordx4 v[168:171], v13, s[40:43], s66 offen sc1\n"
"buffer_store_dwordx4 v[172:175], v14, s[40:43], s66 offen sc1\n"
"s_add_u32 s57, s57, 1\n"
"s_branch Ltopvord36_%=\n"
"Lnonvord36_%=:\n"
"s_max_i32 s68, s60, s61\n"
"s_cmp_ge_i32 s68, 0\n"
"s_cselect_b32 s63, 1, 0\n"
"s_waitcnt lgkmcnt(3)\n"
"v_mfma_f32_32x32x16_bf16 v[16:31], v[80:83], v[128:131], 0\n"
"ds_read_b128 v[96:99], v1 offset:4096\n"
"v_mfma_f32_32x32x16_bf16 v[32:47], v[80:83], a[0:3], 0\n"
"s_add_u32 s90, s57, 1\n"
"s_cmp_lt_u32 s90, s48\n"
"s_cselect_b32 s62, s90, -1\n"
"s_lshl_b32 s90, s90, 2\n"
"s_add_u32 s90, s90, 86528\n"
"v_add_u32 v196, s90, v8\n"
"s_waitcnt lgkmcnt(3)\n"
"v_mfma_f32_32x32x16_bf16 v[16:31], v[84:87], v[132:135], v[16:31]\n"
"ds_read_b128 v[100:103], v1 offset:5120\n"
"ds_read_b32 v196, v196\n"
"s_cmp_ge_i32 s62, 0\n"
"s_cselect_b64 s[94:95], s[78:79], 0\n"
"v_mfma_f32_32x32x16_bf16 v[32:47], v[84:87], a[4:7], v[32:47]\n"
"s_and_b32 s94, s94, 3\n"
"s_mov_b32 s95, 0\n"
"s_waitcnt lgkmcnt(4)\n"
"v_mfma_f32_32x32x16_bf16 v[16:31], v[88:91], v[136:139], v[16:31]\n"
"ds_read_b128 v[80:83], v1 offset:6144\n"
"v_mfma_f32_32x32x16_bf16 v[32:47], v[88:91], a[8:11], v[32:47]\n"
"s_waitcnt lgkmcnt(4)\n"
"v_mfma_f32_32x32x16_bf16 v[16:31], v[92:95], v[140:143], v[16:31]\n"
"ds_read_b128 v[84:87], v1 offset:7168\n"
"s_waitcnt lgkmcnt(2)\n"
"v_add_u32 v196, v196, v9\n"
"v_cndmask_b32_e64 v196, v15, v196, s[94:95]\n"
"v_mfma_f32_32x32x16_bf16 v[32:47], v[92:95], a[12:15], v[32:47]\n"
"s_mov_b32 s91, 87296\n"
"s_cmp_eq_u32 %0, 2\n"
"s_cselect_b32 m0, s91, 87040\n"
"s_nop 0\n"
"buffer_load_dword v196, s[44:47], 0 offen sc1 lds\n"
"v_mfma_f32_32x32x16_bf16 v[16:31], v[96:99], v[144:147], v[16:31]\n"
"ds_read_b128 v[88:91], v1 offset:8192\n"
"v_mfma_f32_32x32x16_bf16 v[32:47], v[96:99], a[16:19], v[32:47]\n"
"v_mfma_f32_32x32x16_bf16 v[16:31], v[100:103], v[148:151], v[16:31]\n"
"ds_read_b128 v[92:95], v1 offset:9216\n"
"v_mfma_f32_32x32x16_bf16 v[32:47], v[100:103], a[20:23], v[32:47]\n"
"s_waitcnt lgkmcnt(3)\n"
"v_mfma_f32_32x32x16_bf16 v[16:31], v[80:83], v[152:155], v[16:31]\n"
"ds_read_b128 v[96:99], v1 offset:10240\n"
"v_mfma_f32_32x32x16_bf16 v[32:47], v[80:83], a[24:27], v[32:47]\n"
"s_waitcnt lgkmcnt(3)\n"
"v_mfma_f32_32x32x16_bf16 v[16:31], v[84:87], v[156:159], v[16:31]\n"
"ds_read_b128 v[100:103], v1 offset:11264\n"
"v_mfma_f32_32x32x16_bf16 v[32:47], v[84:87], a[28:31], v[32:47]\n"
"s_waitcnt lgkmcnt(3)\n"
"v_mfma_f32_32x32x16_bf16 v[16:31], v[88:91], a[144:147], v[16:31]\n"
"ds_read_b128 v[80:83], v1 offset:12288\n"
"v_mfma_f32_32x32x16_bf16 v[32:47], v[88:91], a[32:35], v[32:47]\n"
"s_waitcnt lgkmcnt(3)\n"
"v_mfma_f32_32x32x16_bf16 v[16:31], v[92:95], a[148:151], v[16:31]\n"
"ds_read_b128 v[84:87], v1 offset:13312\n"
"v_mfma_f32_32x32x16_bf16 v[32:47], v[92:95], a[36:39], v[32:47]\n"
"s_waitcnt lgkmcnt(3)\n"
"v_mfma_f32_32x32x16_bf16 v[16:31], v[96:99], a[152:155], v[16:31]\n"
"ds_read_b128 v[88:91], v1 offset:14336\n"
"v_mfma_f32_32x32x16_bf16 v[32:47], v[96:99], a[40:43], v[32:47]\n"
"s_cmp_eq_u32 s63, 0\n"
"s_cbranch_scc1 LnmLvord36_%=\n"
"s_waitcnt vmcnt(1)\n"
"s_barrier\n"
"v_readlane_b32 s93, v6, s60\n"
"s_nop 0\n"
"s_mul_i32 s93, s93, s54\n"
"s_add_u32 s93, s93, s53\n"
"s_cmp_lt_i32 s60, 0\n"
"s_cselect_b32 s93, 0x7ffffff0, s93\n"
"v_add_u32 v197, s93, v3\n"
"buffer_store_dword v4, v197, s[44:47], 0 offen sc1\n"
"v_readlane_b32 s93, v6, s61\n"
"s_nop 0\n"
"s_mul_i32 s93, s93, s54\n"
"s_add_u32 s93, s93, s53\n"
"s_cmp_lt_i32 s61, 0\n"
"s_cselect_b32 s93, 0x7ffffff0, s93\n"
"v_add_u32 v199, s93, v3\n"
"buffer_store_dword v4, v199, s[44:47], 0 offen sc1\n"
"LnmLvord36_%=:\n"
"s_waitcnt lgkmcnt(3)\n"
"v_mfma_f32_32x32x16_bf16 v[16:31], v[100:103], a[156:159], v[16:31]\n"
"ds_read_b128 v[92:95], v1 offset:15360\n"
"v_mfma_f32_32x32x16_bf16 v[32:47], v[100:103], a[44:47], v[32:47]\n"
"s_waitcnt lgkmcnt(3)\n"
"v_mfma_f32_32x32x16_bf16 v[16:31], v[80:83], a[160:163], v[16:31]\n"
"ds_read_b128 v[96:99], v1 offset:16384\n"
"v_mfma_f32_32x32x16_bf16 v[32:47], v[80:83], a[48:51], v[32:47]\n"
"s_waitcnt lgkmcnt(3)\n"
"v_mfma_f32_32x32x16_bf16 v[16:31], v[84:87], a[164:167], v[16:31]\n"
"ds_read_b128 v[100:103], v1 offset:17408\n"
"v_mfma_f32_32x32x16_bf16 v[32:47], v[84:87], a[52:55], v[32:47]\n"
"s_waitcnt lgkmcnt(3)\n"
"v_mfma_f32_32x32x16_bf16 v[16:31], v[88:91], a[168:171], v[16:31]\n"
"ds_read_b128 v[80:83], v1 offset:18432\n"
"v_mfma_f32_32x32x16_bf16 v[32:47], v[88:91], a[56:59], v[32:47]\n"
"s_waitcnt lgkmcnt(3)\n"
"v_mfma_f32_32x32x16_bf16 v[16:31], v[92:95], a[172:175], v[16:31]\n"
"ds_read_b128 v[84:87], v1 offset:19456\n"
"v_mfma_f32_32x32x16_bf16 v[32:47], v[92:95], a[60:63], v[32:47]\n"
"s_mov_b32 s91, 87552\n"
"s_cmp_eq_u32 %0, 2\n"
"s_cselect_b32 m0, s91, 87040\n"
"s_nop 0\n"
"buffer_load_dword v196, s[44:47], 0 offen sc1 lds\n"
"s_waitcnt lgkmcnt(3)\n"
"v_mfma_f32_32x32x16_bf16 v[16:31], v[96:99], a[176:179], v[16:31]\n"
"ds_read_b128 v[88:91], v1 offset:20480\n"
"v_mfma_f32_32x32x16_bf16 v[32:47], v[96:99], a[64:67], v[32:47]\n"
"s_waitcnt lgkmcnt(3)\n"
"v_mfma_f32_32x32x16_bf16 v[16:31], v[100:103], a[180:183], v[16:31]\n"
"ds_read_b128 v[92:95], v1 offset:21504\n"
"v_mfma_f32_32x32x16_bf16 v[32:47], v[100:103], a[68:71], v[32:47]\n"
"s_waitcnt lgkmcnt(3)\n"
"v_mfma_f32_32x32x16_bf16 v[16:31], v[80:83], a[184:187], v[16:31]\n"
"ds_read_b128 v[96:99], v1 offset:22528\n"
"v_mfma_f32_32x32x16_bf16 v[32:47], v[80:83], a[72:75], v[32:47]\n"
"s_waitcnt lgkmcnt(3)\n"
"v_mfma_f32_32x32x16_bf16 v[16:31], v[84:87], a[188:191], v[16:31]\n"
"ds_read_b128 v[100:103], v1 offset:23552\n"
"v_mfma_f32_32x32x16_bf16 v[32:47], v[84:87], a[76:79], v[32:47]\n"
"s_waitcnt lgkmcnt(3)\n"
"v_mfma_f32_32x32x16_bf16 v[16:31], v[88:91], a[192:195], v[16:31]\n"
"ds_read_b128 v[80:83], v1 offset:24576\n"
"v_mfma_f32_32x32x16_bf16 v[32:47], v[88:91], a[80:83], v[32:47]\n"
"s_waitcnt lgkmcnt(3)\n"
"v_mfma_f32_32x32x16_bf16 v[16:31], v[92:95], a[196:199], v[16:31]\n"
"ds_read_b128 v[84:87], v1 offset:25600\n"
"v_mfma_f32_32x32x16_bf16 v[32:47], v[92:95], a[84:87], v[32:47]\n"
"s_waitcnt lgkmcnt(3)\n"
"v_mfma_f32_32x32x16_bf16 v[16:31], v[96:99], a[200:203], v[16:31]\n"
"ds_read_b128 v[88:91], v1 offset:26624\n"
"v_mfma_f32_32x32x16_bf16 v[32:47], v[96:99], a[88:91], v[32:47]\n"
"s_waitcnt lgkmcnt(3)\n"
"v_mfma_f32_32x32x16_bf16 v[16:31], v[100:103], a[204:207], v[16:31]\n"
"ds_read_b128 v[92:95], v1 offset:27648\n"
"v_mfma_f32_32x32x16_bf16 v[32:47], v[100:103], a[92:95], v[32:47]\n"
"s_cmp_eq_u32 s63, 0\n"
"s_cbranch_scc1 LpwLvord36_%=\n"
"s_waitcnt vmcnt(3)\n"
"s_branch LpxLvord36_%=\n"
"LpwLvord36_%=:\n"
"s_waitcnt vmcnt(1)\n"
"LpxLvord36_%=:\n"
"s_barrier\n"
"s_waitcnt lgkmcnt(3)\n"
"v_mfma_f32_32x32x16_bf16 v[16:31], v[80:83], a[208:211], v[16:31]\n"
"ds_read_b128 v[96:99], v1 offset:28672\n"
"ds_read_b32 v195, v212 offset:1280\n"
"s_add_u32 s92, s57, 1\n"
"v_readlane_b32 s92, v6, s92\n"
"s_sub_u32 s64, 36864, s77\n"
"v_mfma_f32_32x32x16_bf16 v[32:47], v[80:83], a[96:99], v[32:47]\n"
"s_add_u32 s64, s64, s55\n"
"s_mul_i32 s92, s92, s50\n"
"s_add_u32 s65, s92, s49\n"
"s_waitcnt lgkmcnt(4)\n"
"v_mfma_f32_32x32x16_bf16 v[16:31], v[84:87], a[212:215], v[16:31]\n"
"ds_read_b128 v[100:103], v1 offset:29696\n"
"v_mfma_f32_32x32x16_bf16 v[32:47], v[84:87], a[100:103], v[32:47]\n"
"s_waitcnt lgkmcnt(1)\n"
"v_cmp_ne_u32_e64 s[80:81], s97, v195\n"
"v_mfma_f32_32x32x16_bf16 v[16:31], v[88:91], a[216:219], v[16:31]\n"
"ds_read_b128 v[80:83], v1 offset:30720\n"
"s_and_b32 s80, s80, 3\n"
"s_mov_b32 s81, 0\n"
"s_ff1_i32_b64 s90, s[80:81]\n"
"s_cmp_lt_i32 s90, 0\n"
"s_cselect_b32 s90, 2, s90\n"
"v_mfma_f32_32x32x16_bf16 v[32:47], v[88:91], a[104:107], v[32:47]\n"
"s_mul_i32 s90, s90, 65536\n"
"s_lshr_b32 s90, s90, 16\n"
"s_sub_u32 s91, s48, s62\n"
"s_min_u32 s90, s90, s91\n"
"s_add_u32 s90, s90, s62\n"
"v_mfma_f32_32x32x16_bf16 v[16:31], v[92:95], a[220:223], v[16:31]\n"
"ds_read_b128 v[84:87], v1 offset:31744\n"
"s_cmp_lt_i32 s62, 0\n"
"s_cselect_b32 s58, s58, s90\n"
"s_add_u32 s90, s57, 1\n"
"v_mfma_f32_32x32x16_bf16 v[32:47], v[92:95], a[108:111], v[32:47]\n"
"s_cmp_lt_u32 s90, s58\n"
"s_cselect_b64 s[94:95], -1, 0\n"
"s_cselect_b32 s87, 1, 0\n"
"v_cndmask_b32_e64 v198, v15, v2, s[94:95]\n"
"v_mfma_f32_32x32x16_bf16 v[16:31], v[96:99], a[224:227], v[16:31]\n"
"ds_read_b128 v[88:91], v1 offset:32768\n"
"s_add_u32 m0, s64, 0\n"
"s_add_u32 s92, s65, 0\n"
"buffer_load_dwordx4 v198, s[36:39], s92 offen sc1 lds\n"
"v_mfma_f32_32x32x16_bf16 v[32:47], v[96:99], a[112:115], v[32:47]\n"
"s_add_u32 m0, s64, 4096\n"
"s_add_u32 s92, s65, 4096\n"
"buffer_load_dwordx4 v198, s[36:39], s92 offen sc1 lds\n"
"s_waitcnt lgkmcnt(3)\n"
"v_mfma_f32_32x32x16_bf16 v[16:31], v[100:103], a[228:231], v[16:31]\n"
"ds_read_b128 v[92:95], v1 offset:33792\n"
"s_add_u32 m0, s64, 8192\n"
"s_add_u32 s92, s65, 8192\n"
"buffer_load_dwordx4 v198, s[36:39], s92 offen sc1 lds\n"
"v_mfma_f32_32x32x16_bf16 v[32:47], v[100:103], a[116:119], v[32:47]\n"
"s_add_u32 m0, s64, 12288\n"
"s_add_u32 s92, s65, 12288\n"
"buffer_load_dwordx4 v198, s[36:39], s92 offen sc1 lds\n"
"s_waitcnt lgkmcnt(3)\n"
"v_mfma_f32_32x32x16_bf16 v[16:31], v[80:83], a[232:235], v[16:31]\n"
"ds_read_b128 v[96:99], v1 offset:34816\n"
"s_add_u32 m0, s64, 16384\n"
"s_add_u32 s92, s65, 16384\n"
"buffer_load_dwordx4 v198, s[36:39], s92 offen sc1 lds\n"
"v_mfma_f32_32x32x16_bf16 v[32:47], v[80:83], a[120:123], v[32:47]\n"
"s_add_u32 m0, s64, 20480\n"
"s_add_u32 s92, s65, 20480\n"
"buffer_load_dwordx4 v198, s[36:39], s92 offen sc1 lds\n"
"s_waitcnt lgkmcnt(3)\n"
"v_mfma_f32_32x32x16_bf16 v[16:31], v[84:87], a[236:239], v[16:31]\n"
"ds_read_b128 v[100:103], v1 offset:35840\n"
"s_add_u32 m0, s64, 24576\n"
"s_add_u32 s92, s65, 24576\n"
"buffer_load_dwordx4 v198, s[36:39], s92 offen sc1 lds\n"
"v_mfma_f32_32x32x16_bf16 v[32:47], v[84:87], a[124:127], v[32:47]\n"
"s_add_u32 m0, s64, 28672\n"
"s_add_u32 s92, s65, 28672\n"
"buffer_load_dwordx4 v198, s[36:39], s92 offen sc1 lds\n"
"s_waitcnt lgkmcnt(3)\n"
"v_mfma_f32_32x32x16_bf16 v[16:31], v[88:91], a[240:243], v[16:31]\n"
"s_add_u32 m0, s64, 32768\n"
"s_add_u32 s92, s65, 32768\n"
"buffer_load_dwordx4 v198, s[36:39], s92 offen sc1 lds\n"
"v_mfma_f32_32x32x16_bf16 v[32:47], v[88:91], a[128:131], v[32:47]\n"
"s_waitcnt lgkmcnt(2)\n"
"v_mfma_f32_32x32x16_bf16 v[16:31], v[92:95], a[244:247], v[16:31]\n"
"v_mfma_f32_32x32x16_bf16 v[32:47], v[92:95], a[132:135], v[32:47]\n"
"s_waitcnt lgkmcnt(1)\n"
"v_mfma_f32_32x32x16_bf16 v[16:31], v[96:99], a[248:251], v[16:31]\n"
"v_mfma_f32_32x32x16_bf16 v[32:47], v[96:99], a[136:139], v[32:47]\n"
"s_waitcnt lgkmcnt(0)\n"
"v_mfma_f32_32x32x16_bf16 v[16:31], v[100:103], a[252:255], v[16:31]\n"
"v_mfma_f32_32x32x16_bf16 v[32:47], v[100:103], a[140:143], v[32:47]\n"
"s_mov_b32 s60, -1\n"
"s_mov_b32 s61, -1\n"
"s_cmp_eq_u32 s87, 0\n"
"s_cbranch_scc0 Lwayvord36_%=\n"
"s_cmp_lt_i32 s62, 0\n"
"s_cbranch_scc1 Lnoldvord36_%=\n"
"s_waitcnt vmcnt(9) lgkmcnt(0)\n"
"s_barrier\n"
"s_sub_u32 s67, 87552, 0\n"
"v_lshrrev_b32 v192, 2, v0\n"
"v_add_u32 v192, s67, v192\n"
"ds_read_b32 v193, v192\n"
"s_waitcnt lgkmcnt(0)\n"
"v_cmp_eq_u32_e64 s[80:81], s97, v193\n"
"s_nop 1\n"
"s_sub_u32 s68, s48, s62\n"
"s_min_u32 s68, s68, 2\n"
"s_mov_b32 s70, 0\n"
"Levs2vord36_%=:\n"
"s_cmp_ge_u32 s70, s68\n"
"s_cbranch_scc1 Lexs2vord36_%=\n"
"s_and_b32 s69, s80, 1\n"
"s_cmp_eq_u32 s69, 1\n"
"s_cbranch_scc0 Lexs2vord36_%=\n"
"s_lshr_b64 s[80:81], s[80:81], 1\n"
"s_add_u32 s70, s70, 1\n"
"s_branch Levs2vord36_%=\n"
"Lexs2vord36_%=:\n"
"s_add_u32 s58, s62, s70\n"
"s_cmp_lt_u32 s62, s58\n"
"s_cbranch_scc0 Lnoldvord36_%=\n"
"v_readlane_b32 s67, v6, s62\n"
"s_nop 0\n"
"s_mul_i32 s67, s67, s50\n"
"s_add_u32 s65, s67, s49\n"
"s_sub_u32 s64, 36864, s77\n"
"s_lshl_b32 s68, %0, 10\n"
"s_add_u32 s64, s64, s68\n"
"s_add_u32 m0, s64, 0\n"
"s_add_u32 s70, s65, 0\n"
"buffer_load_dwordx4 v2, s[36:39], s70 offen sc1 lds\n"
"s_add_u32 m0, s64, 4096\n"
"s_add_u32 s70, s65, 4096\n"
"buffer_load_dwordx4 v2, s[36:39], s70 offen sc1 lds\n"
"s_add_u32 m0, s64, 8192\n"
"s_add_u32 s70, s65, 8192\n"
"buffer_load_dwordx4 v2, s[36:39], s70 offen sc1 lds\n"
"s_add_u32 m0, s64, 12288\n"
"s_add_u32 s70, s65, 12288\n"
"buffer_load_dwordx4 v2, s[36:39], s70 offen sc1 lds\n"
"s_add_u32 m0, s64, 16384\n"
"s_add_u32 s70, s65, 16384\n"
"buffer_load_dwordx4 v2, s[36:39], s70 offen sc1 lds\n"
"s_add_u32 m0, s64, 20480\n"
"s_add_u32 s70, s65, 20480\n"
"buffer_load_dwordx4 v2, s[36:39], s70 offen sc1 lds\n"
"s_add_u32 m0, s64, 24576\n"
"s_add_u32 s70, s65, 24576\n"
"buffer_load_dwordx4 v2, s[36:39], s70 offen sc1 lds\n"
"s_add_u32 m0, s64, 28672\n"
"s_add_u32 s70, s65, 28672\n"
"buffer_load_dwordx4 v2, s[36:39], s70 offen sc1 lds\n"
"s_add_u32 m0, s64, 32768\n"
"s_add_u32 s70, s65, 32768\n"
"buffer_load_dwordx4 v2, s[36:39], s70 offen sc1 lds\n"
"Lwayvord36_%=:\n"
"s_waitcnt lgkmcnt(0)\n"
"s_nop 7\n"
"v_readlane_b32 s67, v6, s57\n"
"s_nop 0\n"
"s_mul_i32 s67, s67, s52\n"
"s_add_u32 s66, s67, s51\n"
"v_cvt_pk_bf16_f32 v160, v16, v17\n"
"v_cvt_pk_bf16_f32 v161, v18, v19\n"
"v_cvt_pk_bf16_f32 v164, v20, v21\n"
"v_cvt_pk_bf16_f32 v165, v22, v23\n"
"v_cvt_pk_bf16_f32 v162, v24, v25\n"
"v_cvt_pk_bf16_f32 v163, v26, v27\n"
"v_cvt_pk_bf16_f32 v166, v28, v29\n"
"v_cvt_pk_bf16_f32 v167, v30, v31\n"
"v_cvt_pk_bf16_f32 v168, v32, v33\n"
"v_cvt_pk_bf16_f32 v169, v34, v35\n"
"v_cvt_pk_bf16_f32 v172, v36, v37\n"
"v_cvt_pk_bf16_f32 v173, v38, v39\n"
"v_cvt_pk_bf16_f32 v170, v40, v41\n"
"v_cvt_pk_bf16_f32 v171, v42, v43\n"
"v_cvt_pk_bf16_f32 v174, v44, v45\n"
"v_cvt_pk_bf16_f32 v175, v46, v47\n"
"buffer_store_dwordx4 v[160:163], v11, s[40:43], s66 offen sc1\n"
"buffer_store_dwordx4 v[164:167], v12, s[40:43], s66 offen sc1\n"
"buffer_store_dwordx4 v[168:171], v13, s[40:43], s66 offen sc1\n"
"buffer_store_dwordx4 v[172:175], v14, s[40:43], s66 offen sc1\n"
"s_waitcnt vmcnt(4) lgkmcnt(0)\n"
"s_barrier\n"
"s_mov_b32 s61, s57\n"
"s_add_u32 s62, s57, 1\n"
"s_sub_u32 s77, 36864, s77\n"
"v_add_u32 v1, s77, v0\n"
"ds_read_b128 v[80:83], v1\n"
"ds_read_b128 v[84:87], v1 offset:1024\n"
"ds_read_b128 v[88:91], v1 offset:2048\n"
"ds_read_b128 v[92:95], v1 offset:3072\n"
"s_add_u32 s57, s57, 1\n"
"s_branch Ltopvord36_%=\n"
"Lnoldvord36_%=:\n"
"s_add_u32 s62, s57, 1\n"
"s_waitcnt lgkmcnt(0)\n"
"s_cmp_lt_u32 s62, s48\n"
"s_cbranch_scc0 Ln1vord36_%=\n"
"s_lshl_b32 s67, s62, 2\n"
"s_add_u32 s67, s67, 86528\n"
"v_add_u32 v192, s67, v8\n"
"ds_read_b32 v192, v192\n"
"s_waitcnt lgkmcnt(0)\n"
"v_add_u32 v192, v192, v9\n"
"s_mov_b32 m0, 87808\n"
"s_and_b32 exec_lo, s78, 3\n"
"s_mov_b32 exec_hi, 0\n"
"buffer_load_dword v192, s[44:47], 0 offen sc1 lds\n"
"s_mov_b64 exec, -1\n"
"s_waitcnt lgkmcnt(0)\n"
"Ln1vord36_%=:\n"
"s_nop 7\n"
"v_readlane_b32 s67, v6, s57\n"
"s_nop 0\n"
"s_mul_i32 s67, s67, s52\n"
"s_add_u32 s66, s67, s51\n"
"v_cvt_pk_bf16_f32 v160, v16, v17\n"
"v_cvt_pk_bf16_f32 v161, v18, v19\n"
"v_cvt_pk_bf16_f32 v164, v20, v21\n"
"v_cvt_pk_bf16_f32 v165, v22, v23\n"
"v_cvt_pk_bf16_f32 v162, v24, v25\n"
"v_cvt_pk_bf16_f32 v163, v26, v27\n"
"v_cvt_pk_bf16_f32 v166, v28, v29\n"
"v_cvt_pk_bf16_f32 v167, v30, v31\n"
"v_cvt_pk_bf16_f32 v168, v32, v33\n"
"v_cvt_pk_bf16_f32 v169, v34, v35\n"
"v_cvt_pk_bf16_f32 v172, v36, v37\n"
"v_cvt_pk_bf16_f32 v173, v38, v39\n"
"v_cvt_pk_bf16_f32 v170, v40, v41\n"
"v_cvt_pk_bf16_f32 v171, v42, v43\n"
"v_cvt_pk_bf16_f32 v174, v44, v45\n"
"v_cvt_pk_bf16_f32 v175, v46, v47\n"
"buffer_store_dwordx4 v[160:163], v11, s[40:43], s66 offen sc1\n"
"buffer_store_dwordx4 v[164:167], v12, s[40:43], s66 offen sc1\n"
"buffer_store_dwordx4 v[168:171], v13, s[40:43], s66 offen sc1\n"
"buffer_store_dwordx4 v[172:175], v14, s[40:43], s66 offen sc1\n"
"s_waitcnt vmcnt(4) lgkmcnt(0)\n"
"s_barrier\n"
"s_cmp_lt_u32 s62, s48\n"
"s_cbranch_scc0 Ln3vord36_%=\n"
"s_sub_u32 s67, 87808, 0\n"
"v_lshrrev_b32 v192, 2, v0\n"
"v_add_u32 v192, s67, v192\n"
"ds_read_b32 v193, v192\n"
"s_waitcnt lgkmcnt(0)\n"
"v_cmp_eq_u32_e64 s[80:81], s97, v193\n"
"s_nop 1\n"
"s_sub_u32 s68, s48, s62\n"
"s_min_u32 s68, s68, 2\n"
"s_mov_b32 s70, 0\n"
"Levnvord36_%=:\n"
"s_cmp_ge_u32 s70, s68\n"
"s_cbranch_scc1 Lexnvord36_%=\n"
"s_and_b32 s69, s80, 1\n"
"s_cmp_eq_u32 s69, 1\n"
"s_cbranch_scc0 Lexnvord36_%=\n"
"s_lshr_b64 s[80:81], s[80:81], 1\n"
"s_add_u32 s70, s70, 1\n"
"s_branch Levnvord36_%=\n"
"Lexnvord36_%=:\n"
"s_add_u32 s58, s62, s70\n"
"s_cmp_lt_u32 s62, s58\n"
"s_cbranch_scc0 Ln2vord36_%=\n"
"v_readlane_b32 s67, v6, s62\n"
"s_nop 0\n"
"s_mul_i32 s67, s67, s50\n"
"s_add_u32 s65, s67, s49\n"
"s_sub_u32 s64, 36864, s77\n"
"s_lshl_b32 s68, %0, 10\n"
"s_add_u32 s64, s64, s68\n"
"s_add_u32 m0, s64, 0\n"
"s_add_u32 s70, s65, 0\n"
"buffer_load_dwordx4 v2, s[36:39], s70 offen sc1 lds\n"
"s_add_u32 m0, s64, 4096\n"
"s_add_u32 s70, s65, 4096\n"
"buffer_load_dwordx4 v2, s[36:39], s70 offen sc1 lds\n"
"s_add_u32 m0, s64, 8192\n"
"s_add_u32 s70, s65, 8192\n"
"buffer_load_dwordx4 v2, s[36:39], s70 offen sc1 lds\n"
"s_add_u32 m0, s64, 12288\n"
"s_add_u32 s70, s65, 12288\n"
"buffer_load_dwordx4 v2, s[36:39], s70 offen sc1 lds\n"
"s_add_u32 m0, s64, 16384\n"
"s_add_u32 s70, s65, 16384\n"
"buffer_load_dwordx4 v2, s[36:39], s70 offen sc1 lds\n"
"s_add_u32 m0, s64, 20480\n"
"s_add_u32 s70, s65, 20480\n"
"buffer_load_dwordx4 v2, s[36:39], s70 offen sc1 lds\n"
"s_add_u32 m0, s64, 24576\n"
"s_add_u32 s70, s65, 24576\n"
"buffer_load_dwordx4 v2, s[36:39], s70 offen sc1 lds\n"
"s_add_u32 m0, s64, 28672\n"
"s_add_u32 s70, s65, 28672\n"
"buffer_load_dwordx4 v2, s[36:39], s70 offen sc1 lds\n"
"s_add_u32 m0, s64, 32768\n"
"s_add_u32 s70, s65, 32768\n"
"buffer_load_dwordx4 v2, s[36:39], s70 offen sc1 lds\n"
"s_waitcnt vmcnt(9) lgkmcnt(0)\n"
"s_barrier\n"
"s_cmp_lt_i32 s57, 0\n"
"s_cbranch_scc1 Lnsn1vord36_%=\n"
"v_readlane_b32 s67, v6, s57\n"
"s_nop 0\n"
"s_mul_i32 s67, s67, s54\n"
"s_add_u32 s67, s67, s53\n"
"buffer_store_dword v4, v3, s[44:47], s67 offen sc1\n"
"Lnsn1vord36_%=:\n"
"s_waitcnt vmcnt(1) lgkmcnt(0)\n"
"s_barrier\n"
"s_branch Ln4vord36_%=\n"
"Ln2vord36_%=:\n"
"s_lshl_b32 s67, s62, 2\n"
"s_add_u32 s67, s67, 86528\n"
"v_add_u32 v192, s67, v8\n"
"ds_read_b32 v192, v192\n"
"s_waitcnt lgkmcnt(0)\n"
"v_add_u32 v192, v192, v9\n"
"s_mov_b32 m0, 87296\n"
"s_and_b32 exec_lo, s78, 3\n"
"s_mov_b32 exec_hi, 0\n"
"buffer_load_dword v192, s[44:47], 0 offen sc1 lds\n"
"s_mov_b64 exec, -1\n"
"s_waitcnt vmcnt(0) lgkmcnt(0)\n"
"s_barrier\n"
"s_cmp_lt_i32 s57, 0\n"
"s_cbranch_scc1 Lnsn2vord36_%=\n"
"v_readlane_b32 s67, v6, s57\n"
"s_nop 0\n"
"s_mul_i32 s67, s67, s54\n"
"s_add_u32 s67, s67, s53\n"
"buffer_store_dword v4, v3, s[44:47], s67 offen sc1\n"
"Lnsn2vord36_%=:\n"
"s_sub_u32 s67, 87296, 0\n"
"v_lshrrev_b32 v192, 2, v0\n"
"v_add_u32 v192, s67, v192\n"
"ds_read_b32 v193, v192\n"
"s_waitcnt lgkmcnt(0)\n"
"v_cmp_eq_u32_e64 s[80:81], s97, v193\n"
"s_nop 1\n"
"s_sub_u32 s68, s48, s62\n"
"s_min_u32 s68, s68, 2\n"
"s_mov_b32 s70, 0\n"
"Levn2vord36_%=:\n"
"s_cmp_ge_u32 s70, s68\n"
"s_cbranch_scc1 Lexn2vord36_%=\n"
"s_and_b32 s69, s80, 1\n"
"s_cmp_eq_u32 s69, 1\n"
"s_cbranch_scc0 Lexn2vord36_%=\n"
"s_lshr_b64 s[80:81], s[80:81], 1\n"
"s_add_u32 s70, s70, 1\n"
"s_branch Levn2vord36_%=\n"
"Lexn2vord36_%=:\n"
"s_add_u32 s58, s62, s70\n"
"s_cmp_lt_u32 s62, s58\n"
"s_cbranch_scc0 Ln5vord36_%=\n"
"v_readlane_b32 s67, v6, s62\n"
"s_nop 0\n"
"s_mul_i32 s67, s67, s50\n"
"s_add_u32 s65, s67, s49\n"
"s_sub_u32 s64, 36864, s77\n"
"s_lshl_b32 s68, %0, 10\n"
"s_add_u32 s64, s64, s68\n"
"s_add_u32 m0, s64, 0\n"
"s_add_u32 s70, s65, 0\n"
"buffer_load_dwordx4 v2, s[36:39], s70 offen sc1 lds\n"
"s_add_u32 m0, s64, 4096\n"
"s_add_u32 s70, s65, 4096\n"
"buffer_load_dwordx4 v2, s[36:39], s70 offen sc1 lds\n"
"s_add_u32 m0, s64, 8192\n"
"s_add_u32 s70, s65, 8192\n"
"buffer_load_dwordx4 v2, s[36:39], s70 offen sc1 lds\n"
"s_add_u32 m0, s64, 12288\n"
"s_add_u32 s70, s65, 12288\n"
"buffer_load_dwordx4 v2, s[36:39], s70 offen sc1 lds\n"
"s_add_u32 m0, s64, 16384\n"
"s_add_u32 s70, s65, 16384\n"
"buffer_load_dwordx4 v2, s[36:39], s70 offen sc1 lds\n"
"s_add_u32 m0, s64, 20480\n"
"s_add_u32 s70, s65, 20480\n"
"buffer_load_dwordx4 v2, s[36:39], s70 offen sc1 lds\n"
"s_add_u32 m0, s64, 24576\n"
"s_add_u32 s70, s65, 24576\n"
"buffer_load_dwordx4 v2, s[36:39], s70 offen sc1 lds\n"
"s_add_u32 m0, s64, 28672\n"
"s_add_u32 s70, s65, 28672\n"
"buffer_load_dwordx4 v2, s[36:39], s70 offen sc1 lds\n"
"s_add_u32 m0, s64, 32768\n"
"s_add_u32 s70, s65, 32768\n"
"buffer_load_dwordx4 v2, s[36:39], s70 offen sc1 lds\n"
"s_waitcnt vmcnt(0) lgkmcnt(0)\n"
"s_barrier\n"
"s_branch Ln4vord36_%=\n"
"Ln5vord36_%=:\n"
"s_cmp_eq_u32 %0, 0\n"
"s_cbranch_scc0 Lbwnvord36_%=\n"
"s_cmp_lg_u32 s75, 0\n"
"s_cbranch_scc1 Lbwnvord36_%=\n"
"v_readlane_b32 s72, v7, s62\n"
"s_nop 3\n"
"s_mov_b32 s71, 0\n"
"s_cmp_lt_i32 s74, 0\n"
"s_cbranch_scc1 Lplnvord36_%=\n"
"s_sub_u32 s67, s62, s74\n"
"s_cmp_gt_u32 s67, 3\n"
"s_cbranch_scc1 Lplnvord36_%=\n"
"s_add_u32 s68, s62, 2\n"
"s_cmp_ge_u32 s68, s48\n"
"s_cbranch_scc1 Lplnvord36_%=\n"
"s_mul_i32 s67, s67, 240\n"
"s_add_u32 s67, s67, s73\n"
"s_sub_u32 s67, s67, 40\n"
"s_mov_b32 s76, 0\n"
"Lslnvord36_%=:\n"
"s_memrealtime s[84:85]\n"
"s_waitcnt lgkmcnt(0)\n"
"s_sub_u32 s68, s84, s67\n"
"s_cmp_ge_i32 s68, 0\n"
"s_cbranch_scc1 Lplnvord36_%=\n"
"s_sleep 2\n"
"s_mov_b32 s71, 1\n"
"s_add_u32 s76, s76, 1\n"
"s_cmp_lt_u32 s76, 32768\n"
"s_cbranch_scc1 Lslnvord36_%=\n"
"Lplnvord36_%=:\n"
"s_mov_b32 s76, 0\n"
"Lppnvord36_%=:\n"
"buffer_load_dword v192, v5, s[44:47], s72 offen sc1\n"
"s_waitcnt vmcnt(0)\n"
"v_cmp_ne_u32_e64 s[80:81], s97, v192\n"
"s_nop 1\n"
"s_and_b64 s[80:81], s[80:81], s[82:83]\n"
"s_cbranch_scc0 Lrdnvord36_%=\n"
"s_sleep 3\n"
"s_add_u32 s76, s76, 1\n"
"s_cmp_lt_u32 s76, 32768\n"
"s_cbranch_scc1 Lppnvord36_%=\n"
"s_mov_b32 s75, 1\n"
"v_mov_b32 v192, 16384\n"
"v_mov_b32 v193, 7000\n"
"buffer_store_dword v193, v192, s[44:47], 0 offen sc1\n"
"Lrdnvord36_%=:\n"
"s_mov_b32 s74, -1\n"
"s_cmp_eq_u32 s76, 0\n"
"s_cbranch_scc1 Lbwnvord36_%=\n"
"s_memrealtime s[84:85]\n"
"s_waitcnt lgkmcnt(0)\n"
"s_mov_b32 s73, s84\n"
"s_mov_b32 s74, s62\n"
"Lbwnvord36_%=:\n"
"s_waitcnt lgkmcnt(0)\n"
"s_barrier\n"
"v_readlane_b32 s67, v6, s62\n"
"s_nop 0\n"
"s_mul_i32 s67, s67, s50\n"
"s_add_u32 s65, s67, s49\n"
"s_sub_u32 s64, 36864, s77\n"
"s_lshl_b32 s68, %0, 10\n"
"s_add_u32 s64, s64, s68\n"
"s_add_u32 m0, s64, 0\n"
"s_add_u32 s70, s65, 0\n"
"buffer_load_dwordx4 v2, s[36:39], s70 offen sc1 lds\n"
"s_add_u32 m0, s64, 4096\n"
"s_add_u32 s70, s65, 4096\n"
"buffer_load_dwordx4 v2, s[36:39], s70 offen sc1 lds\n"
"s_add_u32 m0, s64, 8192\n"
"s_add_u32 s70, s65, 8192\n"
"buffer_load_dwordx4 v2, s[36:39], s70 offen sc1 lds\n"
"s_add_u32 m0, s64, 12288\n"
"s_add_u32 s70, s65, 12288\n"
"buffer_load_dwordx4 v2, s[36:39], s70 offen sc1 lds\n"
"s_add_u32 m0, s64, 16384\n"
"s_add_u32 s70, s65, 16384\n"
"buffer_load_dwordx4 v2, s[36:39], s70 offen sc1 lds\n"
"s_add_u32 m0, s64, 20480\n"
"s_add_u32 s70, s65, 20480\n"
"buffer_load_dwordx4 v2, s[36:39], s70 offen sc1 lds\n"
"s_add_u32 m0, s64, 24576\n"
"s_add_u32 s70, s65, 24576\n"
"buffer_load_dwordx4 v2, s[36:39], s70 offen sc1 lds\n"
"s_add_u32 m0, s64, 28672\n"
"s_add_u32 s70, s65, 28672\n"
"buffer_load_dwordx4 v2, s[36:39], s70 offen sc1 lds\n"
"s_add_u32 m0, s64, 32768\n"
"s_add_u32 s70, s65, 32768\n"
"buffer_load_dwordx4 v2, s[36:39], s70 offen sc1 lds\n"
"s_waitcnt vmcnt(0) lgkmcnt(0)\n"
"s_barrier\n"
"s_add_u32 s58, s62, 1\n"
"Ln4vord36_%=:\n"
"s_sub_u32 s77, 36864, s77\n"
"v_add_u32 v1, s77, v0\n"
"ds_read_b128 v[80:83], v1\n"
"ds_read_b128 v[84:87], v1 offset:1024\n"
"ds_read_b128 v[88:91], v1 offset:2048\n"
"ds_read_b128 v[92:95], v1 offset:3072\n"
"s_add_u32 s57, s57, 1\n"
"s_branch Ltopvord36_%=\n"
"Ln3vord36_%=:\n"
"s_waitcnt vmcnt(0) lgkmcnt(0)\n"
"s_barrier\n"
"s_cmp_lt_i32 s57, 0\n"
"s_cbranch_scc1 Lnsn3vord36_%=\n"
"v_readlane_b32 s67, v6, s57\n"
"s_nop 0\n"
"s_mul_i32 s67, s67, s54\n"
"s_add_u32 s67, s67, s53\n"
"buffer_store_dword v4, v3, s[44:47], s67 offen sc1\n"
"Lnsn3vord36_%=:\n"
"s_waitcnt vmcnt(0) lgkmcnt(0)\n"
"s_barrier\n"
    GEMM_ASM_END
"v0","v1","v2","v3","v4","v5","v6","v7","v8","v9","v10","v11","v12","v13","v14","v15","v16","v17","v18","v19","v20","v21","v22","v23","v24","v25","v26","v27","v28","v29","v30","v31","v32","v33","v34","v35","v36","v37","v38","v39","v40","v41","v42","v43","v44","v45","v46","v47","v48","v49","v50","v51","v52","v53","v54","v55","v56","v57","v58","v59","v60","v61","v62","v63","v64","v65","v66","v67","v68","v69","v70","v71","v72","v73","v74","v75","v76","v77","v78","v79","v80","v81","v82","v83","v84","v85","v86","v87","v88","v89","v90","v91","v92","v93","v94","v95","v96","v97","v98","v99","v100","v101","v102","v103","v104","v105","v106","v107","v108","v109","v110","v111","v112","v113","v114","v115","v116","v117","v118","v119","v120","v121","v122","v123","v124","v125","v126","v128","v129","v130","v131","v132","v133","v134","v135","v136","v137","v138","v139","v140","v141","v142","v143","v144","v145","v146","v147","v148","v149","v150","v151","v152","v153","v154","v155","v156","v157","v158","v159","v160","v161","v162","v163","v164","v165","v166","v167","v168","v169","v170","v171","v172","v173","v174","v175","v176","v177","v178","v179","v180","v181","v182","v183","v184","v185","v186","v187","v188","v189","v190","v191","v192","v193","v194","v195","v196","v197","v198","v199","v200","v201","v202","v203","v204","v205","v206","v207","v208","v209","v210","v211","v212","v213","v214","v215","v216","v217","v218","v219","v220","v221","v222","v223","v224","v225","v226","v227","v228","v229","v230","v231","v232","v233","v234","v235","v236","v237","v238","v239","v240","v241","v242","v243","v244","v245","v246","v247","v248","v249","v250","v251","v252","v253","v254","v255","a0","a1","a2","a3","a4","a5","a6","a7","a8","a9","a10","a11","a12","a13","a14","a15","a16","a17","a18","a19","a20","a21","a22","a23","a24","a25","a26","a27","a28","a29","a30","a31","a32","a33","a34","a35","a36","a37","a38","a39","a40","a41","a42","a43","a44","a45","a46","a47","a48","a49","a50","a51","a52","a53","a54","a55","a56","a57","a58","a59","a60","a61","a62","a63","a64","a65","a66","a67","a68","a69","a70","a71","a72","a73","a74","a75","a76","a77","a78","a79","a80","a81","a82","a83","a84","a85","a86","a87","a88","a89","a90","a91","a92","a93","a94","a95","a96","a97","a98","a99","a100","a101","a102","a103","a104","a105","a106","a107","a108","a109","a110","a111","a112","a113","a114","a115","a116","a117","a118","a119","a120","a121","a122","a123","a124","a125","a126","a127","a128","a129","a130","a131","a132","a133","a134","a135","a136","a137","a138","a139","a140","a141","a142","a143","a144","a145","a146","a147","a148","a149","a150","a151","a152","a153","a154","a155","a156","a157","a158","a159","a160","a161","a162","a163","a164","a165","a166","a167","a168","a169","a170","a171","a172","a173","a174","a175","a176","a177","a178","a179","a180","a181","a182","a183","a184","a185","a186","a187","a188","a189","a190","a191","a192","a193","a194","a195","a196","a197","a198","a199","a200","a201","a202","a203","a204","a205","a206","a207","a208","a209","a210","a211","a212","a213","a214","a215","a216","a217","a218","a219","a220","a221","a222","a223","a224","a225","a226","a227","a228","a229","a230","a231","a232","a233","a234","a235","a236","a237","a238","a239","a240","a241","a242","a243","a244","a245","a246","a247","a248","a249","a250","a251","a252","a253","a254","a255","s36","s37","s38","s39","s40","s41","s42","s43","s44","s45","s46","s47","s48","s49","s50","s51","s52","s53","s54","s55","s56","s57","s58","s59","s60","s61","s62","s63","s64","s65","s66","s67","s68","s69","s70","s71","s72","s73","s74","s75","s76","s77","s78","s79","s80","s81","s82","s83","s84","s85","s86","s87","s88","s89","s90","s91","s92","s93","s94","s95","s96","s97","s98","s99","vcc","scc","memory"
    ); }
}
DEVI int clampcol(int col){ return (col>=576)? col-128 : col; }
DEVI void part_params(GP& p,int lane,int wave,int kq,int ns){
  const int t=lane&31, hf=lane>>5;
  const unsigned vo=(t>>2)*36864+(t&3)*16+hf*64+kq*4608+(ns*24+wave*4)*64;
  p.sv[0]=vo; p.sv[1]=vo+128; p.sv[2]=(wave<2)? vo+16*64 : OOBO; p.sv[3]=(wave<2)? vo+16*64+128 : OOBO;
  p.outb=0; p.outs=294912; p.sgv=(wave==1 && lane==0)? 0u : OOBO; p.segv=lane; p.n=64; p.slot0=64; p.dv=lane*16+wave*1024;
}
DEVI void role_wo(Ctx& c,CArgs& a,int kq,int ns){
  c.lane=fresh_lane();
  const int lane=c.lane, wave=c.wave;
  GP p; p.ri=a.ws+O_O; p.ri_n=SZ_O; p.ro=a.ws+O_YP; p.ro_n=SZ_PART; part_params(p,lane,wave,kq,ns);
  p.inb=kq*32768; p.ins=262144; p.sigb=FL_Y+(kq*3+ns)*128; p.sigs=24*128; p.wstr=16*1152;
  const char* w=(const char*)a.Wo+(size_t)(kq*512+w_row(lane,wave))*1152; const int ch=w_ch(lane,wave);
  p.p0=w+(ns*192+ch*8)*2; p.p1=w+clampcol(ns*192+128+ch*8)*2;
  p.tabv=FL_O+(lane*32+kq*4)*128; p.nf=4; p.pd=8;
  gemm_params(c,a,p);
  GEMM_ASM_BEGIN
"v_mbcnt_lo_u32_b32 v10, -1, 0\n"
"v_mbcnt_hi_u32_b32 v10, -1, v10\n"
"v_lshlrev_b32 v0, 4, v10\n"
"v_lshlrev_b32 v192, 2, v10\n"
"v_add_u32 v192, 87040, v192\n"
"ds_read_b32 v193, v192\n"
"s_lshl_b32 s67, %0, 12\n"
"s_add_u32 s67, s67, 90112\n"
"v_lshlrev_b32 v194, 6, v10\n"
"v_add_u32 v194, s67, v194\n"
"ds_read_b128 v[160:163], v194\n"
"ds_read_b128 v[164:167], v194 offset:16\n"
"ds_read_b128 v[168:171], v194 offset:32\n"
"ds_read_b128 v[172:175], v194 offset:48\n"
"s_waitcnt lgkmcnt(0)\n"
"v_readlane_b32 s36, v193, 0\n"
"v_readlane_b32 s37, v193, 1\n"
"v_readlane_b32 s38, v193, 2\n"
"v_readlane_b32 s40, v193, 3\n"
"v_readlane_b32 s41, v193, 4\n"
"v_readlane_b32 s42, v193, 5\n"
"v_readlane_b32 s44, v193, 6\n"
"v_readlane_b32 s45, v193, 7\n"
"v_readlane_b32 s46, v193, 8\n"
"v_readlane_b32 s48, v193, 9\n"
"v_readlane_b32 s49, v193, 10\n"
"v_readlane_b32 s50, v193, 11\n"
"v_readlane_b32 s51, v193, 12\n"
"v_readlane_b32 s52, v193, 13\n"
"v_readlane_b32 s53, v193, 14\n"
"v_readlane_b32 s54, v193, 15\n"
"v_readlane_b32 s55, v193, 16\n"
"v_readlane_b32 s56, v193, 17\n"
"v_readlane_b32 s97, v193, 18\n"
"v_readlane_b32 s98, v193, 19\n"
"s_nop 1\n"
"s_and_b32 s37, s37, 0xffff\n"
"s_mov_b32 s39, 0x20000\n"
"s_and_b32 s41, s41, 0xffff\n"
"s_mov_b32 s43, 0x20000\n"
"s_and_b32 s45, s45, 0xffff\n"
"s_mov_b32 s47, 0x20000\n"
"v_mov_b32 v176, v160\n"
"v_mov_b32 v177, v161\n"
"v_mov_b32 v178, v162\n"
"v_mov_b32 v179, v163\n"
"v_mov_b32 v2, v164\n"
"v_mov_b32 v11, v165\n"
"v_mov_b32 v12, v166\n"
"v_mov_b32 v13, v167\n"
"v_mov_b32 v14, v168\n"
"v_mov_b32 v3, v169\n"
"v_mov_b32 v8, v170\n"
"v_mov_b32 v9, v171\n"
"v_mov_b32 v180, v172\n"
"v_mov_b32 v181, v173\n"
"v_mov_b32 v6, v174\n"
"v_mov_b32 v7, v175\n"
"v_mov_b32 v4, 1\n"
"v_mov_b32 v15, 0x7ffffff0\n"
"v_lshlrev_b32 v5, 7, v10\n"
"v_cmp_gt_u32_e32 vcc, 4, v10\n"
"s_nop 1\n"
"v_cndmask_b32_e32 v5, v15, v5, vcc\n"
"s_mov_b64 s[82:83], 15\n"
"s_mov_b64 s[78:79], 0\n"
"s_cmp_eq_u32 %0, 2\n"
"s_cbranch_scc0 Lnxpack32_%=\n"
"s_mov_b32 s78, 0xffffffff\n"
"s_mov_b32 s79, 0x0\n"
"Lnxpack32_%=:\n"
"s_mov_b32 s75, 0\n"
"s_mov_b32 s74, -1\n"
"s_mov_b32 s73, 0\n"
"s_barrier\n"
"v_mov_b32 v200, 20480\n"
"buffer_load_dword v200, v200, s[44:47], 0 offen sc1\n"
"v_add_u32 v182, 0x10000, v180\n"
"v_add_u32 v183, 0x10000, v181\n"
"v_add_u32 v184, 0x20000, v180\n"
"v_add_u32 v185, 0x20000, v181\n"
"s_lshl_b32 s69, %0, 10\n"
"s_mov_b32 s96, 0\n"
"s_add_u32 m0, s69, 0\n"
"s_nop 0\n"
"global_load_lds_dwordx4 v[176:177], off\n"
"v_add_co_u32_e32 v176, vcc, s55, v176\n"
"v_addc_co_u32_e32 v177, vcc, 0, v177, vcc\n"
"s_add_u32 m0, s69, 4096\n"
"s_nop 0\n"
"global_load_lds_dwordx4 v[176:177], off\n"
"v_add_co_u32_e32 v176, vcc, s55, v176\n"
"v_addc_co_u32_e32 v177, vcc, 0, v177, vcc\n"
"s_add_u32 m0, s69, 8192\n"
"s_nop 0\n"
"global_load_lds_dwordx4 v[176:177], off\n"
"v_add_co_u32_e32 v176, vcc, s55, v176\n"
"v_addc_co_u32_e32 v177, vcc, 0, v177, vcc\n"
"s_add_u32 m0, s69, 12288\n"
"s_nop 0\n"
"global_load_lds_dwordx4 v[176:177], off\n"
"v_add_co_u32_e32 v176, vcc, s55, v176\n"
"v_addc_co_u32_e32 v177, vcc, 0, v177, vcc\n"
"s_add_u32 m0, s69, 16384\n"
"s_nop 0\n"
"global_load_lds_dwordx4 v[176:177], off\n"
"v_add_co_u32_e32 v176, vcc, s55, v176\n"
"v_addc_co_u32_e32 v177, vcc, 0, v177, vcc\n"
"s_add_u32 m0, s69, 20480\n"
"s_nop 0\n"
"global_load_lds_dwordx4 v[176:177], off\n"
"v_add_co_u32_e32 v176, vcc, s55, v176\n"
"v_addc_co_u32_e32 v177, vcc, 0, v177, vcc\n"
"s_add_u32 m0, s69, 24576\n"
"s_nop 0\n"
"global_load_lds_dwordx4 v[176:177], off\n"
"v_add_co_u32_e32 v176, vcc, s55, v176\n"
"v_addc_co_u32_e32 v177, vcc, 0, v177, vcc\n"
"s_add_u32 m0, s69, 28672\n"
"s_nop 0\n"
"global_load_lds_dwordx4 v[176:177], off\n"
"v_add_co_u32_e32 v176, vcc, s55, v176\n"
"v_addc_co_u32_e32 v177, vcc, 0, v177, vcc\n"
"s_add_u32 m0, s69, 32768\n"
"s_nop 0\n"
"global_load_lds_dwordx4 v[176:177], off\n"
"v_add_co_u32_e32 v176, vcc, s55, v176\n"
"v_addc_co_u32_e32 v177, vcc, 0, v177, vcc\n"
"s_add_u32 m0, s69, 36864\n"
"s_nop 0\n"
"global_load_lds_dwordx4 v[176:177], off\n"
"v_add_co_u32_e32 v176, vcc, s55, v176\n"
"v_addc_co_u32_e32 v177, vcc, 0, v177, vcc\n"
"s_add_u32 m0, s69, 40960\n"
"s_nop 0\n"
"global_load_lds_dwordx4 v[176:177], off\n"
"v_add_co_u32_e32 v176, vcc, s55, v176\n"
"v_addc_co_u32_e32 v177, vcc, 0, v177, vcc\n"
"s_add_u32 m0, s69, 45056\n"
"s_nop 0\n"
"global_load_lds_dwordx4 v[176:177], off\n"
"v_add_co_u32_e32 v176, vcc, s55, v176\n"
"v_addc_co_u32_e32 v177, vcc, 0, v177, vcc\n"
"s_add_u32 m0, s69, 49152\n"
"s_nop 0\n"
"global_load_lds_dwordx4 v[176:177], off\n"
"v_add_co_u32_e32 v176, vcc, s55, v176\n"
"v_addc_co_u32_e32 v177, vcc, 0, v177, vcc\n"
"s_add_u32 m0, s69, 53248\n"
"s_nop 0\n"
"global_load_lds_dwordx4 v[176:177], off\n"
"v_add_co_u32_e32 v176, vcc, s55, v176\n"
"v_addc_co_u32_e32 v177, vcc, 0, v177, vcc\n"
"s_add_u32 m0, s69, 57344\n"
"s_nop 0\n"
"global_load_lds_dwordx4 v[176:177], off\n"
"v_add_co_u32_e32 v176, vcc, s55, v176\n"
"v_addc_co_u32_e32 v177, vcc, 0, v177, vcc\n"
"s_add_u32 m0, s69, 61440\n"
"s_nop 0\n"
"global_load_lds_dwordx4 v[176:177], off\n"
"v_add_co_u32_e32 v176, vcc, s55, v176\n"
"v_addc_co_u32_e32 v177, vcc, 0, v177, vcc\n"
"s_add_u32 m0, s69, 65536\n"
"s_nop 0\n"
"global_load_lds_dwordx4 v[176:177], off\n"
"v_add_co_u32_e32 v176, vcc, s55, v176\n"
"v_addc_co_u32_e32 v177, vcc, 0, v177, vcc\n"
"s_add_u32 m0, s69, 69632\n"
"s_nop 0\n"
"global_load_lds_dwordx4 v[176:177], off\n"
"v_add_co_u32_e32 v176, vcc, s55, v176\n"
"v_addc_co_u32_e32 v177, vcc, 0, v177, vcc\n"
"s_add_u32 m0, s69, 73728\n"
"s_nop 0\n"
"global_load_lds_dwordx4 v[176:177], off\n"
"v_add_co_u32_e32 v176, vcc, s55, v176\n"
"v_addc_co_u32_e32 v177, vcc, 0, v177, vcc\n"
"s_add_u32 m0, s69, 77824\n"
"s_nop 0\n"
"global_load_lds_dwordx4 v[176:177], off\n"
"v_add_co_u32_e32 v176, vcc, s55, v176\n"
"v_addc_co_u32_e32 v177, vcc, 0, v177, vcc\n"
"s_add_u32 m0, s69, 81920\n"
"s_nop 0\n"
"global_load_lds_dwordx4 v[176:177], off\n"
"v_add_co_u32_e32 v176, vcc, s55, v176\n"
"v_addc_co_u32_e32 v177, vcc, 0, v177, vcc\n"
"s_add_u32 m0, s69, 86016\n"
"s_nop 0\n"
"global_load_lds_dwordx4 v[176:177], off\n"
"v_add_co_u32_e32 v176, vcc, s55, v176\n"
"v_addc_co_u32_e32 v177, vcc, 0, v177, vcc\n"
"s_add_u32 m0, s69, 90112\n"
"s_nop 0\n"
"global_load_lds_dwordx4 v[176:177], off\n"
"v_add_co_u32_e32 v176, vcc, s55, v176\n"
"v_addc_co_u32_e32 v177, vcc, 0, v177, vcc\n"
"s_add_u32 m0, s69, 94208\n"
"s_nop 0\n"
"global_load_lds_dwordx4 v[176:177], off\n"
"v_add_co_u32_e32 v176, vcc, s55, v176\n"
"v_addc_co_u32_e32 v177, vcc, 0, v177, vcc\n"
"s_add_u32 m0, s69, 98304\n"
"s_nop 0\n"
"global_load_lds_dwordx4 v[176:177], off\n"
"v_add_co_u32_e32 v176, vcc, s55, v176\n"
"v_addc_co_u32_e32 v177, vcc, 0, v177, vcc\n"
"s_add_u32 m0, s69, 102400\n"
"s_nop 0\n"
"global_load_lds_dwordx4 v[176:177], off\n"
"v_add_co_u32_e32 v176, vcc, s55, v176\n"
"v_addc_co_u32_e32 v177, vcc, 0, v177, vcc\n"
"s_add_u32 m0, s69, 106496\n"
"s_nop 0\n"
"global_load_lds_dwordx4 v[176:177], off\n"
"v_add_co_u32_e32 v176, vcc, s55, v176\n"
"v_addc_co_u32_e32 v177, vcc, 0, v177, vcc\n"
"s_add_u32 m0, s69, 110592\n"
"s_nop 0\n"
"global_load_lds_dwordx4 v[176:177], off\n"
"v_add_co_u32_e32 v176, vcc, s55, v176\n"
"v_addc_co_u32_e32 v177, vcc, 0, v177, vcc\n"
"s_add_u32 m0, s69, 114688\n"
"s_nop 0\n"
"global_load_lds_dwordx4 v[176:177], off\n"
"v_add_co_u32_e32 v176, vcc, s55, v176\n"
"v_addc_co_u32_e32 v177, vcc, 0, v177, vcc\n"
"s_add_u32 m0, s69, 118784\n"
"s_nop 0\n"
"global_load_lds_dwordx4 v[176:177], off\n"
"v_add_co_u32_e32 v176, vcc, s55, v176\n"
"v_addc_co_u32_e32 v177, vcc, 0, v177, vcc\n"
"s_add_u32 m0, s69, 122880\n"
"s_nop 0\n"
"global_load_lds_dwordx4 v[176:177], off\n"
"v_add_co_u32_e32 v176, vcc, s55, v176\n"
"v_addc_co_u32_e32 v177, vcc, 0, v177, vcc\n"
"s_add_u32 m0, s69, 126976\n"
"s_nop 0\n"
"global_load_lds_dwordx4 v[176:177], off\n"
"s_waitcnt vmcnt(24)\n"
"s_barrier\n"
"v_readfirstlane_b32 s90, v200\n"
"s_nop 3\n"
"s_add_u32 s90, s90, 1\n"
"s_cmp_eq_u32 s98, 1\n"
"s_cselect_b32 s97, s90, s97\n"
"v_mov_b32 v4, s97\n"
"v_mov_b32 v201, 163836\n"
"ds_write_b32 v201, v4\n"
"ds_read_b64_tr_b16 a[128:129], v180\n"
"ds_read_b64_tr_b16 a[130:131], v181\n"
"ds_read_b64_tr_b16 a[132:133], v180 offset:4096\n"
"ds_read_b64_tr_b16 a[134:135], v181 offset:4096\n"
"ds_read_b64_tr_b16 a[136:137], v180 offset:8192\n"
"ds_read_b64_tr_b16 a[138:139], v181 offset:8192\n"
"ds_read_b64_tr_b16 a[140:141], v180 offset:12288\n"
"ds_read_b64_tr_b16 a[142:143], v181 offset:12288\n"
"ds_read_b64_tr_b16 a[144:145], v180 offset:16384\n"
"ds_read_b64_tr_b16 a[146:147], v181 offset:16384\n"
"ds_read_b64_tr_b16 a[148:149], v180 offset:20480\n"
"ds_read_b64_tr_b16 a[150:151], v181 offset:20480\n"
"ds_read_b64_tr_b16 a[152:153], v180 offset:24576\n"
"ds_read_b64_tr_b16 a[154:155], v181 offset:24576\n"
"s_waitcnt lgkmcnt(8)\n"
"ds_read_b64_tr_b16 a[156:157], v180 offset:28672\n"
"ds_read_b64_tr_b16 a[158:159], v181 offset:28672\n"
"s_waitcnt lgkmcnt(0)\n"
"s_barrier\n"
"s_add_u32 m0, s69, 0\n"
"s_nop 0\n"
"global_load_lds_dwordx4 v[178:179], off\n"
"v_add_co_u32_e32 v178, vcc, s55, v178\n"
"v_addc_co_u32_e32 v179, vcc, 0, v179, vcc\n"
"s_add_u32 m0, s69, 4096\n"
"s_nop 0\n"
"global_load_lds_dwordx4 v[178:179], off\n"
"v_add_co_u32_e32 v178, vcc, s55, v178\n"
"v_addc_co_u32_e32 v179, vcc, 0, v179, vcc\n"
"s_add_u32 m0, s69, 8192\n"
"s_nop 0\n"
"global_load_lds_dwordx4 v[178:179], off\n"
"v_add_co_u32_e32 v178, vcc, s55, v178\n"
"v_addc_co_u32_e32 v179, vcc, 0, v179, vcc\n"
"s_add_u32 m0, s69, 12288\n"
"s_nop 0\n"
"global_load_lds_dwordx4 v[178:179], off\n"
"v_add_co_u32_e32 v178, vcc, s55, v178\n"
"v_addc_co_u32_e32 v179, vcc, 0, v179, vcc\n"
"s_add_u32 m0, s69, 16384\n"
"s_nop 0\n"
"global_load_lds_dwordx4 v[178:179], off\n"
"v_add_co_u32_e32 v178, vcc, s55, v178\n"
"v_addc_co_u32_e32 v179, vcc, 0, v179, vcc\n"
"s_add_u32 m0, s69, 20480\n"
"s_nop 0\n"
"global_load_lds_dwordx4 v[178:179], off\n"
"v_add_co_u32_e32 v178, vcc, s55, v178\n"
"v_addc_co_u32_e32 v179, vcc, 0, v179, vcc\n"
"s_add_u32 m0, s69, 24576\n"
"s_nop 0\n"
"global_load_lds_dwordx4 v[178:179], off\n"
"v_add_co_u32_e32 v178, vcc, s55, v178\n"
"v_addc_co_u32_e32 v179, vcc, 0, v179, vcc\n"
"s_add_u32 m0, s69, 28672\n"
"s_nop 0\n"
"global_load_lds_dwordx4 v[178:179], off\n"
"v_add_co_u32_e32 v178, vcc, s55, v178\n"
"v_addc_co_u32_e32 v179, vcc, 0, v179, vcc\n"
"s_waitcnt vmcnt(24)\n"
"s_barrier\n"
"ds_read_b64_tr_b16 a[160:161], v180 offset:32768\n"
"ds_read_b64_tr_b16 a[162:163], v181 offset:32768\n"
"ds_read_b64_tr_b16 a[164:165], v180 offset:36864\n"
"ds_read_b64_tr_b16 a[166:167], v181 offset:36864\n"
"ds_read_b64_tr_b16 a[168:169], v180 offset:40960\n"
"ds_read_b64_tr_b16 a[170:171], v181 offset:40960\n"
"ds_read_b64_tr_b16 a[172:173], v180 offset:45056\n"
"ds_read_b64_tr_b16 a[174:175], v181 offset:45056\n"
"ds_read_b64_tr_b16 a[176:177], v180 offset:49152\n"
"ds_read_b64_tr_b16 a[178:179], v181 offset:49152\n"
"ds_read_b64_tr_b16 a[180:181], v180 offset:53248\n"
"ds_read_b64_tr_b16 a[182:183], v181 offset:53248\n"
"ds_read_b64_tr_b16 a[184:185], v180 offset:57344\n"
"ds_read_b64_tr_b16 a[186:187], v181 offset:57344\n"
"ds_read_b64_tr_b16 a[188:189], v180 offset:61440\n"
"s_waitcnt lgkmcnt(8)\n"
"ds_read_b64_tr_b16 a[190:191], v181 offset:61440\n"
"s_waitcnt lgkmcnt(0)\n"
"s_barrier\n"
"s_add_u32 m0, s69, 32768\n"
"s_nop 0\n"
"global_load_lds_dwordx4 v[178:179], off\n"
"v_add_co_u32_e32 v178, vcc, s55, v178\n"
"v_addc_co_u32_e32 v179, vcc, 0, v179, vcc\n"
"s_add_u32 m0, s69, 36864\n"
"s_nop 0\n"
"global_load_lds_dwordx4 v[178:179], off\n"
"v_add_co_u32_e32 v178, vcc, s55, v178\n"
"v_addc_co_u32_e32 v179, vcc, 0, v179, vcc\n"
"s_add_u32 m0, s69, 40960\n"
"s_nop 0\n"
"global_load_lds_dwordx4 v[178:179], off\n"
"v_add_co_u32_e32 v178, vcc, s55, v178\n"
"v_addc_co_u32_e32 v179, vcc, 0, v179, vcc\n"
"s_add_u32 m0, s69, 45056\n"
"s_nop 0\n"
"global_load_lds_dwordx4 v[178:179], off\n"
"v_add_co_u32_e32 v178, vcc, s55, v178\n"
"v_addc_co_u32_e32 v179, vcc, 0, v179, vcc\n"
"s_add_u32 m0, s69, 49152\n"
"s_nop 0\n"
"global_load_lds_dwordx4 v[178:179], off\n"
"v_add_co_u32_e32 v178, vcc, s55, v178\n"
"v_addc_co_u32_e32 v179, vcc, 0, v179, vcc\n"
"s_add_u32 m0, s69, 53248\n"
"s_nop 0\n"
"global_load_lds_dwordx4 v[178:179], off\n"
"v_add_co_u32_e32 v178, vcc, s55, v178\n"
"v_addc_co_u32_e32 v179, vcc, 0, v179, vcc\n"
"s_add_u32 m0, s69, 57344\n"
"s_nop 0\n"
"global_load_lds_dwordx4 v[178:179], off\n"
"v_add_co_u32_e32 v178, vcc, s55, v178\n"
"v_addc_co_u32_e32 v179, vcc, 0, v179, vcc\n"
"s_add_u32 m0, s69, 61440\n"
"s_nop 0\n"
"global_load_lds_dwordx4 v[178:179], off\n"
"v_add_co_u32_e32 v178, vcc, s55, v178\n"
"v_addc_co_u32_e32 v179, vcc, 0, v179, vcc\n"
"s_waitcnt vmcnt(24)\n"
"s_barrier\n"
"ds_read_b64_tr_b16 a[192:193], v182\n"
"ds_read_b64_tr_b16 a[194:195], v183\n"
"ds_read_b64_tr_b16 a[196:197], v182 offset:4096\n"
"ds_read_b64_tr_b16 a[198:199], v183 offset:4096\n"
"ds_read_b64_tr_b16 a[200:201], v182 offset:8192\n"
"ds_read_b64_tr_b16 a[202:203], v183 offset:8192\n"
"ds_read_b64_tr_b16 a[204:205], v182 offset:12288\n"
"ds_read_b64_tr_b16 a[206:207], v183 offset:12288\n"
"ds_read_b64_tr_b16 a[208:209], v182 offset:16384\n"
"ds_read_b64_tr_b16 a[210:211], v183 offset:16384\n"
"ds_read_b64_tr_b16 a[212:213], v182 offset:20480\n"
"ds_read_b64_tr_b16 a[214:215], v183 offset:20480\n"
"ds_read_b64_tr_b16 a[216:217], v182 offset:24576\n"
"ds_read_b64_tr_b16 a[218:219], v183 offset:24576\n"
"ds_read_b64_tr_b16 a[220:221], v182 offset:28672\n"
"s_waitcnt lgkmcnt(8)\n"
"ds_read_b64_tr_b16 a[222:223], v183 offset:28672\n"
"s_waitcnt lgkmcnt(0)\n"
"s_barrier\n"
"s_add_u32 m0, s69, 65536\n"
"s_nop 0\n"
"global_load_lds_dwordx4 v[178:179], off\n"
"v_add_co_u32_e32 v178, vcc, s55, v178\n"
"v_addc_co_u32_e32 v179, vcc, 0, v179, vcc\n"
"s_add_u32 m0, s69, 69632\n"
"s_nop 0\n"
"global_load_lds_dwordx4 v[178:179], off\n"
"v_add_co_u32_e32 v178, vcc, s55, v178\n"
"v_addc_co_u32_e32 v179, vcc, 0, v179, vcc\n"
"s_add_u32 m0, s69, 73728\n"
"s_nop 0\n"
"global_load_lds_dwordx4 v[178:179], off\n"
"v_add_co_u32_e32 v178, vcc, s55, v178\n"
"v_addc_co_u32_e32 v179, vcc, 0, v179, vcc\n"
"s_add_u32 m0, s69, 77824\n"
"s_nop 0\n"
"global_load_lds_dwordx4 v[178:179], off\n"
"v_add_co_u32_e32 v178, vcc, s55, v178\n"
"v_addc_co_u32_e32 v179, vcc, 0, v179, vcc\n"
"s_add_u32 m0, s69, 81920\n"
"s_nop 0\n"
"global_load_lds_dwordx4 v[178:179], off\n"
"v_add_co_u32_e32 v178, vcc, s55, v178\n"
"v_addc_co_u32_e32 v179, vcc, 0, v179, vcc\n"
"s_add_u32 m0, s69, 86016\n"
"s_nop 0\n"
"global_load_lds_dwordx4 v[178:179], off\n"
"v_add_co_u32_e32 v178, vcc, s55, v178\n"
"v_addc_co_u32_e32 v179, vcc, 0, v179, vcc\n"
"s_add_u32 m0, s69, 90112\n"
"s_nop 0\n"
"global_load_lds_dwordx4 v[178:179], off\n"
"v_add_co_u32_e32 v178, vcc, s55, v178\n"
"v_addc_co_u32_e32 v179, vcc, 0, v179, vcc\n"
"s_add_u32 m0, s69, 94208\n"
"s_nop 0\n"
"global_load_lds_dwordx4 v[178:179], off\n"
"v_add_co_u32_e32 v178, vcc, s55, v178\n"
"v_addc_co_u32_e32 v179, vcc, 0, v179, vcc\n"
"s_waitcnt vmcnt(24)\n"
"s_barrier\n"
"ds_read_b64_tr_b16 a[224:225], v182 offset:32768\n"
"ds_read_b64_tr_b16 a[226:227], v183 offset:32768\n"
"ds_read_b64_tr_b16 a[228:229], v182 offset:36864\n"
"ds_read_b64_tr_b16 a[230:231], v183 offset:36864\n"
"ds_read_b64_tr_b16 a[232:233], v182 offset:40960\n"
"ds_read_b64_tr_b16 a[234:235], v183 offset:40960\n"
"ds_read_b64_tr_b16 a[236:237], v182 offset:45056\n"
"ds_read_b64_tr_b16 a[238:239], v183 offset:45056\n"
"ds_read_b64_tr_b16 a[240:241], v182 offset:49152\n"
"ds_read_b64_tr_b16 a[242:243], v183 offset:49152\n"
"ds_read_b64_tr_b16 a[244:245], v182 offset:53248\n"
"ds_read_b64_tr_b16 a[246:247], v183 offset:53248\n"
"ds_read_b64_tr_b16 a[248:249], v182 offset:57344\n"
"ds_read_b64_tr_b16 a[250:251], v183 offset:57344\n"
"ds_read_b64_tr_b16 a[252:253], v182 offset:61440\n"
"s_waitcnt lgkmcnt(8)\n"
"ds_read_b64_tr_b16 a[254:255], v183 offset:61440\n"
"s_waitcnt lgkmcnt(0)\n"
"s_barrier\n"
"s_waitcnt vmcnt(16)\n"
"s_barrier\n"
"ds_read_b64_tr_b16 a[0:1], v180\n"
"ds_read_b64_tr_b16 a[2:3], v181\n"
"ds_read_b64_tr_b16 a[4:5], v180 offset:4096\n"
"ds_read_b64_tr_b16 a[6:7], v181 offset:4096\n"
"ds_read_b64_tr_b16 a[8:9], v180 offset:8192\n"
"ds_read_b64_tr_b16 a[10:11], v181 offset:8192\n"
"ds_read_b64_tr_b16 a[12:13], v180 offset:12288\n"
"ds_read_b64_tr_b16 a[14:15], v181 offset:12288\n"
"ds_read_b64_tr_b16 a[16:17], v180 offset:16384\n"
"ds_read_b64_tr_b16 a[18:19], v181 offset:16384\n"
"ds_read_b64_tr_b16 a[20:21], v180 offset:20480\n"
"ds_read_b64_tr_b16 a[22:23], v181 offset:20480\n"
"ds_read_b64_tr_b16 a[24:25], v180 offset:24576\n"
"ds_read_b64_tr_b16 a[26:27], v181 offset:24576\n"
"ds_read_b64_tr_b16 a[28:29], v180 offset:28672\n"
"s_waitcnt lgkmcnt(8)\n"
"ds_read_b64_tr_b16 a[30:31], v181 offset:28672\n"
"s_waitcnt lgkmcnt(0)\n"
"s_barrier\n"
"s_waitcnt vmcnt(8)\n"
"s_barrier\n"
"ds_read_b64_tr_b16 a[32:33], v180 offset:32768\n"
"ds_read_b64_tr_b16 a[34:35], v181 offset:32768\n"
"ds_read_b64_tr_b16 a[36:37], v180 offset:36864\n"
"ds_read_b64_tr_b16 a[38:39], v181 offset:36864\n"
"ds_read_b64_tr_b16 a[40:41], v180 offset:40960\n"
"ds_read_b64_tr_b16 a[42:43], v181 offset:40960\n"
"ds_read_b64_tr_b16 a[44:45], v180 offset:45056\n"
"ds_read_b64_tr_b16 a[46:47], v181 offset:45056\n"
"ds_read_b64_tr_b16 a[48:49], v180 offset:49152\n"
"ds_read_b64_tr_b16 a[50:51], v181 offset:49152\n"
"ds_read_b64_tr_b16 a[52:53], v180 offset:53248\n"
"ds_read_b64_tr_b16 a[54:55], v181 offset:53248\n"
"ds_read_b64_tr_b16 a[56:57], v180 offset:57344\n"
"ds_read_b64_tr_b16 a[58:59], v181 offset:57344\n"
"ds_read_b64_tr_b16 a[60:61], v180 offset:61440\n"
"s_waitcnt lgkmcnt(8)\n"
"ds_read_b64_tr_b16 a[62:63], v181 offset:61440\n"
"s_waitcnt lgkmcnt(0)\n"
"s_barrier\n"
"v_readlane_b32 s72, v7, 0\n"
"s_nop 4\n"
"buffer_load_dword v198, v5, s[44:47], s72 offen sc1\n"
"s_add_u32 m0, s69, 98304\n"
"s_nop 0\n"
"global_load_lds_dwordx4 v[178:179], off\n"
"v_add_co_u32_e32 v178, vcc, s55, v178\n"
"v_addc_co_u32_e32 v179, vcc, 0, v179, vcc\n"
"s_add_u32 m0, s69, 102400\n"
"s_nop 0\n"
"global_load_lds_dwordx4 v[178:179], off\n"
"v_add_co_u32_e32 v178, vcc, s55, v178\n"
"v_addc_co_u32_e32 v179, vcc, 0, v179, vcc\n"
"s_add_u32 m0, s69, 106496\n"
"s_nop 0\n"
"global_load_lds_dwordx4 v[178:179], off\n"
"v_add_co_u32_e32 v178, vcc, s55, v178\n"
"v_addc_co_u32_e32 v179, vcc, 0, v179, vcc\n"
"s_add_u32 m0, s69, 110592\n"
"s_nop 0\n"
"global_load_lds_dwordx4 v[178:179], off\n"
"v_add_co_u32_e32 v178, vcc, s55, v178\n"
"v_addc_co_u32_e32 v179, vcc, 0, v179, vcc\n"
"s_add_u32 m0, s69, 114688\n"
"s_nop 0\n"
"global_load_lds_dwordx4 v[178:179], off\n"
"v_add_co_u32_e32 v178, vcc, s55, v178\n"
"v_addc_co_u32_e32 v179, vcc, 0, v179, vcc\n"
"s_add_u32 m0, s69, 118784\n"
"s_nop 0\n"
"global_load_lds_dwordx4 v[178:179], off\n"
"v_add_co_u32_e32 v178, vcc, s55, v178\n"
"v_addc_co_u32_e32 v179, vcc, 0, v179, vcc\n"
"s_add_u32 m0, s69, 122880\n"
"s_nop 0\n"
"global_load_lds_dwordx4 v[178:179], off\n"
"v_add_co_u32_e32 v178, vcc, s55, v178\n"
"v_addc_co_u32_e32 v179, vcc, 0, v179, vcc\n"
"s_add_u32 m0, s69, 126976\n"
"s_nop 0\n"
"global_load_lds_dwordx4 v[178:179], off\n"
"s_waitcnt vmcnt(8)\n"
"s_barrier\n"
"ds_read_b64_tr_b16 a[64:65], v182\n"
"ds_read_b64_tr_b16 a[66:67], v183\n"
"ds_read_b64_tr_b16 a[68:69], v182 offset:4096\n"
"ds_read_b64_tr_b16 a[70:71], v183 offset:4096\n"
"ds_read_b64_tr_b16 a[72:73], v182 offset:8192\n"
"ds_read_b64_tr_b16 a[74:75], v183 offset:8192\n"
"ds_read_b64_tr_b16 a[76:77], v182 offset:12288\n"
"ds_read_b64_tr_b16 a[78:79], v183 offset:12288\n"
"ds_read_b64_tr_b16 a[80:81], v182 offset:16384\n"
"ds_read_b64_tr_b16 a[82:83], v183 offset:16384\n"
"ds_read_b64_tr_b16 a[84:85], v182 offset:20480\n"
"ds_read_b64_tr_b16 a[86:87], v183 offset:20480\n"
"ds_read_b64_tr_b16 a[88:89], v182 offset:24576\n"
"ds_read_b64_tr_b16 a[90:91], v183 offset:24576\n"
"ds_read_b64_tr_b16 a[92:93], v182 offset:28672\n"
"s_waitcnt lgkmcnt(8)\n"
"ds_read_b64_tr_b16 a[94:95], v183 offset:28672\n"
"s_waitcnt lgkmcnt(0)\n"
"s_barrier\n"
"v_cmp_ne_u32_e64 s[80:81], s97, v198\n"
"s_nop 1\n"
"s_and_b64 s[80:81], s[80:81], s[82:83]\n"
"s_cbranch_scc1 Lx0npack32_%=\n"
"s_mov_b32 s96, 1\n"
"s_mov_b32 s57, 0\n"
"s_mov_b32 s77, 32768\n"
"v_readlane_b32 s67, v6, s57\n"
"s_nop 0\n"
"s_mul_i32 s67, s67, s50\n"
"s_add_u32 s65, s67, s49\n"
"s_sub_u32 s64, 32768, s77\n"
"s_lshl_b32 s68, %0, 10\n"
"s_add_u32 s64, s64, s68\n"
"s_add_u32 m0, s64, 0\n"
"s_add_u32 s70, s65, 0\n"
"buffer_load_dwordx4 v2, s[36:39], s70 offen sc1 lds\n"
"s_add_u32 m0, s64, 4096\n"
"s_add_u32 s70, s65, 4096\n"
"buffer_load_dwordx4 v2, s[36:39], s70 offen sc1 lds\n"
"s_add_u32 m0, s64, 8192\n"
"s_add_u32 s70, s65, 8192\n"
"buffer_load_dwordx4 v2, s[36:39], s70 offen sc1 lds\n"
"s_add_u32 m0, s64, 12288\n"
"s_add_u32 s70, s65, 12288\n"
"buffer_load_dwordx4 v2, s[36:39], s70 offen sc1 lds\n"
"s_add_u32 m0, s64, 16384\n"
"s_add_u32 s70, s65, 16384\n"
"buffer_load_dwordx4 v2, s[36:39], s70 offen sc1 lds\n"
"s_add_u32 m0, s64, 20480\n"
"s_add_u32 s70, s65, 20480\n"
"buffer_load_dwordx4 v2, s[36:39], s70 offen sc1 lds\n"
"s_add_u32 m0, s64, 24576\n"
"s_add_u32 s70, s65, 24576\n"
"buffer_load_dwordx4 v2, s[36:39], s70 offen sc1 lds\n"
"s_add_u32 m0, s64, 28672\n"
"s_add_u32 s70, s65, 28672\n"
"buffer_load_dwordx4 v2, s[36:39], s70 offen sc1 lds\n"
"Lx0npack32_%=:\n"
"s_cmp_eq_u32 s96, 0\n"
"s_cbranch_scc1 Lw7apack32_%=\n"
"s_waitcnt vmcnt(8)\n"
"s_branch Lw7bpack32_%=\n"
"Lw7apack32_%=:\n"
"s_waitcnt vmcnt(0)\n"
"Lw7bpack32_%=:\n"
"s_barrier\n"
"ds_read_b64_tr_b16 a[96:97], v182 offset:32768\n"
"ds_read_b64_tr_b16 a[98:99], v183 offset:32768\n"
"ds_read_b64_tr_b16 a[100:101], v182 offset:36864\n"
"ds_read_b64_tr_b16 a[102:103], v183 offset:36864\n"
"ds_read_b64_tr_b16 a[104:105], v182 offset:40960\n"
"ds_read_b64_tr_b16 a[106:107], v183 offset:40960\n"
"ds_read_b64_tr_b16 a[108:109], v182 offset:45056\n"
"ds_read_b64_tr_b16 a[110:111], v183 offset:45056\n"
"ds_read_b64_tr_b16 a[112:113], v182 offset:49152\n"
"ds_read_b64_tr_b16 a[114:115], v183 offset:49152\n"
"ds_read_b64_tr_b16 a[116:117], v182 offset:53248\n"
"ds_read_b64_tr_b16 a[118:119], v183 offset:53248\n"
"ds_read_b64_tr_b16 a[120:121], v182 offset:57344\n"
"ds_read_b64_tr_b16 a[122:123], v183 offset:57344\n"
"ds_read_b64_tr_b16 a[124:125], v182 offset:61440\n"
"s_waitcnt lgkmcnt(8)\n"
"ds_read_b64_tr_b16 a[126:127], v183 offset:61440\n"
"s_waitcnt lgkmcnt(0)\n"
"s_barrier\n"
"s_lshl_b32 s55, %0, 10\n"
"s_mov_b32 s88, 0xffffffff\n"
"s_mov_b32 s89, 0x0\n"
"v_lshrrev_b32 v212, 2, v0\n"
"v_add_u32 v212, 86016, v212\n"
"v_lshlrev_b32 v192, 2, v10\n"
"v_add_u32 v192, 86528, v192\n"
"ds_write_b32 v192, v7\n"
"ds_write_b32 v192, v15 offset:256\n"
"s_mov_b32 s57, 0\n"
"s_mov_b32 s59, 0\n"
"s_mov_b32 s60, -1\n"
"s_mov_b32 s61, -1\n"
"s_mov_b32 s77, 32768\n"
"s_waitcnt lgkmcnt(0)\n"
"s_cmp_lg_u32 s96, 0\n"
"s_cbranch_scc1 Lbwipack32_%=\n"
"s_cmp_eq_u32 %0, 0\n"
"s_cbranch_scc0 Lbwipack32_%=\n"
"s_cmp_lg_u32 s75, 0\n"
"s_cbranch_scc1 Lbwipack32_%=\n"
"v_readlane_b32 s72, v7, s57\n"
"s_nop 3\n"
"s_mov_b32 s71, 0\n"
"s_cmp_lt_i32 s74, 0\n"
"s_cbranch_scc1 Lplipack32_%=\n"
"s_sub_u32 s67, s57, s74\n"
"s_cmp_gt_u32 s67, 3\n"
"s_cbranch_scc1 Lplipack32_%=\n"
"s_add_u32 s68, s57, 2\n"
"s_cmp_ge_u32 s68, s48\n"
"s_cbranch_scc1 Lplipack32_%=\n"
"s_mul_i32 s67, s67, 240\n"
"s_add_u32 s67, s67, s73\n"
"s_sub_u32 s67, s67, 40\n"
"s_mov_b32 s76, 0\n"
"Lslipack32_%=:\n"
"s_memrealtime s[84:85]\n"
"s_waitcnt lgkmcnt(0)\n"
"s_sub_u32 s68, s84, s67\n"
"s_cmp_ge_i32 s68, 0\n"
"s_cbranch_scc1 Lplipack32_%=\n"
"s_sleep 2\n"
"s_mov_b32 s71, 1\n"
"s_add_u32 s76, s76, 1\n"
"s_cmp_lt_u32 s76, 32768\n"
"s_cbranch_scc1 Lslipack32_%=\n"
"Lplipack32_%=:\n"
"s_mov_b32 s76, 0\n"
"Lppipack32_%=:\n"
"buffer_load_dword v192, v5, s[44:47], s72 offen sc1\n"
"s_waitcnt vmcnt(0)\n"
"v_cmp_ne_u32_e64 s[80:81], s97, v192\n"
"s_nop 1\n"
"s_and_b64 s[80:81], s[80:81], s[82:83]\n"
"s_cbranch_scc0 Lrdipack32_%=\n"
"s_sleep 12\n"
"s_add_u32 s76, s76, 1\n"
"s_cmp_lt_u32 s76, 32768\n"
"s_cbranch_scc1 Lppipack32_%=\n"
"s_mov_b32 s75, 1\n"
"v_mov_b32 v192, 16384\n"
"v_mov_b32 v193, 7000\n"
"buffer_store_dword v193, v192, s[44:47], 0 offen sc1\n"
"Lrdipack32_%=:\n"
"s_mov_b32 s74, -1\n"
"s_cmp_eq_u32 s76, 0\n"
"s_cbranch_scc1 Lbwipack32_%=\n"
"s_memrealtime s[84:85]\n"
"s_waitcnt lgkmcnt(0)\n"
"s_mov_b32 s73, s84\n"
"s_mov_b32 s74, s57\n"
"Lbwipack32_%=:\n"
"s_barrier\n"
"s_cmp_lg_u32 s96, 0\n"
"s_cbranch_scc1 Lx0spack32_%=\n"
"v_readlane_b32 s67, v6, s57\n"
"s_nop 0\n"
"s_mul_i32 s67, s67, s50\n"
"s_add_u32 s65, s67, s49\n"
"s_sub_u32 s64, 32768, s77\n"
"s_lshl_b32 s68, %0, 10\n"
"s_add_u32 s64, s64, s68\n"
"s_add_u32 m0, s64, 0\n"
"s_add_u32 s70, s65, 0\n"
"buffer_load_dwordx4 v2, s[36:39], s70 offen sc1 lds\n"
"s_add_u32 m0, s64, 4096\n"
"s_add_u32 s70, s65, 4096\n"
"buffer_load_dwordx4 v2, s[36:39], s70 offen sc1 lds\n"
"s_add_u32 m0, s64, 8192\n"
"s_add_u32 s70, s65, 8192\n"
"buffer_load_dwordx4 v2, s[36:39], s70 offen sc1 lds\n"
"s_add_u32 m0, s64, 12288\n"
"s_add_u32 s70, s65, 12288\n"
"buffer_load_dwordx4 v2, s[36:39], s70 offen sc1 lds\n"
"s_add_u32 m0, s64, 16384\n"
"s_add_u32 s70, s65, 16384\n"
"buffer_load_dwordx4 v2, s[36:39], s70 offen sc1 lds\n"
"s_add_u32 m0, s64, 20480\n"
"s_add_u32 s70, s65, 20480\n"
"buffer_load_dwordx4 v2, s[36:39], s70 offen sc1 lds\n"
"s_add_u32 m0, s64, 24576\n"
"s_add_u32 s70, s65, 24576\n"
"buffer_load_dwordx4 v2, s[36:39], s70 offen sc1 lds\n"
"s_add_u32 m0, s64, 28672\n"
"s_add_u32 s70, s65, 28672\n"
"buffer_load_dwordx4 v2, s[36:39], s70 offen sc1 lds\n"
"Lx0spack32_%=:\n"
"s_waitcnt vmcnt(0) lgkmcnt(0)\n"
"s_barrier\n"
"s_sub_u32 s77, 32768, s77\n"
"v_add_u32 v1, s77, v0\n"
"ds_read_b128 v[80:83], v1\n"
"ds_read_b128 v[84:87], v1 offset:1024\n"
"ds_read_b128 v[88:91], v1 offset:2048\n"
"ds_read_b128 v[92:95], v1 offset:3072\n"
"s_mov_b32 s58, 1\n"
"Ltoppack32_%=:\n"
"s_cmp_eq_u32 s59, 1\n"
"s_cbranch_scc1 LPApack32_%=\n"
"s_cmp_eq_u32 s59, 2\n"
"s_cbranch_scc1 LPBpack32_%=\n"
"s_add_u32 s67, s57, 1\n"
"s_cmp_lt_u32 s67, s58\n"
"s_cbranch_scc0 Lnonpack32_%=\n"
"v_readlane_b32 s67, v6, s67\n"
"s_nop 0\n"
"s_mul_i32 s67, s67, s50\n"
"s_add_u32 s65, s67, s49\n"
"s_sub_u32 s64, 32768, s77\n"
"s_lshl_b32 s68, %0, 10\n"
"s_add_u32 s64, s64, s68\n"
"s_mov_b32 s62, -1\n"
"s_cmp_ge_u32 s58, s48\n"
"s_cbranch_scc1 Lq1pack32_%=\n"
"s_sub_u32 s68, s58, s57\n"
"s_cmp_le_u32 s68, 4\n"
"s_cselect_b32 s62, s58, -1\n"
"Lq1pack32_%=:\n"
"s_max_i32 s68, s60, s61\n"
"s_mov_b32 s63, 0\n"
"s_cmp_lt_i32 s68, 0\n"
"s_cbranch_scc1 Lq2pack32_%=\n"
"s_mov_b32 s63, 1\n"
"Lq2pack32_%=:\n"
"s_waitcnt lgkmcnt(3)\n"
"v_mfma_f32_32x32x16_bf16 v[16:31], a[128:131], v[80:83], 0\n"
"ds_read_b128 v[96:99], v1 offset:4096\n"
"v_mfma_f32_32x32x16_bf16 v[32:47], a[0:3], v[80:83], 0\n"
"s_waitcnt lgkmcnt(3)\n"
"v_mfma_f32_32x32x16_bf16 v[16:31], a[132:135], v[84:87], v[16:31]\n"
"ds_read_b128 v[100:103], v1 offset:5120\n"
"v_mfma_f32_32x32x16_bf16 v[32:47], a[4:7], v[84:87], v[32:47]\n"
"s_add_u32 m0, s64, 0\n"
"s_add_u32 s70, s65, 0\n"
"buffer_load_dwordx4 v2, s[36:39], s70 offen sc1 lds\n"
"s_waitcnt lgkmcnt(3)\n"
"v_mfma_f32_32x32x16_bf16 v[16:31], a[136:139], v[88:91], v[16:31]\n"
"ds_read_b128 v[104:107], v1 offset:6144\n"
"v_mfma_f32_32x32x16_bf16 v[32:47], a[8:11], v[88:91], v[32:47]\n"
"s_waitcnt lgkmcnt(3)\n"
"v_mfma_f32_32x32x16_bf16 v[16:31], a[140:143], v[92:95], v[16:31]\n"
"ds_read_b128 v[108:111], v1 offset:7168\n"
"v_mfma_f32_32x32x16_bf16 v[32:47], a[12:15], v[92:95], v[32:47]\n"
"s_add_u32 m0, s64, 4096\n"
"s_add_u32 s70, s65, 4096\n"
"buffer_load_dwordx4 v2, s[36:39], s70 offen sc1 lds\n"
"s_waitcnt lgkmcnt(3)\n"
"v_mfma_f32_32x32x16_bf16 v[16:31], a[144:147], v[96:99], v[16:31]\n"
"ds_read_b128 v[80:83], v1 offset:8192\n"
"v_mfma_f32_32x32x16_bf16 v[32:47], a[16:19], v[96:99], v[32:47]\n"
"s_waitcnt lgkmcnt(3)\n"
"v_mfma_f32_32x32x16_bf16 v[16:31], a[148:151], v[100:103], v[16:31]\n"
"ds_read_b128 v[84:87], v1 offset:9216\n"
"v_mfma_f32_32x32x16_bf16 v[32:47], a[20:23], v[100:103], v[32:47]\n"
"s_add_u32 m0, s64, 8192\n"
"s_add_u32 s70, s65, 8192\n"
"buffer_load_dwordx4 v2, s[36:39], s70 offen sc1 lds\n"
"s_waitcnt lgkmcnt(3)\n"
"v_mfma_f32_32x32x16_bf16 v[16:31], a[152:155], v[104:107], v[16:31]\n"
"ds_read_b128 v[88:91], v1 offset:10240\n"
"v_mfma_f32_32x32x16_bf16 v[32:47], a[24:27], v[104:107], v[32:47]\n"
"s_waitcnt lgkmcnt(3)\n"
"v_mfma_f32_32x32x16_bf16 v[16:31], a[156:159], v[108:111], v[16:31]\n"
"ds_read_b128 v[92:95], v1 offset:11264\n"
"v_mfma_f32_32x32x16_bf16 v[32:47], a[28:31], v[108:111], v[32:47]\n"
"s_add_u32 m0, s64, 12288\n"
"s_add_u32 s70, s65, 12288\n"
"buffer_load_dwordx4 v2, s[36:39], s70 offen sc1 lds\n"
"s_waitcnt lgkmcnt(3)\n"
"v_mfma_f32_32x32x16_bf16 v[16:31], a[160:163], v[80:83], v[16:31]\n"
"ds_read_b128 v[96:99], v1 offset:12288\n"
"v_mfma_f32_32x32x16_bf16 v[32:47], a[32:35], v[80:83], v[32:47]\n"
"s_waitcnt lgkmcnt(3)\n"
"v_mfma_f32_32x32x16_bf16 v[16:31], a[164:167], v[84:87], v[16:31]\n"
"ds_read_b128 v[100:103], v1 offset:13312\n"
"v_mfma_f32_32x32x16_bf16 v[32:47], a[36:39], v[84:87], v[32:47]\n"
"s_add_u32 m0, s64, 16384\n"
"s_add_u32 s70, s65, 16384\n"
"buffer_load_dwordx4 v2, s[36:39], s70 offen sc1 lds\n"
"s_waitcnt lgkmcnt(3)\n"
"v_mfma_f32_32x32x16_bf16 v[16:31], a[168:171], v[88:91], v[16:31]\n"
"ds_read_b128 v[104:107], v1 offset:14336\n"
"v_mfma_f32_32x32x16_bf16 v[32:47], a[40:43], v[88:91], v[32:47]\n"
"s_waitcnt lgkmcnt(3)\n"
"v_mfma_f32_32x32x16_bf16 v[16:31], a[172:175], v[92:95], v[16:31]\n"
"ds_read_b128 v[108:111], v1 offset:15360\n"
"v_mfma_f32_32x32x16_bf16 v[32:47], a[44:47], v[92:95], v[32:47]\n"
"s_add_u32 m0, s64, 20480\n"
"s_add_u32 s70, s65, 20480\n"
"buffer_load_dwordx4 v2, s[36:39], s70 offen sc1 lds\n"
"s_waitcnt lgkmcnt(3)\n"
"v_mfma_f32_32x32x16_bf16 v[16:31], a[176:179], v[96:99], v[16:31]\n"
"ds_read_b128 v[80:83], v1 offset:16384\n"
"v_mfma_f32_32x32x16_bf16 v[32:47], a[48:51], v[96:99], v[32:47]\n"
"s_waitcnt lgkmcnt(3)\n"
"v_mfma_f32_32x32x16_bf16 v[16:31], a[180:183], v[100:103], v[16:31]\n"
"ds_read_b128 v[84:87], v1 offset:17408\n"
"v_mfma_f32_32x32x16_bf16 v[32:47], a[52:55], v[100:103], v[32:47]\n"
"s_add_u32 m0, s64, 24576\n"
"s_add_u32 s70, s65, 24576\n"
"buffer_load_dwordx4 v2, s[36:39], s70 offen sc1 lds\n"
"s_waitcnt lgkmcnt(3)\n"
"v_mfma_f32_32x32x16_bf16 v[16:31], a[184:187], v[104:107], v[16:31]\n"
"ds_read_b128 v[88:91], v1 offset:18432\n"
"v_mfma_f32_32x32x16_bf16 v[32:47], a[56:59], v[104:107], v[32:47]\n"
"s_cmp_eq_u32 s63, 0\n"
"s_cbranch_scc1 LnmDpack32_%=\n"
"s_waitcnt vmcnt(7)\n"
"s_barrier\n"
"s_cmp_lt_i32 s60, 0\n"
"s_cbranch_scc1 LnsmaDpack32_%=\n"
"v_readlane_b32 s67, v6, s60\n"
"s_nop 0\n"
"s_mul_i32 s67, s67, s54\n"
"s_add_u32 s67, s67, s53\n"
"buffer_store_dword v4, v3, s[44:47], s67 offen sc1\n"
"LnsmaDpack32_%=:\n"
"s_cmp_lt_i32 s61, 0\n"
"s_cbranch_scc1 LnsmbDpack32_%=\n"
"v_readlane_b32 s67, v6, s61\n"
"s_nop 0\n"
"s_mul_i32 s67, s67, s54\n"
"s_add_u32 s67, s67, s53\n"
"buffer_store_dword v4, v3, s[44:47], s67 offen sc1\n"
"LnsmbDpack32_%=:\n"
"LnmDpack32_%=:\n"
"s_waitcnt lgkmcnt(3)\n"
"v_mfma_f32_32x32x16_bf16 v[16:31], a[188:191], v[108:111], v[16:31]\n"
"ds_read_b128 v[92:95], v1 offset:19456\n"
"v_mfma_f32_32x32x16_bf16 v[32:47], a[60:63], v[108:111], v[32:47]\n"
"s_add_u32 m0, s64, 28672\n"
"s_add_u32 s70, s65, 28672\n"
"buffer_load_dwordx4 v2, s[36:39], s70 offen sc1 lds\n"
"s_waitcnt lgkmcnt(3)\n"
"v_mfma_f32_32x32x16_bf16 v[16:31], a[192:195], v[80:83], v[16:31]\n"
"ds_read_b128 v[96:99], v1 offset:20480\n"
"v_mfma_f32_32x32x16_bf16 v[32:47], a[64:67], v[80:83], v[32:47]\n"
"s_cmp_lt_i32 s62, 0\n"
"s_cbranch_scc1 LnpDpack32_%=\n"
"s_and_b32 s72, s57, 1\n"
"s_lshl_b32 s72, s72, 8\n"
"s_add_u32 s72, s72, 86016\n"
"s_waitcnt lgkmcnt(0)\n"
"s_lshl_b32 s67, s62, 2\n"
"s_add_u32 s67, s67, 86528\n"
"v_add_u32 v192, s67, v8\n"
"ds_read_b32 v192, v192\n"
"s_waitcnt lgkmcnt(0)\n"
"v_add_u32 v192, v192, v9\n"
"s_mov_b32 m0, s72\n"
"s_mov_b64 exec, s[78:79]\n"
"buffer_load_dword v192, s[44:47], 0 offen sc1 lds\n"
"s_mov_b64 exec, -1\n"
"s_waitcnt lgkmcnt(0)\n"
"LnpDpack32_%=:\n"
"s_waitcnt lgkmcnt(3)\n"
"v_mfma_f32_32x32x16_bf16 v[16:31], a[196:199], v[84:87], v[16:31]\n"
"ds_read_b128 v[100:103], v1 offset:21504\n"
"v_mfma_f32_32x32x16_bf16 v[32:47], a[68:71], v[84:87], v[32:47]\n"
"s_waitcnt lgkmcnt(3)\n"
"v_mfma_f32_32x32x16_bf16 v[16:31], a[200:203], v[88:91], v[16:31]\n"
"ds_read_b128 v[104:107], v1 offset:22528\n"
"v_mfma_f32_32x32x16_bf16 v[32:47], a[72:75], v[88:91], v[32:47]\n"
"s_waitcnt lgkmcnt(3)\n"
"v_mfma_f32_32x32x16_bf16 v[16:31], a[204:207], v[92:95], v[16:31]\n"
"ds_read_b128 v[108:111], v1 offset:23552\n"
"v_mfma_f32_32x32x16_bf16 v[32:47], a[76:79], v[92:95], v[32:47]\n"
"s_waitcnt lgkmcnt(3)\n"
"v_mfma_f32_32x32x16_bf16 v[16:31], a[208:211], v[96:99], v[16:31]\n"
"ds_read_b128 v[80:83], v1 offset:24576\n"
"v_mfma_f32_32x32x16_bf16 v[32:47], a[80:83], v[96:99], v[32:47]\n"
"s_waitcnt lgkmcnt(3)\n"
"v_mfma_f32_32x32x16_bf16 v[16:31], a[212:215], v[100:103], v[16:31]\n"
"ds_read_b128 v[84:87], v1 offset:25600\n"
"v_mfma_f32_32x32x16_bf16 v[32:47], a[84:87], v[100:103], v[32:47]\n"
"s_waitcnt lgkmcnt(3)\n"
"v_mfma_f32_32x32x16_bf16 v[16:31], a[216:219], v[104:107], v[16:31]\n"
"ds_read_b128 v[88:91], v1 offset:26624\n"
"v_mfma_f32_32x32x16_bf16 v[32:47], a[88:91], v[104:107], v[32:47]\n"
"s_waitcnt lgkmcnt(3)\n"
"v_mfma_f32_32x32x16_bf16 v[16:31], a[220:223], v[108:111], v[16:31]\n"
"ds_read_b128 v[92:95], v1 offset:27648\n"
"v_mfma_f32_32x32x16_bf16 v[32:47], a[92:95], v[108:111], v[32:47]\n"
"s_waitcnt lgkmcnt(3)\n"
"v_mfma_f32_32x32x16_bf16 v[16:31], a[224:227], v[80:83], v[16:31]\n"
"ds_read_b128 v[96:99], v1 offset:28672\n"
"v_mfma_f32_32x32x16_bf16 v[32:47], a[96:99], v[80:83], v[32:47]\n"
"s_waitcnt lgkmcnt(3)\n"
"v_mfma_f32_32x32x16_bf16 v[16:31], a[228:231], v[84:87], v[16:31]\n"
"ds_read_b128 v[100:103], v1 offset:29696\n"
"v_mfma_f32_32x32x16_bf16 v[32:47], a[100:103], v[84:87], v[32:47]\n"
"s_waitcnt lgkmcnt(3)\n"
"v_mfma_f32_32x32x16_bf16 v[16:31], a[232:235], v[88:91], v[16:31]\n"
"ds_read_b128 v[104:107], v1 offset:30720\n"
"v_mfma_f32_32x32x16_bf16 v[32:47], a[104:107], v[88:91], v[32:47]\n"
"s_waitcnt lgkmcnt(3)\n"
"v_mfma_f32_32x32x16_bf16 v[16:31], a[236:239], v[92:95], v[16:31]\n"
"ds_read_b128 v[108:111], v1 offset:31744\n"
"v_mfma_f32_32x32x16_bf16 v[32:47], a[108:111], v[92:95], v[32:47]\n"
"s_waitcnt lgkmcnt(3)\n"
"v_mfma_f32_32x32x16_bf16 v[16:31], a[240:243], v[96:99], v[16:31]\n"
"v_mfma_f32_32x32x16_bf16 v[32:47], a[112:115], v[96:99], v[32:47]\n"
"s_waitcnt lgkmcnt(2)\n"
"v_mfma_f32_32x32x16_bf16 v[16:31], a[244:247], v[100:103], v[16:31]\n"
"v_mfma_f32_32x32x16_bf16 v[32:47], a[116:119], v[100:103], v[32:47]\n"
"s_waitcnt lgkmcnt(1)\n"
"v_mfma_f32_32x32x16_bf16 v[16:31], a[248:251], v[104:107], v[16:31]\n"
"v_mfma_f32_32x32x16_bf16 v[32:47], a[120:123], v[104:107], v[32:47]\n"
"s_waitcnt lgkmcnt(0)\n"
"v_mfma_f32_32x32x16_bf16 v[16:31], a[252:255], v[108:111], v[16:31]\n"
"v_mfma_f32_32x32x16_bf16 v[32:47], a[124:127], v[108:111], v[32:47]\n"
"s_waitcnt vmcnt(0) lgkmcnt(0)\n"
"s_barrier\n"
"s_mov_b32 s99, 0\n"
"s_sub_u32 s77, 32768, s77\n"
"v_add_u32 v1, s77, v0\n"
"ds_read_b128 v[80:83], v1\n"
"ds_read_b128 v[84:87], v1 offset:1024\n"
"ds_read_b128 v[88:91], v1 offset:2048\n"
"ds_read_b128 v[92:95], v1 offset:3072\n"
"s_branch Le1pack32_%=\n"
"LPApack32_%=:\n"
"s_waitcnt lgkmcnt(3)\n"
"v_mfma_f32_32x32x16_bf16 v[48:63], a[128:131], v[80:83], 0\n"
"ds_read_b128 v[96:99], v1 offset:4096\n"
"s_add_u32 s92, s57, 1\n"
"v_readlane_b32 s92, v6, s92\n"
"s_sub_u32 s64, 32768, s77\n"
"v_mfma_f32_32x32x16_bf16 v[64:79], a[0:3], v[80:83], 0\n"
"s_add_u32 s64, s64, s55\n"
"s_mul_i32 s92, s92, s50\n"
"s_add_u32 s65, s92, s49\n"
"s_sub_u32 s90, s57, 1\n"
"s_and_b32 s90, s90, 1\n"
"s_waitcnt lgkmcnt(3)\n"
"v_mfma_f32_32x32x16_bf16 v[48:63], a[132:135], v[84:87], v[48:63]\n"
"ds_read_b128 v[100:103], v1 offset:5120\n"
"s_lshl_b32 s90, s90, 8\n"
"v_add_u32 v194, s90, v212\n"
"ds_read_b32 v195, v194\n"
"v_mfma_f32_32x32x16_bf16 v[64:79], a[4:7], v[84:87], v[64:79]\n"
"s_add_u32 m0, s64, 0\n"
"s_add_u32 s92, s65, 0\n"
"buffer_load_dwordx4 v2, s[36:39], s92 offen sc1 lds\n"
"s_sub_u32 s93, s57, 1\n"
"s_waitcnt lgkmcnt(4)\n"
"v_mfma_f32_32x32x16_bf16 v[48:63], a[136:139], v[88:91], v[48:63]\n"
"ds_read_b128 v[104:107], v1 offset:6144\n"
"v_readlane_b32 s93, v6, s93\n"
"v_mfma_f32_32x32x16_bf16 v[64:79], a[8:11], v[88:91], v[64:79]\n"
"s_add_u32 m0, s64, 4096\n"
"s_add_u32 s92, s65, 4096\n"
"buffer_load_dwordx4 v2, s[36:39], s92 offen sc1 lds\n"
"s_mul_i32 s93, s93, s52\n"
"s_waitcnt lgkmcnt(4)\n"
"v_mfma_f32_32x32x16_bf16 v[48:63], a[140:143], v[92:95], v[48:63]\n"
"ds_read_b128 v[108:111], v1 offset:7168\n"
"s_waitcnt lgkmcnt(2)\n"
"v_cmp_ne_u32_e64 s[80:81], s97, v195\n"
"s_add_u32 s66, s93, s51\n"
"v_readlane_b32 s93, v6, s60\n"
"v_mfma_f32_32x32x16_bf16 v[64:79], a[12:15], v[92:95], v[64:79]\n"
"s_add_u32 m0, s64, 8192\n"
"s_add_u32 s92, s65, 8192\n"
"buffer_load_dwordx4 v2, s[36:39], s92 offen sc1 lds\n"
"s_and_b64 s[80:81], s[80:81], s[88:89]\n"
"v_mfma_f32_32x32x16_bf16 v[48:63], a[144:147], v[96:99], v[48:63]\n"
"ds_read_b128 v[80:83], v1 offset:8192\n"
"s_ff1_i32_b64 s90, s[80:81]\n"
"s_cmp_lt_i32 s90, 0\n"
"s_cselect_b32 s90, 32, s90\n"
"s_mul_i32 s90, s90, 16384\n"
"v_mfma_f32_32x32x16_bf16 v[64:79], a[16:19], v[96:99], v[64:79]\n"
"s_add_u32 m0, s64, 12288\n"
"s_add_u32 s92, s65, 12288\n"
"buffer_load_dwordx4 v2, s[36:39], s92 offen sc1 lds\n"
"s_lshr_b32 s90, s90, 16\n"
"v_mfma_f32_32x32x16_bf16 v[48:63], a[148:151], v[100:103], v[48:63]\n"
"ds_read_b128 v[84:87], v1 offset:9216\n"
"s_sub_u32 s91, s48, s86\n"
"s_min_u32 s90, s90, s91\n"
"s_add_u32 s90, s90, s86\n"
"s_mul_i32 s93, s93, s54\n"
"v_mfma_f32_32x32x16_bf16 v[64:79], a[20:23], v[100:103], v[64:79]\n"
"s_add_u32 m0, s64, 16384\n"
"s_add_u32 s92, s65, 16384\n"
"buffer_load_dwordx4 v2, s[36:39], s92 offen sc1 lds\n"
"s_add_u32 s93, s93, s53\n"
"s_waitcnt lgkmcnt(3)\n"
"v_mfma_f32_32x32x16_bf16 v[48:63], a[152:155], v[104:107], v[48:63]\n"
"ds_read_b128 v[88:91], v1 offset:10240\n"
"s_cmp_lt_i32 s86, 0\n"
"s_cselect_b32 s58, s58, s90\n"
"v_mfma_f32_32x32x16_bf16 v[64:79], a[24:27], v[104:107], v[64:79]\n"
"s_add_u32 m0, s64, 20480\n"
"s_add_u32 s92, s65, 20480\n"
"buffer_load_dwordx4 v2, s[36:39], s92 offen sc1 lds\n"
"s_waitcnt lgkmcnt(3)\n"
"v_mfma_f32_32x32x16_bf16 v[48:63], a[156:159], v[108:111], v[48:63]\n"
"ds_read_b128 v[92:95], v1 offset:11264\n"
"s_cmp_lt_u32 s58, s48\n"
"s_cselect_b32 s62, s58, -1\n"
"s_sub_u32 s90, s58, s57\n"
"v_mfma_f32_32x32x16_bf16 v[64:79], a[28:31], v[108:111], v[64:79]\n"
"s_add_u32 m0, s64, 24576\n"
"s_add_u32 s92, s65, 24576\n"
"buffer_load_dwordx4 v2, s[36:39], s92 offen sc1 lds\n"
"s_waitcnt lgkmcnt(3)\n"
"v_mfma_f32_32x32x16_bf16 v[48:63], a[160:163], v[80:83], v[48:63]\n"
"ds_read_b128 v[96:99], v1 offset:12288\n"
"s_cmp_le_u32 s90, 4\n"
"s_cselect_b32 s62, s62, -1\n"
"s_max_i32 s90, s62, 0\n"
"v_mfma_f32_32x32x16_bf16 v[64:79], a[32:35], v[80:83], v[64:79]\n"
"s_add_u32 m0, s64, 28672\n"
"s_add_u32 s92, s65, 28672\n"
"buffer_load_dwordx4 v2, s[36:39], s92 offen sc1 lds\n"
"s_lshl_b32 s90, s90, 2\n"
"s_waitcnt lgkmcnt(3)\n"
"v_mfma_f32_32x32x16_bf16 v[48:63], a[164:167], v[84:87], v[48:63]\n"
"ds_read_b128 v[100:103], v1 offset:13312\n"
"s_add_u32 s90, s90, 86528\n"
"v_add_u32 v196, s90, v8\n"
"ds_read_b32 v196, v196\n"
"v_mfma_f32_32x32x16_bf16 v[64:79], a[36:39], v[84:87], v[64:79]\n"
"s_cmp_ge_i32 s62, 0\n"
"s_cselect_b64 s[94:95], s[78:79], 0\n"
"s_and_b32 s91, s57, 1\n"
"s_lshl_b32 s91, s91, 8\n"
"s_add_u32 s91, s91, 86016\n"
"s_waitcnt lgkmcnt(4)\n"
"v_mfma_f32_32x32x16_bf16 v[48:63], a[168:171], v[88:91], v[48:63]\n"
"ds_read_b128 v[104:107], v1 offset:14336\n"
"s_add_u32 s90, s57, 2\n"
"s_cmp_lt_u32 s90, s58\n"
"s_cselect_b32 s87, 1, 0\n"
"v_mfma_f32_32x32x16_bf16 v[64:79], a[40:43], v[88:91], v[64:79]\n"
"s_cmp_lt_i32 s60, 0\n"
"s_cselect_b32 s93, 0x7ffffff0, s93\n"
"v_add_u32 v197, s93, v3\n"
"v_cvt_pk_bf16_f32 v160, v16, v17\n"
"v_cvt_pk_bf16_f32 v161, v18, v19\n"
"s_waitcnt lgkmcnt(4)\n"
"v_mfma_f32_32x32x16_bf16 v[48:63], a[172:175], v[92:95], v[48:63]\n"
"ds_read_b128 v[108:111], v1 offset:15360\n"
"v_cvt_pk_bf16_f32 v162, v20, v21\n"
"v_cvt_pk_bf16_f32 v163, v22, v23\n"
"v_cvt_pk_bf16_f32 v164, v24, v25\n"
"v_mfma_f32_32x32x16_bf16 v[64:79], a[44:47], v[92:95], v[64:79]\n"
"s_waitcnt lgkmcnt(2)\n"
"v_add_u32 v196, v196, v9\n"
"s_mov_b32 m0, s91\n"
"s_mov_b64 exec, s[94:95]\n"
"buffer_load_dword v196, s[44:47], 0 offen sc1 lds\n"
"s_mov_b64 exec, -1\n"
"v_mfma_f32_32x32x16_bf16 v[48:63], a[176:179], v[96:99], v[48:63]\n"
"ds_read_b128 v[80:83], v1 offset:16384\n"
"v_cvt_pk_bf16_f32 v165, v26, v27\n"
"v_cvt_pk_bf16_f32 v166, v28, v29\n"
"v_cvt_pk_bf16_f32 v167, v30, v31\n"
"v_cvt_pk_bf16_f32 v168, v32, v33\n"
"v_mfma_f32_32x32x16_bf16 v[64:79], a[48:51], v[96:99], v[64:79]\n"
"v_cvt_pk_bf16_f32 v169, v34, v35\n"
"v_cvt_pk_bf16_f32 v170, v36, v37\n"
"v_cvt_pk_bf16_f32 v171, v38, v39\n"
"v_cvt_pk_bf16_f32 v172, v40, v41\n"
"v_cvt_pk_bf16_f32 v173, v42, v43\n"
"v_mfma_f32_32x32x16_bf16 v[48:63], a[180:183], v[100:103], v[48:63]\n"
"ds_read_b128 v[84:87], v1 offset:17408\n"
"v_cvt_pk_bf16_f32 v174, v44, v45\n"
"v_cvt_pk_bf16_f32 v175, v46, v47\n"
"v_permlane32_swap_b32 v160, v162\n"
"v_permlane32_swap_b32 v161, v163\n"
"v_mfma_f32_32x32x16_bf16 v[64:79], a[52:55], v[100:103], v[64:79]\n"
"v_permlane32_swap_b32 v164, v166\n"
"v_permlane32_swap_b32 v165, v167\n"
"v_permlane32_swap_b32 v168, v170\n"
"v_permlane32_swap_b32 v169, v171\n"
"v_permlane32_swap_b32 v172, v174\n"
"s_waitcnt lgkmcnt(3)\n"
"v_mfma_f32_32x32x16_bf16 v[48:63], a[184:187], v[104:107], v[48:63]\n"
"ds_read_b128 v[88:91], v1 offset:18432\n"
"v_permlane32_swap_b32 v173, v175\n"
"buffer_store_dwordx4 v[160:163], v11, s[40:43], s66 offen sc1\n"
"v_mfma_f32_32x32x16_bf16 v[64:79], a[56:59], v[104:107], v[64:79]\n"
"buffer_store_dwordx4 v[164:167], v12, s[40:43], s66 offen sc1\n"
"s_waitcnt lgkmcnt(3)\n"
"v_mfma_f32_32x32x16_bf16 v[48:63], a[188:191], v[108:111], v[48:63]\n"
"ds_read_b128 v[92:95], v1 offset:19456\n"
"buffer_store_dwordx4 v[168:171], v13, s[40:43], s66 offen sc1\n"
"v_mfma_f32_32x32x16_bf16 v[64:79], a[60:63], v[108:111], v[64:79]\n"
"buffer_store_dwordx4 v[172:175], v14, s[40:43], s66 offen sc1\n"
"s_waitcnt lgkmcnt(3)\n"
"v_mfma_f32_32x32x16_bf16 v[48:63], a[192:195], v[80:83], v[48:63]\n"
"ds_read_b128 v[96:99], v1 offset:20480\n"
"v_mfma_f32_32x32x16_bf16 v[64:79], a[64:67], v[80:83], v[64:79]\n"
"s_waitcnt lgkmcnt(3)\n"
"v_mfma_f32_32x32x16_bf16 v[48:63], a[196:199], v[84:87], v[48:63]\n"
"ds_read_b128 v[100:103], v1 offset:21504\n"
"v_mfma_f32_32x32x16_bf16 v[64:79], a[68:71], v[84:87], v[64:79]\n"
"s_waitcnt lgkmcnt(3)\n"
"v_mfma_f32_32x32x16_bf16 v[48:63], a[200:203], v[88:91], v[48:63]\n"
"ds_read_b128 v[104:107], v1 offset:22528\n"
"v_mfma_f32_32x32x16_bf16 v[64:79], a[72:75], v[88:91], v[64:79]\n"
"s_waitcnt lgkmcnt(3)\n"
"v_mfma_f32_32x32x16_bf16 v[48:63], a[204:207], v[92:95], v[48:63]\n"
"ds_read_b128 v[108:111], v1 offset:23552\n"
"v_mfma_f32_32x32x16_bf16 v[64:79], a[76:79], v[92:95], v[64:79]\n"
"s_waitcnt lgkmcnt(3)\n"
"v_mfma_f32_32x32x16_bf16 v[48:63], a[208:211], v[96:99], v[48:63]\n"
"ds_read_b128 v[80:83], v1 offset:24576\n"
"v_mfma_f32_32x32x16_bf16 v[64:79], a[80:83], v[96:99], v[64:79]\n"
"s_waitcnt lgkmcnt(3)\n"
"v_mfma_f32_32x32x16_bf16 v[48:63], a[212:215], v[100:103], v[48:63]\n"
"ds_read_b128 v[84:87], v1 offset:25600\n"
"v_mfma_f32_32x32x16_bf16 v[64:79], a[84:87], v[100:103], v[64:79]\n"
"s_waitcnt lgkmcnt(3)\n"
"v_mfma_f32_32x32x16_bf16 v[48:63], a[216:219], v[104:107], v[48:63]\n"
"ds_read_b128 v[88:91], v1 offset:26624\n"
"v_mfma_f32_32x32x16_bf16 v[64:79], a[88:91], v[104:107], v[64:79]\n"
"s_waitcnt lgkmcnt(3)\n"
"v_mfma_f32_32x32x16_bf16 v[48:63], a[220:223], v[108:111], v[48:63]\n"
"ds_read_b128 v[92:95], v1 offset:27648\n"
"v_mfma_f32_32x32x16_bf16 v[64:79], a[92:95], v[108:111], v[64:79]\n"
"s_waitcnt lgkmcnt(3)\n"
"v_mfma_f32_32x32x16_bf16 v[48:63], a[224:227], v[80:83], v[48:63]\n"
"ds_read_b128 v[96:99], v1 offset:28672\n"
"v_mfma_f32_32x32x16_bf16 v[64:79], a[96:99], v[80:83], v[64:79]\n"
"s_waitcnt lgkmcnt(3)\n"
"v_mfma_f32_32x32x16_bf16 v[48:63], a[228:231], v[84:87], v[48:63]\n"
"ds_read_b128 v[100:103], v1 offset:29696\n"
"v_mfma_f32_32x32x16_bf16 v[64:79], a[100:103], v[84:87], v[64:79]\n"
"s_waitcnt lgkmcnt(3)\n"
"v_mfma_f32_32x32x16_bf16 v[48:63], a[232:235], v[88:91], v[48:63]\n"
"ds_read_b128 v[104:107], v1 offset:30720\n"
"v_mfma_f32_32x32x16_bf16 v[64:79], a[104:107], v[88:91], v[64:79]\n"
"s_waitcnt lgkmcnt(3)\n"
"v_mfma_f32_32x32x16_bf16 v[48:63], a[236:239], v[92:95], v[48:63]\n"
"ds_read_b128 v[108:111], v1 offset:31744\n"
"v_mfma_f32_32x32x16_bf16 v[64:79], a[108:111], v[92:95], v[64:79]\n"
"s_waitcnt lgkmcnt(3)\n"
"v_mfma_f32_32x32x16_bf16 v[48:63], a[240:243], v[96:99], v[48:63]\n"
"v_mfma_f32_32x32x16_bf16 v[64:79], a[112:115], v[96:99], v[64:79]\n"
"s_waitcnt vmcnt(4) lgkmcnt(0)\n"
"s_barrier\n"
"v_mfma_f32_32x32x16_bf16 v[48:63], a[244:247], v[100:103], v[48:63]\n"
"s_sub_u32 s77, 32768, s77\n"
"v_add_u32 v1, s77, v0\n"
"ds_read_b128 v[80:83], v1\n"
"ds_read_b128 v[84:87], v1 offset:1024\n"
"v_mfma_f32_32x32x16_bf16 v[64:79], a[116:119], v[100:103], v[64:79]\n"
"buffer_store_dword v4, v197, s[44:47], 0 offen sc1\n"
"v_mfma_f32_32x32x16_bf16 v[48:63], a[248:251], v[104:107], v[48:63]\n"
"ds_read_b128 v[88:91], v1 offset:2048\n"
"v_mfma_f32_32x32x16_bf16 v[64:79], a[120:123], v[104:107], v[64:79]\n"
"s_sub_u32 s60, s57, 1\n"
"s_mov_b32 s86, s62\n"
"s_add_u32 s57, s57, 1\n"
"s_cmp_eq_u32 s87, 0\n"
"v_mfma_f32_32x32x16_bf16 v[48:63], a[252:255], v[108:111], v[48:63]\n"
"ds_read_b128 v[92:95], v1 offset:3072\n"
"v_mfma_f32_32x32x16_bf16 v[64:79], a[124:127], v[108:111], v[64:79]\n"
"s_cbranch_scc1 LfxApack32_%=\n"
"LPBpack32_%=:\n"
"s_waitcnt lgkmcnt(3)\n"
"v_mfma_f32_32x32x16_bf16 v[16:31], a[128:131], v[80:83], 0\n"
"ds_read_b128 v[96:99], v1 offset:4096\n"
"s_add_u32 s92, s57, 1\n"
"v_readlane_b32 s92, v6, s92\n"
"s_sub_u32 s64, 32768, s77\n"
"v_mfma_f32_32x32x16_bf16 v[32:47], a[0:3], v[80:83], 0\n"
"s_add_u32 s64, s64, s55\n"
"s_mul_i32 s92, s92, s50\n"
"s_add_u32 s65, s92, s49\n"
"s_sub_u32 s90, s57, 1\n"
"s_and_b32 s90, s90, 1\n"
"s_waitcnt lgkmcnt(3)\n"
"v_mfma_f32_32x32x16_bf16 v[16:31], a[132:135], v[84:87], v[16:31]\n"
"ds_read_b128 v[100:103], v1 offset:5120\n"
"s_lshl_b32 s90, s90, 8\n"
"v_add_u32 v194, s90, v212\n"
"ds_read_b32 v195, v194\n"
"v_mfma_f32_32x32x16_bf16 v[32:47], a[4:7], v[84:87], v[32:47]\n"
"s_add_u32 m0, s64, 0\n"
"s_add_u32 s92, s65, 0\n"
"buffer_load_dwordx4 v2, s[36:39], s92 offen sc1 lds\n"
"s_sub_u32 s93, s57, 1\n"
"s_waitcnt lgkmcnt(4)\n"
"v_mfma_f32_32x32x16_bf16 v[16:31], a[136:139], v[88:91], v[16:31]\n"
"ds_read_b128 v[104:107], v1 offset:6144\n"
"v_readlane_b32 s93, v6, s93\n"
"v_mfma_f32_32x32x16_bf16 v[32:47], a[8:11], v[88:91], v[32:47]\n"
"s_add_u32 m0, s64, 4096\n"
"s_add_u32 s92, s65, 4096\n"
"buffer_load_dwordx4 v2, s[36:39], s92 offen sc1 lds\n"
"s_mul_i32 s93, s93, s52\n"
"s_waitcnt lgkmcnt(4)\n"
"v_mfma_f32_32x32x16_bf16 v[16:31], a[140:143], v[92:95], v[16:31]\n"
"ds_read_b128 v[108:111], v1 offset:7168\n"
"s_waitcnt lgkmcnt(2)\n"
"v_cmp_ne_u32_e64 s[80:81], s97, v195\n"
"s_add_u32 s66, s93, s51\n"
"v_readlane_b32 s93, v6, s60\n"
"v_mfma_f32_32x32x16_bf16 v[32:47], a[12:15], v[92:95], v[32:47]\n"
"s_add_u32 m0, s64, 8192\n"
"s_add_u32 s92, s65, 8192\n"
"buffer_load_dwordx4 v2, s[36:39], s92 offen sc1 lds\n"
"s_and_b64 s[80:81], s[80:81], s[88:89]\n"
"v_mfma_f32_32x32x16_bf16 v[16:31], a[144:147], v[96:99], v[16:31]\n"
"ds_read_b128 v[80:83], v1 offset:8192\n"
"s_ff1_i32_b64 s90, s[80:81]\n"
"s_cmp_lt_i32 s90, 0\n"
"s_cselect_b32 s90, 32, s90\n"
"s_mul_i32 s90, s90, 16384\n"
"v_mfma_f32_32x32x16_bf16 v[32:47], a[16:19], v[96:99], v[32:47]\n"
"s_add_u32 m0, s64, 12288\n"
"s_add_u32 s92, s65, 12288\n"
"buffer_load_dwordx4 v2, s[36:39], s92 offen sc1 lds\n"
"s_lshr_b32 s90, s90, 16\n"
"v_mfma_f32_32x32x16_bf16 v[16:31], a[148:151], v[100:103], v[16:31]\n"
"ds_read_b128 v[84:87], v1 offset:9216\n"
"s_sub_u32 s91, s48, s86\n"
"s_min_u32 s90, s90, s91\n"
"s_add_u32 s90, s90, s86\n"
"s_mul_i32 s93, s93, s54\n"
"v_mfma_f32_32x32x16_bf16 v[32:47], a[20:23], v[100:103], v[32:47]\n"
"s_add_u32 m0, s64, 16384\n"
"s_add_u32 s92, s65, 16384\n"
"buffer_load_dwordx4 v2, s[36:39], s92 offen sc1 lds\n"
"s_add_u32 s93, s93, s53\n"
"s_waitcnt lgkmcnt(3)\n"
"v_mfma_f32_32x32x16_bf16 v[16:31], a[152:155], v[104:107], v[16:31]\n"
"ds_read_b128 v[88:91], v1 offset:10240\n"
"s_cmp_lt_i32 s86, 0\n"
"s_cselect_b32 s58, s58, s90\n"
"v_mfma_f32_32x32x16_bf16 v[32:47], a[24:27], v[104:107], v[32:47]\n"
"s_add_u32 m0, s64, 20480\n"
"s_add_u32 s92, s65, 20480\n"
"buffer_load_dwordx4 v2, s[36:39], s92 offen sc1 lds\n"
"s_waitcnt lgkmcnt(3)\n"
"v_mfma_f32_32x32x16_bf16 v[16:31], a[156:159], v[108:111], v[16:31]\n"
"ds_read_b128 v[92:95], v1 offset:11264\n"
"s_cmp_lt_u32 s58, s48\n"
"s_cselect_b32 s62, s58, -1\n"
"s_sub_u32 s90, s58, s57\n"
"v_mfma_f32_32x32x16_bf16 v[32:47], a[28:31], v[108:111], v[32:47]\n"
"s_add_u32 m0, s64, 24576\n"
"s_add_u32 s92, s65, 24576\n"
"buffer_load_dwordx4 v2, s[36:39], s92 offen sc1 lds\n"
"s_waitcnt lgkmcnt(3)\n"
"v_mfma_f32_32x32x16_bf16 v[16:31], a[160:163], v[80:83], v[16:31]\n"
"ds_read_b128 v[96:99], v1 offset:12288\n"
"s_cmp_le_u32 s90, 4\n"
"s_cselect_b32 s62, s62, -1\n"
"s_max_i32 s90, s62, 0\n"
"v_mfma_f32_32x32x16_bf16 v[32:47], a[32:35], v[80:83], v[32:47]\n"
"s_add_u32 m0, s64, 28672\n"
"s_add_u32 s92, s65, 28672\n"
"buffer_load_dwordx4 v2, s[36:39], s92 offen sc1 lds\n"
"s_lshl_b32 s90, s90, 2\n"
"s_waitcnt lgkmcnt(3)\n"
"v_mfma_f32_32x32x16_bf16 v[16:31], a[164:167], v[84:87], v[16:31]\n"
"ds_read_b128 v[100:103], v1 offset:13312\n"
"s_add_u32 s90, s90, 86528\n"
"v_add_u32 v196, s90, v8\n"
"ds_read_b32 v196, v196\n"
"v_mfma_f32_32x32x16_bf16 v[32:47], a[36:39], v[84:87], v[32:47]\n"
"s_cmp_ge_i32 s62, 0\n"
"s_cselect_b64 s[94:95], s[78:79], 0\n"
"s_and_b32 s91, s57, 1\n"
"s_lshl_b32 s91, s91, 8\n"
"s_add_u32 s91, s91, 86016\n"
"s_waitcnt lgkmcnt(4)\n"
"v_mfma_f32_32x32x16_bf16 v[16:31], a[168:171], v[88:91], v[16:31]\n"
"ds_read_b128 v[104:107], v1 offset:14336\n"
"s_add_u32 s90, s57, 2\n"
"s_cmp_lt_u32 s90, s58\n"
"s_cselect_b32 s87, 1, 0\n"
"v_mfma_f32_32x32x16_bf16 v[32:47], a[40:43], v[88:91], v[32:47]\n"
"s_cmp_lt_i32 s60, 0\n"
"s_cselect_b32 s93, 0x7ffffff0, s93\n"
"v_add_u32 v197, s93, v3\n"
"v_cvt_pk_bf16_f32 v160, v48, v49\n"
"v_cvt_pk_bf16_f32 v161, v50, v51\n"
"s_waitcnt lgkmcnt(4)\n"
"v_mfma_f32_32x32x16_bf16 v[16:31], a[172:175], v[92:95], v[16:31]\n"
"ds_read_b128 v[108:111], v1 offset:15360\n"
"v_cvt_pk_bf16_f32 v162, v52, v53\n"
"v_cvt_pk_bf16_f32 v163, v54, v55\n"
"v_cvt_pk_bf16_f32 v164, v56, v57\n"
"v_mfma_f32_32x32x16_bf16 v[32:47], a[44:47], v[92:95], v[32:47]\n"
"s_waitcnt lgkmcnt(2)\n"
"v_add_u32 v196, v196, v9\n"
"s_mov_b32 m0, s91\n"
"s_mov_b64 exec, s[94:95]\n"
"buffer_load_dword v196, s[44:47], 0 offen sc1 lds\n"
"s_mov_b64 exec, -1\n"
"v_mfma_f32_32x32x16_bf16 v[16:31], a[176:179], v[96:99], v[16:31]\n"
"ds_read_b128 v[80:83], v1 offset:16384\n"
"v_cvt_pk_bf16_f32 v165, v58, v59\n"
"v_cvt_pk_bf16_f32 v166, v60, v61\n"
"v_cvt_pk_bf16_f32 v167, v62, v63\n"
"v_cvt_pk_bf16_f32 v168, v64, v65\n"
"v_mfma_f32_32x32x16_bf16 v[32:47], a[48:51], v[96:99], v[32:47]\n"
"v_cvt_pk_bf16_f32 v169, v66, v67\n"
"v_cvt_pk_bf16_f32 v170, v68, v69\n"
"v_cvt_pk_bf16_f32 v171, v70, v71\n"
"v_cvt_pk_bf16_f32 v172, v72, v73\n"
"v_cvt_pk_bf16_f32 v173, v74, v75\n"
"v_mfma_f32_32x32x16_bf16 v[16:31], a[180:183], v[100:103], v[16:31]\n"
"ds_read_b128 v[84:87], v1 offset:17408\n"
"v_cvt_pk_bf16_f32 v174, v76, v77\n"
"v_cvt_pk_bf16_f32 v175, v78, v79\n"
"v_permlane32_swap_b32 v160, v162\n"
"v_permlane32_swap_b32 v161, v163\n"
"v_mfma_f32_32x32x16_bf16 v[32:47], a[52:55], v[100:103], v[32:47]\n"
"v_permlane32_swap_b32 v164, v166\n"
"v_permlane32_swap_b32 v165, v167\n"
"v_permlane32_swap_b32 v168, v170\n"
"v_permlane32_swap_b32 v169, v171\n"
"v_permlane32_swap_b32 v172, v174\n"
"s_waitcnt lgkmcnt(3)\n"
"v_mfma_f32_32x32x16_bf16 v[16:31], a[184:187], v[104:107], v[16:31]\n"
"ds_read_b128 v[88:91], v1 offset:18432\n"
"v_permlane32_swap_b32 v173, v175\n"
"buffer_store_dwordx4 v[160:163], v11, s[40:43], s66 offen sc1\n"
"v_mfma_f32_32x32x16_bf16 v[32:47], a[56:59], v[104:107], v[32:47]\n"
"buffer_store_dwordx4 v[164:167], v12, s[40:43], s66 offen sc1\n"
"s_waitcnt lgkmcnt(3)\n"
"v_mfma_f32_32x32x16_bf16 v[16:31], a[188:191], v[108:111], v[16:31]\n"
"ds_read_b128 v[92:95], v1 offset:19456\n"
"buffer_store_dwordx4 v[168:171], v13, s[40:43], s66 offen sc1\n"
"v_mfma_f32_32x32x16_bf16 v[32:47], a[60:63], v[108:111], v[32:47]\n"
"buffer_store_dwordx4 v[172:175], v14, s[40:43], s66 offen sc1\n"
"s_waitcnt lgkmcnt(3)\n"
"v_mfma_f32_32x32x16_bf16 v[16:31], a[192:195], v[80:83], v[16:31]\n"
"ds_read_b128 v[96:99], v1 offset:20480\n"
"v_mfma_f32_32x32x16_bf16 v[32:47], a[64:67], v[80:83], v[32:47]\n"
"s_waitcnt lgkmcnt(3)\n"
"v_mfma_f32_32x32x16_bf16 v[16:31], a[196:199], v[84:87], v[16:31]\n"
"ds_read_b128 v[100:103], v1 offset:21504\n"
"v_mfma_f32_32x32x16_bf16 v[32:47], a[68:71], v[84:87], v[32:47]\n"
"s_waitcnt lgkmcnt(3)\n"
"v_mfma_f32_32x32x16_bf16 v[16:31], a[200:203], v[88:91], v[16:31]\n"
"ds_read_b128 v[104:107], v1 offset:22528\n"
"v_mfma_f32_32x32x16_bf16 v[32:47], a[72:75], v[88:91], v[32:47]\n"
"s_waitcnt lgkmcnt(3)\n"
"v_mfma_f32_32x32x16_bf16 v[16:31], a[204:207], v[92:95], v[16:31]\n"
"ds_read_b128 v[108:111], v1 offset:23552\n"
"v_mfma_f32_32x32x16_bf16 v[32:47], a[76:79], v[92:95], v[32:47]\n"
"s_waitcnt lgkmcnt(3)\n"
"v_mfma_f32_32x32x16_bf16 v[16:31], a[208:211], v[96:99], v[16:31]\n"
"ds_read_b128 v[80:83], v1 offset:24576\n"
"v_mfma_f32_32x32x16_bf16 v[32:47], a[80:83], v[96:99], v[32:47]\n"
"s_waitcnt lgkmcnt(3)\n"
"v_mfma_f32_32x32x16_bf16 v[16:31], a[212:215], v[100:103], v[16:31]\n"
"ds_read_b128 v[84:87], v1 offset:25600\n"
"v_mfma_f32_32x32x16_bf16 v[32:47], a[84:87], v[100:103], v[32:47]\n"
"s_waitcnt lgkmcnt(3)\n"
"v_mfma_f32_32x32x16_bf16 v[16:31], a[216:219], v[104:107], v[16:31]\n"
"ds_read_b128 v[88:91], v1 offset:26624\n"
"v_mfma_f32_32x32x16_bf16 v[32:47], a[88:91], v[104:107], v[32:47]\n"
"s_waitcnt lgkmcnt(3)\n"
"v_mfma_f32_32x32x16_bf16 v[16:31], a[220:223], v[108:111], v[16:31]\n"
"ds_read_b128 v[92:95], v1 offset:27648\n"
"v_mfma_f32_32x32x16_bf16 v[32:47], a[92:95], v[108:111], v[32:47]\n"
"s_waitcnt lgkmcnt(3)\n"
"v_mfma_f32_32x32x16_bf16 v[16:31], a[224:227], v[80:83], v[16:31]\n"
"ds_read_b128 v[96:99], v1 offset:28672\n"
"v_mfma_f32_32x32x16_bf16 v[32:47], a[96:99], v[80:83], v[32:47]\n"
"s_waitcnt lgkmcnt(3)\n"
"v_mfma_f32_32x32x16_bf16 v[16:31], a[228:231], v[84:87], v[16:31]\n"
"ds_read_b128 v[100:103], v1 offset:29696\n"
"v_mfma_f32_32x32x16_bf16 v[32:47], a[100:103], v[84:87], v[32:47]\n"
"s_waitcnt lgkmcnt(3)\n"
"v_mfma_f32_32x32x16_bf16 v[16:31], a[232:235], v[88:91], v[16:31]\n"
"ds_read_b128 v[104:107], v1 offset:30720\n"
"v_mfma_f32_32x32x16_bf16 v[32:47], a[104:107], v[88:91], v[32:47]\n"
"s_waitcnt lgkmcnt(3)\n"
"v_mfma_f32_32x32x16_bf16 v[16:31], a[236:239], v[92:95], v[16:31]\n"
"ds_read_b128 v[108:111], v1 offset:31744\n"
"v_mfma_f32_32x32x16_bf16 v[32:47], a[108:111], v[92:95], v[32:47]\n"
"s_waitcnt lgkmcnt(3)\n"
"v_mfma_f32_32x32x16_bf16 v[16:31], a[240:243], v[96:99], v[16:31]\n"
"v_mfma_f32_32x32x16_bf16 v[32:47], a[112:115], v[96:99], v[32:47]\n"
"s_waitcnt vmcnt(4) lgkmcnt(0)\n"
"s_barrier\n"
"v_mfma_f32_32x32x16_bf16 v[16:31], a[244:247], v[100:103], v[16:31]\n"
"s_sub_u32 s77, 32768, s77\n"
"v_add_u32 v1, s77, v0\n"
"ds_read_b128 v[80:83], v1\n"
"ds_read_b128 v[84:87], v1 offset:1024\n"
"v_mfma_f32_32x32x16_bf16 v[32:47], a[116:119], v[100:103], v[32:47]\n"
"buffer_store_dword v4, v197, s[44:47], 0 offen sc1\n"
"v_mfma_f32_32x32x16_bf16 v[16:31], a[248:251], v[104:107], v[16:31]\n"
"ds_read_b128 v[88:91], v1 offset:2048\n"
"v_mfma_f32_32x32x16_bf16 v[32:47], a[120:123], v[104:107], v[32:47]\n"
"s_sub_u32 s60, s57, 1\n"
"s_mov_b32 s86, s62\n"
"s_add_u32 s57, s57, 1\n"
"s_cmp_eq_u32 s87, 0\n"
"v_mfma_f32_32x32x16_bf16 v[16:31], a[252:255], v[108:111], v[16:31]\n"
"ds_read_b128 v[92:95], v1 offset:3072\n"
"v_mfma_f32_32x32x16_bf16 v[32:47], a[124:127], v[108:111], v[32:47]\n"
"s_cbranch_scc0 LPApack32_%=\n"
"s_mov_b32 s99, 0\n"
"s_sub_u32 s57, s57, 1\n"
"s_branch Le1pack32_%=\n"
"LfxApack32_%=:\n"
"s_mov_b32 s99, 1\n"
"s_sub_u32 s57, s57, 1\n"
"Le1pack32_%=:\n"
"s_mov_b32 s60, -1\n"
"s_mov_b32 s61, -1\n"
"s_cmp_eq_u32 s59, 0\n"
"s_cbranch_scc1 Le2pack32_%=\n"
"s_sub_u32 s60, s57, 1\n"
"Le2pack32_%=:\n"
"s_cmp_lt_i32 s62, 0\n"
"s_cbranch_scc1 Le3pack32_%=\n"
"s_and_b32 s72, s57, 1\n"
"s_lshl_b32 s72, s72, 8\n"
"s_add_u32 s72, s72, 86016\n"
"s_sub_u32 s67, s72, 0\n"
"v_lshrrev_b32 v192, 2, v0\n"
"v_add_u32 v192, s67, v192\n"
"ds_read_b32 v193, v192\n"
"s_waitcnt lgkmcnt(0)\n"
"v_cmp_eq_u32_e64 s[80:81], s97, v193\n"
"s_nop 1\n"
"s_sub_u32 s68, s48, s62\n"
"s_min_u32 s68, s68, 8\n"
"s_mov_b32 s70, 0\n"
"Levepack32_%=:\n"
"s_cmp_ge_u32 s70, s68\n"
"s_cbranch_scc1 Lexepack32_%=\n"
"s_and_b32 s69, s80, 15\n"
"s_cmp_eq_u32 s69, 15\n"
"s_cbranch_scc0 Lexepack32_%=\n"
"s_lshr_b64 s[80:81], s[80:81], 4\n"
"s_add_u32 s70, s70, 1\n"
"s_branch Levepack32_%=\n"
"Lexepack32_%=:\n"
"s_add_u32 s58, s58, s70\n"
"s_waitcnt lgkmcnt(0)\n"
"Le3pack32_%=:\n"
"s_mov_b32 s59, 0\n"
"s_add_u32 s68, s57, 2\n"
"s_cmp_lt_u32 s68, s58\n"
"s_cbranch_scc0 Le4pack32_%=\n"
"s_cmp_ge_u32 s57, 3\n"
"s_cbranch_scc0 Le4pack32_%=\n"
"s_add_u32 s59, s99, 1\n"
"s_mov_b32 s86, -1\n"
"s_add_u32 s57, s57, 1\n"
"s_branch Ltoppack32_%=\n"
"Le4pack32_%=:\n"
"s_mov_b32 s61, s57\n"
"s_cmp_eq_u32 s99, 0\n"
"s_cbranch_scc0 Le5pack32_%=\n"
"s_nop 3\n"
"v_readlane_b32 s67, v6, s57\n"
"s_nop 0\n"
"s_mul_i32 s67, s67, s52\n"
"s_add_u32 s66, s67, s51\n"
"v_cvt_pk_bf16_f32 v160, v16, v17\n"
"v_cvt_pk_bf16_f32 v161, v18, v19\n"
"v_cvt_pk_bf16_f32 v162, v20, v21\n"
"v_cvt_pk_bf16_f32 v163, v22, v23\n"
"v_cvt_pk_bf16_f32 v164, v24, v25\n"
"v_cvt_pk_bf16_f32 v165, v26, v27\n"
"v_cvt_pk_bf16_f32 v166, v28, v29\n"
"v_cvt_pk_bf16_f32 v167, v30, v31\n"
"v_cvt_pk_bf16_f32 v168, v32, v33\n"
"v_cvt_pk_bf16_f32 v169, v34, v35\n"
"v_cvt_pk_bf16_f32 v170, v36, v37\n"
"v_cvt_pk_bf16_f32 v171, v38, v39\n"
"v_cvt_pk_bf16_f32 v172, v40, v41\n"
"v_cvt_pk_bf16_f32 v173, v42, v43\n"
"v_cvt_pk_bf16_f32 v174, v44, v45\n"
"v_cvt_pk_bf16_f32 v175, v46, v47\n"
"v_permlane32_swap_b32 v160, v162\n"
"v_permlane32_swap_b32 v161, v163\n"
"v_permlane32_swap_b32 v164, v166\n"
"v_permlane32_swap_b32 v165, v167\n"
"v_permlane32_swap_b32 v168, v170\n"
"v_permlane32_swap_b32 v169, v171\n"
"v_permlane32_swap_b32 v172, v174\n"
"v_permlane32_swap_b32 v173, v175\n"
"buffer_store_dwordx4 v[160:163], v11, s[40:43], s66 offen sc1\n"
"buffer_store_dwordx4 v[164:167], v12, s[40:43], s66 offen sc1\n"
"buffer_store_dwordx4 v[168:171], v13, s[40:43], s66 offen sc1\n"
"buffer_store_dwordx4 v[172:175], v14, s[40:43], s66 offen sc1\n"
"s_add_u32 s57, s57, 1\n"
"s_branch Ltoppack32_%=\n"
"Le5pack32_%=:\n"
"s_nop 3\n"
"v_readlane_b32 s67, v6, s57\n"
"s_nop 0\n"
"s_mul_i32 s67, s67, s52\n"
"s_add_u32 s66, s67, s51\n"
"v_cvt_pk_bf16_f32 v160, v48, v49\n"
"v_cvt_pk_bf16_f32 v161, v50, v51\n"
"v_cvt_pk_bf16_f32 v162, v52, v53\n"
"v_cvt_pk_bf16_f32 v163, v54, v55\n"
"v_cvt_pk_bf16_f32 v164, v56, v57\n"
"v_cvt_pk_bf16_f32 v165, v58, v59\n"
"v_cvt_pk_bf16_f32 v166, v60, v61\n"
"v_cvt_pk_bf16_f32 v167, v62, v63\n"
"v_cvt_pk_bf16_f32 v168, v64, v65\n"
"v_cvt_pk_bf16_f32 v169, v66, v67\n"
"v_cvt_pk_bf16_f32 v170, v68, v69\n"
"v_cvt_pk_bf16_f32 v171, v70, v71\n"
"v_cvt_pk_bf16_f32 v172, v72, v73\n"
"v_cvt_pk_bf16_f32 v173, v74, v75\n"
"v_cvt_pk_bf16_f32 v174, v76, v77\n"
"v_cvt_pk_bf16_f32 v175, v78, v79\n"
"v_permlane32_swap_b32 v160, v162\n"
"v_permlane32_swap_b32 v161, v163\n"
"v_permlane32_swap_b32 v164, v166\n"
"v_permlane32_swap_b32 v165, v167\n"
"v_permlane32_swap_b32 v168, v170\n"
"v_permlane32_swap_b32 v169, v171\n"
"v_permlane32_swap_b32 v172, v174\n"
"v_permlane32_swap_b32 v173, v175\n"
"buffer_store_dwordx4 v[160:163], v11, s[40:43], s66 offen sc1\n"
"buffer_store_dwordx4 v[164:167], v12, s[40:43], s66 offen sc1\n"
"buffer_store_dwordx4 v[168:171], v13, s[40:43], s66 offen sc1\n"
"buffer_store_dwordx4 v[172:175], v14, s[40:43], s66 offen sc1\n"
"s_add_u32 s57, s57, 1\n"
"s_branch Ltoppack32_%=\n"
"Lnonpack32_%=:\n"
"s_max_i32 s68, s60, s61\n"
"s_cmp_ge_i32 s68, 0\n"
"s_cselect_b32 s63, 1, 0\n"
"s_waitcnt lgkmcnt(3)\n"
"v_mfma_f32_32x32x16_bf16 v[16:31], a[128:131], v[80:83], 0\n"
"ds_read_b128 v[96:99], v1 offset:4096\n"
"v_mfma_f32_32x32x16_bf16 v[32:47], a[0:3], v[80:83], 0\n"
"s_add_u32 s90, s57, 1\n"
"s_cmp_lt_u32 s90, s48\n"
"s_cselect_b32 s62, s90, -1\n"
"s_lshl_b32 s90, s90, 2\n"
"s_add_u32 s90, s90, 86528\n"
"v_add_u32 v196, s90, v8\n"
"s_waitcnt lgkmcnt(3)\n"
"v_mfma_f32_32x32x16_bf16 v[16:31], a[132:135], v[84:87], v[16:31]\n"
"ds_read_b128 v[100:103], v1 offset:5120\n"
"ds_read_b32 v196, v196\n"
"s_cmp_ge_i32 s62, 0\n"
"s_cselect_b64 s[94:95], s[78:79], 0\n"
"v_mfma_f32_32x32x16_bf16 v[32:47], a[4:7], v[84:87], v[32:47]\n"
"s_and_b32 s94, s94, 255\n"
"s_mov_b32 s95, 0\n"
"s_waitcnt lgkmcnt(4)\n"
"v_mfma_f32_32x32x16_bf16 v[16:31], a[136:139], v[88:91], v[16:31]\n"
"ds_read_b128 v[104:107], v1 offset:6144\n"
"v_mfma_f32_32x32x16_bf16 v[32:47], a[8:11], v[88:91], v[32:47]\n"
"s_waitcnt lgkmcnt(4)\n"
"v_mfma_f32_32x32x16_bf16 v[16:31], a[140:143], v[92:95], v[16:31]\n"
"ds_read_b128 v[108:111], v1 offset:7168\n"
"s_waitcnt lgkmcnt(2)\n"
"v_add_u32 v196, v196, v9\n"
"v_cndmask_b32_e64 v196, v15, v196, s[94:95]\n"
"v_mfma_f32_32x32x16_bf16 v[32:47], a[12:15], v[92:95], v[32:47]\n"
"s_mov_b32 s91, 87296\n"
"s_cmp_eq_u32 %0, 2\n"
"s_cselect_b32 m0, s91, 87040\n"
"s_nop 0\n"
"buffer_load_dword v196, s[44:47], 0 offen sc1 lds\n"
"v_mfma_f32_32x32x16_bf16 v[16:31], a[144:147], v[96:99], v[16:31]\n"
"ds_read_b128 v[80:83], v1 offset:8192\n"
"v_mfma_f32_32x32x16_bf16 v[32:47], a[16:19], v[96:99], v[32:47]\n"
"v_mfma_f32_32x32x16_bf16 v[16:31], a[148:151], v[100:103], v[16:31]\n"
"ds_read_b128 v[84:87], v1 offset:9216\n"
"v_mfma_f32_32x32x16_bf16 v[32:47], a[20:23], v[100:103], v[32:47]\n"
"s_waitcnt lgkmcnt(3)\n"
"v_mfma_f32_32x32x16_bf16 v[16:31], a[152:155], v[104:107], v[16:31]\n"
"ds_read_b128 v[88:91], v1 offset:10240\n"
"v_mfma_f32_32x32x16_bf16 v[32:47], a[24:27], v[104:107], v[32:47]\n"
"s_waitcnt lgkmcnt(3)\n"
"v_mfma_f32_32x32x16_bf16 v[16:31], a[156:159], v[108:111], v[16:31]\n"
"ds_read_b128 v[92:95], v1 offset:11264\n"
"v_mfma_f32_32x32x16_bf16 v[32:47], a[28:31], v[108:111], v[32:47]\n"
"s_waitcnt lgkmcnt(3)\n"
"v_mfma_f32_32x32x16_bf16 v[16:31], a[160:163], v[80:83], v[16:31]\n"
"ds_read_b128 v[96:99], v1 offset:12288\n"
"v_mfma_f32_32x32x16_bf16 v[32:47], a[32:35], v[80:83], v[32:47]\n"
"s_waitcnt lgkmcnt(3)\n"
"v_mfma_f32_32x32x16_bf16 v[16:31], a[164:167], v[84:87], v[16:31]\n"
"ds_read_b128 v[100:103], v1 offset:13312\n"
"v_mfma_f32_32x32x16_bf16 v[32:47], a[36:39], v[84:87], v[32:47]\n"
"s_waitcnt lgkmcnt(3)\n"
"v_mfma_f32_32x32x16_bf16 v[16:31], a[168:171], v[88:91], v[16:31]\n"
"ds_read_b128 v[104:107], v1 offset:14336\n"
"v_mfma_f32_32x32x16_bf16 v[32:47], a[40:43], v[88:91], v[32:47]\n"
"s_cmp_eq_u32 s63, 0\n"
"s_cbranch_scc1 LnmLpack32_%=\n"
"s_waitcnt vmcnt(1)\n"
"s_barrier\n"
"v_readlane_b32 s93, v6, s60\n"
"s_nop 0\n"
"s_mul_i32 s93, s93, s54\n"
"s_add_u32 s93, s93, s53\n"
"s_cmp_lt_i32 s60, 0\n"
"s_cselect_b32 s93, 0x7ffffff0, s93\n"
"v_add_u32 v197, s93, v3\n"
"buffer_store_dword v4, v197, s[44:47], 0 offen sc1\n"
"v_readlane_b32 s93, v6, s61\n"
"s_nop 0\n"
"s_mul_i32 s93, s93, s54\n"
"s_add_u32 s93, s93, s53\n"
"s_cmp_lt_i32 s61, 0\n"
"s_cselect_b32 s93, 0x7ffffff0, s93\n"
"v_add_u32 v199, s93, v3\n"
"buffer_store_dword v4, v199, s[44:47], 0 offen sc1\n"
"LnmLpack32_%=:\n"
"s_waitcnt lgkmcnt(3)\n"
"v_mfma_f32_32x32x16_bf16 v[16:31], a[172:175], v[92:95], v[16:31]\n"
"ds_read_b128 v[108:111], v1 offset:15360\n"
"v_mfma_f32_32x32x16_bf16 v[32:47], a[44:47], v[92:95], v[32:47]\n"
"s_waitcnt lgkmcnt(3)\n"
"v_mfma_f32_32x32x16_bf16 v[16:31], a[176:179], v[96:99], v[16:31]\n"
"ds_read_b128 v[80:83], v1 offset:16384\n"
"v_mfma_f32_32x32x16_bf16 v[32:47], a[48:51], v[96:99], v[32:47]\n"
"s_waitcnt lgkmcnt(3)\n"
"v_mfma_f32_32x32x16_bf16 v[16:31], a[180:183], v[100:103], v[16:31]\n"
"ds_read_b128 v[84:87], v1 offset:17408\n"
"v_mfma_f32_32x32x16_bf16 v[32:47], a[52:55], v[100:103], v[32:47]\n"
"s_waitcnt lgkmcnt(3)\n"
"v_mfma_f32_32x32x16_bf16 v[16:31], a[184:187], v[104:107], v[16:31]\n"
"ds_read_b128 v[88:91], v1 offset:18432\n"
"v_mfma_f32_32x32x16_bf16 v[32:47], a[56:59], v[104:107], v[32:47]\n"
"s_waitcnt lgkmcnt(3)\n"
"v_mfma_f32_32x32x16_bf16 v[16:31], a[188:191], v[108:111], v[16:31]\n"
"ds_read_b128 v[92:95], v1 offset:19456\n"
"v_mfma_f32_32x32x16_bf16 v[32:47], a[60:63], v[108:111], v[32:47]\n"
"s_mov_b32 s91, 87552\n"
"s_cmp_eq_u32 %0, 2\n"
"s_cselect_b32 m0, s91, 87040\n"
"s_nop 0\n"
"buffer_load_dword v196, s[44:47], 0 offen sc1 lds\n"
"s_waitcnt lgkmcnt(3)\n"
"v_mfma_f32_32x32x16_bf16 v[16:31], a[192:195], v[80:83], v[16:31]\n"
"ds_read_b128 v[96:99], v1 offset:20480\n"
"v_mfma_f32_32x32x16_bf16 v[32:47], a[64:67], v[80:83], v[32:47]\n"
"s_waitcnt lgkmcnt(3)\n"
"v_mfma_f32_32x32x16_bf16 v[16:31], a[196:199], v[84:87], v[16:31]\n"
"ds_read_b128 v[100:103], v1 offset:21504\n"
"v_mfma_f32_32x32x16_bf16 v[32:47], a[68:71], v[84:87], v[32:47]\n"
"s_waitcnt lgkmcnt(3)\n"
"v_mfma_f32_32x32x16_bf16 v[16:31], a[200:203], v[88:91], v[16:31]\n"
"ds_read_b128 v[104:107], v1 offset:22528\n"
"v_mfma_f32_32x32x16_bf16 v[32:47], a[72:75], v[88:91], v[32:47]\n"
"s_waitcnt lgkmcnt(3)\n"
"v_mfma_f32_32x32x16_bf16 v[16:31], a[204:207], v[92:95], v[16:31]\n"
"ds_read_b128 v[108:111], v1 offset:23552\n"
"v_mfma_f32_32x32x16_bf16 v[32:47], a[76:79], v[92:95], v[32:47]\n"
"s_cmp_eq_u32 s63, 0\n"
"s_cbranch_scc1 LpwLpack32_%=\n"
"s_waitcnt vmcnt(3)\n"
"s_branch LpxLpack32_%=\n"
"LpwLpack32_%=:\n"
"s_waitcnt vmcnt(1)\n"
"LpxLpack32_%=:\n"
"s_barrier\n"
"s_waitcnt lgkmcnt(3)\n"
"v_mfma_f32_32x32x16_bf16 v[16:31], a[208:211], v[96:99], v[16:31]\n"
"ds_read_b128 v[80:83], v1 offset:24576\n"
"ds_read_b32 v195, v212 offset:1280\n"
"s_add_u32 s92, s57, 1\n"
"v_readlane_b32 s92, v6, s92\n"
"s_sub_u32 s64, 32768, s77\n"
"v_mfma_f32_32x32x16_bf16 v[32:47], a[80:83], v[96:99], v[32:47]\n"
"s_add_u32 s64, s64, s55\n"
"s_mul_i32 s92, s92, s50\n"
"s_add_u32 s65, s92, s49\n"
"s_waitcnt lgkmcnt(4)\n"
"v_mfma_f32_32x32x16_bf16 v[16:31], a[212:215], v[100:103], v[16:31]\n"
"ds_read_b128 v[84:87], v1 offset:25600\n"
"v_mfma_f32_32x32x16_bf16 v[32:47], a[84:87], v[100:103], v[32:47]\n"
"s_waitcnt lgkmcnt(1)\n"
"v_cmp_ne_u32_e64 s[80:81], s97, v195\n"
"v_mfma_f32_32x32x16_bf16 v[16:31], a[216:219], v[104:107], v[16:31]\n"
"ds_read_b128 v[88:91], v1 offset:26624\n"
"s_and_b32 s80, s80, 255\n"
"s_mov_b32 s81, 0\n"
"s_ff1_i32_b64 s90, s[80:81]\n"
"s_cmp_lt_i32 s90, 0\n"
"s_cselect_b32 s90, 8, s90\n"
"v_mfma_f32_32x32x16_bf16 v[32:47], a[88:91], v[104:107], v[32:47]\n"
"s_mul_i32 s90, s90, 16384\n"
"s_lshr_b32 s90, s90, 16\n"
"s_sub_u32 s91, s48, s62\n"
"s_min_u32 s90, s90, s91\n"
"s_add_u32 s90, s90, s62\n"
"v_mfma_f32_32x32x16_bf16 v[16:31], a[220:223], v[108:111], v[16:31]\n"
"ds_read_b128 v[92:95], v1 offset:27648\n"
"s_cmp_lt_i32 s62, 0\n"
"s_cselect_b32 s58, s58, s90\n"
"s_add_u32 s90, s57, 1\n"
"v_mfma_f32_32x32x16_bf16 v[32:47], a[92:95], v[108:111], v[32:47]\n"
"s_cmp_lt_u32 s90, s58\n"
"s_cselect_b64 s[94:95], -1, 0\n"
"s_cselect_b32 s87, 1, 0\n"
"v_cndmask_b32_e64 v198, v15, v2, s[94:95]\n"
"v_mfma_f32_32x32x16_bf16 v[16:31], a[224:227], v[80:83], v[16:31]\n"
"ds_read_b128 v[96:99], v1 offset:28672\n"
"s_add_u32 m0, s64, 0\n"
"s_add_u32 s92, s65, 0\n"
"buffer_load_dwordx4 v198, s[36:39], s92 offen sc1 lds\n"
"v_mfma_f32_32x32x16_bf16 v[32:47], a[96:99], v[80:83], v[32:47]\n"
"s_add_u32 m0, s64, 4096\n"
"s_add_u32 s92, s65, 4096\n"
"buffer_load_dwordx4 v198, s[36:39], s92 offen sc1 lds\n"
"s_waitcnt lgkmcnt(3)\n"
"v_mfma_f32_32x32x16_bf16 v[16:31], a[228:231], v[84:87], v[16:31]\n"
"ds_read_b128 v[100:103], v1 offset:29696\n"
"s_add_u32 m0, s64, 8192\n"
"s_add_u32 s92, s65, 8192\n"
"buffer_load_dwordx4 v198, s[36:39], s92 offen sc1 lds\n"
"v_mfma_f32_32x32x16_bf16 v[32:47], a[100:103], v[84:87], v[32:47]\n"
"s_add_u32 m0, s64, 12288\n"
"s_add_u32 s92, s65, 12288\n"
"buffer_load_dwordx4 v198, s[36:39], s92 offen sc1 lds\n"
"s_waitcnt lgkmcnt(3)\n"
"v_mfma_f32_32x32x16_bf16 v[16:31], a[232:235], v[88:91], v[16:31]\n"
"ds_read_b128 v[104:107], v1 offset:30720\n"
"s_add_u32 m0, s64, 16384\n"
"s_add_u32 s92, s65, 16384\n"
"buffer_load_dwordx4 v198, s[36:39], s92 offen sc1 lds\n"
"v_mfma_f32_32x32x16_bf16 v[32:47], a[104:107], v[88:91], v[32:47]\n"
"s_add_u32 m0, s64, 20480\n"
"s_add_u32 s92, s65, 20480\n"
"buffer_load_dwordx4 v198, s[36:39], s92 offen sc1 lds\n"
"s_waitcnt lgkmcnt(3)\n"
"v_mfma_f32_32x32x16_bf16 v[16:31], a[236:239], v[92:95], v[16:31]\n"
"ds_read_b128 v[108:111], v1 offset:31744\n"
"s_add_u32 m0, s64, 24576\n"
"s_add_u32 s92, s65, 24576\n"
"buffer_load_dwordx4 v198, s[36:39], s92 offen sc1 lds\n"
"v_mfma_f32_32x32x16_bf16 v[32:47], a[108:111], v[92:95], v[32:47]\n"
"s_add_u32 m0, s64, 28672\n"
"s_add_u32 s92, s65, 28672\n"
"buffer_load_dwordx4 v198, s[36:39], s92 offen sc1 lds\n"
"s_waitcnt lgkmcnt(3)\n"
"v_mfma_f32_32x32x16_bf16 v[16:31], a[240:243], v[96:99], v[16:31]\n"
"v_mfma_f32_32x32x16_bf16 v[32:47], a[112:115], v[96:99], v[32:47]\n"
"s_waitcnt lgkmcnt(2)\n"
"v_mfma_f32_32x32x16_bf16 v[16:31], a[244:247], v[100:103], v[16:31]\n"
"v_mfma_f32_32x32x16_bf16 v[32:47], a[116:119], v[100:103], v[32:47]\n"
"s_waitcnt lgkmcnt(1)\n"
"v_mfma_f32_32x32x16_bf16 v[16:31], a[248:251], v[104:107], v[16:31]\n"
"v_mfma_f32_32x32x16_bf16 v[32:47], a[120:123], v[104:107], v[32:47]\n"
"s_waitcnt lgkmcnt(0)\n"
"v_mfma_f32_32x32x16_bf16 v[16:31], a[252:255], v[108:111], v[16:31]\n"
"v_mfma_f32_32x32x16_bf16 v[32:47], a[124:127], v[108:111], v[32:47]\n"
"s_mov_b32 s60, -1\n"
"s_mov_b32 s61, -1\n"
"s_cmp_eq_u32 s87, 0\n"
"s_cbranch_scc0 Lwaypack32_%=\n"
"s_cmp_lt_i32 s62, 0\n"
"s_cbranch_scc1 Lnoldpack32_%=\n"
"s_waitcnt vmcnt(8) lgkmcnt(0)\n"
"s_barrier\n"
"s_sub_u32 s67, 87552, 0\n"
"v_lshrrev_b32 v192, 2, v0\n"
"v_add_u32 v192, s67, v192\n"
"ds_read_b32 v193, v192\n"
"s_waitcnt lgkmcnt(0)\n"
"v_cmp_eq_u32_e64 s[80:81], s97, v193\n"
"s_nop 1\n"
"s_sub_u32 s68, s48, s62\n"
"s_min_u32 s68, s68, 2\n"
"s_mov_b32 s70, 0\n"
"Levs2pack32_%=:\n"
"s_cmp_ge_u32 s70, s68\n"
"s_cbranch_scc1 Lexs2pack32_%=\n"
"s_and_b32 s69, s80, 15\n"
"s_cmp_eq_u32 s69, 15\n"
"s_cbranch_scc0 Lexs2pack32_%=\n"
"s_lshr_b64 s[80:81], s[80:81], 4\n"
"s_add_u32 s70, s70, 1\n"
"s_branch Levs2pack32_%=\n"
"Lexs2pack32_%=:\n"
"s_add_u32 s58, s62, s70\n"
"s_cmp_lt_u32 s62, s58\n"
"s_cbranch_scc0 Lnoldpack32_%=\n"
"v_readlane_b32 s67, v6, s62\n"
"s_nop 0\n"
"s_mul_i32 s67, s67, s50\n"
"s_add_u32 s65, s67, s49\n"
"s_sub_u32 s64, 32768, s77\n"
"s_lshl_b32 s68, %0, 10\n"
"s_add_u32 s64, s64, s68\n"
"s_add_u32 m0, s64, 0\n"
"s_add_u32 s70, s65, 0\n"
"buffer_load_dwordx4 v2, s[36:39], s70 offen sc1 lds\n"
"s_add_u32 m0, s64, 4096\n"
"s_add_u32 s70, s65, 4096\n"
"buffer_load_dwordx4 v2, s[36:39], s70 offen sc1 lds\n"
"s_add_u32 m0, s64, 8192\n"
"s_add_u32 s70, s65, 8192\n"
"buffer_load_dwordx4 v2, s[36:39], s70 offen sc1 lds\n"
"s_add_u32 m0, s64, 12288\n"
"s_add_u32 s70, s65, 12288\n"
"buffer_load_dwordx4 v2, s[36:39], s70 offen sc1 lds\n"
"s_add_u32 m0, s64, 16384\n"
"s_add_u32 s70, s65, 16384\n"
"buffer_load_dwordx4 v2, s[36:39], s70 offen sc1 lds\n"
"s_add_u32 m0, s64, 20480\n"
"s_add_u32 s70, s65, 20480\n"
"buffer_load_dwordx4 v2, s[36:39], s70 offen sc1 lds\n"
"s_add_u32 m0, s64, 24576\n"
"s_add_u32 s70, s65, 24576\n"
"buffer_load_dwordx4 v2, s[36:39], s70 offen sc1 lds\n"
"s_add_u32 m0, s64, 28672\n"
"s_add_u32 s70, s65, 28672\n"
"buffer_load_dwordx4 v2, s[36:39], s70 offen sc1 lds\n"
"Lwaypack32_%=:\n"
"s_waitcnt lgkmcnt(0)\n"
"s_nop 7\n"
"v_readlane_b32 s67, v6, s57\n"
"s_nop 0\n"
"s_mul_i32 s67, s67, s52\n"
"s_add_u32 s66, s67, s51\n"
"v_cvt_pk_bf16_f32 v160, v16, v17\n"
"v_cvt_pk_bf16_f32 v161, v18, v19\n"
"v_cvt_pk_bf16_f32 v162, v20, v21\n"
"v_cvt_pk_bf16_f32 v163, v22, v23\n"
"v_cvt_pk_bf16_f32 v164, v24, v25\n"
"v_cvt_pk_bf16_f32 v165, v26, v27\n"
"v_cvt_pk_bf16_f32 v166, v28, v29\n"
"v_cvt_pk_bf16_f32 v167, v30, v31\n"
"v_cvt_pk_bf16_f32 v168, v32, v33\n"
"v_cvt_pk_bf16_f32 v169, v34, v35\n"
"v_cvt_pk_bf16_f32 v170, v36, v37\n"
"v_cvt_pk_bf16_f32 v171, v38, v39\n"
"v_cvt_pk_bf16_f32 v172, v40, v41\n"
"v_cvt_pk_bf16_f32 v173, v42, v43\n"
"v_cvt_pk_bf16_f32 v174, v44, v45\n"
"v_cvt_pk_bf16_f32 v175, v46, v47\n"
"v_permlane32_swap_b32 v160, v162\n"
"v_permlane32_swap_b32 v161, v163\n"
"v_permlane32_swap_b32 v164, v166\n"
"v_permlane32_swap_b32 v165, v167\n"
"v_permlane32_swap_b32 v168, v170\n"
"v_permlane32_swap_b32 v169, v171\n"
"v_permlane32_swap_b32 v172, v174\n"
"v_permlane32_swap_b32 v173, v175\n"
"buffer_store_dwordx4 v[160:163], v11, s[40:43], s66 offen sc1\n"
"buffer_store_dwordx4 v[164:167], v12, s[40:43], s66 offen sc1\n"
"buffer_store_dwordx4 v[168:171], v13, s[40:43], s66 offen sc1\n"
"buffer_store_dwordx4 v[172:175], v14, s[40:43], s66 offen sc1\n"
"s_waitcnt vmcnt(4) lgkmcnt(0)\n"
"s_barrier\n"
"s_mov_b32 s61, s57\n"
"s_add_u32 s62, s57, 1\n"
"s_sub_u32 s77, 32768, s77\n"
"v_add_u32 v1, s77, v0\n"
"ds_read_b128 v[80:83], v1\n"
"ds_read_b128 v[84:87], v1 offset:1024\n"
"ds_read_b128 v[88:91], v1 offset:2048\n"
"ds_read_b128 v[92:95], v1 offset:3072\n"
"s_add_u32 s57, s57, 1\n"
"s_branch Ltoppack32_%=\n"
"Lnoldpack32_%=:\n"
"s_add_u32 s62, s57, 1\n"
"s_waitcnt lgkmcnt(0)\n"
"s_cmp_lt_u32 s62, s48\n"
"s_cbranch_scc0 Ln1pack32_%=\n"
"s_lshl_b32 s67, s62, 2\n"
"s_add_u32 s67, s67, 86528\n"
"v_add_u32 v192, s67, v8\n"
"ds_read_b32 v192, v192\n"
"s_waitcnt lgkmcnt(0)\n"
"v_add_u32 v192, v192, v9\n"
"s_mov_b32 m0, 87808\n"
"s_and_b32 exec_lo, s78, 255\n"
"s_mov_b32 exec_hi, 0\n"
"buffer_load_dword v192, s[44:47], 0 offen sc1 lds\n"
"s_mov_b64 exec, -1\n"
"s_waitcnt lgkmcnt(0)\n"
"Ln1pack32_%=:\n"
"s_nop 7\n"
"v_readlane_b32 s67, v6, s57\n"
"s_nop 0\n"
"s_mul_i32 s67, s67, s52\n"
"s_add_u32 s66, s67, s51\n"
"v_cvt_pk_bf16_f32 v160, v16, v17\n"
"v_cvt_pk_bf16_f32 v161, v18, v19\n"
"v_cvt_pk_bf16_f32 v162, v20, v21\n"
"v_cvt_pk_bf16_f32 v163, v22, v23\n"
"v_cvt_pk_bf16_f32 v164, v24, v25\n"
"v_cvt_pk_bf16_f32 v165, v26, v27\n"
"v_cvt_pk_bf16_f32 v166, v28, v29\n"
"v_cvt_pk_bf16_f32 v167, v30, v31\n"
"v_cvt_pk_bf16_f32 v168, v32, v33\n"
"v_cvt_pk_bf16_f32 v169, v34, v35\n"
"v_cvt_pk_bf16_f32 v170, v36, v37\n"
"v_cvt_pk_bf16_f32 v171, v38, v39\n"
"v_cvt_pk_bf16_f32 v172, v40, v41\n"
"v_cvt_pk_bf16_f32 v173, v42, v43\n"
"v_cvt_pk_bf16_f32 v174, v44, v45\n"
"v_cvt_pk_bf16_f32 v175, v46, v47\n"
"v_permlane32_swap_b32 v160, v162\n"
"v_permlane32_swap_b32 v161, v163\n"
"v_permlane32_swap_b32 v164, v166\n"
"v_permlane32_swap_b32 v165, v167\n"
"v_permlane32_swap_b32 v168, v170\n"
"v_permlane32_swap_b32 v169, v171\n"
"v_permlane32_swap_b32 v172, v174\n"
"v_permlane32_swap_b32 v173, v175\n"
"buffer_store_dwordx4 v[160:163], v11, s[40:43], s66 offen sc1\n"
"buffer_store_dwordx4 v[164:167], v12, s[40:43], s66 offen sc1\n"
"buffer_store_dwordx4 v[168:171], v13, s[40:43], s66 offen sc1\n"
"buffer_store_dwordx4 v[172:175], v14, s[40:43], s66 offen sc1\n"
"s_waitcnt vmcnt(4) lgkmcnt(0)\n"
"s_barrier\n"
"s_cmp_lt_u32 s62, s48\n"
"s_cbranch_scc0 Ln3pack32_%=\n"
"s_sub_u32 s67, 87808, 0\n"
"v_lshrrev_b32 v192, 2, v0\n"
"v_add_u32 v192, s67, v192\n"
"ds_read_b32 v193, v192\n"
"s_waitcnt lgkmcnt(0)\n"
"v_cmp_eq_u32_e64 s[80:81], s97, v193\n"
"s_nop 1\n"
"s_sub_u32 s68, s48, s62\n"
"s_min_u32 s68, s68, 2\n"
"s_mov_b32 s70, 0\n"
"Levnpack32_%=:\n"
"s_cmp_ge_u32 s70, s68\n"
"s_cbranch_scc1 Lexnpack32_%=\n"
"s_and_b32 s69, s80, 15\n"
"s_cmp_eq_u32 s69, 15\n"
"s_cbranch_scc0 Lexnpack32_%=\n"
"s_lshr_b64 s[80:81], s[80:81], 4\n"
"s_add_u32 s70, s70, 1\n"
"s_branch Levnpack32_%=\n"
"Lexnpack32_%=:\n"
"s_add_u32 s58, s62, s70\n"
"s_cmp_lt_u32 s62, s58\n"
"s_cbranch_scc0 Ln2pack32_%=\n"
"v_readlane_b32 s67, v6, s62\n"
"s_nop 0\n"
"s_mul_i32 s67, s67, s50\n"
"s_add_u32 s65, s67, s49\n"
"s_sub_u32 s64, 32768, s77\n"
"s_lshl_b32 s68, %0, 10\n"
"s_add_u32 s64, s64, s68\n"
"s_add_u32 m0, s64, 0\n"
"s_add_u32 s70, s65, 0\n"
"buffer_load_dwordx4 v2, s[36:39], s70 offen sc1 lds\n"
"s_add_u32 m0, s64, 4096\n"
"s_add_u32 s70, s65, 4096\n"
"buffer_load_dwordx4 v2, s[36:39], s70 offen sc1 lds\n"
"s_add_u32 m0, s64, 8192\n"
"s_add_u32 s70, s65, 8192\n"
"buffer_load_dwordx4 v2, s[36:39], s70 offen sc1 lds\n"
"s_add_u32 m0, s64, 12288\n"
"s_add_u32 s70, s65, 12288\n"
"buffer_load_dwordx4 v2, s[36:39], s70 offen sc1 lds\n"
"s_add_u32 m0, s64, 16384\n"
"s_add_u32 s70, s65, 16384\n"
"buffer_load_dwordx4 v2, s[36:39], s70 offen sc1 lds\n"
"s_add_u32 m0, s64, 20480\n"
"s_add_u32 s70, s65, 20480\n"
"buffer_load_dwordx4 v2, s[36:39], s70 offen sc1 lds\n"
"s_add_u32 m0, s64, 24576\n"
"s_add_u32 s70, s65, 24576\n"
"buffer_load_dwordx4 v2, s[36:39], s70 offen sc1 lds\n"
"s_add_u32 m0, s64, 28672\n"
"s_add_u32 s70, s65, 28672\n"
"buffer_load_dwordx4 v2, s[36:39], s70 offen sc1 lds\n"
"s_waitcnt vmcnt(8) lgkmcnt(0)\n"
"s_barrier\n"
"s_cmp_lt_i32 s57, 0\n"
"s_cbranch_scc1 Lnsn1pack32_%=\n"
"v_readlane_b32 s67, v6, s57\n"
"s_nop 0\n"
"s_mul_i32 s67, s67, s54\n"
"s_add_u32 s67, s67, s53\n"
"buffer_store_dword v4, v3, s[44:47], s67 offen sc1\n"
"Lnsn1pack32_%=:\n"
"s_waitcnt vmcnt(1) lgkmcnt(0)\n"
"s_barrier\n"
"s_branch Ln4pack32_%=\n"
"Ln2pack32_%=:\n"
"s_lshl_b32 s67, s62, 2\n"
"s_add_u32 s67, s67, 86528\n"
"v_add_u32 v192, s67, v8\n"
"ds_read_b32 v192, v192\n"
"s_waitcnt lgkmcnt(0)\n"
"v_add_u32 v192, v192, v9\n"
"s_mov_b32 m0, 87296\n"
"s_and_b32 exec_lo, s78, 255\n"
"s_mov_b32 exec_hi, 0\n"
"buffer_load_dword v192, s[44:47], 0 offen sc1 lds\n"
"s_mov_b64 exec, -1\n"
"s_waitcnt vmcnt(0) lgkmcnt(0)\n"
"s_barrier\n"
"s_cmp_lt_i32 s57, 0\n"
"s_cbranch_scc1 Lnsn2pack32_%=\n"
"v_readlane_b32 s67, v6, s57\n"
"s_nop 0\n"
"s_mul_i32 s67, s67, s54\n"
"s_add_u32 s67, s67, s53\n"
"buffer_store_dword v4, v3, s[44:47], s67 offen sc1\n"
"Lnsn2pack32_%=:\n"
"s_sub_u32 s67, 87296, 0\n"
"v_lshrrev_b32 v192, 2, v0\n"
"v_add_u32 v192, s67, v192\n"
"ds_read_b32 v193, v192\n"
"s_waitcnt lgkmcnt(0)\n"
"v_cmp_eq_u32_e64 s[80:81], s97, v193\n"
"s_nop 1\n"
"s_sub_u32 s68, s48, s62\n"
"s_min_u32 s68, s68, 2\n"
"s_mov_b32 s70, 0\n"
"Levn2pack32_%=:\n"
"s_cmp_ge_u32 s70, s68\n"
"s_cbranch_scc1 Lexn2pack32_%=\n"
"s_and_b32 s69, s80, 15\n"
"s_cmp_eq_u32 s69, 15\n"
"s_cbranch_scc0 Lexn2pack32_%=\n"
"s_lshr_b64 s[80:81], s[80:81], 4\n"
"s_add_u32 s70, s70, 1\n"
"s_branch Levn2pack32_%=\n"
"Lexn2pack32_%=:\n"
"s_add_u32 s58, s62, s70\n"
"s_cmp_lt_u32 s62, s58\n"
"s_cbranch_scc0 Ln5pack32_%=\n"
"v_readlane_b32 s67, v6, s62\n"
"s_nop 0\n"
"s_mul_i32 s67, s67, s50\n"
"s_add_u32 s65, s67, s49\n"
"s_sub_u32 s64, 32768, s77\n"
"s_lshl_b32 s68, %0, 10\n"
"s_add_u32 s64, s64, s68\n"
"s_add_u32 m0, s64, 0\n"
"s_add_u32 s70, s65, 0\n"
"buffer_load_dwordx4 v2, s[36:39], s70 offen sc1 lds\n"
"s_add_u32 m0, s64, 4096\n"
"s_add_u32 s70, s65, 4096\n"
"buffer_load_dwordx4 v2, s[36:39], s70 offen sc1 lds\n"
"s_add_u32 m0, s64, 8192\n"
"s_add_u32 s70, s65, 8192\n"
"buffer_load_dwordx4 v2, s[36:39], s70 offen sc1 lds\n"
"s_add_u32 m0, s64, 12288\n"
"s_add_u32 s70, s65, 12288\n"
"buffer_load_dwordx4 v2, s[36:39], s70 offen sc1 lds\n"
"s_add_u32 m0, s64, 16384\n"
"s_add_u32 s70, s65, 16384\n"
"buffer_load_dwordx4 v2, s[36:39], s70 offen sc1 lds\n"
"s_add_u32 m0, s64, 20480\n"
"s_add_u32 s70, s65, 20480\n"
"buffer_load_dwordx4 v2, s[36:39], s70 offen sc1 lds\n"
"s_add_u32 m0, s64, 24576\n"
"s_add_u32 s70, s65, 24576\n"
"buffer_load_dwordx4 v2, s[36:39], s70 offen sc1 lds\n"
"s_add_u32 m0, s64, 28672\n"
"s_add_u32 s70, s65, 28672\n"
"buffer_load_dwordx4 v2, s[36:39], s70 offen sc1 lds\n"
"s_waitcnt vmcnt(0) lgkmcnt(0)\n"
"s_barrier\n"
"s_branch Ln4pack32_%=\n"
"Ln5pack32_%=:\n"
"s_cmp_eq_u32 %0, 0\n"
"s_cbranch_scc0 Lbwnpack32_%=\n"
"s_cmp_lg_u32 s75, 0\n"
"s_cbranch_scc1 Lbwnpack32_%=\n"
"v_readlane_b32 s72, v7, s62\n"
"s_nop 3\n"
"s_mov_b32 s71, 0\n"
"s_cmp_lt_i32 s74, 0\n"
"s_cbranch_scc1 Lplnpack32_%=\n"
"s_sub_u32 s67, s62, s74\n"
"s_cmp_gt_u32 s67, 3\n"
"s_cbranch_scc1 Lplnpack32_%=\n"
"s_add_u32 s68, s62, 2\n"
"s_cmp_ge_u32 s68, s48\n"
"s_cbranch_scc1 Lplnpack32_%=\n"
"s_mul_i32 s67, s67, 240\n"
"s_add_u32 s67, s67, s73\n"
"s_sub_u32 s67, s67, 40\n"
"s_mov_b32 s76, 0\n"
"Lslnpack32_%=:\n"
"s_memrealtime s[84:85]\n"
"s_waitcnt lgkmcnt(0)\n"
"s_sub_u32 s68, s84, s67\n"
"s_cmp_ge_i32 s68, 0\n"
"s_cbranch_scc1 Lplnpack32_%=\n"
"s_sleep 2\n"
"s_mov_b32 s71, 1\n"
"s_add_u32 s76, s76, 1\n"
"s_cmp_lt_u32 s76, 32768\n"
"s_cbranch_scc1 Lslnpack32_%=\n"
"Lplnpack32_%=:\n"
"s_mov_b32 s76, 0\n"
"Lppnpack32_%=:\n"
"buffer_load_dword v192, v5, s[44:47], s72 offen sc1\n"
"s_waitcnt vmcnt(0)\n"
"v_cmp_ne_u32_e64 s[80:81], s97, v192\n"
"s_nop 1\n"
"s_and_b64 s[80:81], s[80:81], s[82:83]\n"
"s_cbranch_scc0 Lrdnpack32_%=\n"
"s_sleep 3\n"
"s_add_u32 s76, s76, 1\n"
"s_cmp_lt_u32 s76, 32768\n"
"s_cbranch_scc1 Lppnpack32_%=\n"
"s_mov_b32 s75, 1\n"
"v_mov_b32 v192, 16384\n"
"v_mov_b32 v193, 7000\n"
"buffer_store_dword v193, v192, s[44:47], 0 offen sc1\n"
"Lrdnpack32_%=:\n"
"s_mov_b32 s74, -1\n"
"s_cmp_eq_u32 s76, 0\n"
"s_cbranch_scc1 Lbwnpack32_%=\n"
"s_memrealtime s[84:85]\n"
"s_waitcnt lgkmcnt(0)\n"
"s_mov_b32 s73, s84\n"
"s_mov_b32 s74, s62\n"
"Lbwnpack32_%=:\n"
"s_waitcnt lgkmcnt(0)\n"
"s_barrier\n"
"v_readlane_b32 s67, v6, s62\n"
"s_nop 0\n"
"s_mul_i32 s67, s67, s50\n"
"s_add_u32 s65, s67, s49\n"
"s_sub_u32 s64, 32768, s77\n"
"s_lshl_b32 s68, %0, 10\n"
"s_add_u32 s64, s64, s68\n"
"s_add_u32 m0, s64, 0\n"
"s_add_u32 s70, s65, 0\n"
"buffer_load_dwordx4 v2, s[36:39], s70 offen sc1 lds\n"
"s_add_u32 m0, s64, 4096\n"
"s_add_u32 s70, s65, 4096\n"
"buffer_load_dwordx4 v2, s[36:39], s70 offen sc1 lds\n"
"s_add_u32 m0, s64, 8192\n"
"s_add_u32 s70, s65, 8192\n"
"buffer_load_dwordx4 v2, s[36:39], s70 offen sc1 lds\n"
"s_add_u32 m0, s64, 12288\n"
"s_add_u32 s70, s65, 12288\n"
"buffer_load_dwordx4 v2, s[36:39], s70 offen sc1 lds\n"
"s_add_u32 m0, s64, 16384\n"
"s_add_u32 s70, s65, 16384\n"
"buffer_load_dwordx4 v2, s[36:39], s70 offen sc1 lds\n"
"s_add_u32 m0, s64, 20480\n"
"s_add_u32 s70, s65, 20480\n"
"buffer_load_dwordx4 v2, s[36:39], s70 offen sc1 lds\n"
"s_add_u32 m0, s64, 24576\n"
"s_add_u32 s70, s65, 24576\n"
"buffer_load_dwordx4 v2, s[36:39], s70 offen sc1 lds\n"
"s_add_u32 m0, s64, 28672\n"
"s_add_u32 s70, s65, 28672\n"
"buffer_load_dwordx4 v2, s[36:39], s70 offen sc1 lds\n"
"s_waitcnt vmcnt(0) lgkmcnt(0)\n"
"s_barrier\n"
"s_add_u32 s58, s62, 1\n"
"Ln4pack32_%=:\n"
"s_sub_u32 s77, 32768, s77\n"
"v_add_u32 v1, s77, v0\n"
"ds_read_b128 v[80:83], v1\n"
"ds_read_b128 v[84:87], v1 offset:1024\n"
"ds_read_b128 v[88:91], v1 offset:2048\n"
"ds_read_b128 v[92:95], v1 offset:3072\n"
"s_add_u32 s57, s57, 1\n"
"s_branch Ltoppack32_%=\n"
"Ln3pack32_%=:\n"
"s_waitcnt vmcnt(0) lgkmcnt(0)\n"
"s_barrier\n"
"s_cmp_lt_i32 s57, 0\n"
"s_cbranch_scc1 Lnsn3pack32_%=\n"
"v_readlane_b32 s67, v6, s57\n"
"s_nop 0\n"
"s_mul_i32 s67, s67, s54\n"
"s_add_u32 s67, s67, s53\n"
"buffer_store_dword v4, v3, s[44:47], s67 offen sc1\n"
"Lnsn3pack32_%=:\n"
"s_waitcnt vmcnt(0) lgkmcnt(0)\n"
"s_barrier\n"
  GEMM_ASM_END
"v0","v1","v2","v3","v4","v5","v6","v7","v8","v9","v10","v11","v12","v13","v14","v15","v16","v17","v18","v19","v20","v21","v22","v23","v24","v25","v26","v27","v28","v29","v30","v31","v32","v33","v34","v35","v36","v37","v38","v39","v40","v41","v42","v43","v44","v45","v46","v47","v48","v49","v50","v51","v52","v53","v54","v55","v56","v57","v58","v59","v60","v61","v62","v63","v64","v65","v66","v67","v68","v69","v70","v71","v72","v73","v74","v75","v76","v77","v78","v79","v80","v81","v82","v83","v84","v85","v86","v87","v88","v89","v90","v91","v92","v93","v94","v95","v96","v97","v98","v99","v100","v101","v102","v103","v104","v105","v106","v107","v108","v109","v110","v111","v112","v113","v114","v115","v116","v117","v118","v119","v120","v121","v122","v123","v124","v125","v126","v128","v129","v130","v131","v132","v133","v134","v135","v136","v137","v138","v139","v140","v141","v142","v143","v144","v145","v146","v147","v148","v149","v150","v151","v152","v153","v154","v155","v156","v157","v158","v159","v160","v161","v162","v163","v164","v165","v166","v167","v168","v169","v170","v171","v172","v173","v174","v175","v176","v177","v178","v179","v180","v181","v182","v183","v184","v185","v186","v187","v188","v189","v190","v191","v192","v193","v194","v195","v196","v197","v198","v199","v200","v201","v202","v203","v204","v205","v206","v207","v208","v209","v210","v211","v212","v213","v214","v215","v216","v217","v218","v219","v220","v221","v222","v223","v224","v225","v226","v227","v228","v229","v230","v231","v232","v233","v234","v235","v236","v237","v238","v239","v240","v241","v242","v243","v244","v245","v246","v247","v248","v249","v250","v251","v252","v253","v254","v255","a0","a1","a2","a3","a4","a5","a6","a7","a8","a9","a10","a11","a12","a13","a14","a15","a16","a17","a18","a19","a20","a21","a22","a23","a24","a25","a26","a27","a28","a29","a30","a31","a32","a33","a34","a35","a36","a37","a38","a39","a40","a41","a42","a43","a44","a45","a46","a47","a48","a49","a50","a51","a52","a53","a54","a55","a56","a57","a58","a59","a60","a61","a62","a63","a64","a65","a66","a67","a68","a69","a70","a71","a72","a73","a74","a75","a76","a77","a78","a79","a80","a81","a82","a83","a84","a85","a86","a87","a88","a89","a90","a91","a92","a93","a94","a95","a96","a97","a98","a99","a100","a101","a102","a103","a104","a105","a106","a107","a108","a109","a110","a111","a112","a113","a114","a115","a116","a117","a118","a119","a120","a121","a122","a123","a124","a125","a126","a127","a128","a129","a130","a131","a132","a133","a134","a135","a136","a137","a138","a139","a140","a141","a142","a143","a144","a145","a146","a147","a148","a149","a150","a151","a152","a153","a154","a155","a156","a157","a158","a159","a160","a161","a162","a163","a164","a165","a166","a167","a168","a169","a170","a171","a172","a173","a174","a175","a176","a177","a178","a179","a180","a181","a182","a183","a184","a185","a186","a187","a188","a189","a190","a191","a192","a193","a194","a195","a196","a197","a198","a199","a200","a201","a202","a203","a204","a205","a206","a207","a208","a209","a210","a211","a212","a213","a214","a215","a216","a217","a218","a219","a220","a221","a222","a223","a224","a225","a226","a227","a228","a229","a230","a231","a232","a233","a234","a235","a236","a237","a238","a239","a240","a241","a242","a243","a244","a245","a246","a247","a248","a249","a250","a251","a252","a253","a254","a255","s36","s37","s38","s39","s40","s41","s42","s43","s44","s45","s46","s47","s48","s49","s50","s51","s52","s53","s54","s55","s56","s57","s58","s59","s60","s61","s62","s63","s64","s65","s66","s67","s68","s69","s70","s71","s72","s73","s74","s75","s76","s77","s78","s79","s80","s81","s82","s83","s84","s85","s86","s87","s88","s89","s90","s91","s92","s93","s94","s95","s96","s97","s98","s99","vcc","scc","memory"
  );
}
DEVI void role_f1(Ctx& c,CArgs& a,int f){
  c.lane=fresh_lane();
  const int lane=c.lane, wave=c.wave, tok=lane&31;
  GP p; p.ri=a.ws+O_H2; p.ri_n=SZ_T576; p.ro=a.ws+O_ACT; p.ro_n=SZ_ACT;
  p.n=64; p.inb=0; p.ins=36864; p.outb=0; p.outs=294912; p.sigb=FL_ACT+f*128; p.sigs=36*128; p.wstr=16*9216; p.slot0=64;
  const int ch=w_ch(lane,wave); const int col=f*128+(ch>>2)*16+(ch&1)*8;
  p.p0=(const char*)(((ch>>1)&1)? a.wu : a.wg)+(size_t)w_row(lane,wave)*9216+col*2; p.p1=p.p0+128;
  p.dv=(tok>>2)*4608+(lane>>5)*64+(tok&3)*16+wave*128;
  p.sv[0]=lane*16+(f*16+wave*2)*512; p.sv[1]=lane*16+(f*16+8+wave*2)*512; p.sv[2]=OOBO; p.sv[3]=OOBO;
  p.sgv=(wave==1 && lane==0)? 0u : OOBO; p.segv=lane; p.tabv=FL_H2+lane*8*128; p.nf=8; p.pd=7;
  gemm_params(c,a,p);
  GEMM_ASM_BEGIN
"v_mbcnt_lo_u32_b32 v10, -1, 0\n"
"v_mbcnt_hi_u32_b32 v10, -1, v10\n"
"v_lshlrev_b32 v0, 4, v10\n"
"v_lshlrev_b32 v192, 2, v10\n"
"v_add_u32 v192, 87040, v192\n"
"ds_read_b32 v193, v192\n"
"s_lshl_b32 s67, %0, 12\n"
"s_add_u32 s67, s67, 90112\n"
"v_lshlrev_b32 v194, 6, v10\n"
"v_add_u32 v194, s67, v194\n"
"ds_read_b128 v[160:163], v194\n"
"ds_read_b128 v[164:167], v194 offset:16\n"
"ds_read_b128 v[168:171], v194 offset:32\n"
"ds_read_b128 v[172:175], v194 offset:48\n"
"s_waitcnt lgkmcnt(0)\n"
"v_readlane_b32 s36, v193, 0\n"
"v_readlane_b32 s37, v193, 1\n"
"v_readlane_b32 s38, v193, 2\n"
"v_readlane_b32 s40, v193, 3\n"
"v_readlane_b32 s41, v193, 4\n"
"v_readlane_b32 s42, v193, 5\n"
"v_readlane_b32 s44, v193, 6\n"
"v_readlane_b32 s45, v193, 7\n"
"v_readlane_b32 s46, v193, 8\n"
"v_readlane_b32 s48, v193, 9\n"
"v_readlane_b32 s49, v193, 10\n"
"v_readlane_b32 s50, v193, 11\n"
"v_readlane_b32 s51, v193, 12\n"
"v_readlane_b32 s52, v193, 13\n"
"v_readlane_b32 s53, v193, 14\n"
"v_readlane_b32 s54, v193, 15\n"
"v_readlane_b32 s55, v193, 16\n"
"v_readlane_b32 s56, v193, 17\n"
"v_readlane_b32 s97, v193, 18\n"
"v_readlane_b32 s98, v193, 19\n"
"s_nop 1\n"
"s_and_b32 s37, s37, 0xffff\n"
"s_mov_b32 s39, 0x20000\n"
"s_and_b32 s41, s41, 0xffff\n"
"s_mov_b32 s43, 0x20000\n"
"s_and_b32 s45, s45, 0xffff\n"
"s_mov_b32 s47, 0x20000\n"
"v_mov_b32 v176, v160\n"
"v_mov_b32 v177, v161\n"
"v_mov_b32 v178, v162\n"
"v_mov_b32 v179, v163\n"
"v_mov_b32 v2, v164\n"
"v_mov_b32 v11, v165\n"
"v_mov_b32 v12, v166\n"
"v_mov_b32 v13, v167\n"
"v_mov_b32 v14, v168\n"
"v_mov_b32 v3, v169\n"
"v_mov_b32 v8, v170\n"
"v_mov_b32 v9, v171\n"
"v_mov_b32 v180, v172\n"
"v_mov_b32 v181, v173\n"
"v_mov_b32 v6, v174\n"
"v_mov_b32 v7, v175\n"
"v_mov_b32 v4, 1\n"
"v_mov_b32 v15, 0x7ffffff0\n"
"v_lshlrev_b32 v5, 7, v10\n"
"v_cmp_gt_u32_e32 vcc, 8, v10\n"
"s_nop 1\n"
"v_cndmask_b32_e32 v5, v15, v5, vcc\n"
"s_mov_b64 s[82:83], 255\n"
"s_mov_b64 s[78:79], 0\n"
"s_cmp_eq_u32 %0, 2\n"
"s_cbranch_scc0 Lnxsilu36_%=\n"
"s_mov_b32 s78, 0xffffffff\n"
"s_mov_b32 s79, 0xffffff\n"
"Lnxsilu36_%=:\n"
"s_mov_b32 s75, 0\n"
"s_mov_b32 s74, -1\n"
"s_mov_b32 s73, 0\n"
"s_barrier\n"
"v_mov_b32 v200, 20480\n"
"buffer_load_dword v200, v200, s[44:47], 0 offen sc1\n"
"v_add_u32 v182, 0x10000, v180\n"
"v_add_u32 v183, 0x10000, v181\n"
"v_add_u32 v184, 0x20000, v180\n"
"v_add_u32 v185, 0x20000, v181\n"
"s_lshl_b32 s69, %0, 10\n"
"s_mov_b32 s96, 0\n"
"s_add_u32 m0, s69, 0\n"
"s_nop 0\n"
"global_load_lds_dwordx4 v[176:177], off\n"
"v_add_co_u32_e32 v176, vcc, s55, v176\n"
"v_addc_co_u32_e32 v177, vcc, 0, v177, vcc\n"
"s_add_u32 m0, s69, 4096\n"
"s_nop 0\n"
"global_load_lds_dwordx4 v[176:177], off\n"
"v_add_co_u32_e32 v176, vcc, s55, v176\n"
"v_addc_co_u32_e32 v177, vcc, 0, v177, vcc\n"
"s_add_u32 m0, s69, 8192\n"
"s_nop 0\n"
"global_load_lds_dwordx4 v[176:177], off\n"
"v_add_co_u32_e32 v176, vcc, s55, v176\n"
"v_addc_co_u32_e32 v177, vcc, 0, v177, vcc\n"
"s_add_u32 m0, s69, 12288\n"
"s_nop 0\n"
"global_load_lds_dwordx4 v[176:177], off\n"
"v_add_co_u32_e32 v176, vcc, s55, v176\n"
"v_addc_co_u32_e32 v177, vcc, 0, v177, vcc\n"
"s_add_u32 m0, s69, 16384\n"
"s_nop 0\n"
"global_load_lds_dwordx4 v[176:177], off\n"
"v_add_co_u32_e32 v176, vcc, s55, v176\n"
"v_addc_co_u32_e32 v177, vcc, 0, v177, vcc\n"
"s_add_u32 m0, s69, 20480\n"
"s_nop 0\n"
"global_load_lds_dwordx4 v[176:177], off\n"
"v_add_co_u32_e32 v176, vcc, s55, v176\n"
"v_addc_co_u32_e32 v177, vcc, 0, v177, vcc\n"
"s_add_u32 m0, s69, 24576\n"
"s_nop 0\n"
"global_load_lds_dwordx4 v[176:177], off\n"
"v_add_co_u32_e32 v176, vcc, s55, v176\n"
"v_addc_co_u32_e32 v177, vcc, 0, v177, vcc\n"
"s_add_u32 m0, s69, 28672\n"
"s_nop 0\n"
"global_load_lds_dwordx4 v[176:177], off\n"
"v_add_co_u32_e32 v176, vcc, s55, v176\n"
"v_addc_co_u32_e32 v177, vcc, 0, v177, vcc\n"
"s_add_u32 m0, s69, 32768\n"
"s_nop 0\n"
"global_load_lds_dwordx4 v[176:177], off\n"
"v_add_co_u32_e32 v176, vcc, s55, v176\n"
"v_addc_co_u32_e32 v177, vcc, 0, v177, vcc\n"
"s_add_u32 m0, s69, 36864\n"
"s_nop 0\n"
"global_load_lds_dwordx4 v[176:177], off\n"
"v_add_co_u32_e32 v176, vcc, s55, v176\n"
"v_addc_co_u32_e32 v177, vcc, 0, v177, vcc\n"
"s_add_u32 m0, s69, 40960\n"
"s_nop 0\n"
"global_load_lds_dwordx4 v[176:177], off\n"
"v_add_co_u32_e32 v176, vcc, s55, v176\n"
"v_addc_co_u32_e32 v177, vcc, 0, v177, vcc\n"
"s_add_u32 m0, s69, 45056\n"
"s_nop 0\n"
"global_load_lds_dwordx4 v[176:177], off\n"
"v_add_co_u32_e32 v176, vcc, s55, v176\n"
"v_addc_co_u32_e32 v177, vcc, 0, v177, vcc\n"
"s_add_u32 m0, s69, 49152\n"
"s_nop 0\n"
"global_load_lds_dwordx4 v[176:177], off\n"
"v_add_co_u32_e32 v176, vcc, s55, v176\n"
"v_addc_co_u32_e32 v177, vcc, 0, v177, vcc\n"
"s_add_u32 m0, s69, 53248\n"
"s_nop 0\n"
"global_load_lds_dwordx4 v[176:177], off\n"
"v_add_co_u32_e32 v176, vcc, s55, v176\n"
"v_addc_co_u32_e32 v177, vcc, 0, v177, vcc\n"
"s_add_u32 m0, s69, 57344\n"
"s_nop 0\n"
"global_load_lds_dwordx4 v[176:177], off\n"
"v_add_co_u32_e32 v176, vcc, s55, v176\n"
"v_addc_co_u32_e32 v177, vcc, 0, v177, vcc\n"
"s_add_u32 m0, s69, 61440\n"
"s_nop 0\n"
"global_load_lds_dwordx4 v[176:177], off\n"
"v_add_co_u32_e32 v176, vcc, s55, v176\n"
"v_addc_co_u32_e32 v177, vcc, 0, v177, vcc\n"
"s_add_u32 m0, s69, 65536\n"
"s_nop 0\n"
"global_load_lds_dwordx4 v[176:177], off\n"
"v_add_co_u32_e32 v176, vcc, s55, v176\n"
"v_addc_co_u32_e32 v177, vcc, 0, v177, vcc\n"
"s_add_u32 m0, s69, 69632\n"
"s_nop 0\n"
"global_load_lds_dwordx4 v[176:177], off\n"
"v_add_co_u32_e32 v176, vcc, s55, v176\n"
"v_addc_co_u32_e32 v177, vcc, 0, v177, vcc\n"
"s_add_u32 m0, s69, 73728\n"
"s_nop 0\n"
"global_load_lds_dwordx4 v[176:177], off\n"
"v_add_co_u32_e32 v176, vcc, s55, v176\n"
"v_addc_co_u32_e32 v177, vcc, 0, v177, vcc\n"
"s_add_u32 m0, s69, 77824\n"
"s_nop 0\n"
"global_load_lds_dwordx4 v[176:177], off\n"
"v_add_co_u32_e32 v176, vcc, s55, v176\n"
"v_addc_co_u32_e32 v177, vcc, 0, v177, vcc\n"
"s_add_u32 m0, s69, 81920\n"
"s_nop 0\n"
"global_load_lds_dwordx4 v[176:177], off\n"
"v_add_co_u32_e32 v176, vcc, s55, v176\n"
"v_addc_co_u32_e32 v177, vcc, 0, v177, vcc\n"
"s_add_u32 m0, s69, 86016\n"
"s_nop 0\n"
"global_load_lds_dwordx4 v[176:177], off\n"
"v_add_co_u32_e32 v176, vcc, s55, v176\n"
"v_addc_co_u32_e32 v177, vcc, 0, v177, vcc\n"
"s_add_u32 m0, s69, 90112\n"
"s_nop 0\n"
"global_load_lds_dwordx4 v[176:177], off\n"
"v_add_co_u32_e32 v176, vcc, s55, v176\n"
"v_addc_co_u32_e32 v177, vcc, 0, v177, vcc\n"
"s_add_u32 m0, s69, 94208\n"
"s_nop 0\n"
"global_load_lds_dwordx4 v[176:177], off\n"
"v_add_co_u32_e32 v176, vcc, s55, v176\n"
"v_addc_co_u32_e32 v177, vcc, 0, v177, vcc\n"
"s_add_u32 m0, s69, 98304\n"
"s_nop 0\n"
"global_load_lds_dwordx4 v[176:177], off\n"
"v_add_co_u32_e32 v176, vcc, s55, v176\n"
"v_addc_co_u32_e32 v177, vcc, 0, v177, vcc\n"
"s_add_u32 m0, s69, 102400\n"
"s_nop 0\n"
"global_load_lds_dwordx4 v[176:177], off\n"
"v_add_co_u32_e32 v176, vcc, s55, v176\n"
"v_addc_co_u32_e32 v177, vcc, 0, v177, vcc\n"
"s_add_u32 m0, s69, 106496\n"
"s_nop 0\n"
"global_load_lds_dwordx4 v[176:177], off\n"
"v_add_co_u32_e32 v176, vcc, s55, v176\n"
"v_addc_co_u32_e32 v177, vcc, 0, v177, vcc\n"
"s_add_u32 m0, s69, 110592\n"
"s_nop 0\n"
"global_load_lds_dwordx4 v[176:177], off\n"
"v_add_co_u32_e32 v176, vcc, s55, v176\n"
"v_addc_co_u32_e32 v177, vcc, 0, v177, vcc\n"
"s_add_u32 m0, s69, 114688\n"
"s_nop 0\n"
"global_load_lds_dwordx4 v[176:177], off\n"
"v_add_co_u32_e32 v176, vcc, s55, v176\n"
"v_addc_co_u32_e32 v177, vcc, 0, v177, vcc\n"
"s_add_u32 m0, s69, 118784\n"
"s_nop 0\n"
"global_load_lds_dwordx4 v[176:177], off\n"
"v_add_co_u32_e32 v176, vcc, s55, v176\n"
"v_addc_co_u32_e32 v177, vcc, 0, v177, vcc\n"
"s_add_u32 m0, s69, 122880\n"
"s_nop 0\n"
"global_load_lds_dwordx4 v[176:177], off\n"
"v_add_co_u32_e32 v176, vcc, s55, v176\n"
"v_addc_co_u32_e32 v177, vcc, 0, v177, vcc\n"
"s_add_u32 m0, s69, 126976\n"
"s_nop 0\n"
"global_load_lds_dwordx4 v[176:177], off\n"
"v_add_co_u32_e32 v176, vcc, s55, v176\n"
"v_addc_co_u32_e32 v177, vcc, 0, v177, vcc\n"
"s_add_u32 m0, s69, 131072\n"
"s_nop 0\n"
"global_load_lds_dwordx4 v[176:177], off\n"
"v_add_co_u32_e32 v176, vcc, s55, v176\n"
"v_addc_co_u32_e32 v177, vcc, 0, v177, vcc\n"
"s_add_u32 m0, s69, 135168\n"
"s_nop 0\n"
"global_load_lds_dwordx4 v[176:177], off\n"
"v_add_co_u32_e32 v176, vcc, s55, v176\n"
"v_addc_co_u32_e32 v177, vcc, 0, v177, vcc\n"
"s_add_u32 m0, s69, 139264\n"
"s_nop 0\n"
"global_load_lds_dwordx4 v[176:177], off\n"
"v_add_co_u32_e32 v176, vcc, s55, v176\n"
"v_addc_co_u32_e32 v177, vcc, 0, v177, vcc\n"
"s_add_u32 m0, s69, 143360\n"
"s_nop 0\n"
"global_load_lds_dwordx4 v[176:177], off\n"
"s_waitcnt vmcnt(27)\n"
"s_barrier\n"
"v_readfirstlane_b32 s90, v200\n"
"s_nop 3\n"
"s_add_u32 s90, s90, 1\n"
"s_cmp_eq_u32 s98, 1\n"
"s_cselect_b32 s97, s90, s97\n"
"v_mov_b32 v4, s97\n"
"v_mov_b32 v201, 163836\n"
"ds_write_b32 v201, v4\n"
"ds_read_b64_tr_b16 v[128:129], v180\n"
"ds_read_b64_tr_b16 v[130:131], v181\n"
"ds_read_b64_tr_b16 v[132:133], v180 offset:4096\n"
"ds_read_b64_tr_b16 v[134:135], v181 offset:4096\n"
"ds_read_b64_tr_b16 v[136:137], v180 offset:8192\n"
"ds_read_b64_tr_b16 v[138:139], v181 offset:8192\n"
"ds_read_b64_tr_b16 v[140:141], v180 offset:12288\n"
"ds_read_b64_tr_b16 v[142:143], v181 offset:12288\n"
"ds_read_b64_tr_b16 v[144:145], v180 offset:16384\n"
"ds_read_b64_tr_b16 v[146:147], v181 offset:16384\n"
"ds_read_b64_tr_b16 v[148:149], v180 offset:20480\n"
"ds_read_b64_tr_b16 v[150:151], v181 offset:20480\n"
"ds_read_b64_tr_b16 v[152:153], v180 offset:24576\n"
"ds_read_b64_tr_b16 v[154:155], v181 offset:24576\n"
"s_waitcnt lgkmcnt(8)\n"
"ds_read_b64_tr_b16 v[156:157], v180 offset:28672\n"
"ds_read_b64_tr_b16 v[158:159], v181 offset:28672\n"
"ds_read_b64_tr_b16 a[144:145], v180 offset:32768\n"
"ds_read_b64_tr_b16 a[146:147], v181 offset:32768\n"
"s_waitcnt lgkmcnt(0)\n"
"s_barrier\n"
"s_add_u32 m0, s69, 0\n"
"s_nop 0\n"
"global_load_lds_dwordx4 v[178:179], off\n"
"v_add_co_u32_e32 v178, vcc, s55, v178\n"
"v_addc_co_u32_e32 v179, vcc, 0, v179, vcc\n"
"s_add_u32 m0, s69, 4096\n"
"s_nop 0\n"
"global_load_lds_dwordx4 v[178:179], off\n"
"v_add_co_u32_e32 v178, vcc, s55, v178\n"
"v_addc_co_u32_e32 v179, vcc, 0, v179, vcc\n"
"s_add_u32 m0, s69, 8192\n"
"s_nop 0\n"
"global_load_lds_dwordx4 v[178:179], off\n"
"v_add_co_u32_e32 v178, vcc, s55, v178\n"
"v_addc_co_u32_e32 v179, vcc, 0, v179, vcc\n"
"s_add_u32 m0, s69, 12288\n"
"s_nop 0\n"
"global_load_lds_dwordx4 v[178:179], off\n"
"v_add_co_u32_e32 v178, vcc, s55, v178\n"
"v_addc_co_u32_e32 v179, vcc, 0, v179, vcc\n"
"s_add_u32 m0, s69, 16384\n"
"s_nop 0\n"
"global_load_lds_dwordx4 v[178:179], off\n"
"v_add_co_u32_e32 v178, vcc, s55, v178\n"
"v_addc_co_u32_e32 v179, vcc, 0, v179, vcc\n"
"s_add_u32 m0, s69, 20480\n"
"s_nop 0\n"
"global_load_lds_dwordx4 v[178:179], off\n"
"v_add_co_u32_e32 v178, vcc, s55, v178\n"
"v_addc_co_u32_e32 v179, vcc, 0, v179, vcc\n"
"s_add_u32 m0, s69, 24576\n"
"s_nop 0\n"
"global_load_lds_dwordx4 v[178:179], off\n"
"v_add_co_u32_e32 v178, vcc, s55, v178\n"
"v_addc_co_u32_e32 v179, vcc, 0, v179, vcc\n"
"s_add_u32 m0, s69, 28672\n"
"s_nop 0\n"
"global_load_lds_dwordx4 v[178:179], off\n"
"v_add_co_u32_e32 v178, vcc, s55, v178\n"
"v_addc_co_u32_e32 v179, vcc, 0, v179, vcc\n"
"s_add_u32 m0, s69, 32768\n"
"s_nop 0\n"
"global_load_lds_dwordx4 v[178:179], off\n"
"v_add_co_u32_e32 v178, vcc, s55, v178\n"
"v_addc_co_u32_e32 v179, vcc, 0, v179, vcc\n"
"s_waitcnt vmcnt(27)\n"
"s_barrier\n"
"ds_read_b64_tr_b16 a[148:149], v180 offset:36864\n"
"ds_read_b64_tr_b16 a[150:151], v181 offset:36864\n"
"ds_read_b64_tr_b16 a[152:153], v180 offset:40960\n"
"ds_read_b64_tr_b16 a[154:155], v181 offset:40960\n"
"ds_read_b64_tr_b16 a[156:157], v180 offset:45056\n"
"ds_read_b64_tr_b16 a[158:159], v181 offset:45056\n"
"ds_read_b64_tr_b16 a[160:161], v180 offset:49152\n"
"ds_read_b64_tr_b16 a[162:163], v181 offset:49152\n"
"ds_read_b64_tr_b16 a[164:165], v180 offset:53248\n"
"ds_read_b64_tr_b16 a[166:167], v181 offset:53248\n"
"ds_read_b64_tr_b16 a[168:169], v180 offset:57344\n"
"ds_read_b64_tr_b16 a[170:171], v181 offset:57344\n"
"ds_read_b64_tr_b16 a[172:173], v180 offset:61440\n"
"ds_read_b64_tr_b16 a[174:175], v181 offset:61440\n"
"ds_read_b64_tr_b16 a[176:177], v182\n"
"s_waitcnt lgkmcnt(8)\n"
"ds_read_b64_tr_b16 a[178:179], v183\n"
"ds_read_b64_tr_b16 a[180:181], v182 offset:4096\n"
"ds_read_b64_tr_b16 a[182:183], v183 offset:4096\n"
"s_waitcnt lgkmcnt(0)\n"
"s_barrier\n"
"s_add_u32 m0, s69, 36864\n"
"s_nop 0\n"
"global_load_lds_dwordx4 v[178:179], off\n"
"v_add_co_u32_e32 v178, vcc, s55, v178\n"
"v_addc_co_u32_e32 v179, vcc, 0, v179, vcc\n"
"s_add_u32 m0, s69, 40960\n"
"s_nop 0\n"
"global_load_lds_dwordx4 v[178:179], off\n"
"v_add_co_u32_e32 v178, vcc, s55, v178\n"
"v_addc_co_u32_e32 v179, vcc, 0, v179, vcc\n"
"s_add_u32 m0, s69, 45056\n"
"s_nop 0\n"
"global_load_lds_dwordx4 v[178:179], off\n"
"v_add_co_u32_e32 v178, vcc, s55, v178\n"
"v_addc_co_u32_e32 v179, vcc, 0, v179, vcc\n"
"s_add_u32 m0, s69, 49152\n"
"s_nop 0\n"
"global_load_lds_dwordx4 v[178:179], off\n"
"v_add_co_u32_e32 v178, vcc, s55, v178\n"
"v_addc_co_u32_e32 v179, vcc, 0, v179, vcc\n"
"s_add_u32 m0, s69, 53248\n"
"s_nop 0\n"
"global_load_lds_dwordx4 v[178:179], off\n"
"v_add_co_u32_e32 v178, vcc, s55, v178\n"
"v_addc_co_u32_e32 v179, vcc, 0, v179, vcc\n"
"s_add_u32 m0, s69, 57344\n"
"s_nop 0\n"
"global_load_lds_dwordx4 v[178:179], off\n"
"v_add_co_u32_e32 v178, vcc, s55, v178\n"
"v_addc_co_u32_e32 v179, vcc, 0, v179, vcc\n"
"s_add_u32 m0, s69, 61440\n"
"s_nop 0\n"
"global_load_lds_dwordx4 v[178:179], off\n"
"v_add_co_u32_e32 v178, vcc, s55, v178\n"
"v_addc_co_u32_e32 v179, vcc, 0, v179, vcc\n"
"s_add_u32 m0, s69, 65536\n"
"s_nop 0\n"
"global_load_lds_dwordx4 v[178:179], off\n"
"v_add_co_u32_e32 v178, vcc, s55, v178\n"
"v_addc_co_u32_e32 v179, vcc, 0, v179, vcc\n"
"s_add_u32 m0, s69, 69632\n"
"s_nop 0\n"
"global_load_lds_dwordx4 v[178:179], off\n"
"v_add_co_u32_e32 v178, vcc, s55, v178\n"
"v_addc_co_u32_e32 v179, vcc, 0, v179, vcc\n"
"s_waitcnt vmcnt(27)\n"
"s_barrier\n"
"ds_read_b64_tr_b16 a[184:185], v182 offset:8192\n"
"ds_read_b64_tr_b16 a[186:187], v183 offset:8192\n"
"ds_read_b64_tr_b16 a[188:189], v182 offset:12288\n"
"ds_read_b64_tr_b16 a[190:191], v183 offset:12288\n"
"ds_read_b64_tr_b16 a[192:193], v182 offset:16384\n"
"ds_read_b64_tr_b16 a[194:195], v183 offset:16384\n"
"ds_read_b64_tr_b16 a[196:197], v182 offset:20480\n"
"ds_read_b64_tr_b16 a[198:199], v183 offset:20480\n"
"ds_read_b64_tr_b16 a[200:201], v182 offset:24576\n"
"ds_read_b64_tr_b16 a[202:203], v183 offset:24576\n"
"ds_read_b64_tr_b16 a[204:205], v182 offset:28672\n"
"ds_read_b64_tr_b16 a[206:207], v183 offset:28672\n"
"ds_read_b64_tr_b16 a[208:209], v182 offset:32768\n"
"ds_read_b64_tr_b16 a[210:211], v183 offset:32768\n"
"ds_read_b64_tr_b16 a[212:213], v182 offset:36864\n"
"s_waitcnt lgkmcnt(8)\n"
"ds_read_b64_tr_b16 a[214:215], v183 offset:36864\n"
"ds_read_b64_tr_b16 a[216:217], v182 offset:40960\n"
"ds_read_b64_tr_b16 a[218:219], v183 offset:40960\n"
"s_waitcnt lgkmcnt(0)\n"
"s_barrier\n"
"s_add_u32 m0, s69, 73728\n"
"s_nop 0\n"
"global_load_lds_dwordx4 v[178:179], off\n"
"v_add_co_u32_e32 v178, vcc, s55, v178\n"
"v_addc_co_u32_e32 v179, vcc, 0, v179, vcc\n"
"s_add_u32 m0, s69, 77824\n"
"s_nop 0\n"
"global_load_lds_dwordx4 v[178:179], off\n"
"v_add_co_u32_e32 v178, vcc, s55, v178\n"
"v_addc_co_u32_e32 v179, vcc, 0, v179, vcc\n"
"s_add_u32 m0, s69, 81920\n"
"s_nop 0\n"
"global_load_lds_dwordx4 v[178:179], off\n"
"v_add_co_u32_e32 v178, vcc, s55, v178\n"
"v_addc_co_u32_e32 v179, vcc, 0, v179, vcc\n"
"s_add_u32 m0, s69, 86016\n"
"s_nop 0\n"
"global_load_lds_dwordx4 v[178:179], off\n"
"v_add_co_u32_e32 v178, vcc, s55, v178\n"
"v_addc_co_u32_e32 v179, vcc, 0, v179, vcc\n"
"s_add_u32 m0, s69, 90112\n"
"s_nop 0\n"
"global_load_lds_dwordx4 v[178:179], off\n"
"v_add_co_u32_e32 v178, vcc, s55, v178\n"
"v_addc_co_u32_e32 v179, vcc, 0, v179, vcc\n"
"s_add_u32 m0, s69, 94208\n"
"s_nop 0\n"
"global_load_lds_dwordx4 v[178:179], off\n"
"v_add_co_u32_e32 v178, vcc, s55, v178\n"
"v_addc_co_u32_e32 v179, vcc, 0, v179, vcc\n"
"s_add_u32 m0, s69, 98304\n"
"s_nop 0\n"
"global_load_lds_dwordx4 v[178:179], off\n"
"v_add_co_u32_e32 v178, vcc, s55, v178\n"
"v_addc_co_u32_e32 v179, vcc, 0, v179, vcc\n"
"s_add_u32 m0, s69, 102400\n"
"s_nop 0\n"
"global_load_lds_dwordx4 v[178:179], off\n"
"v_add_co_u32_e32 v178, vcc, s55, v178\n"
"v_addc_co_u32_e32 v179, vcc, 0, v179, vcc\n"
"s_add_u32 m0, s69, 106496\n"
"s_nop 0\n"
"global_load_lds_dwordx4 v[178:179], off\n"
"v_add_co_u32_e32 v178, vcc, s55, v178\n"
"v_addc_co_u32_e32 v179, vcc, 0, v179, vcc\n"
"s_waitcnt vmcnt(27)\n"
"s_barrier\n"
"ds_read_b64_tr_b16 a[220:221], v182 offset:45056\n"
"ds_read_b64_tr_b16 a[222:223], v183 offset:45056\n"
"ds_read_b64_tr_b16 a[224:225], v182 offset:49152\n"
"ds_read_b64_tr_b16 a[226:227], v183 offset:49152\n"
"ds_read_b64_tr_b16 a[228:229], v182 offset:53248\n"
"ds_read_b64_tr_b16 a[230:231], v183 offset:53248\n"
"ds_read_b64_tr_b16 a[232:233], v182 offset:57344\n"
"ds_read_b64_tr_b16 a[234:235], v183 offset:57344\n"
"ds_read_b64_tr_b16 a[236:237], v182 offset:61440\n"
"ds_read_b64_tr_b16 a[238:239], v183 offset:61440\n"
"ds_read_b64_tr_b16 a[240:241], v184\n"
"ds_read_b64_tr_b16 a[242:243], v185\n"
"ds_read_b64_tr_b16 a[244:245], v184 offset:4096\n"
"ds_read_b64_tr_b16 a[246:247], v185 offset:4096\n"
"ds_read_b64_tr_b16 a[248:249], v184 offset:8192\n"
"s_waitcnt lgkmcnt(8)\n"
"ds_read_b64_tr_b16 a[250:251], v185 offset:8192\n"
"ds_read_b64_tr_b16 a[252:253], v184 offset:12288\n"
"ds_read_b64_tr_b16 a[254:255], v185 offset:12288\n"
"s_waitcnt lgkmcnt(0)\n"
"s_barrier\n"
"s_waitcnt vmcnt(18)\n"
"s_barrier\n"
"ds_read_b64_tr_b16 a[0:1], v180\n"
"ds_read_b64_tr_b16 a[2:3], v181\n"
"ds_read_b64_tr_b16 a[4:5], v180 offset:4096\n"
"ds_read_b64_tr_b16 a[6:7], v181 offset:4096\n"
"ds_read_b64_tr_b16 a[8:9], v180 offset:8192\n"
"ds_read_b64_tr_b16 a[10:11], v181 offset:8192\n"
"ds_read_b64_tr_b16 a[12:13], v180 offset:12288\n"
"ds_read_b64_tr_b16 a[14:15], v181 offset:12288\n"
"ds_read_b64_tr_b16 a[16:17], v180 offset:16384\n"
"ds_read_b64_tr_b16 a[18:19], v181 offset:16384\n"
"ds_read_b64_tr_b16 a[20:21], v180 offset:20480\n"
"ds_read_b64_tr_b16 a[22:23], v181 offset:20480\n"
"ds_read_b64_tr_b16 a[24:25], v180 offset:24576\n"
"ds_read_b64_tr_b16 a[26:27], v181 offset:24576\n"
"ds_read_b64_tr_b16 a[28:29], v180 offset:28672\n"
"s_waitcnt lgkmcnt(8)\n"
"ds_read_b64_tr_b16 a[30:31], v181 offset:28672\n"
"ds_read_b64_tr_b16 a[32:33], v180 offset:32768\n"
"ds_read_b64_tr_b16 a[34:35], v181 offset:32768\n"
"s_waitcnt lgkmcnt(0)\n"
"s_barrier\n"
"s_waitcnt vmcnt(9)\n"
"s_barrier\n"
"ds_read_b64_tr_b16 a[36:37], v180 offset:36864\n"
"ds_read_b64_tr_b16 a[38:39], v181 offset:36864\n"
"ds_read_b64_tr_b16 a[40:41], v180 offset:40960\n"
"ds_read_b64_tr_b16 a[42:43], v181 offset:40960\n"
"ds_read_b64_tr_b16 a[44:45], v180 offset:45056\n"
"ds_read_b64_tr_b16 a[46:47], v181 offset:45056\n"
"ds_read_b64_tr_b16 a[48:49], v180 offset:49152\n"
"ds_read_b64_tr_b16 a[50:51], v181 offset:49152\n"
"ds_read_b64_tr_b16 a[52:53], v180 offset:53248\n"
"ds_read_b64_tr_b16 a[54:55], v181 offset:53248\n"
"ds_read_b64_tr_b16 a[56:57], v180 offset:57344\n"
"ds_read_b64_tr_b16 a[58:59], v181 offset:57344\n"
"ds_read_b64_tr_b16 a[60:61], v180 offset:61440\n"
"ds_read_b64_tr_b16 a[62:63], v181 offset:61440\n"
"ds_read_b64_tr_b16 a[64:65], v182\n"
"s_waitcnt lgkmcnt(8)\n"
"ds_read_b64_tr_b16 a[66:67], v183\n"
"ds_read_b64_tr_b16 a[68:69], v182 offset:4096\n"
"ds_read_b64_tr_b16 a[70:71], v183 offset:4096\n"
"s_waitcnt lgkmcnt(0)\n"
"s_barrier\n"
"v_readlane_b32 s72, v7, 0\n"
"s_nop 4\n"
"buffer_load_dword v198, v5, s[44:47], s72 offen sc1\n"
"s_add_u32 m0, s69, 110592\n"
"s_nop 0\n"
"global_load_lds_dwordx4 v[178:179], off\n"
"v_add_co_u32_e32 v178, vcc, s55, v178\n"
"v_addc_co_u32_e32 v179, vcc, 0, v179, vcc\n"
"s_add_u32 m0, s69, 114688\n"
"s_nop 0\n"
"global_load_lds_dwordx4 v[178:179], off\n"
"v_add_co_u32_e32 v178, vcc, s55, v178\n"
"v_addc_co_u32_e32 v179, vcc, 0, v179, vcc\n"
"s_add_u32 m0, s69, 118784\n"
"s_nop 0\n"
"global_load_lds_dwordx4 v[178:179], off\n"
"v_add_co_u32_e32 v178, vcc, s55, v178\n"
"v_addc_co_u32_e32 v179, vcc, 0, v179, vcc\n"
"s_add_u32 m0, s69, 122880\n"
"s_nop 0\n"
"global_load_lds_dwordx4 v[178:179], off\n"
"v_add_co_u32_e32 v178, vcc, s55, v178\n"
"v_addc_co_u32_e32 v179, vcc, 0, v179, vcc\n"
"s_add_u32 m0, s69, 126976\n"
"s_nop 0\n"
"global_load_lds_dwordx4 v[178:179], off\n"
"v_add_co_u32_e32 v178, vcc, s55, v178\n"
"v_addc_co_u32_e32 v179, vcc, 0, v179, vcc\n"
"s_add_u32 m0, s69, 131072\n"
"s_nop 0\n"
"global_load_lds_dwordx4 v[178:179], off\n"
"v_add_co_u32_e32 v178, vcc, s55, v178\n"
"v_addc_co_u32_e32 v179, vcc, 0, v179, vcc\n"
"s_add_u32 m0, s69, 135168\n"
"s_nop 0\n"
"global_load_lds_dwordx4 v[178:179], off\n"
"v_add_co_u32_e32 v178, vcc, s55, v178\n"
"v_addc_co_u32_e32 v179, vcc, 0, v179, vcc\n"
"s_add_u32 m0, s69, 139264\n"
"s_nop 0\n"
"global_load_lds_dwordx4 v[178:179], off\n"
"v_add_co_u32_e32 v178, vcc, s55, v178\n"
"v_addc_co_u32_e32 v179, vcc, 0, v179, vcc\n"
"s_add_u32 m0, s69, 143360\n"
"s_nop 0\n"
"global_load_lds_dwordx4 v[178:179], off\n"
"s_waitcnt vmcnt(9)\n"
"s_barrier\n"
"ds_read_b64_tr_b16 a[72:73], v182 offset:8192\n"
"ds_read_b64_tr_b16 a[74:75], v183 offset:8192\n"
"ds_read_b64_tr_b16 a[76:77], v182 offset:12288\n"
"ds_read_b64_tr_b16 a[78:79], v183 offset:12288\n"
"ds_read_b64_tr_b16 a[80:81], v182 offset:16384\n"
"ds_read_b64_tr_b16 a[82:83], v183 offset:16384\n"
"ds_read_b64_tr_b16 a[84:85], v182 offset:20480\n"
"ds_read_b64_tr_b16 a[86:87], v183 offset:20480\n"
"ds_read_b64_tr_b16 a[88:89], v182 offset:24576\n"
"ds_read_b64_tr_b16 a[90:91], v183 offset:24576\n"
"ds_read_b64_tr_b16 a[92:93], v182 offset:28672\n"
"ds_read_b64_tr_b16 a[94:95], v183 offset:28672\n"
"ds_read_b64_tr_b16 a[96:97], v182 offset:32768\n"
"ds_read_b64_tr_b16 a[98:99], v183 offset:32768\n"
"ds_read_b64_tr_b16 a[100:101], v182 offset:36864\n"
"s_waitcnt lgkmcnt(8)\n"
"ds_read_b64_tr_b16 a[102:103], v183 offset:36864\n"
"ds_read_b64_tr_b16 a[104:105], v182 offset:40960\n"
"ds_read_b64_tr_b16 a[106:107], v183 offset:40960\n"
"s_waitcnt lgkmcnt(0)\n"
"s_barrier\n"
"v_cmp_ne_u32_e64 s[80:81], s97, v198\n"
"s_nop 1\n"
"s_and_b64 s[80:81], s[80:81], s[82:83]\n"
"s_cbranch_scc1 Lx0nsilu36_%=\n"
"s_mov_b32 s96, 1\n"
"s_mov_b32 s57, 0\n"
"s_mov_b32 s77, 36864\n"
"v_readlane_b32 s67, v6, s57\n"
"s_nop 0\n"
"s_mul_i32 s67, s67, s50\n"
"s_add_u32 s65, s67, s49\n"
"s_sub_u32 s64, 36864, s77\n"
"s_lshl_b32 s68, %0, 10\n"
"s_add_u32 s64, s64, s68\n"
"s_add_u32 m0, s64, 0\n"
"s_add_u32 s70, s65, 0\n"
"buffer_load_dwordx4 v2, s[36:39], s70 offen sc1 lds\n"
"s_add_u32 m0, s64, 4096\n"
"s_add_u32 s70, s65, 512\n"
"buffer_load_dwordx4 v2, s[36:39], s70 offen sc1 lds\n"
"s_add_u32 m0, s64, 8192\n"
"s_add_u32 s70, s65, 1024\n"
"buffer_load_dwordx4 v2, s[36:39], s70 offen sc1 lds\n"
"s_add_u32 m0, s64, 12288\n"
"s_add_u32 s70, s65, 1536\n"
"buffer_load_dwordx4 v2, s[36:39], s70 offen sc1 lds\n"
"s_add_u32 m0, s64, 16384\n"
"s_add_u32 s70, s65, 2048\n"
"buffer_load_dwordx4 v2, s[36:39], s70 offen sc1 lds\n"
"s_add_u32 m0, s64, 20480\n"
"s_add_u32 s70, s65, 2560\n"
"buffer_load_dwordx4 v2, s[36:39], s70 offen sc1 lds\n"
"s_add_u32 m0, s64, 24576\n"
"s_add_u32 s70, s65, 3072\n"
"buffer_load_dwordx4 v2, s[36:39], s70 offen sc1 lds\n"
"s_add_u32 m0, s64, 28672\n"
"s_add_u32 s70, s65, 3584\n"
"buffer_load_dwordx4 v2, s[36:39], s70 offen sc1 lds\n"
"s_add_u32 m0, s64, 32768\n"
"s_add_u32 s70, s65, 4096\n"
"buffer_load_dwordx4 v2, s[36:39], s70 offen sc1 lds\n"
"Lx0nsilu36_%=:\n"
"s_cmp_eq_u32 s96, 0\n"
"s_cbranch_scc1 Lw7asilu36_%=\n"
"s_waitcnt vmcnt(9)\n"
"s_branch Lw7bsilu36_%=\n"
"Lw7asilu36_%=:\n"
"s_waitcnt vmcnt(0)\n"
"Lw7bsilu36_%=:\n"
"s_barrier\n"
"ds_read_b64_tr_b16 a[108:109], v182 offset:45056\n"
"ds_read_b64_tr_b16 a[110:111], v183 offset:45056\n"
"ds_read_b64_tr_b16 a[112:113], v182 offset:49152\n"
"ds_read_b64_tr_b16 a[114:115], v183 offset:49152\n"
"ds_read_b64_tr_b16 a[116:117], v182 offset:53248\n"
"ds_read_b64_tr_b16 a[118:119], v183 offset:53248\n"
"ds_read_b64_tr_b16 a[120:121], v182 offset:57344\n"
"ds_read_b64_tr_b16 a[122:123], v183 offset:57344\n"
"ds_read_b64_tr_b16 a[124:125], v182 offset:61440\n"
"ds_read_b64_tr_b16 a[126:127], v183 offset:61440\n"
"ds_read_b64_tr_b16 a[128:129], v184\n"
"ds_read_b64_tr_b16 a[130:131], v185\n"
"ds_read_b64_tr_b16 a[132:133], v184 offset:4096\n"
"ds_read_b64_tr_b16 a[134:135], v185 offset:4096\n"
"ds_read_b64_tr_b16 a[136:137], v184 offset:8192\n"
"s_waitcnt lgkmcnt(8)\n"
"ds_read_b64_tr_b16 a[138:139], v185 offset:8192\n"
"ds_read_b64_tr_b16 a[140:141], v184 offset:12288\n"
"ds_read_b64_tr_b16 a[142:143], v185 offset:12288\n"
"s_waitcnt lgkmcnt(0)\n"
"s_barrier\n"
"s_lshl_b32 s55, %0, 10\n"
"s_mov_b32 s88, 0xffffffff\n"
"s_mov_b32 s89, 0xffffff\n"
"v_lshrrev_b32 v212, 2, v0\n"
"v_add_u32 v212, 86016, v212\n"
"v_lshlrev_b32 v192, 2, v10\n"
"v_add_u32 v192, 86528, v192\n"
"ds_write_b32 v192, v7\n"
"ds_write_b32 v192, v15 offset:256\n"
"s_mov_b32 s57, 0\n"
"s_mov_b32 s59, 0\n"
"s_mov_b32 s60, -1\n"
"s_mov_b32 s61, -1\n"
"s_mov_b32 s77, 36864\n"
"s_waitcnt lgkmcnt(0)\n"
"s_cmp_lg_u32 s96, 0\n"
"s_cbranch_scc1 Lbwisilu36_%=\n"
"s_cmp_eq_u32 %0, 0\n"
"s_cbranch_scc0 Lbwisilu36_%=\n"
"s_cmp_lg_u32 s75, 0\n"
"s_cbranch_scc1 Lbwisilu36_%=\n"
"v_readlane_b32 s72, v7, s57\n"
"s_nop 3\n"
"s_mov_b32 s71, 0\n"
"s_cmp_lt_i32 s74, 0\n"
"s_cbranch_scc1 Lplisilu36_%=\n"
"s_sub_u32 s67, s57, s74\n"
"s_cmp_gt_u32 s67, 3\n"
"s_cbranch_scc1 Lplisilu36_%=\n"
"s_add_u32 s68, s57, 2\n"
"s_cmp_ge_u32 s68, s48\n"
"s_cbranch_scc1 Lplisilu36_%=\n"
"s_mul_i32 s67, s67, 240\n"
"s_add_u32 s67, s67, s73\n"
"s_sub_u32 s67, s67, 40\n"
"s_mov_b32 s76, 0\n"
"Lslisilu36_%=:\n"
"s_memrealtime s[84:85]\n"
"s_waitcnt lgkmcnt(0)\n"
"s_sub_u32 s68, s84, s67\n"
"s_cmp_ge_i32 s68, 0\n"
"s_cbranch_scc1 Lplisilu36_%=\n"
"s_sleep 2\n"
"s_mov_b32 s71, 1\n"
"s_add_u32 s76, s76, 1\n"
"s_cmp_lt_u32 s76, 32768\n"
"s_cbranch_scc1 Lslisilu36_%=\n"
"Lplisilu36_%=:\n"
"s_mov_b32 s76, 0\n"
"Lppisilu36_%=:\n"
"buffer_load_dword v192, v5, s[44:47], s72 offen sc1\n"
"s_waitcnt vmcnt(0)\n"
"v_cmp_ne_u32_e64 s[80:81], s97, v192\n"
"s_nop 1\n"
"s_and_b64 s[80:81], s[80:81], s[82:83]\n"
"s_cbranch_scc0 Lrdisilu36_%=\n"
"s_sleep 12\n"
"s_add_u32 s76, s76, 1\n"
"s_cmp_lt_u32 s76, 32768\n"
"s_cbranch_scc1 Lppisilu36_%=\n"
"s_mov_b32 s75, 1\n"
"v_mov_b32 v192, 16384\n"
"v_mov_b32 v193, 7000\n"
"buffer_store_dword v193, v192, s[44:47], 0 offen sc1\n"
"Lrdisilu36_%=:\n"
"s_mov_b32 s74, -1\n"
"s_cmp_eq_u32 s76, 0\n"
"s_cbranch_scc1 Lbwisilu36_%=\n"
"s_memrealtime s[84:85]\n"
"s_waitcnt lgkmcnt(0)\n"
"s_mov_b32 s73, s84\n"
"s_mov_b32 s74, s57\n"
"Lbwisilu36_%=:\n"
"s_barrier\n"
"s_cmp_lg_u32 s96, 0\n"
"s_cbranch_scc1 Lx0ssilu36_%=\n"
"v_readlane_b32 s67, v6, s57\n"
"s_nop 0\n"
"s_mul_i32 s67, s67, s50\n"
"s_add_u32 s65, s67, s49\n"
"s_sub_u32 s64, 36864, s77\n"
"s_lshl_b32 s68, %0, 10\n"
"s_add_u32 s64, s64, s68\n"
"s_add_u32 m0, s64, 0\n"
"s_add_u32 s70, s65, 0\n"
"buffer_load_dwordx4 v2, s[36:39], s70 offen sc1 lds\n"
"s_add_u32 m0, s64, 4096\n"
"s_add_u32 s70, s65, 512\n"
"buffer_load_dwordx4 v2, s[36:39], s70 offen sc1 lds\n"
"s_add_u32 m0, s64, 8192\n"
"s_add_u32 s70, s65, 1024\n"
"buffer_load_dwordx4 v2, s[36:39], s70 offen sc1 lds\n"
"s_add_u32 m0, s64, 12288\n"
"s_add_u32 s70, s65, 1536\n"
"buffer_load_dwordx4 v2, s[36:39], s70 offen sc1 lds\n"
"s_add_u32 m0, s64, 16384\n"
"s_add_u32 s70, s65, 2048\n"
"buffer_load_dwordx4 v2, s[36:39], s70 offen sc1 lds\n"
"s_add_u32 m0, s64, 20480\n"
"s_add_u32 s70, s65, 2560\n"
"buffer_load_dwordx4 v2, s[36:39], s70 offen sc1 lds\n"
"s_add_u32 m0, s64, 24576\n"
"s_add_u32 s70, s65, 3072\n"
"buffer_load_dwordx4 v2, s[36:39], s70 offen sc1 lds\n"
"s_add_u32 m0, s64, 28672\n"
"s_add_u32 s70, s65, 3584\n"
"buffer_load_dwordx4 v2, s[36:39], s70 offen sc1 lds\n"
"s_add_u32 m0, s64, 32768\n"
"s_add_u32 s70, s65, 4096\n"
"buffer_load_dwordx4 v2, s[36:39], s70 offen sc1 lds\n"
"Lx0ssilu36_%=:\n"
"s_waitcnt vmcnt(0) lgkmcnt(0)\n"
"s_barrier\n"
"s_sub_u32 s77, 36864, s77\n"
"v_add_u32 v1, s77, v0\n"
"ds_read_b128 v[80:83], v1\n"
"ds_read_b128 v[84:87], v1 offset:1024\n"
"ds_read_b128 v[88:91], v1 offset:2048\n"
"ds_read_b128 v[92:95], v1 offset:3072\n"
"s_mov_b32 s58, 1\n"
"Ltopsilu36_%=:\n"
"s_cmp_eq_u32 s59, 1\n"
"s_cbranch_scc1 LPAsilu36_%=\n"
"s_cmp_eq_u32 s59, 2\n"
"s_cbranch_scc1 LPBsilu36_%=\n"
"s_add_u32 s67, s57, 1\n"
"s_cmp_lt_u32 s67, s58\n"
"s_cbranch_scc0 Lnonsilu36_%=\n"
"v_readlane_b32 s67, v6, s67\n"
"s_nop 0\n"
"s_mul_i32 s67, s67, s50\n"
"s_add_u32 s65, s67, s49\n"
"s_sub_u32 s64, 36864, s77\n"
"s_lshl_b32 s68, %0, 10\n"
"s_add_u32 s64, s64, s68\n"
"s_mov_b32 s62, -1\n"
"s_cmp_ge_u32 s58, s48\n"
"s_cbranch_scc1 Lq1silu36_%=\n"
"s_sub_u32 s68, s58, s57\n"
"s_cmp_le_u32 s68, 4\n"
"s_cselect_b32 s62, s58, -1\n"
"Lq1silu36_%=:\n"
"s_max_i32 s68, s60, s61\n"
"s_mov_b32 s63, 0\n"
"s_cmp_lt_i32 s68, 0\n"
"s_cbranch_scc1 Lq2silu36_%=\n"
"s_mov_b32 s63, 1\n"
"Lq2silu36_%=:\n"
"s_waitcnt lgkmcnt(3)\n"
"v_mfma_f32_32x32x16_bf16 v[16:31], v[128:131], v[80:83], 0\n"
"ds_read_b128 v[96:99], v1 offset:4096\n"
"v_mfma_f32_32x32x16_bf16 v[32:47], a[0:3], v[80:83], 0\n"
"s_waitcnt lgkmcnt(3)\n"
"v_mfma_f32_32x32x16_bf16 v[16:31], v[132:135], v[84:87], v[16:31]\n"
"ds_read_b128 v[100:103], v1 offset:5120\n"
"v_mfma_f32_32x32x16_bf16 v[32:47], a[4:7], v[84:87], v[32:47]\n"
"s_add_u32 m0, s64, 0\n"
"s_add_u32 s70, s65, 0\n"
"buffer_load_dwordx4 v2, s[36:39], s70 offen sc1 lds\n"
"s_waitcnt lgkmcnt(3)\n"
"v_mfma_f32_32x32x16_bf16 v[16:31], v[136:139], v[88:91], v[16:31]\n"
"ds_read_b128 v[80:83], v1 offset:6144\n"
"v_mfma_f32_32x32x16_bf16 v[32:47], a[8:11], v[88:91], v[32:47]\n"
"s_waitcnt lgkmcnt(3)\n"
"v_mfma_f32_32x32x16_bf16 v[16:31], v[140:143], v[92:95], v[16:31]\n"
"ds_read_b128 v[84:87], v1 offset:7168\n"
"v_mfma_f32_32x32x16_bf16 v[32:47], a[12:15], v[92:95], v[32:47]\n"
"s_add_u32 m0, s64, 4096\n"
"s_add_u32 s70, s65, 512\n"
"buffer_load_dwordx4 v2, s[36:39], s70 offen sc1 lds\n"
"s_waitcnt lgkmcnt(3)\n"
"v_mfma_f32_32x32x16_bf16 v[16:31], v[144:147], v[96:99], v[16:31]\n"
"ds_read_b128 v[88:91], v1 offset:8192\n"
"v_mfma_f32_32x32x16_bf16 v[32:47], a[16:19], v[96:99], v[32:47]\n"
"s_waitcnt lgkmcnt(3)\n"
"v_mfma_f32_32x32x16_bf16 v[16:31], v[148:151], v[100:103], v[16:31]\n"
"ds_read_b128 v[92:95], v1 offset:9216\n"
"v_mfma_f32_32x32x16_bf16 v[32:47], a[20:23], v[100:103], v[32:47]\n"
"s_add_u32 m0, s64, 8192\n"
"s_add_u32 s70, s65, 1024\n"
"buffer_load_dwordx4 v2, s[36:39], s70 offen sc1 lds\n"
"s_waitcnt lgkmcnt(3)\n"
"v_mfma_f32_32x32x16_bf16 v[16:31], v[152:155], v[80:83], v[16:31]\n"
"ds_read_b128 v[96:99], v1 offset:10240\n"
"v_mfma_f32_32x32x16_bf16 v[32:47], a[24:27], v[80:83], v[32:47]\n"
"s_waitcnt lgkmcnt(3)\n"
"v_mfma_f32_32x32x16_bf16 v[16:31], v[156:159], v[84:87], v[16:31]\n"
"ds_read_b128 v[100:103], v1 offset:11264\n"
"v_mfma_f32_32x32x16_bf16 v[32:47], a[28:31], v[84:87], v[32:47]\n"
"s_add_u32 m0, s64, 12288\n"
"s_add_u32 s70, s65, 1536\n"
"buffer_load_dwordx4 v2, s[36:39], s70 offen sc1 lds\n"
"s_waitcnt lgkmcnt(3)\n"
"v_mfma_f32_32x32x16_bf16 v[16:31], a[144:147], v[88:91], v[16:31]\n"
"ds_read_b128 v[80:83], v1 offset:12288\n"
"v_mfma_f32_32x32x16_bf16 v[32:47], a[32:35], v[88:91], v[32:47]\n"
"s_waitcnt lgkmcnt(3)\n"
"v_mfma_f32_32x32x16_bf16 v[16:31], a[148:151], v[92:95], v[16:31]\n"
"ds_read_b128 v[84:87], v1 offset:13312\n"
"v_mfma_f32_32x32x16_bf16 v[32:47], a[36:39], v[92:95], v[32:47]\n"
"s_add_u32 m0, s64, 16384\n"
"s_add_u32 s70, s65, 2048\n"
"buffer_load_dwordx4 v2, s[36:39], s70 offen sc1 lds\n"
"s_waitcnt lgkmcnt(3)\n"
"v_mfma_f32_32x32x16_bf16 v[16:31], a[152:155], v[96:99], v[16:31]\n"
"ds_read_b128 v[88:91], v1 offset:14336\n"
"v_mfma_f32_32x32x16_bf16 v[32:47], a[40:43], v[96:99], v[32:47]\n"
"s_waitcnt lgkmcnt(3)\n"
"v_mfma_f32_32x32x16_bf16 v[16:31], a[156:159], v[100:103], v[16:31]\n"
"ds_read_b128 v[92:95], v1 offset:15360\n"
"v_mfma_f32_32x32x16_bf16 v[32:47], a[44:47], v[100:103], v[32:47]\n"
"s_add_u32 m0, s64, 20480\n"
"s_add_u32 s70, s65, 2560\n"
"buffer_load_dwordx4 v2, s[36:39], s70 offen sc1 lds\n"
"s_waitcnt lgkmcnt(3)\n"
"v_mfma_f32_32x32x16_bf16 v[16:31], a[160:163], v[80:83], v[16:31]\n"
"ds_read_b128 v[96:99], v1 offset:16384\n"
"v_mfma_f32_32x32x16_bf16 v[32:47], a[48:51], v[80:83], v[32:47]\n"
"s_waitcnt lgkmcnt(3)\n"
"v_mfma_f32_32x32x16_bf16 v[16:31], a[164:167], v[84:87], v[16:31]\n"
"ds_read_b128 v[100:103], v1 offset:17408\n"
"v_mfma_f32_32x32x16_bf16 v[32:47], a[52:55], v[84:87], v[32:47]\n"
"s_add_u32 m0, s64, 24576\n"
"s_add_u32 s70, s65, 3072\n"
"buffer_load_dwordx4 v2, s[36:39], s70 offen sc1 lds\n"
"s_waitcnt lgkmcnt(3)\n"
"v_mfma_f32_32x32x16_bf16 v[16:31], a[168:171], v[88:91], v[16:31]\n"
"ds_read_b128 v[80:83], v1 offset:18432\n"
"v_mfma_f32_32x32x16_bf16 v[32:47], a[56:59], v[88:91], v[32:47]\n"
"s_cmp_eq_u32 s63, 0\n"
"s_cbranch_scc1 LnmDsilu36_%=\n"
"s_waitcnt vmcnt(7)\n"
"s_barrier\n"
"s_cmp_lt_i32 s60, 0\n"
"s_cbranch_scc1 LnsmaDsilu36_%=\n"
"v_readlane_b32 s67, v6, s60\n"
"s_nop 0\n"
"s_mul_i32 s67, s67, s54\n"
"s_add_u32 s67, s67, s53\n"
"buffer_store_dword v4, v3, s[44:47], s67 offen sc1\n"
"LnsmaDsilu36_%=:\n"
"s_cmp_lt_i32 s61, 0\n"
"s_cbranch_scc1 LnsmbDsilu36_%=\n"
"v_readlane_b32 s67, v6, s61\n"
"s_nop 0\n"
"s_mul_i32 s67, s67, s54\n"
"s_add_u32 s67, s67, s53\n"
"buffer_store_dword v4, v3, s[44:47], s67 offen sc1\n"
"LnsmbDsilu36_%=:\n"
"LnmDsilu36_%=:\n"
"s_waitcnt lgkmcnt(3)\n"
"v_mfma_f32_32x32x16_bf16 v[16:31], a[172:175], v[92:95], v[16:31]\n"
"ds_read_b128 v[84:87], v1 offset:19456\n"
"v_mfma_f32_32x32x16_bf16 v[32:47], a[60:63], v[92:95], v[32:47]\n"
"s_add_u32 m0, s64, 28672\n"
"s_add_u32 s70, s65, 3584\n"
"buffer_load_dwordx4 v2, s[36:39], s70 offen sc1 lds\n"
"s_waitcnt lgkmcnt(3)\n"
"v_mfma_f32_32x32x16_bf16 v[16:31], a[176:179], v[96:99], v[16:31]\n"
"ds_read_b128 v[88:91], v1 offset:20480\n"
"v_mfma_f32_32x32x16_bf16 v[32:47], a[64:67], v[96:99], v[32:47]\n"
"s_waitcnt lgkmcnt(3)\n"
"v_mfma_f32_32x32x16_bf16 v[16:31], a[180:183], v[100:103], v[16:31]\n"
"ds_read_b128 v[92:95], v1 offset:21504\n"
"v_mfma_f32_32x32x16_bf16 v[32:47], a[68:71], v[100:103], v[32:47]\n"
"s_add_u32 m0, s64, 32768\n"
"s_add_u32 s70, s65, 4096\n"
"buffer_load_dwordx4 v2, s[36:39], s70 offen sc1 lds\n"
"s_waitcnt lgkmcnt(3)\n"
"v_mfma_f32_32x32x16_bf16 v[16:31], a[184:187], v[80:83], v[16:31]\n"
"ds_read_b128 v[96:99], v1 offset:22528\n"
"v_mfma_f32_32x32x16_bf16 v[32:47], a[72:75], v[80:83], v[32:47]\n"
"s_cmp_lt_i32 s62, 0\n"
"s_cbranch_scc1 LnpDsilu36_%=\n"
"s_and_b32 s72, s57, 1\n"
"s_lshl_b32 s72, s72, 8\n"
"s_add_u32 s72, s72, 86016\n"
"s_waitcnt lgkmcnt(0)\n"
"s_lshl_b32 s67, s62, 2\n"
"s_add_u32 s67, s67, 86528\n"
"v_add_u32 v192, s67, v8\n"
"ds_read_b32 v192, v192\n"
"s_waitcnt lgkmcnt(0)\n"
"v_add_u32 v192, v192, v9\n"
"s_mov_b32 m0, s72\n"
"s_mov_b64 exec, s[78:79]\n"
"buffer_load_dword v192, s[44:47], 0 offen sc1 lds\n"
"s_mov_b64 exec, -1\n"
"s_waitcnt lgkmcnt(0)\n"
"LnpDsilu36_%=:\n"
"s_waitcnt lgkmcnt(3)\n"
"v_mfma_f32_32x32x16_bf16 v[16:31], a[188:191], v[84:87], v[16:31]\n"
"ds_read_b128 v[100:103], v1 offset:23552\n"
"v_mfma_f32_32x32x16_bf16 v[32:47], a[76:79], v[84:87], v[32:47]\n"
"s_waitcnt lgkmcnt(3)\n"
"v_mfma_f32_32x32x16_bf16 v[16:31], a[192:195], v[88:91], v[16:31]\n"
"ds_read_b128 v[80:83], v1 offset:24576\n"
"v_mfma_f32_32x32x16_bf16 v[32:47], a[80:83], v[88:91], v[32:47]\n"
"s_waitcnt lgkmcnt(3)\n"
"v_mfma_f32_32x32x16_bf16 v[16:31], a[196:199], v[92:95], v[16:31]\n"
"ds_read_b128 v[84:87], v1 offset:25600\n"
"v_mfma_f32_32x32x16_bf16 v[32:47], a[84:87], v[92:95], v[32:47]\n"
"s_waitcnt lgkmcnt(3)\n"
"v_mfma_f32_32x32x16_bf16 v[16:31], a[200:203], v[96:99], v[16:31]\n"
"ds_read_b128 v[88:91], v1 offset:26624\n"
"v_mfma_f32_32x32x16_bf16 v[32:47], a[88:91], v[96:99], v[32:47]\n"
"s_waitcnt lgkmcnt(3)\n"
"v_mfma_f32_32x32x16_bf16 v[16:31], a[204:207], v[100:103], v[16:31]\n"
"ds_read_b128 v[92:95], v1 offset:27648\n"
"v_mfma_f32_32x32x16_bf16 v[32:47], a[92:95], v[100:103], v[32:47]\n"
"s_waitcnt lgkmcnt(3)\n"
"v_mfma_f32_32x32x16_bf16 v[16:31], a[208:211], v[80:83], v[16:31]\n"
"ds_read_b128 v[96:99], v1 offset:28672\n"
"v_mfma_f32_32x32x16_bf16 v[32:47], a[96:99], v[80:83], v[32:47]\n"
"s_waitcnt lgkmcnt(3)\n"
"v_mfma_f32_32x32x16_bf16 v[16:31], a[212:215], v[84:87], v[16:31]\n"
"ds_read_b128 v[100:103], v1 offset:29696\n"
"v_mfma_f32_32x32x16_bf16 v[32:47], a[100:103], v[84:87], v[32:47]\n"
"s_waitcnt lgkmcnt(3)\n"
"v_mfma_f32_32x32x16_bf16 v[16:31], a[216:219], v[88:91], v[16:31]\n"
"ds_read_b128 v[80:83], v1 offset:30720\n"
"v_mfma_f32_32x32x16_bf16 v[32:47], a[104:107], v[88:91], v[32:47]\n"
"s_waitcnt lgkmcnt(3)\n"
"v_mfma_f32_32x32x16_bf16 v[16:31], a[220:223], v[92:95], v[16:31]\n"
"ds_read_b128 v[84:87], v1 offset:31744\n"
"v_mfma_f32_32x32x16_bf16 v[32:47], a[108:111], v[92:95], v[32:47]\n"
"s_waitcnt lgkmcnt(3)\n"
"v_mfma_f32_32x32x16_bf16 v[16:31], a[224:227], v[96:99], v[16:31]\n"
"ds_read_b128 v[88:91], v1 offset:32768\n"
"v_mfma_f32_32x32x16_bf16 v[32:47], a[112:115], v[96:99], v[32:47]\n"
"s_waitcnt lgkmcnt(3)\n"
"v_mfma_f32_32x32x16_bf16 v[16:31], a[228:231], v[100:103], v[16:31]\n"
"ds_read_b128 v[92:95], v1 offset:33792\n"
"v_mfma_f32_32x32x16_bf16 v[32:47], a[116:119], v[100:103], v[32:47]\n"
"s_waitcnt lgkmcnt(3)\n"
"v_mfma_f32_32x32x16_bf16 v[16:31], a[232:235], v[80:83], v[16:31]\n"
"ds_read_b128 v[96:99], v1 offset:34816\n"
"v_mfma_f32_32x32x16_bf16 v[32:47], a[120:123], v[80:83], v[32:47]\n"
"s_waitcnt lgkmcnt(3)\n"
"v_mfma_f32_32x32x16_bf16 v[16:31], a[236:239], v[84:87], v[16:31]\n"
"ds_read_b128 v[100:103], v1 offset:35840\n"
"v_mfma_f32_32x32x16_bf16 v[32:47], a[124:127], v[84:87], v[32:47]\n"
"s_waitcnt lgkmcnt(3)\n"
"v_mfma_f32_32x32x16_bf16 v[16:31], a[240:243], v[88:91], v[16:31]\n"
"v_mfma_f32_32x32x16_bf16 v[32:47], a[128:131], v[88:91], v[32:47]\n"
"s_waitcnt lgkmcnt(2)\n"
"v_mfma_f32_32x32x16_bf16 v[16:31], a[244:247], v[92:95], v[16:31]\n"
"v_mfma_f32_32x32x16_bf16 v[32:47], a[132:135], v[92:95], v[32:47]\n"
"s_waitcnt lgkmcnt(1)\n"
"v_mfma_f32_32x32x16_bf16 v[16:31], a[248:251], v[96:99], v[16:31]\n"
"v_mfma_f32_32x32x16_bf16 v[32:47], a[136:139], v[96:99], v[32:47]\n"
"s_waitcnt lgkmcnt(0)\n"
"v_mfma_f32_32x32x16_bf16 v[16:31], a[252:255], v[100:103], v[16:31]\n"
"v_mfma_f32_32x32x16_bf16 v[32:47], a[140:143], v[100:103], v[32:47]\n"
"s_waitcnt vmcnt(0) lgkmcnt(0)\n"
"s_barrier\n"
"s_mov_b32 s99, 0\n"
"s_sub_u32 s77, 36864, s77\n"
"v_add_u32 v1, s77, v0\n"
"ds_read_b128 v[80:83], v1\n"
"ds_read_b128 v[84:87], v1 offset:1024\n"
"ds_read_b128 v[88:91], v1 offset:2048\n"
"ds_read_b128 v[92:95], v1 offset:3072\n"
"s_branch Le1silu36_%=\n"
"LPAsilu36_%=:\n"
"s_waitcnt lgkmcnt(3)\n"
"v_mfma_f32_32x32x16_bf16 v[48:63], v[128:131], v[80:83], 0\n"
"ds_read_b128 v[96:99], v1 offset:4096\n"
"s_add_u32 s92, s57, 1\n"
"v_readlane_b32 s92, v6, s92\n"
"s_sub_u32 s64, 36864, s77\n"
"v_mfma_f32_32x32x16_bf16 v[64:79], a[0:3], v[80:83], 0\n"
"s_add_u32 s64, s64, s55\n"
"s_mul_i32 s92, s92, s50\n"
"s_add_u32 s65, s92, s49\n"
"s_sub_u32 s90, s57, 1\n"
"s_and_b32 s90, s90, 1\n"
"s_waitcnt lgkmcnt(3)\n"
"v_mfma_f32_32x32x16_bf16 v[48:63], v[132:135], v[84:87], v[48:63]\n"
"ds_read_b128 v[100:103], v1 offset:5120\n"
"s_lshl_b32 s90, s90, 8\n"
"v_add_u32 v194, s90, v212\n"
"ds_read_b32 v195, v194\n"
"v_mfma_f32_32x32x16_bf16 v[64:79], a[4:7], v[84:87], v[64:79]\n"
"s_add_u32 m0, s64, 0\n"
"s_add_u32 s92, s65, 0\n"
"buffer_load_dwordx4 v2, s[36:39], s92 offen sc1 lds\n"
"s_sub_u32 s93, s57, 1\n"
"s_waitcnt lgkmcnt(4)\n"
"v_mfma_f32_32x32x16_bf16 v[48:63], v[136:139], v[88:91], v[48:63]\n"
"ds_read_b128 v[80:83], v1 offset:6144\n"
"v_readlane_b32 s93, v6, s93\n"
"v_mfma_f32_32x32x16_bf16 v[64:79], a[8:11], v[88:91], v[64:79]\n"
"s_add_u32 m0, s64, 4096\n"
"s_add_u32 s92, s65, 512\n"
"buffer_load_dwordx4 v2, s[36:39], s92 offen sc1 lds\n"
"s_mul_i32 s93, s93, s52\n"
"s_waitcnt lgkmcnt(4)\n"
"v_mfma_f32_32x32x16_bf16 v[48:63], v[140:143], v[92:95], v[48:63]\n"
"ds_read_b128 v[84:87], v1 offset:7168\n"
"s_waitcnt lgkmcnt(2)\n"
"v_cmp_ne_u32_e64 s[80:81], s97, v195\n"
"s_add_u32 s66, s93, s51\n"
"v_readlane_b32 s93, v6, s60\n"
"v_mfma_f32_32x32x16_bf16 v[64:79], a[12:15], v[92:95], v[64:79]\n"
"s_add_u32 m0, s64, 8192\n"
"s_add_u32 s92, s65, 1024\n"
"buffer_load_dwordx4 v2, s[36:39], s92 offen sc1 lds\n"
"s_and_b64 s[80:81], s[80:81], s[88:89]\n"
"v_mfma_f32_32x32x16_bf16 v[48:63], v[144:147], v[96:99], v[48:63]\n"
"ds_read_b128 v[88:91], v1 offset:8192\n"
"s_ff1_i32_b64 s90, s[80:81]\n"
"s_cmp_lt_i32 s90, 0\n"
"s_cselect_b32 s90, 56, s90\n"
"s_mul_i32 s90, s90, 8192\n"
"v_mfma_f32_32x32x16_bf16 v[64:79], a[16:19], v[96:99], v[64:79]\n"
"s_add_u32 m0, s64, 12288\n"
"s_add_u32 s92, s65, 1536\n"
"buffer_load_dwordx4 v2, s[36:39], s92 offen sc1 lds\n"
"s_lshr_b32 s90, s90, 16\n"
"v_mfma_f32_32x32x16_bf16 v[48:63], v[148:151], v[100:103], v[48:63]\n"
"ds_read_b128 v[92:95], v1 offset:9216\n"
"s_sub_u32 s91, s48, s86\n"
"s_min_u32 s90, s90, s91\n"
"s_add_u32 s90, s90, s86\n"
"s_mul_i32 s93, s93, s54\n"
"v_mfma_f32_32x32x16_bf16 v[64:79], a[20:23], v[100:103], v[64:79]\n"
"s_add_u32 m0, s64, 16384\n"
"s_add_u32 s92, s65, 2048\n"
"buffer_load_dwordx4 v2, s[36:39], s92 offen sc1 lds\n"
"s_add_u32 s93, s93, s53\n"
"s_waitcnt lgkmcnt(3)\n"
"v_mfma_f32_32x32x16_bf16 v[48:63], v[152:155], v[80:83], v[48:63]\n"
"ds_read_b128 v[96:99], v1 offset:10240\n"
"s_cmp_lt_i32 s86, 0\n"
"s_cselect_b32 s58, s58, s90\n"
"v_mfma_f32_32x32x16_bf16 v[64:79], a[24:27], v[80:83], v[64:79]\n"
"s_add_u32 m0, s64, 20480\n"
"s_add_u32 s92, s65, 2560\n"
"buffer_load_dwordx4 v2, s[36:39], s92 offen sc1 lds\n"
"s_waitcnt lgkmcnt(3)\n"
"v_mfma_f32_32x32x16_bf16 v[48:63], v[156:159], v[84:87], v[48:63]\n"
"ds_read_b128 v[100:103], v1 offset:11264\n"
"s_cmp_lt_u32 s58, s48\n"
"s_cselect_b32 s62, s58, -1\n"
"s_sub_u32 s90, s58, s57\n"
"v_mfma_f32_32x32x16_bf16 v[64:79], a[28:31], v[84:87], v[64:79]\n"
"s_add_u32 m0, s64, 24576\n"
"s_add_u32 s92, s65, 3072\n"
"buffer_load_dwordx4 v2, s[36:39], s92 offen sc1 lds\n"
"s_waitcnt lgkmcnt(3)\n"
"v_mfma_f32_32x32x16_bf16 v[48:63], a[144:147], v[88:91], v[48:63]\n"
"ds_read_b128 v[80:83], v1 offset:12288\n"
"s_cmp_le_u32 s90, 4\n"
"s_cselect_b32 s62, s62, -1\n"
"s_max_i32 s90, s62, 0\n"
"v_mfma_f32_32x32x16_bf16 v[64:79], a[32:35], v[88:91], v[64:79]\n"
"s_add_u32 m0, s64, 28672\n"
"s_add_u32 s92, s65, 3584\n"
"buffer_load_dwordx4 v2, s[36:39], s92 offen sc1 lds\n"
"s_lshl_b32 s90, s90, 2\n"
"s_waitcnt lgkmcnt(3)\n"
"v_mfma_f32_32x32x16_bf16 v[48:63], a[148:151], v[92:95], v[48:63]\n"
"ds_read_b128 v[84:87], v1 offset:13312\n"
"s_add_u32 s90, s90, 86528\n"
"v_add_u32 v196, s90, v8\n"
"ds_read_b32 v196, v196\n"
"v_mfma_f32_32x32x16_bf16 v[64:79], a[36:39], v[92:95], v[64:79]\n"
"s_add_u32 m0, s64, 32768\n"
"s_add_u32 s92, s65, 4096\n"
"buffer_load_dwordx4 v2, s[36:39], s92 offen sc1 lds\n"
"s_waitcnt lgkmcnt(4)\n"
"v_mfma_f32_32x32x16_bf16 v[48:63], a[152:155], v[96:99], v[48:63]\n"
"ds_read_b128 v[88:91], v1 offset:14336\n"
"s_cmp_ge_i32 s62, 0\n"
"s_cselect_b64 s[94:95], s[78:79], 0\n"
"s_and_b32 s91, s57, 1\n"
"v_mfma_f32_32x32x16_bf16 v[64:79], a[40:43], v[96:99], v[64:79]\n"
"s_lshl_b32 s91, s91, 8\n"
"s_add_u32 s91, s91, 86016\n"
"s_add_u32 s90, s57, 2\n"
"s_cmp_lt_u32 s90, s58\n"
"s_cselect_b32 s87, 1, 0\n"
"s_waitcnt lgkmcnt(4)\n"
"v_mfma_f32_32x32x16_bf16 v[48:63], a[156:159], v[100:103], v[48:63]\n"
"ds_read_b128 v[92:95], v1 offset:15360\n"
"s_cmp_lt_i32 s60, 0\n"
"s_cselect_b32 s93, 0x7ffffff0, s93\n"
"v_add_u32 v197, s93, v3\n"
"v_mfma_f32_32x32x16_bf16 v[64:79], a[44:47], v[100:103], v[64:79]\n"
"v_mul_f32 v176, 0xbfb8aa3b, v16\n"
"v_mul_f32 v177, 0xbfb8aa3b, v17\n"
"v_mul_f32 v178, 0xbfb8aa3b, v18\n"
"v_mul_f32 v179, 0xbfb8aa3b, v19\n"
"s_waitcnt lgkmcnt(4)\n"
"v_mfma_f32_32x32x16_bf16 v[48:63], a[160:163], v[80:83], v[48:63]\n"
"ds_read_b128 v[96:99], v1 offset:16384\n"
"s_waitcnt lgkmcnt(3)\n"
"v_add_u32 v196, v196, v9\n"
"v_exp_f32 v176, v176\n"
"v_mfma_f32_32x32x16_bf16 v[64:79], a[48:51], v[80:83], v[64:79]\n"
"s_mov_b32 m0, s91\n"
"s_mov_b64 exec, s[94:95]\n"
"buffer_load_dword v196, s[44:47], 0 offen sc1 lds\n"
"s_mov_b64 exec, -1\n"
"v_mfma_f32_32x32x16_bf16 v[48:63], a[164:167], v[84:87], v[48:63]\n"
"ds_read_b128 v[100:103], v1 offset:17408\n"
"v_exp_f32 v177, v177\n"
"v_exp_f32 v178, v178\n"
"v_mfma_f32_32x32x16_bf16 v[64:79], a[52:55], v[84:87], v[64:79]\n"
"v_exp_f32 v179, v179\n"
"v_add_f32 v176, 1.0, v176\n"
"v_add_f32 v177, 1.0, v177\n"
"v_add_f32 v178, 1.0, v178\n"
"s_waitcnt lgkmcnt(3)\n"
"v_mfma_f32_32x32x16_bf16 v[48:63], a[168:171], v[88:91], v[48:63]\n"
"ds_read_b128 v[80:83], v1 offset:18432\n"
"v_add_f32 v179, 1.0, v179\n"
"v_rcp_f32 v176, v176\n"
"v_mfma_f32_32x32x16_bf16 v[64:79], a[56:59], v[88:91], v[64:79]\n"
"v_rcp_f32 v177, v177\n"
"v_rcp_f32 v178, v178\n"
"s_waitcnt lgkmcnt(3)\n"
"v_mfma_f32_32x32x16_bf16 v[48:63], a[172:175], v[92:95], v[48:63]\n"
"ds_read_b128 v[84:87], v1 offset:19456\n"
"v_rcp_f32 v179, v179\n"
"v_mul_f32 v16, v16, v24\n"
"v_mfma_f32_32x32x16_bf16 v[64:79], a[60:63], v[92:95], v[64:79]\n"
"v_mul_f32 v17, v17, v25\n"
"v_mul_f32 v18, v18, v26\n"
"v_mul_f32 v19, v19, v27\n"
"v_mul_f32 v16, v16, v176\n"
"v_mul_f32 v17, v17, v177\n"
"s_waitcnt lgkmcnt(3)\n"
"v_mfma_f32_32x32x16_bf16 v[48:63], a[176:179], v[96:99], v[48:63]\n"
"ds_read_b128 v[88:91], v1 offset:20480\n"
"v_mul_f32 v18, v18, v178\n"
"v_mul_f32 v19, v19, v179\n"
"v_mul_f32 v180, 0xbfb8aa3b, v20\n"
"v_mfma_f32_32x32x16_bf16 v[64:79], a[64:67], v[96:99], v[64:79]\n"
"v_mul_f32 v181, 0xbfb8aa3b, v21\n"
"v_mul_f32 v182, 0xbfb8aa3b, v22\n"
"v_mul_f32 v183, 0xbfb8aa3b, v23\n"
"v_exp_f32 v180, v180\n"
"s_waitcnt lgkmcnt(3)\n"
"v_mfma_f32_32x32x16_bf16 v[48:63], a[180:183], v[100:103], v[48:63]\n"
"ds_read_b128 v[92:95], v1 offset:21504\n"
"v_exp_f32 v181, v181\n"
"v_mfma_f32_32x32x16_bf16 v[64:79], a[68:71], v[100:103], v[64:79]\n"
"v_exp_f32 v182, v182\n"
"v_exp_f32 v183, v183\n"
"v_add_f32 v180, 1.0, v180\n"
"s_waitcnt lgkmcnt(3)\n"
"v_mfma_f32_32x32x16_bf16 v[48:63], a[184:187], v[80:83], v[48:63]\n"
"ds_read_b128 v[96:99], v1 offset:22528\n"
"v_add_f32 v181, 1.0, v181\n"
"v_add_f32 v182, 1.0, v182\n"
"v_add_f32 v183, 1.0, v183\n"
"v_mfma_f32_32x32x16_bf16 v[64:79], a[72:75], v[80:83], v[64:79]\n"
"v_rcp_f32 v180, v180\n"
"v_rcp_f32 v181, v181\n"
"s_waitcnt lgkmcnt(3)\n"
"v_mfma_f32_32x32x16_bf16 v[48:63], a[188:191], v[84:87], v[48:63]\n"
"ds_read_b128 v[100:103], v1 offset:23552\n"
"v_rcp_f32 v182, v182\n"
"v_mfma_f32_32x32x16_bf16 v[64:79], a[76:79], v[84:87], v[64:79]\n"
"v_rcp_f32 v183, v183\n"
"v_mul_f32 v20, v20, v28\n"
"v_mul_f32 v21, v21, v29\n"
"v_mul_f32 v22, v22, v30\n"
"s_waitcnt lgkmcnt(3)\n"
"v_mfma_f32_32x32x16_bf16 v[48:63], a[192:195], v[88:91], v[48:63]\n"
"ds_read_b128 v[80:83], v1 offset:24576\n"
"v_mul_f32 v23, v23, v31\n"
"v_mul_f32 v20, v20, v180\n"
"v_mul_f32 v21, v21, v181\n"
"v_mfma_f32_32x32x16_bf16 v[64:79], a[80:83], v[88:91], v[64:79]\n"
"v_mul_f32 v22, v22, v182\n"
"v_mul_f32 v23, v23, v183\n"
"v_mul_f32 v176, 0xbfb8aa3b, v32\n"
"v_mul_f32 v177, 0xbfb8aa3b, v33\n"
"v_mul_f32 v178, 0xbfb8aa3b, v34\n"
"s_waitcnt lgkmcnt(3)\n"
"v_mfma_f32_32x32x16_bf16 v[48:63], a[196:199], v[92:95], v[48:63]\n"
"ds_read_b128 v[84:87], v1 offset:25600\n"
"v_mul_f32 v179, 0xbfb8aa3b, v35\n"
"v_exp_f32 v176, v176\n"
"v_mfma_f32_32x32x16_bf16 v[64:79], a[84:87], v[92:95], v[64:79]\n"
"v_exp_f32 v177, v177\n"
"v_exp_f32 v178, v178\n"
"s_waitcnt lgkmcnt(3)\n"
"v_mfma_f32_32x32x16_bf16 v[48:63], a[200:203], v[96:99], v[48:63]\n"
"ds_read_b128 v[88:91], v1 offset:26624\n"
"v_exp_f32 v179, v179\n"
"v_add_f32 v176, 1.0, v176\n"
"v_mfma_f32_32x32x16_bf16 v[64:79], a[88:91], v[96:99], v[64:79]\n"
"v_add_f32 v177, 1.0, v177\n"
"v_add_f32 v178, 1.0, v178\n"
"v_add_f32 v179, 1.0, v179\n"
"v_rcp_f32 v176, v176\n"
"s_waitcnt lgkmcnt(3)\n"
"v_mfma_f32_32x32x16_bf16 v[48:63], a[204:207], v[100:103], v[48:63]\n"
"ds_read_b128 v[92:95], v1 offset:27648\n"
"v_rcp_f32 v177, v177\n"
"v_mfma_f32_32x32x16_bf16 v[64:79], a[92:95], v[100:103], v[64:79]\n"
"v_rcp_f32 v178, v178\n"
"v_rcp_f32 v179, v179\n"
"v_mul_f32 v32, v32, v40\n"
"s_waitcnt lgkmcnt(3)\n"
"v_mfma_f32_32x32x16_bf16 v[48:63], a[208:211], v[80:83], v[48:63]\n"
"ds_read_b128 v[96:99], v1 offset:28672\n"
"v_mul_f32 v33, v33, v41\n"
"v_mul_f32 v34, v34, v42\n"
"v_mul_f32 v35, v35, v43\n"
"v_mfma_f32_32x32x16_bf16 v[64:79], a[96:99], v[80:83], v[64:79]\n"
"v_mul_f32 v32, v32, v176\n"
"v_mul_f32 v33, v33, v177\n"
"v_mul_f32 v34, v34, v178\n"
"v_mul_f32 v35, v35, v179\n"
"v_mul_f32 v180, 0xbfb8aa3b, v36\n"
"s_waitcnt lgkmcnt(3)\n"
"v_mfma_f32_32x32x16_bf16 v[48:63], a[212:215], v[84:87], v[48:63]\n"
"ds_read_b128 v[100:103], v1 offset:29696\n"
"v_mul_f32 v181, 0xbfb8aa3b, v37\n"
"v_mul_f32 v182, 0xbfb8aa3b, v38\n"
"v_mul_f32 v183, 0xbfb8aa3b, v39\n"
"v_mfma_f32_32x32x16_bf16 v[64:79], a[100:103], v[84:87], v[64:79]\n"
"v_exp_f32 v180, v180\n"
"v_exp_f32 v181, v181\n"
"s_waitcnt lgkmcnt(3)\n"
"v_mfma_f32_32x32x16_bf16 v[48:63], a[216:219], v[88:91], v[48:63]\n"
"ds_read_b128 v[80:83], v1 offset:30720\n"
"v_exp_f32 v182, v182\n"
"v_mfma_f32_32x32x16_bf16 v[64:79], a[104:107], v[88:91], v[64:79]\n"
"v_exp_f32 v183, v183\n"
"v_add_f32 v180, 1.0, v180\n"
"v_add_f32 v181, 1.0, v181\n"
"v_add_f32 v182, 1.0, v182\n"
"s_waitcnt lgkmcnt(3)\n"
"v_mfma_f32_32x32x16_bf16 v[48:63], a[220:223], v[92:95], v[48:63]\n"
"ds_read_b128 v[84:87], v1 offset:31744\n"
"v_add_f32 v183, 1.0, v183\n"
"v_rcp_f32 v180, v180\n"
"v_mfma_f32_32x32x16_bf16 v[64:79], a[108:111], v[92:95], v[64:79]\n"
"v_rcp_f32 v181, v181\n"
"v_rcp_f32 v182, v182\n"
"s_waitcnt lgkmcnt(3)\n"
"v_mfma_f32_32x32x16_bf16 v[48:63], a[224:227], v[96:99], v[48:63]\n"
"ds_read_b128 v[88:91], v1 offset:32768\n"
"v_rcp_f32 v183, v183\n"
"v_mul_f32 v36, v36, v44\n"
"v_mfma_f32_32x32x16_bf16 v[64:79], a[112:115], v[96:99], v[64:79]\n"
"v_mul_f32 v37, v37, v45\n"
"v_mul_f32 v38, v38, v46\n"
"v_mul_f32 v39, v39, v47\n"
"v_mul_f32 v36, v36, v180\n"
"v_mul_f32 v37, v37, v181\n"
"s_waitcnt lgkmcnt(3)\n"
"v_mfma_f32_32x32x16_bf16 v[48:63], a[228:231], v[100:103], v[48:63]\n"
"ds_read_b128 v[92:95], v1 offset:33792\n"
"v_mul_f32 v38, v38, v182\n"
"v_mul_f32 v39, v39, v183\n"
"v_cvt_pk_bf16_f32 v160, v16, v17\n"
"v_mfma_f32_32x32x16_bf16 v[64:79], a[116:119], v[100:103], v[64:79]\n"
"v_cvt_pk_bf16_f32 v161, v18, v19\n"
"v_cvt_pk_bf16_f32 v162, v20, v21\n"
"v_cvt_pk_bf16_f32 v163, v22, v23\n"
"v_cvt_pk_bf16_f32 v164, v32, v33\n"
"v_cvt_pk_bf16_f32 v165, v34, v35\n"
"s_waitcnt lgkmcnt(3)\n"
"v_mfma_f32_32x32x16_bf16 v[48:63], a[232:235], v[80:83], v[48:63]\n"
"ds_read_b128 v[96:99], v1 offset:34816\n"
"v_cvt_pk_bf16_f32 v166, v36, v37\n"
"v_cvt_pk_bf16_f32 v167, v38, v39\n"
"v_permlane32_swap_b32 v160, v162\n"
"v_mfma_f32_32x32x16_bf16 v[64:79], a[120:123], v[80:83], v[64:79]\n"
"v_permlane32_swap_b32 v161, v163\n"
"v_permlane32_swap_b32 v164, v166\n"
"v_permlane32_swap_b32 v165, v167\n"
"buffer_store_dwordx4 v[160:163], v11, s[40:43], s66 offen sc1\n"
"s_waitcnt lgkmcnt(3)\n"
"v_mfma_f32_32x32x16_bf16 v[48:63], a[236:239], v[84:87], v[48:63]\n"
"ds_read_b128 v[100:103], v1 offset:35840\n"
"buffer_store_dwordx4 v[164:167], v12, s[40:43], s66 offen sc1\n"
"v_mfma_f32_32x32x16_bf16 v[64:79], a[124:127], v[84:87], v[64:79]\n"
"s_waitcnt lgkmcnt(3)\n"
"v_mfma_f32_32x32x16_bf16 v[48:63], a[240:243], v[88:91], v[48:63]\n"
"v_mfma_f32_32x32x16_bf16 v[64:79], a[128:131], v[88:91], v[64:79]\n"
"s_waitcnt vmcnt(2) lgkmcnt(0)\n"
"s_barrier\n"
"v_mfma_f32_32x32x16_bf16 v[48:63], a[244:247], v[92:95], v[48:63]\n"
"s_sub_u32 s77, 36864, s77\n"
"v_add_u32 v1, s77, v0\n"
"ds_read_b128 v[80:83], v1\n"
"ds_read_b128 v[84:87], v1 offset:1024\n"
"v_mfma_f32_32x32x16_bf16 v[64:79], a[132:135], v[92:95], v[64:79]\n"
"buffer_store_dword v4, v197, s[44:47], 0 offen sc1\n"
"v_mfma_f32_32x32x16_bf16 v[48:63], a[248:251], v[96:99], v[48:63]\n"
"ds_read_b128 v[88:91], v1 offset:2048\n"
"v_mfma_f32_32x32x16_bf16 v[64:79], a[136:139], v[96:99], v[64:79]\n"
"s_sub_u32 s60, s57, 1\n"
"s_mov_b32 s86, s62\n"
"s_add_u32 s57, s57, 1\n"
"s_cmp_eq_u32 s87, 0\n"
"v_mfma_f32_32x32x16_bf16 v[48:63], a[252:255], v[100:103], v[48:63]\n"
"ds_read_b128 v[92:95], v1 offset:3072\n"
"v_mfma_f32_32x32x16_bf16 v[64:79], a[140:143], v[100:103], v[64:79]\n"
"s_cbranch_scc1 LfxAsilu36_%=\n"
"LPBsilu36_%=:\n"
"s_waitcnt lgkmcnt(3)\n"
"v_mfma_f32_32x32x16_bf16 v[16:31], v[128:131], v[80:83], 0\n"
"ds_read_b128 v[96:99], v1 offset:4096\n"
"s_add_u32 s92, s57, 1\n"
"v_readlane_b32 s92, v6, s92\n"
"s_sub_u32 s64, 36864, s77\n"
"v_mfma_f32_32x32x16_bf16 v[32:47], a[0:3], v[80:83], 0\n"
"s_add_u32 s64, s64, s55\n"
"s_mul_i32 s92, s92, s50\n"
"s_add_u32 s65, s92, s49\n"
"s_sub_u32 s90, s57, 1\n"
"s_and_b32 s90, s90, 1\n"
"s_waitcnt lgkmcnt(3)\n"
"v_mfma_f32_32x32x16_bf16 v[16:31], v[132:135], v[84:87], v[16:31]\n"
"ds_read_b128 v[100:103], v1 offset:5120\n"
"s_lshl_b32 s90, s90, 8\n"
"v_add_u32 v194, s90, v212\n"
"ds_read_b32 v195, v194\n"
"v_mfma_f32_32x32x16_bf16 v[32:47], a[4:7], v[84:87], v[32:47]\n"
"s_add_u32 m0, s64, 0\n"
"s_add_u32 s92, s65, 0\n"
"buffer_load_dwordx4 v2, s[36:39], s92 offen sc1 lds\n"
"s_sub_u32 s93, s57, 1\n"
"s_waitcnt lgkmcnt(4)\n"
"v_mfma_f32_32x32x16_bf16 v[16:31], v[136:139], v[88:91], v[16:31]\n"
"ds_read_b128 v[80:83], v1 offset:6144\n"
"v_readlane_b32 s93, v6, s93\n"
"v_mfma_f32_32x32x16_bf16 v[32:47], a[8:11], v[88:91], v[32:47]\n"
"s_add_u32 m0, s64, 4096\n"
"s_add_u32 s92, s65, 512\n"
"buffer_load_dwordx4 v2, s[36:39], s92 offen sc1 lds\n"
"s_mul_i32 s93, s93, s52\n"
"s_waitcnt lgkmcnt(4)\n"
"v_mfma_f32_32x32x16_bf16 v[16:31], v[140:143], v[92:95], v[16:31]\n"
"ds_read_b128 v[84:87], v1 offset:7168\n"
"s_waitcnt lgkmcnt(2)\n"
"v_cmp_ne_u32_e64 s[80:81], s97, v195\n"
"s_add_u32 s66, s93, s51\n"
"v_readlane_b32 s93, v6, s60\n"
"v_mfma_f32_32x32x16_bf16 v[32:47], a[12:15], v[92:95], v[32:47]\n"
"s_add_u32 m0, s64, 8192\n"
"s_add_u32 s92, s65, 1024\n"
"buffer_load_dwordx4 v2, s[36:39], s92 offen sc1 lds\n"
"s_and_b64 s[80:81], s[80:81], s[88:89]\n"
"v_mfma_f32_32x32x16_bf16 v[16:31], v[144:147], v[96:99], v[16:31]\n"
"ds_read_b128 v[88:91], v1 offset:8192\n"
"s_ff1_i32_b64 s90, s[80:81]\n"
"s_cmp_lt_i32 s90, 0\n"
"s_cselect_b32 s90, 56, s90\n"
"s_mul_i32 s90, s90, 8192\n"
"v_mfma_f32_32x32x16_bf16 v[32:47], a[16:19], v[96:99], v[32:47]\n"
"s_add_u32 m0, s64, 12288\n"
"s_add_u32 s92, s65, 1536\n"
"buffer_load_dwordx4 v2, s[36:39], s92 offen sc1 lds\n"
"s_lshr_b32 s90, s90, 16\n"
"v_mfma_f32_32x32x16_bf16 v[16:31], v[148:151], v[100:103], v[16:31]\n"
"ds_read_b128 v[92:95], v1 offset:9216\n"
"s_sub_u32 s91, s48, s86\n"
"s_min_u32 s90, s90, s91\n"
"s_add_u32 s90, s90, s86\n"
"s_mul_i32 s93, s93, s54\n"
"v_mfma_f32_32x32x16_bf16 v[32:47], a[20:23], v[100:103], v[32:47]\n"
"s_add_u32 m0, s64, 16384\n"
"s_add_u32 s92, s65, 2048\n"
"buffer_load_dwordx4 v2, s[36:39], s92 offen sc1 lds\n"
"s_add_u32 s93, s93, s53\n"
"s_waitcnt lgkmcnt(3)\n"
"v_mfma_f32_32x32x16_bf16 v[16:31], v[152:155], v[80:83], v[16:31]\n"
"ds_read_b128 v[96:99], v1 offset:10240\n"
"s_cmp_lt_i32 s86, 0\n"
"s_cselect_b32 s58, s58, s90\n"
"v_mfma_f32_32x32x16_bf16 v[32:47], a[24:27], v[80:83], v[32:47]\n"
"s_add_u32 m0, s64, 20480\n"
"s_add_u32 s92, s65, 2560\n"
"buffer_load_dwordx4 v2, s[36:39], s92 offen sc1 lds\n"
"s_waitcnt lgkmcnt(3)\n"
"v_mfma_f32_32x32x16_bf16 v[16:31], v[156:159], v[84:87], v[16:31]\n"
"ds_read_b128 v[100:103], v1 offset:11264\n"
"s_cmp_lt_u32 s58, s48\n"
"s_cselect_b32 s62, s58, -1\n"
"s_sub_u32 s90, s58, s57\n"
"v_mfma_f32_32x32x16_bf16 v[32:47], a[28:31], v[84:87], v[32:47]\n"
"s_add_u32 m0, s64, 24576\n"
"s_add_u32 s92, s65, 3072\n"
"buffer_load_dwordx4 v2, s[36:39], s92 offen sc1 lds\n"
"s_waitcnt lgkmcnt(3)\n"
"v_mfma_f32_32x32x16_bf16 v[16:31], a[144:147], v[88:91], v[16:31]\n"
"ds_read_b128 v[80:83], v1 offset:12288\n"
"s_cmp_le_u32 s90, 4\n"
"s_cselect_b32 s62, s62, -1\n"
"s_max_i32 s90, s62, 0\n"
"v_mfma_f32_32x32x16_bf16 v[32:47], a[32:35], v[88:91], v[32:47]\n"
"s_add_u32 m0, s64, 28672\n"
"s_add_u32 s92, s65, 3584\n"
"buffer_load_dwordx4 v2, s[36:39], s92 offen sc1 lds\n"
"s_lshl_b32 s90, s90, 2\n"
"s_waitcnt lgkmcnt(3)\n"
"v_mfma_f32_32x32x16_bf16 v[16:31], a[148:151], v[92:95], v[16:31]\n"
"ds_read_b128 v[84:87], v1 offset:13312\n"
"s_add_u32 s90, s90, 86528\n"
"v_add_u32 v196, s90, v8\n"
"ds_read_b32 v196, v196\n"
"v_mfma_f32_32x32x16_bf16 v[32:47], a[36:39], v[92:95], v[32:47]\n"
"s_add_u32 m0, s64, 32768\n"
"s_add_u32 s92, s65, 4096\n"
"buffer_load_dwordx4 v2, s[36:39], s92 offen sc1 lds\n"
"s_waitcnt lgkmcnt(4)\n"
"v_mfma_f32_32x32x16_bf16 v[16:31], a[152:155], v[96:99], v[16:31]\n"
"ds_read_b128 v[88:91], v1 offset:14336\n"
"s_cmp_ge_i32 s62, 0\n"
"s_cselect_b64 s[94:95], s[78:79], 0\n"
"s_and_b32 s91, s57, 1\n"
"v_mfma_f32_32x32x16_bf16 v[32:47], a[40:43], v[96:99], v[32:47]\n"
"s_lshl_b32 s91, s91, 8\n"
"s_add_u32 s91, s91, 86016\n"
"s_add_u32 s90, s57, 2\n"
"s_cmp_lt_u32 s90, s58\n"
"s_cselect_b32 s87, 1, 0\n"
"s_waitcnt lgkmcnt(4)\n"
"v_mfma_f32_32x32x16_bf16 v[16:31], a[156:159], v[100:103], v[16:31]\n"
"ds_read_b128 v[92:95], v1 offset:15360\n"
"s_cmp_lt_i32 s60, 0\n"
"s_cselect_b32 s93, 0x7ffffff0, s93\n"
"v_add_u32 v197, s93, v3\n"
"v_mfma_f32_32x32x16_bf16 v[32:47], a[44:47], v[100:103], v[32:47]\n"
"v_mul_f32 v176, 0xbfb8aa3b, v48\n"
"v_mul_f32 v177, 0xbfb8aa3b, v49\n"
"v_mul_f32 v178, 0xbfb8aa3b, v50\n"
"v_mul_f32 v179, 0xbfb8aa3b, v51\n"
"s_waitcnt lgkmcnt(4)\n"
"v_mfma_f32_32x32x16_bf16 v[16:31], a[160:163], v[80:83], v[16:31]\n"
"ds_read_b128 v[96:99], v1 offset:16384\n"
"s_waitcnt lgkmcnt(3)\n"
"v_add_u32 v196, v196, v9\n"
"v_exp_f32 v176, v176\n"
"v_mfma_f32_32x32x16_bf16 v[32:47], a[48:51], v[80:83], v[32:47]\n"
"s_mov_b32 m0, s91\n"
"s_mov_b64 exec, s[94:95]\n"
"buffer_load_dword v196, s[44:47], 0 offen sc1 lds\n"
"s_mov_b64 exec, -1\n"
"v_mfma_f32_32x32x16_bf16 v[16:31], a[164:167], v[84:87], v[16:31]\n"
"ds_read_b128 v[100:103], v1 offset:17408\n"
"v_exp_f32 v177, v177\n"
"v_exp_f32 v178, v178\n"
"v_mfma_f32_32x32x16_bf16 v[32:47], a[52:55], v[84:87], v[32:47]\n"
"v_exp_f32 v179, v179\n"
"v_add_f32 v176, 1.0, v176\n"
"v_add_f32 v177, 1.0, v177\n"
"v_add_f32 v178, 1.0, v178\n"
"s_waitcnt lgkmcnt(3)\n"
"v_mfma_f32_32x32x16_bf16 v[16:31], a[168:171], v[88:91], v[16:31]\n"
"ds_read_b128 v[80:83], v1 offset:18432\n"
"v_add_f32 v179, 1.0, v179\n"
"v_rcp_f32 v176, v176\n"
"v_mfma_f32_32x32x16_bf16 v[32:47], a[56:59], v[88:91], v[32:47]\n"
"v_rcp_f32 v177, v177\n"
"v_rcp_f32 v178, v178\n"
"s_waitcnt lgkmcnt(3)\n"
"v_mfma_f32_32x32x16_bf16 v[16:31], a[172:175], v[92:95], v[16:31]\n"
"ds_read_b128 v[84:87], v1 offset:19456\n"
"v_rcp_f32 v179, v179\n"
"v_mul_f32 v48, v48, v56\n"
"v_mfma_f32_32x32x16_bf16 v[32:47], a[60:63], v[92:95], v[32:47]\n"
"v_mul_f32 v49, v49, v57\n"
"v_mul_f32 v50, v50, v58\n"
"v_mul_f32 v51, v51, v59\n"
"v_mul_f32 v48, v48, v176\n"
"v_mul_f32 v49, v49, v177\n"
"s_waitcnt lgkmcnt(3)\n"
"v_mfma_f32_32x32x16_bf16 v[16:31], a[176:179], v[96:99], v[16:31]\n"
"ds_read_b128 v[88:91], v1 offset:20480\n"
"v_mul_f32 v50, v50, v178\n"
"v_mul_f32 v51, v51, v179\n"
"v_mul_f32 v180, 0xbfb8aa3b, v52\n"
"v_mfma_f32_32x32x16_bf16 v[32:47], a[64:67], v[96:99], v[32:47]\n"
"v_mul_f32 v181, 0xbfb8aa3b, v53\n"
"v_mul_f32 v182, 0xbfb8aa3b, v54\n"
"v_mul_f32 v183, 0xbfb8aa3b, v55\n"
"v_exp_f32 v180, v180\n"
"s_waitcnt lgkmcnt(3)\n"
"v_mfma_f32_32x32x16_bf16 v[16:31], a[180:183], v[100:103], v[16:31]\n"
"ds_read_b128 v[92:95], v1 offset:21504\n"
"v_exp_f32 v181, v181\n"
"v_mfma_f32_32x32x16_bf16 v[32:47], a[68:71], v[100:103], v[32:47]\n"
"v_exp_f32 v182, v182\n"
"v_exp_f32 v183, v183\n"
"v_add_f32 v180, 1.0, v180\n"
"s_waitcnt lgkmcnt(3)\n"
"v_mfma_f32_32x32x16_bf16 v[16:31], a[184:187], v[80:83], v[16:31]\n"
"ds_read_b128 v[96:99], v1 offset:22528\n"
"v_add_f32 v181, 1.0, v181\n"
"v_add_f32 v182, 1.0, v182\n"
"v_add_f32 v183, 1.0, v183\n"
"v_mfma_f32_32x32x16_bf16 v[32:47], a[72:75], v[80:83], v[32:47]\n"
"v_rcp_f32 v180, v180\n"
"v_rcp_f32 v181, v181\n"
"s_waitcnt lgkmcnt(3)\n"
"v_mfma_f32_32x32x16_bf16 v[16:31], a[188:191], v[84:87], v[16:31]\n"
"ds_read_b128 v[100:103], v1 offset:23552\n"
"v_rcp_f32 v182, v182\n"
"v_mfma_f32_32x32x16_bf16 v[32:47], a[76:79], v[84:87], v[32:47]\n"
"v_rcp_f32 v183, v183\n"
"v_mul_f32 v52, v52, v60\n"
"v_mul_f32 v53, v53, v61\n"
"v_mul_f32 v54, v54, v62\n"
"s_waitcnt lgkmcnt(3)\n"
"v_mfma_f32_32x32x16_bf16 v[16:31], a[192:195], v[88:91], v[16:31]\n"
"ds_read_b128 v[80:83], v1 offset:24576\n"
"v_mul_f32 v55, v55, v63\n"
"v_mul_f32 v52, v52, v180\n"
"v_mul_f32 v53, v53, v181\n"
"v_mfma_f32_32x32x16_bf16 v[32:47], a[80:83], v[88:91], v[32:47]\n"
"v_mul_f32 v54, v54, v182\n"
"v_mul_f32 v55, v55, v183\n"
"v_mul_f32 v176, 0xbfb8aa3b, v64\n"
"v_mul_f32 v177, 0xbfb8aa3b, v65\n"
"v_mul_f32 v178, 0xbfb8aa3b, v66\n"
"s_waitcnt lgkmcnt(3)\n"
"v_mfma_f32_32x32x16_bf16 v[16:31], a[196:199], v[92:95], v[16:31]\n"
"ds_read_b128 v[84:87], v1 offset:25600\n"
"v_mul_f32 v179, 0xbfb8aa3b, v67\n"
"v_exp_f32 v176, v176\n"
"v_mfma_f32_32x32x16_bf16 v[32:47], a[84:87], v[92:95], v[32:47]\n"
"v_exp_f32 v177, v177\n"
"v_exp_f32 v178, v178\n"
"s_waitcnt lgkmcnt(3)\n"
"v_mfma_f32_32x32x16_bf16 v[16:31], a[200:203], v[96:99], v[16:31]\n"
"ds_read_b128 v[88:91], v1 offset:26624\n"
"v_exp_f32 v179, v179\n"
"v_add_f32 v176, 1.0, v176\n"
"v_mfma_f32_32x32x16_bf16 v[32:47], a[88:91], v[96:99], v[32:47]\n"
"v_add_f32 v177, 1.0, v177\n"
"v_add_f32 v178, 1.0, v178\n"
"v_add_f32 v179, 1.0, v179\n"
"v_rcp_f32 v176, v176\n"
"s_waitcnt lgkmcnt(3)\n"
"v_mfma_f32_32x32x16_bf16 v[16:31], a[204:207], v[100:103], v[16:31]\n"
"ds_read_b128 v[92:95], v1 offset:27648\n"
"v_rcp_f32 v177, v177\n"
"v_mfma_f32_32x32x16_bf16 v[32:47], a[92:95], v[100:103], v[32:47]\n"
"v_rcp_f32 v178, v178\n"
"v_rcp_f32 v179, v179\n"
"v_mul_f32 v64, v64, v72\n"
"s_waitcnt lgkmcnt(3)\n"
"v_mfma_f32_32x32x16_bf16 v[16:31], a[208:211], v[80:83], v[16:31]\n"
"ds_read_b128 v[96:99], v1 offset:28672\n"
"v_mul_f32 v65, v65, v73\n"
"v_mul_f32 v66, v66, v74\n"
"v_mul_f32 v67, v67, v75\n"
"v_mfma_f32_32x32x16_bf16 v[32:47], a[96:99], v[80:83], v[32:47]\n"
"v_mul_f32 v64, v64, v176\n"
"v_mul_f32 v65, v65, v177\n"
"v_mul_f32 v66, v66, v178\n"
"v_mul_f32 v67, v67, v179\n"
"v_mul_f32 v180, 0xbfb8aa3b, v68\n"
"s_waitcnt lgkmcnt(3)\n"
"v_mfma_f32_32x32x16_bf16 v[16:31], a[212:215], v[84:87], v[16:31]\n"
"ds_read_b128 v[100:103], v1 offset:29696\n"
"v_mul_f32 v181, 0xbfb8aa3b, v69\n"
"v_mul_f32 v182, 0xbfb8aa3b, v70\n"
"v_mul_f32 v183, 0xbfb8aa3b, v71\n"
"v_mfma_f32_32x32x16_bf16 v[32:47], a[100:103], v[84:87], v[32:47]\n"
"v_exp_f32 v180, v180\n"
"v_exp_f32 v181, v181\n"
"s_waitcnt lgkmcnt(3)\n"
"v_mfma_f32_32x32x16_bf16 v[16:31], a[216:219], v[88:91], v[16:31]\n"
"ds_read_b128 v[80:83], v1 offset:30720\n"
"v_exp_f32 v182, v182\n"
"v_mfma_f32_32x32x16_bf16 v[32:47], a[104:107], v[88:91], v[32:47]\n"
"v_exp_f32 v183, v183\n"
"v_add_f32 v180, 1.0, v180\n"
"v_add_f32 v181, 1.0, v181\n"
"v_add_f32 v182, 1.0, v182\n"
"s_waitcnt lgkmcnt(3)\n"
"v_mfma_f32_32x32x16_bf16 v[16:31], a[220:223], v[92:95], v[16:31]\n"
"ds_read_b128 v[84:87], v1 offset:31744\n"
"v_add_f32 v183, 1.0, v183\n"
"v_rcp_f32 v180, v180\n"
"v_mfma_f32_32x32x16_bf16 v[32:47], a[108:111], v[92:95], v[32:47]\n"
"v_rcp_f32 v181, v181\n"
"v_rcp_f32 v182, v182\n"
"s_waitcnt lgkmcnt(3)\n"
"v_mfma_f32_32x32x16_bf16 v[16:31], a[224:227], v[96:99], v[16:31]\n"
"ds_read_b128 v[88:91], v1 offset:32768\n"
"v_rcp_f32 v183, v183\n"
"v_mul_f32 v68, v68, v76\n"
"v_mfma_f32_32x32x16_bf16 v[32:47], a[112:115], v[96:99], v[32:47]\n"
"v_mul_f32 v69, v69, v77\n"
"v_mul_f32 v70, v70, v78\n"
"v_mul_f32 v71, v71, v79\n"
"v_mul_f32 v68, v68, v180\n"
"v_mul_f32 v69, v69, v181\n"
"s_waitcnt lgkmcnt(3)\n"
"v_mfma_f32_32x32x16_bf16 v[16:31], a[228:231], v[100:103], v[16:31]\n"
"ds_read_b128 v[92:95], v1 offset:33792\n"
"v_mul_f32 v70, v70, v182\n"
"v_mul_f32 v71, v71, v183\n"
"v_cvt_pk_bf16_f32 v160, v48, v49\n"
"v_mfma_f32_32x32x16_bf16 v[32:47], a[116:119], v[100:103], v[32:47]\n"
"v_cvt_pk_bf16_f32 v161, v50, v51\n"
"v_cvt_pk_bf16_f32 v162, v52, v53\n"
"v_cvt_pk_bf16_f32 v163, v54, v55\n"
"v_cvt_pk_bf16_f32 v164, v64, v65\n"
"v_cvt_pk_bf16_f32 v165, v66, v67\n"
"s_waitcnt lgkmcnt(3)\n"
"v_mfma_f32_32x32x16_bf16 v[16:31], a[232:235], v[80:83], v[16:31]\n"
"ds_read_b128 v[96:99], v1 offset:34816\n"
"v_cvt_pk_bf16_f32 v166, v68, v69\n"
"v_cvt_pk_bf16_f32 v167, v70, v71\n"
"v_permlane32_swap_b32 v160, v162\n"
"v_mfma_f32_32x32x16_bf16 v[32:47], a[120:123], v[80:83], v[32:47]\n"
"v_permlane32_swap_b32 v161, v163\n"
"v_permlane32_swap_b32 v164, v166\n"
"v_permlane32_swap_b32 v165, v167\n"
"buffer_store_dwordx4 v[160:163], v11, s[40:43], s66 offen sc1\n"
"s_waitcnt lgkmcnt(3)\n"
"v_mfma_f32_32x32x16_bf16 v[16:31], a[236:239], v[84:87], v[16:31]\n"
"ds_read_b128 v[100:103], v1 offset:35840\n"
"buffer_store_dwordx4 v[164:167], v12, s[40:43], s66 offen sc1\n"
"v_mfma_f32_32x32x16_bf16 v[32:47], a[124:127], v[84:87], v[32:47]\n"
"s_waitcnt lgkmcnt(3)\n"
"v_mfma_f32_32x32x16_bf16 v[16:31], a[240:243], v[88:91], v[16:31]\n"
"v_mfma_f32_32x32x16_bf16 v[32:47], a[128:131], v[88:91], v[32:47]\n"
"s_waitcnt vmcnt(2) lgkmcnt(0)\n"
"s_barrier\n"
"v_mfma_f32_32x32x16_bf16 v[16:31], a[244:247], v[92:95], v[16:31]\n"
"s_sub_u32 s77, 36864, s77\n"
"v_add_u32 v1, s77, v0\n"
"ds_read_b128 v[80:83], v1\n"
"ds_read_b128 v[84:87], v1 offset:1024\n"
"v_mfma_f32_32x32x16_bf16 v[32:47], a[132:135], v[92:95], v[32:47]\n"
"buffer_store_dword v4, v197, s[44:47], 0 offen sc1\n"
"v_mfma_f32_32x32x16_bf16 v[16:31], a[248:251], v[96:99], v[16:31]\n"
"ds_read_b128 v[88:91], v1 offset:2048\n"
"v_mfma_f32_32x32x16_bf16 v[32:47], a[136:139], v[96:99], v[32:47]\n"
"s_sub_u32 s60, s57, 1\n"
"s_mov_b32 s86, s62\n"
"s_add_u32 s57, s57, 1\n"
"s_cmp_eq_u32 s87, 0\n"
"v_mfma_f32_32x32x16_bf16 v[16:31], a[252:255], v[100:103], v[16:31]\n"
"ds_read_b128 v[92:95], v1 offset:3072\n"
"v_mfma_f32_32x32x16_bf16 v[32:47], a[140:143], v[100:103], v[32:47]\n"
"s_cbranch_scc0 LPAsilu36_%=\n"
"s_mov_b32 s99, 0\n"
"s_sub_u32 s57, s57, 1\n"
"s_branch Le1silu36_%=\n"
"LfxAsilu36_%=:\n"
"s_mov_b32 s99, 1\n"
"s_sub_u32 s57, s57, 1\n"
"Le1silu36_%=:\n"
"s_mov_b32 s60, -1\n"
"s_mov_b32 s61, -1\n"
"s_cmp_eq_u32 s59, 0\n"
"s_cbranch_scc1 Le2silu36_%=\n"
"s_sub_u32 s60, s57, 1\n"
"Le2silu36_%=:\n"
"s_cmp_lt_i32 s62, 0\n"
"s_cbranch_scc1 Le3silu36_%=\n"
"s_and_b32 s72, s57, 1\n"
"s_lshl_b32 s72, s72, 8\n"
"s_add_u32 s72, s72, 86016\n"
"s_sub_u32 s67, s72, 0\n"
"v_lshrrev_b32 v192, 2, v0\n"
"v_add_u32 v192, s67, v192\n"
"ds_read_b32 v193, v192\n"
"s_waitcnt lgkmcnt(0)\n"
"v_cmp_eq_u32_e64 s[80:81], s97, v193\n"
"s_nop 1\n"
"s_sub_u32 s68, s48, s62\n"
"s_min_u32 s68, s68, 7\n"
"s_mov_b32 s70, 0\n"
"Levesilu36_%=:\n"
"s_cmp_ge_u32 s70, s68\n"
"s_cbranch_scc1 Lexesilu36_%=\n"
"s_and_b32 s69, s80, 255\n"
"s_cmp_eq_u32 s69, 255\n"
"s_cbranch_scc0 Lexesilu36_%=\n"
"s_lshr_b64 s[80:81], s[80:81], 8\n"
"s_add_u32 s70, s70, 1\n"
"s_branch Levesilu36_%=\n"
"Lexesilu36_%=:\n"
"s_add_u32 s58, s58, s70\n"
"s_waitcnt lgkmcnt(0)\n"
"Le3silu36_%=:\n"
"s_mov_b32 s59, 0\n"
"s_add_u32 s68, s57, 2\n"
"s_cmp_lt_u32 s68, s58\n"
"s_cbranch_scc0 Le4silu36_%=\n"
"s_cmp_ge_u32 s57, 3\n"
"s_cbranch_scc0 Le4silu36_%=\n"
"s_add_u32 s59, s99, 1\n"
"s_mov_b32 s86, -1\n"
"s_add_u32 s57, s57, 1\n"
"s_branch Ltopsilu36_%=\n"
"Le4silu36_%=:\n"
"s_mov_b32 s61, s57\n"
"s_cmp_eq_u32 s99, 0\n"
"s_cbranch_scc0 Le5silu36_%=\n"
"s_nop 3\n"
"v_readlane_b32 s67, v6, s57\n"
"s_nop 0\n"
"s_mul_i32 s67, s67, s52\n"
"s_add_u32 s66, s67, s51\n"
"v_mul_f32 v176, 0xbfb8aa3b, v16\n"
"v_mul_f32 v177, 0xbfb8aa3b, v17\n"
"v_mul_f32 v178, 0xbfb8aa3b, v18\n"
"v_mul_f32 v179, 0xbfb8aa3b, v19\n"
"v_exp_f32 v176, v176\n"
"v_exp_f32 v177, v177\n"
"v_exp_f32 v178, v178\n"
"v_exp_f32 v179, v179\n"
"v_add_f32 v176, 1.0, v176\n"
"v_add_f32 v177, 1.0, v177\n"
"v_add_f32 v178, 1.0, v178\n"
"v_add_f32 v179, 1.0, v179\n"
"v_rcp_f32 v176, v176\n"
"v_rcp_f32 v177, v177\n"
"v_rcp_f32 v178, v178\n"
"v_rcp_f32 v179, v179\n"
"v_mul_f32 v16, v16, v24\n"
"v_mul_f32 v17, v17, v25\n"
"v_mul_f32 v18, v18, v26\n"
"v_mul_f32 v19, v19, v27\n"
"v_mul_f32 v16, v16, v176\n"
"v_mul_f32 v17, v17, v177\n"
"v_mul_f32 v18, v18, v178\n"
"v_mul_f32 v19, v19, v179\n"
"v_mul_f32 v180, 0xbfb8aa3b, v20\n"
"v_mul_f32 v181, 0xbfb8aa3b, v21\n"
"v_mul_f32 v182, 0xbfb8aa3b, v22\n"
"v_mul_f32 v183, 0xbfb8aa3b, v23\n"
"v_exp_f32 v180, v180\n"
"v_exp_f32 v181, v181\n"
"v_exp_f32 v182, v182\n"
"v_exp_f32 v183, v183\n"
"v_add_f32 v180, 1.0, v180\n"
"v_add_f32 v181, 1.0, v181\n"
"v_add_f32 v182, 1.0, v182\n"
"v_add_f32 v183, 1.0, v183\n"
"v_rcp_f32 v180, v180\n"
"v_rcp_f32 v181, v181\n"
"v_rcp_f32 v182, v182\n"
"v_rcp_f32 v183, v183\n"
"v_mul_f32 v20, v20, v28\n"
"v_mul_f32 v21, v21, v29\n"
"v_mul_f32 v22, v22, v30\n"
"v_mul_f32 v23, v23, v31\n"
"v_mul_f32 v20, v20, v180\n"
"v_mul_f32 v21, v21, v181\n"
"v_mul_f32 v22, v22, v182\n"
"v_mul_f32 v23, v23, v183\n"
"v_mul_f32 v176, 0xbfb8aa3b, v32\n"
"v_mul_f32 v177, 0xbfb8aa3b, v33\n"
"v_mul_f32 v178, 0xbfb8aa3b, v34\n"
"v_mul_f32 v179, 0xbfb8aa3b, v35\n"
"v_exp_f32 v176, v176\n"
"v_exp_f32 v177, v177\n"
"v_exp_f32 v178, v178\n"
"v_exp_f32 v179, v179\n"
"v_add_f32 v176, 1.0, v176\n"
"v_add_f32 v177, 1.0, v177\n"
"v_add_f32 v178, 1.0, v178\n"
"v_add_f32 v179, 1.0, v179\n"
"v_rcp_f32 v176, v176\n"
"v_rcp_f32 v177, v177\n"
"v_rcp_f32 v178, v178\n"
"v_rcp_f32 v179, v179\n"
"v_mul_f32 v32, v32, v40\n"
"v_mul_f32 v33, v33, v41\n"
"v_mul_f32 v34, v34, v42\n"
"v_mul_f32 v35, v35, v43\n"
"v_mul_f32 v32, v32, v176\n"
"v_mul_f32 v33, v33, v177\n"
"v_mul_f32 v34, v34, v178\n"
"v_mul_f32 v35, v35, v179\n"
"v_mul_f32 v180, 0xbfb8aa3b, v36\n"
"v_mul_f32 v181, 0xbfb8aa3b, v37\n"
"v_mul_f32 v182, 0xbfb8aa3b, v38\n"
"v_mul_f32 v183, 0xbfb8aa3b, v39\n"
"v_exp_f32 v180, v180\n"
"v_exp_f32 v181, v181\n"
"v_exp_f32 v182, v182\n"
"v_exp_f32 v183, v183\n"
"v_add_f32 v180, 1.0, v180\n"
"v_add_f32 v181, 1.0, v181\n"
"v_add_f32 v182, 1.0, v182\n"
"v_add_f32 v183, 1.0, v183\n"
"v_rcp_f32 v180, v180\n"
"v_rcp_f32 v181, v181\n"
"v_rcp_f32 v182, v182\n"
"v_rcp_f32 v183, v183\n"
"v_mul_f32 v36, v36, v44\n"
"v_mul_f32 v37, v37, v45\n"
"v_mul_f32 v38, v38, v46\n"
"v_mul_f32 v39, v39, v47\n"
"v_mul_f32 v36, v36, v180\n"
"v_mul_f32 v37, v37, v181\n"
"v_mul_f32 v38, v38, v182\n"
"v_mul_f32 v39, v39, v183\n"
"v_cvt_pk_bf16_f32 v160, v16, v17\n"
"v_cvt_pk_bf16_f32 v161, v18, v19\n"
"v_cvt_pk_bf16_f32 v162, v20, v21\n"
"v_cvt_pk_bf16_f32 v163, v22, v23\n"
"v_cvt_pk_bf16_f32 v164, v32, v33\n"
"v_cvt_pk_bf16_f32 v165, v34, v35\n"
"v_cvt_pk_bf16_f32 v166, v36, v37\n"
"v_cvt_pk_bf16_f32 v167, v38, v39\n"
"v_permlane32_swap_b32 v160, v162\n"
"v_permlane32_swap_b32 v161, v163\n"
"v_permlane32_swap_b32 v164, v166\n"
"v_permlane32_swap_b32 v165, v167\n"
"buffer_store_dwordx4 v[160:163], v11, s[40:43], s66 offen sc1\n"
"buffer_store_dwordx4 v[164:167], v12, s[40:43], s66 offen sc1\n"
"s_add_u32 s57, s57, 1\n"
"s_branch Ltopsilu36_%=\n"
"Le5silu36_%=:\n"
"s_nop 3\n"
"v_readlane_b32 s67, v6, s57\n"
"s_nop 0\n"
"s_mul_i32 s67, s67, s52\n"
"s_add_u32 s66, s67, s51\n"
"v_mul_f32 v176, 0xbfb8aa3b, v48\n"
"v_mul_f32 v177, 0xbfb8aa3b, v49\n"
"v_mul_f32 v178, 0xbfb8aa3b, v50\n"
"v_mul_f32 v179, 0xbfb8aa3b, v51\n"
"v_exp_f32 v176, v176\n"
"v_exp_f32 v177, v177\n"
"v_exp_f32 v178, v178\n"
"v_exp_f32 v179, v179\n"
"v_add_f32 v176, 1.0, v176\n"
"v_add_f32 v177, 1.0, v177\n"
"v_add_f32 v178, 1.0, v178\n"
"v_add_f32 v179, 1.0, v179\n"
"v_rcp_f32 v176, v176\n"
"v_rcp_f32 v177, v177\n"
"v_rcp_f32 v178, v178\n"
"v_rcp_f32 v179, v179\n"
"v_mul_f32 v48, v48, v56\n"
"v_mul_f32 v49, v49, v57\n"
"v_mul_f32 v50, v50, v58\n"
"v_mul_f32 v51, v51, v59\n"
"v_mul_f32 v48, v48, v176\n"
"v_mul_f32 v49, v49, v177\n"
"v_mul_f32 v50, v50, v178\n"
"v_mul_f32 v51, v51, v179\n"
"v_mul_f32 v180, 0xbfb8aa3b, v52\n"
"v_mul_f32 v181, 0xbfb8aa3b, v53\n"
"v_mul_f32 v182, 0xbfb8aa3b, v54\n"
"v_mul_f32 v183, 0xbfb8aa3b, v55\n"
"v_exp_f32 v180, v180\n"
"v_exp_f32 v181, v181\n"
"v_exp_f32 v182, v182\n"
"v_exp_f32 v183, v183\n"
"v_add_f32 v180, 1.0, v180\n"
"v_add_f32 v181, 1.0, v181\n"
"v_add_f32 v182, 1.0, v182\n"
"v_add_f32 v183, 1.0, v183\n"
"v_rcp_f32 v180, v180\n"
"v_rcp_f32 v181, v181\n"
"v_rcp_f32 v182, v182\n"
"v_rcp_f32 v183, v183\n"
"v_mul_f32 v52, v52, v60\n"
"v_mul_f32 v53, v53, v61\n"
"v_mul_f32 v54, v54, v62\n"
"v_mul_f32 v55, v55, v63\n"
"v_mul_f32 v52, v52, v180\n"
"v_mul_f32 v53, v53, v181\n"
"v_mul_f32 v54, v54, v182\n"
"v_mul_f32 v55, v55, v183\n"
"v_mul_f32 v176, 0xbfb8aa3b, v64\n"
"v_mul_f32 v177, 0xbfb8aa3b, v65\n"
"v_mul_f32 v178, 0xbfb8aa3b, v66\n"
"v_mul_f32 v179, 0xbfb8aa3b, v67\n"
"v_exp_f32 v176, v176\n"
"v_exp_f32 v177, v177\n"
"v_exp_f32 v178, v178\n"
"v_exp_f32 v179, v179\n"
"v_add_f32 v176, 1.0, v176\n"
"v_add_f32 v177, 1.0, v177\n"
"v_add_f32 v178, 1.0, v178\n"
"v_add_f32 v179, 1.0, v179\n"
"v_rcp_f32 v176, v176\n"
"v_rcp_f32 v177, v177\n"
"v_rcp_f32 v178, v178\n"
"v_rcp_f32 v179, v179\n"
"v_mul_f32 v64, v64, v72\n"
"v_mul_f32 v65, v65, v73\n"
"v_mul_f32 v66, v66, v74\n"
"v_mul_f32 v67, v67, v75\n"
"v_mul_f32 v64, v64, v176\n"
"v_mul_f32 v65, v65, v177\n"
"v_mul_f32 v66, v66, v178\n"
"v_mul_f32 v67, v67, v179\n"
"v_mul_f32 v180, 0xbfb8aa3b, v68\n"
"v_mul_f32 v181, 0xbfb8aa3b, v69\n"
"v_mul_f32 v182, 0xbfb8aa3b, v70\n"
"v_mul_f32 v183, 0xbfb8aa3b, v71\n"
"v_exp_f32 v180, v180\n"
"v_exp_f32 v181, v181\n"
"v_exp_f32 v182, v182\n"
"v_exp_f32 v183, v183\n"
"v_add_f32 v180, 1.0, v180\n"
"v_add_f32 v181, 1.0, v181\n"
"v_add_f32 v182, 1.0, v182\n"
"v_add_f32 v183, 1.0, v183\n"
"v_rcp_f32 v180, v180\n"
"v_rcp_f32 v181, v181\n"
"v_rcp_f32 v182, v182\n"
"v_rcp_f32 v183, v183\n"
"v_mul_f32 v68, v68, v76\n"
"v_mul_f32 v69, v69, v77\n"
"v_mul_f32 v70, v70, v78\n"
"v_mul_f32 v71, v71, v79\n"
"v_mul_f32 v68, v68, v180\n"
"v_mul_f32 v69, v69, v181\n"
"v_mul_f32 v70, v70, v182\n"
"v_mul_f32 v71, v71, v183\n"
"v_cvt_pk_bf16_f32 v160, v48, v49\n"
"v_cvt_pk_bf16_f32 v161, v50, v51\n"
"v_cvt_pk_bf16_f32 v162, v52, v53\n"
"v_cvt_pk_bf16_f32 v163, v54, v55\n"
"v_cvt_pk_bf16_f32 v164, v64, v65\n"
"v_cvt_pk_bf16_f32 v165, v66, v67\n"
"v_cvt_pk_bf16_f32 v166, v68, v69\n"
"v_cvt_pk_bf16_f32 v167, v70, v71\n"
"v_permlane32_swap_b32 v160, v162\n"
"v_permlane32_swap_b32 v161, v163\n"
"v_permlane32_swap_b32 v164, v166\n"
"v_permlane32_swap_b32 v165, v167\n"
"buffer_store_dwordx4 v[160:163], v11, s[40:43], s66 offen sc1\n"
"buffer_store_dwordx4 v[164:167], v12, s[40:43], s66 offen sc1\n"
"s_add_u32 s57, s57, 1\n"
"s_branch Ltopsilu36_%=\n"
"Lnonsilu36_%=:\n"
"s_max_i32 s68, s60, s61\n"
"s_cmp_ge_i32 s68, 0\n"
"s_cselect_b32 s63, 1, 0\n"
"s_waitcnt lgkmcnt(3)\n"
"v_mfma_f32_32x32x16_bf16 v[16:31], v[128:131], v[80:83], 0\n"
"ds_read_b128 v[96:99], v1 offset:4096\n"
"v_mfma_f32_32x32x16_bf16 v[32:47], a[0:3], v[80:83], 0\n"
"s_add_u32 s90, s57, 1\n"
"s_cmp_lt_u32 s90, s48\n"
"s_cselect_b32 s62, s90, -1\n"
"s_lshl_b32 s90, s90, 2\n"
"s_add_u32 s90, s90, 86528\n"
"v_add_u32 v196, s90, v8\n"
"s_waitcnt lgkmcnt(3)\n"
"v_mfma_f32_32x32x16_bf16 v[16:31], v[132:135], v[84:87], v[16:31]\n"
"ds_read_b128 v[100:103], v1 offset:5120\n"
"ds_read_b32 v196, v196\n"
"s_cmp_ge_i32 s62, 0\n"
"s_cselect_b64 s[94:95], s[78:79], 0\n"
"v_mfma_f32_32x32x16_bf16 v[32:47], a[4:7], v[84:87], v[32:47]\n"
"s_and_b32 s94, s94, 65535\n"
"s_mov_b32 s95, 0\n"
"s_waitcnt lgkmcnt(4)\n"
"v_mfma_f32_32x32x16_bf16 v[16:31], v[136:139], v[88:91], v[16:31]\n"
"ds_read_b128 v[80:83], v1 offset:6144\n"
"v_mfma_f32_32x32x16_bf16 v[32:47], a[8:11], v[88:91], v[32:47]\n"
"s_waitcnt lgkmcnt(4)\n"
"v_mfma_f32_32x32x16_bf16 v[16:31], v[140:143], v[92:95], v[16:31]\n"
"ds_read_b128 v[84:87], v1 offset:7168\n"
"s_waitcnt lgkmcnt(2)\n"
"v_add_u32 v196, v196, v9\n"
"v_cndmask_b32_e64 v196, v15, v196, s[94:95]\n"
"v_mfma_f32_32x32x16_bf16 v[32:47], a[12:15], v[92:95], v[32:47]\n"
"s_mov_b32 s91, 87296\n"
"s_cmp_eq_u32 %0, 2\n"
"s_cselect_b32 m0, s91, 87040\n"
"s_nop 0\n"
"buffer_load_dword v196, s[44:47], 0 offen sc1 lds\n"
"v_mfma_f32_32x32x16_bf16 v[16:31], v[144:147], v[96:99], v[16:31]\n"
"ds_read_b128 v[88:91], v1 offset:8192\n"
"v_mfma_f32_32x32x16_bf16 v[32:47], a[16:19], v[96:99], v[32:47]\n"
"v_mfma_f32_32x32x16_bf16 v[16:31], v[148:151], v[100:103], v[16:31]\n"
"ds_read_b128 v[92:95], v1 offset:9216\n"
"v_mfma_f32_32x32x16_bf16 v[32:47], a[20:23], v[100:103], v[32:47]\n"
"s_waitcnt lgkmcnt(3)\n"
"v_mfma_f32_32x32x16_bf16 v[16:31], v[152:155], v[80:83], v[16:31]\n"
"ds_read_b128 v[96:99], v1 offset:10240\n"
"v_mfma_f32_32x32x16_bf16 v[32:47], a[24:27], v[80:83], v[32:47]\n"
"s_waitcnt lgkmcnt(3)\n"
"v_mfma_f32_32x32x16_bf16 v[16:31], v[156:159], v[84:87], v[16:31]\n"
"ds_read_b128 v[100:103], v1 offset:11264\n"
"v_mfma_f32_32x32x16_bf16 v[32:47], a[28:31], v[84:87], v[32:47]\n"
"s_waitcnt lgkmcnt(3)\n"
"v_mfma_f32_32x32x16_bf16 v[16:31], a[144:147], v[88:91], v[16:31]\n"
"ds_read_b128 v[80:83], v1 offset:12288\n"
"v_mfma_f32_32x32x16_bf16 v[32:47], a[32:35], v[88:91], v[32:47]\n"
"s_waitcnt lgkmcnt(3)\n"
"v_mfma_f32_32x32x16_bf16 v[16:31], a[148:151], v[92:95], v[16:31]\n"
"ds_read_b128 v[84:87], v1 offset:13312\n"
"v_mfma_f32_32x32x16_bf16 v[32:47], a[36:39], v[92:95], v[32:47]\n"
"s_waitcnt lgkmcnt(3)\n"
"v_mfma_f32_32x32x16_bf16 v[16:31], a[152:155], v[96:99], v[16:31]\n"
"ds_read_b128 v[88:91], v1 offset:14336\n"
"v_mfma_f32_32x32x16_bf16 v[32:47], a[40:43], v[96:99], v[32:47]\n"
"s_cmp_eq_u32 s63, 0\n"
"s_cbranch_scc1 LnmLsilu36_%=\n"
"s_waitcnt vmcnt(1)\n"
"s_barrier\n"
"v_readlane_b32 s93, v6, s60\n"
"s_nop 0\n"
"s_mul_i32 s93, s93, s54\n"
"s_add_u32 s93, s93, s53\n"
"s_cmp_lt_i32 s60, 0\n"
"s_cselect_b32 s93, 0x7ffffff0, s93\n"
"v_add_u32 v197, s93, v3\n"
"buffer_store_dword v4, v197, s[44:47], 0 offen sc1\n"
"v_readlane_b32 s93, v6, s61\n"
"s_nop 0\n"
"s_mul_i32 s93, s93, s54\n"
"s_add_u32 s93, s93, s53\n"
"s_cmp_lt_i32 s61, 0\n"
"s_cselect_b32 s93, 0x7ffffff0, s93\n"
"v_add_u32 v199, s93, v3\n"
"buffer_store_dword v4, v199, s[44:47], 0 offen sc1\n"
"LnmLsilu36_%=:\n"
"s_waitcnt lgkmcnt(3)\n"
"v_mfma_f32_32x32x16_bf16 v[16:31], a[156:159], v[100:103], v[16:31]\n"
"ds_read_b128 v[92:95], v1 offset:15360\n"
"v_mfma_f32_32x32x16_bf16 v[32:47], a[44:47], v[100:103], v[32:47]\n"
"s_waitcnt lgkmcnt(3)\n"
"v_mfma_f32_32x32x16_bf16 v[16:31], a[160:163], v[80:83], v[16:31]\n"
"ds_read_b128 v[96:99], v1 offset:16384\n"
"v_mfma_f32_32x32x16_bf16 v[32:47], a[48:51], v[80:83], v[32:47]\n"
"s_waitcnt lgkmcnt(3)\n"
"v_mfma_f32_32x32x16_bf16 v[16:31], a[164:167], v[84:87], v[16:31]\n"
"ds_read_b128 v[100:103], v1 offset:17408\n"
"v_mfma_f32_32x32x16_bf16 v[32:47], a[52:55], v[84:87], v[32:47]\n"
"s_waitcnt lgkmcnt(3)\n"
"v_mfma_f32_32x32x16_bf16 v[16:31], a[168:171], v[88:91], v[16:31]\n"
"ds_read_b128 v[80:83], v1 offset:18432\n"
"v_mfma_f32_32x32x16_bf16 v[32:47], a[56:59], v[88:91], v[32:47]\n"
"s_waitcnt lgkmcnt(3)\n"
"v_mfma_f32_32x32x16_bf16 v[16:31], a[172:175], v[92:95], v[16:31]\n"
"ds_read_b128 v[84:87], v1 offset:19456\n"
"v_mfma_f32_32x32x16_bf16 v[32:47], a[60:63], v[92:95], v[32:47]\n"
"s_mov_b32 s91, 87552\n"
"s_cmp_eq_u32 %0, 2\n"
"s_cselect_b32 m0, s91, 87040\n"
"s_nop 0\n"
"buffer_load_dword v196, s[44:47], 0 offen sc1 lds\n"
"s_waitcnt lgkmcnt(3)\n"
"v_mfma_f32_32x32x16_bf16 v[16:31], a[176:179], v[96:99], v[16:31]\n"
"ds_read_b128 v[88:91], v1 offset:20480\n"
"v_mfma_f32_32x32x16_bf16 v[32:47], a[64:67], v[96:99], v[32:47]\n"
"s_waitcnt lgkmcnt(3)\n"
"v_mfma_f32_32x32x16_bf16 v[16:31], a[180:183], v[100:103], v[16:31]\n"
"ds_read_b128 v[92:95], v1 offset:21504\n"
"v_mfma_f32_32x32x16_bf16 v[32:47], a[68:71], v[100:103], v[32:47]\n"
"s_waitcnt lgkmcnt(3)\n"
"v_mfma_f32_32x32x16_bf16 v[16:31], a[184:187], v[80:83], v[16:31]\n"
"ds_read_b128 v[96:99], v1 offset:22528\n"
"v_mfma_f32_32x32x16_bf16 v[32:47], a[72:75], v[80:83], v[32:47]\n"
"s_waitcnt lgkmcnt(3)\n"
"v_mfma_f32_32x32x16_bf16 v[16:31], a[188:191], v[84:87], v[16:31]\n"
"ds_read_b128 v[100:103], v1 offset:23552\n"
"v_mfma_f32_32x32x16_bf16 v[32:47], a[76:79], v[84:87], v[32:47]\n"
"s_waitcnt lgkmcnt(3)\n"
"v_mfma_f32_32x32x16_bf16 v[16:31], a[192:195], v[88:91], v[16:31]\n"
"ds_read_b128 v[80:83], v1 offset:24576\n"
"v_mfma_f32_32x32x16_bf16 v[32:47], a[80:83], v[88:91], v[32:47]\n"
"s_waitcnt lgkmcnt(3)\n"
"v_mfma_f32_32x32x16_bf16 v[16:31], a[196:199], v[92:95], v[16:31]\n"
"ds_read_b128 v[84:87], v1 offset:25600\n"
"v_mfma_f32_32x32x16_bf16 v[32:47], a[84:87], v[92:95], v[32:47]\n"
"s_waitcnt lgkmcnt(3)\n"
"v_mfma_f32_32x32x16_bf16 v[16:31], a[200:203], v[96:99], v[16:31]\n"
"ds_read_b128 v[88:91], v1 offset:26624\n"
"v_mfma_f32_32x32x16_bf16 v[32:47], a[88:91], v[96:99], v[32:47]\n"
"s_waitcnt lgkmcnt(3)\n"
"v_mfma_f32_32x32x16_bf16 v[16:31], a[204:207], v[100:103], v[16:31]\n"
"ds_read_b128 v[92:95], v1 offset:27648\n"
"v_mfma_f32_32x32x16_bf16 v[32:47], a[92:95], v[100:103], v[32:47]\n"
"s_cmp_eq_u32 s63, 0\n"
"s_cbranch_scc1 LpwLsilu36_%=\n"
"s_waitcnt vmcnt(3)\n"
"s_branch LpxLsilu36_%=\n"
"LpwLsilu36_%=:\n"
"s_waitcnt vmcnt(1)\n"
"LpxLsilu36_%=:\n"
"s_barrier\n"
"s_waitcnt lgkmcnt(3)\n"
"v_mfma_f32_32x32x16_bf16 v[16:31], a[208:211], v[80:83], v[16:31]\n"
"ds_read_b128 v[96:99], v1 offset:28672\n"
"ds_read_b32 v195, v212 offset:1280\n"
"s_add_u32 s92, s57, 1\n"
"v_readlane_b32 s92, v6, s92\n"
"s_sub_u32 s64, 36864, s77\n"
"v_mfma_f32_32x32x16_bf16 v[32:47], a[96:99], v[80:83], v[32:47]\n"
"s_add_u32 s64, s64, s55\n"
"s_mul_i32 s92, s92, s50\n"
"s_add_u32 s65, s92, s49\n"
"s_waitcnt lgkmcnt(4)\n"
"v_mfma_f32_32x32x16_bf16 v[16:31], a[212:215], v[84:87], v[16:31]\n"
"ds_read_b128 v[100:103], v1 offset:29696\n"
"v_mfma_f32_32x32x16_bf16 v[32:47], a[100:103], v[84:87], v[32:47]\n"
"s_waitcnt lgkmcnt(1)\n"
"v_cmp_ne_u32_e64 s[80:81], s97, v195\n"
"v_mfma_f32_32x32x16_bf16 v[16:31], a[216:219], v[88:91], v[16:31]\n"
"ds_read_b128 v[80:83], v1 offset:30720\n"
"s_and_b32 s80, s80, 65535\n"
"s_mov_b32 s81, 0\n"
"s_ff1_i32_b64 s90, s[80:81]\n"
"s_cmp_lt_i32 s90, 0\n"
"s_cselect_b32 s90, 16, s90\n"
"v_mfma_f32_32x32x16_bf16 v[32:47], a[104:107], v[88:91], v[32:47]\n"
"s_mul_i32 s90, s90, 8192\n"
"s_lshr_b32 s90, s90, 16\n"
"s_sub_u32 s91, s48, s62\n"
"s_min_u32 s90, s90, s91\n"
"s_add_u32 s90, s90, s62\n"
"v_mfma_f32_32x32x16_bf16 v[16:31], a[220:223], v[92:95], v[16:31]\n"
"ds_read_b128 v[84:87], v1 offset:31744\n"
"s_cmp_lt_i32 s62, 0\n"
"s_cselect_b32 s58, s58, s90\n"
"s_add_u32 s90, s57, 1\n"
"v_mfma_f32_32x32x16_bf16 v[32:47], a[108:111], v[92:95], v[32:47]\n"
"s_cmp_lt_u32 s90, s58\n"
"s_cselect_b64 s[94:95], -1, 0\n"
"s_cselect_b32 s87, 1, 0\n"
"v_cndmask_b32_e64 v198, v15, v2, s[94:95]\n"
"v_mfma_f32_32x32x16_bf16 v[16:31], a[224:227], v[96:99], v[16:31]\n"
"ds_read_b128 v[88:91], v1 offset:32768\n"
"s_add_u32 m0, s64, 0\n"
"s_add_u32 s92, s65, 0\n"
"buffer_load_dwordx4 v198, s[36:39], s92 offen sc1 lds\n"
"v_mfma_f32_32x32x16_bf16 v[32:47], a[112:115], v[96:99], v[32:47]\n"
"s_add_u32 m0, s64, 4096\n"
"s_add_u32 s92, s65, 512\n"
"buffer_load_dwordx4 v198, s[36:39], s92 offen sc1 lds\n"
"s_waitcnt lgkmcnt(3)\n"
"v_mfma_f32_32x32x16_bf16 v[16:31], a[228:231], v[100:103], v[16:31]\n"
"ds_read_b128 v[92:95], v1 offset:33792\n"
"s_add_u32 m0, s64, 8192\n"
"s_add_u32 s92, s65, 1024\n"
"buffer_load_dwordx4 v198, s[36:39], s92 offen sc1 lds\n"
"v_mfma_f32_32x32x16_bf16 v[32:47], a[116:119], v[100:103], v[32:47]\n"
"s_add_u32 m0, s64, 12288\n"
"s_add_u32 s92, s65, 1536\n"
"buffer_load_dwordx4 v198, s[36:39], s92 offen sc1 lds\n"
"s_waitcnt lgkmcnt(3)\n"
"v_mfma_f32_32x32x16_bf16 v[16:31], a[232:235], v[80:83], v[16:31]\n"
"ds_read_b128 v[96:99], v1 offset:34816\n"
"s_add_u32 m0, s64, 16384\n"
"s_add_u32 s92, s65, 2048\n"
"buffer_load_dwordx4 v198, s[36:39], s92 offen sc1 lds\n"
"v_mfma_f32_32x32x16_bf16 v[32:47], a[120:123], v[80:83], v[32:47]\n"
"s_add_u32 m0, s64, 20480\n"
"s_add_u32 s92, s65, 2560\n"
"buffer_load_dwordx4 v198, s[36:39], s92 offen sc1 lds\n"
"s_waitcnt lgkmcnt(3)\n"
"v_mfma_f32_32x32x16_bf16 v[16:31], a[236:239], v[84:87], v[16:31]\n"
"ds_read_b128 v[100:103], v1 offset:35840\n"
"s_add_u32 m0, s64, 24576\n"
"s_add_u32 s92, s65, 3072\n"
"buffer_load_dwordx4 v198, s[36:39], s92 offen sc1 lds\n"
"v_mfma_f32_32x32x16_bf16 v[32:47], a[124:127], v[84:87], v[32:47]\n"
"s_add_u32 m0, s64, 28672\n"
"s_add_u32 s92, s65, 3584\n"
"buffer_load_dwordx4 v198, s[36:39], s92 offen sc1 lds\n"
"s_waitcnt lgkmcnt(3)\n"
"v_mfma_f32_32x32x16_bf16 v[16:31], a[240:243], v[88:91], v[16:31]\n"
"s_add_u32 m0, s64, 32768\n"
"s_add_u32 s92, s65, 4096\n"
"buffer_load_dwordx4 v198, s[36:39], s92 offen sc1 lds\n"
"v_mfma_f32_32x32x16_bf16 v[32:47], a[128:131], v[88:91], v[32:47]\n"
"s_waitcnt lgkmcnt(2)\n"
"v_mfma_f32_32x32x16_bf16 v[16:31], a[244:247], v[92:95], v[16:31]\n"
"v_mfma_f32_32x32x16_bf16 v[32:47], a[132:135], v[92:95], v[32:47]\n"
"s_waitcnt lgkmcnt(1)\n"
"v_mfma_f32_32x32x16_bf16 v[16:31], a[248:251], v[96:99], v[16:31]\n"
"v_mfma_f32_32x32x16_bf16 v[32:47], a[136:139], v[96:99], v[32:47]\n"
"s_waitcnt lgkmcnt(0)\n"
"v_mfma_f32_32x32x16_bf16 v[16:31], a[252:255], v[100:103], v[16:31]\n"
"v_mfma_f32_32x32x16_bf16 v[32:47], a[140:143], v[100:103], v[32:47]\n"
"s_mov_b32 s60, -1\n"
"s_mov_b32 s61, -1\n"
"s_cmp_eq_u32 s87, 0\n"
"s_cbranch_scc0 Lwaysilu36_%=\n"
"s_cmp_lt_i32 s62, 0\n"
"s_cbranch_scc1 Lnoldsilu36_%=\n"
"s_waitcnt vmcnt(9) lgkmcnt(0)\n"
"s_barrier\n"
"s_sub_u32 s67, 87552, 0\n"
"v_lshrrev_b32 v192, 2, v0\n"
"v_add_u32 v192, s67, v192\n"
"ds_read_b32 v193, v192\n"
"s_waitcnt lgkmcnt(0)\n"
"v_cmp_eq_u32_e64 s[80:81], s97, v193\n"
"s_nop 1\n"
"s_sub_u32 s68, s48, s62\n"
"s_min_u32 s68, s68, 2\n"
"s_mov_b32 s70, 0\n"
"Levs2silu36_%=:\n"
"s_cmp_ge_u32 s70, s68\n"
"s_cbranch_scc1 Lexs2silu36_%=\n"
"s_and_b32 s69, s80, 255\n"
"s_cmp_eq_u32 s69, 255\n"
"s_cbranch_scc0 Lexs2silu36_%=\n"
"s_lshr_b64 s[80:81], s[80:81], 8\n"
"s_add_u32 s70, s70, 1\n"
"s_branch Levs2silu36_%=\n"
"Lexs2silu36_%=:\n"
"s_add_u32 s58, s62, s70\n"
"s_cmp_lt_u32 s62, s58\n"
"s_cbranch_scc0 Lnoldsilu36_%=\n"
"v_readlane_b32 s67, v6, s62\n"
"s_nop 0\n"
"s_mul_i32 s67, s67, s50\n"
"s_add_u32 s65, s67, s49\n"
"s_sub_u32 s64, 36864, s77\n"
"s_lshl_b32 s68, %0, 10\n"
"s_add_u32 s64, s64, s68\n"
"s_add_u32 m0, s64, 0\n"
"s_add_u32 s70, s65, 0\n"
"buffer_load_dwordx4 v2, s[36:39], s70 offen sc1 lds\n"
"s_add_u32 m0, s64, 4096\n"
"s_add_u32 s70, s65, 512\n"
"buffer_load_dwordx4 v2, s[36:39], s70 offen sc1 lds\n"
"s_add_u32 m0, s64, 8192\n"
"s_add_u32 s70, s65, 1024\n"
"buffer_load_dwordx4 v2, s[36:39], s70 offen sc1 lds\n"
"s_add_u32 m0, s64, 12288\n"
"s_add_u32 s70, s65, 1536\n"
"buffer_load_dwordx4 v2, s[36:39], s70 offen sc1 lds\n"
"s_add_u32 m0, s64, 16384\n"
"s_add_u32 s70, s65, 2048\n"
"buffer_load_dwordx4 v2, s[36:39], s70 offen sc1 lds\n"
"s_add_u32 m0, s64, 20480\n"
"s_add_u32 s70, s65, 2560\n"
"buffer_load_dwordx4 v2, s[36:39], s70 offen sc1 lds\n"
"s_add_u32 m0, s64, 24576\n"
"s_add_u32 s70, s65, 3072\n"
"buffer_load_dwordx4 v2, s[36:39], s70 offen sc1 lds\n"
"s_add_u32 m0, s64, 28672\n"
"s_add_u32 s70, s65, 3584\n"
"buffer_load_dwordx4 v2, s[36:39], s70 offen sc1 lds\n"
"s_add_u32 m0, s64, 32768\n"
"s_add_u32 s70, s65, 4096\n"
"buffer_load_dwordx4 v2, s[36:39], s70 offen sc1 lds\n"
"Lwaysilu36_%=:\n"
"s_waitcnt lgkmcnt(0)\n"
"s_nop 7\n"
"v_readlane_b32 s67, v6, s57\n"
"s_nop 0\n"
"s_mul_i32 s67, s67, s52\n"
"s_add_u32 s66, s67, s51\n"
"v_mul_f32 v176, 0xbfb8aa3b, v16\n"
"v_mul_f32 v177, 0xbfb8aa3b, v17\n"
"v_mul_f32 v178, 0xbfb8aa3b, v18\n"
"v_mul_f32 v179, 0xbfb8aa3b, v19\n"
"v_exp_f32 v176, v176\n"
"v_exp_f32 v177, v177\n"
"v_exp_f32 v178, v178\n"
"v_exp_f32 v179, v179\n"
"v_add_f32 v176, 1.0, v176\n"
"v_add_f32 v177, 1.0, v177\n"
"v_add_f32 v178, 1.0, v178\n"
"v_add_f32 v179, 1.0, v179\n"
"v_rcp_f32 v176, v176\n"
"v_rcp_f32 v177, v177\n"
"v_rcp_f32 v178, v178\n"
"v_rcp_f32 v179, v179\n"
"v_mul_f32 v16, v16, v24\n"
"v_mul_f32 v17, v17, v25\n"
"v_mul_f32 v18, v18, v26\n"
"v_mul_f32 v19, v19, v27\n"
"v_mul_f32 v16, v16, v176\n"
"v_mul_f32 v17, v17, v177\n"
"v_mul_f32 v18, v18, v178\n"
"v_mul_f32 v19, v19, v179\n"
"v_mul_f32 v180, 0xbfb8aa3b, v20\n"
"v_mul_f32 v181, 0xbfb8aa3b, v21\n"
"v_mul_f32 v182, 0xbfb8aa3b, v22\n"
"v_mul_f32 v183, 0xbfb8aa3b, v23\n"
"v_exp_f32 v180, v180\n"
"v_exp_f32 v181, v181\n"
"v_exp_f32 v182, v182\n"
"v_exp_f32 v183, v183\n"
"v_add_f32 v180, 1.0, v180\n"
"v_add_f32 v181, 1.0, v181\n"
"v_add_f32 v182, 1.0, v182\n"
"v_add_f32 v183, 1.0, v183\n"
"v_rcp_f32 v180, v180\n"
"v_rcp_f32 v181, v181\n"
"v_rcp_f32 v182, v182\n"
"v_rcp_f32 v183, v183\n"
"v_mul_f32 v20, v20, v28\n"
"v_mul_f32 v21, v21, v29\n"
"v_mul_f32 v22, v22, v30\n"
"v_mul_f32 v23, v23, v31\n"
"v_mul_f32 v20, v20, v180\n"
"v_mul_f32 v21, v21, v181\n"
"v_mul_f32 v22, v22, v182\n"
"v_mul_f32 v23, v23, v183\n"
"v_mul_f32 v176, 0xbfb8aa3b, v32\n"
"v_mul_f32 v177, 0xbfb8aa3b, v33\n"
"v_mul_f32 v178, 0xbfb8aa3b, v34\n"
"v_mul_f32 v179, 0xbfb8aa3b, v35\n"
"v_exp_f32 v176, v176\n"
"v_exp_f32 v177, v177\n"
"v_exp_f32 v178, v178\n"
"v_exp_f32 v179, v179\n"
"v_add_f32 v176, 1.0, v176\n"
"v_add_f32 v177, 1.0, v177\n"
"v_add_f32 v178, 1.0, v178\n"
"v_add_f32 v179, 1.0, v179\n"
"v_rcp_f32 v176, v176\n"
"v_rcp_f32 v177, v177\n"
"v_rcp_f32 v178, v178\n"
"v_rcp_f32 v179, v179\n"
"v_mul_f32 v32, v32, v40\n"
"v_mul_f32 v33, v33, v41\n"
"v_mul_f32 v34, v34, v42\n"
"v_mul_f32 v35, v35, v43\n"
"v_mul_f32 v32, v32, v176\n"
"v_mul_f32 v33, v33, v177\n"
"v_mul_f32 v34, v34, v178\n"
"v_mul_f32 v35, v35, v179\n"
"v_mul_f32 v180, 0xbfb8aa3b, v36\n"
"v_mul_f32 v181, 0xbfb8aa3b, v37\n"
"v_mul_f32 v182, 0xbfb8aa3b, v38\n"
"v_mul_f32 v183, 0xbfb8aa3b, v39\n"
"v_exp_f32 v180, v180\n"
"v_exp_f32 v181, v181\n"
"v_exp_f32 v182, v182\n"
"v_exp_f32 v183, v183\n"
"v_add_f32 v180, 1.0, v180\n"
"v_add_f32 v181, 1.0, v181\n"
"v_add_f32 v182, 1.0, v182\n"
"v_add_f32 v183, 1.0, v183\n"
"v_rcp_f32 v180, v180\n"
"v_rcp_f32 v181, v181\n"
"v_rcp_f32 v182, v182\n"
"v_rcp_f32 v183, v183\n"
"v_mul_f32 v36, v36, v44\n"
"v_mul_f32 v37, v37, v45\n"
"v_mul_f32 v38, v38, v46\n"
"v_mul_f32 v39, v39, v47\n"
"v_mul_f32 v36, v36, v180\n"
"v_mul_f32 v37, v37, v181\n"
"v_mul_f32 v38, v38, v182\n"
"v_mul_f32 v39, v39, v183\n"
"v_cvt_pk_bf16_f32 v160, v16, v17\n"
"v_cvt_pk_bf16_f32 v161, v18, v19\n"
"v_cvt_pk_bf16_f32 v162, v20, v21\n"
"v_cvt_pk_bf16_f32 v163, v22, v23\n"
"v_cvt_pk_bf16_f32 v164, v32, v33\n"
"v_cvt_pk_bf16_f32 v165, v34, v35\n"
"v_cvt_pk_bf16_f32 v166, v36, v37\n"
"v_cvt_pk_bf16_f32 v167, v38, v39\n"
"v_permlane32_swap_b32 v160, v162\n"
"v_permlane32_swap_b32 v161, v163\n"
"v_permlane32_swap_b32 v164, v166\n"
"v_permlane32_swap_b32 v165, v167\n"
"buffer_store_dwordx4 v[160:163], v11, s[40:43], s66 offen sc1\n"
"buffer_store_dwordx4 v[164:167], v12, s[40:43], s66 offen sc1\n"
"s_waitcnt vmcnt(2) lgkmcnt(0)\n"
"s_barrier\n"
"s_mov_b32 s61, s57\n"
"s_add_u32 s62, s57, 1\n"
"s_sub_u32 s77, 36864, s77\n"
"v_add_u32 v1, s77, v0\n"
"ds_read_b128 v[80:83], v1\n"
"ds_read_b128 v[84:87], v1 offset:1024\n"
"ds_read_b128 v[88:91], v1 offset:2048\n"
"ds_read_b128 v[92:95], v1 offset:3072\n"
"s_add_u32 s57, s57, 1\n"
"s_branch Ltopsilu36_%=\n"
"Lnoldsilu36_%=:\n"
"s_add_u32 s62, s57, 1\n"
"s_waitcnt lgkmcnt(0)\n"
"s_cmp_lt_u32 s62, s48\n"
"s_cbranch_scc0 Ln1silu36_%=\n"
"s_lshl_b32 s67, s62, 2\n"
"s_add_u32 s67, s67, 86528\n"
"v_add_u32 v192, s67, v8\n"
"ds_read_b32 v192, v192\n"
"s_waitcnt lgkmcnt(0)\n"
"v_add_u32 v192, v192, v9\n"
"s_mov_b32 m0, 87808\n"
"s_and_b32 exec_lo, s78, 65535\n"
"s_mov_b32 exec_hi, 0\n"
"buffer_load_dword v192, s[44:47], 0 offen sc1 lds\n"
"s_mov_b64 exec, -1\n"
"s_waitcnt lgkmcnt(0)\n"
"Ln1silu36_%=:\n"
"s_nop 7\n"
"v_readlane_b32 s67, v6, s57\n"
"s_nop 0\n"
"s_mul_i32 s67, s67, s52\n"
"s_add_u32 s66, s67, s51\n"
"v_mul_f32 v176, 0xbfb8aa3b, v16\n"
"v_mul_f32 v177, 0xbfb8aa3b, v17\n"
"v_mul_f32 v178, 0xbfb8aa3b, v18\n"
"v_mul_f32 v179, 0xbfb8aa3b, v19\n"
"v_exp_f32 v176, v176\n"
"v_exp_f32 v177, v177\n"
"v_exp_f32 v178, v178\n"
"v_exp_f32 v179, v179\n"
"v_add_f32 v176, 1.0, v176\n"
"v_add_f32 v177, 1.0, v177\n"
"v_add_f32 v178, 1.0, v178\n"
"v_add_f32 v179, 1.0, v179\n"
"v_rcp_f32 v176, v176\n"
"v_rcp_f32 v177, v177\n"
"v_rcp_f32 v178, v178\n"
"v_rcp_f32 v179, v179\n"
"v_mul_f32 v16, v16, v24\n"
"v_mul_f32 v17, v17, v25\n"
"v_mul_f32 v18, v18, v26\n"
"v_mul_f32 v19, v19, v27\n"
"v_mul_f32 v16, v16, v176\n"
"v_mul_f32 v17, v17, v177\n"
"v_mul_f32 v18, v18, v178\n"
"v_mul_f32 v19, v19, v179\n"
"v_mul_f32 v180, 0xbfb8aa3b, v20\n"
"v_mul_f32 v181, 0xbfb8aa3b, v21\n"
"v_mul_f32 v182, 0xbfb8aa3b, v22\n"
"v_mul_f32 v183, 0xbfb8aa3b, v23\n"
"v_exp_f32 v180, v180\n"
"v_exp_f32 v181, v181\n"
"v_exp_f32 v182, v182\n"
"v_exp_f32 v183, v183\n"
"v_add_f32 v180, 1.0, v180\n"
"v_add_f32 v181, 1.0, v181\n"
"v_add_f32 v182, 1.0, v182\n"
"v_add_f32 v183, 1.0, v183\n"
"v_rcp_f32 v180, v180\n"
"v_rcp_f32 v181, v181\n"
"v_rcp_f32 v182, v182\n"
"v_rcp_f32 v183, v183\n"
"v_mul_f32 v20, v20, v28\n"
"v_mul_f32 v21, v21, v29\n"
"v_mul_f32 v22, v22, v30\n"
"v_mul_f32 v23, v23, v31\n"
"v_mul_f32 v20, v20, v180\n"
"v_mul_f32 v21, v21, v181\n"
"v_mul_f32 v22, v22, v182\n"
"v_mul_f32 v23, v23, v183\n"
"v_mul_f32 v176, 0xbfb8aa3b, v32\n"
"v_mul_f32 v177, 0xbfb8aa3b, v33\n"
"v_mul_f32 v178, 0xbfb8aa3b, v34\n"
"v_mul_f32 v179, 0xbfb8aa3b, v35\n"
"v_exp_f32 v176, v176\n"
"v_exp_f32 v177, v177\n"
"v_exp_f32 v178, v178\n"
"v_exp_f32 v179, v179\n"
"v_add_f32 v176, 1.0, v176\n"
"v_add_f32 v177, 1.0, v177\n"
"v_add_f32 v178, 1.0, v178\n"
"v_add_f32 v179, 1.0, v179\n"
"v_rcp_f32 v176, v176\n"
"v_rcp_f32 v177, v177\n"
"v_rcp_f32 v178, v178\n"
"v_rcp_f32 v179, v179\n"
"v_mul_f32 v32, v32, v40\n"
"v_mul_f32 v33, v33, v41\n"
"v_mul_f32 v34, v34, v42\n"
"v_mul_f32 v35, v35, v43\n"
"v_mul_f32 v32, v32, v176\n"
"v_mul_f32 v33, v33, v177\n"
"v_mul_f32 v34, v34, v178\n"
"v_mul_f32 v35, v35, v179\n"
"v_mul_f32 v180, 0xbfb8aa3b, v36\n"
"v_mul_f32 v181, 0xbfb8aa3b, v37\n"
"v_mul_f32 v182, 0xbfb8aa3b, v38\n"
"v_mul_f32 v183, 0xbfb8aa3b, v39\n"
"v_exp_f32 v180, v180\n"
"v_exp_f32 v181, v181\n"
"v_exp_f32 v182, v182\n"
"v_exp_f32 v183, v183\n"
"v_add_f32 v180, 1.0, v180\n"
"v_add_f32 v181, 1.0, v181\n"
"v_add_f32 v182, 1.0, v182\n"
"v_add_f32 v183, 1.0, v183\n"
"v_rcp_f32 v180, v180\n"
"v_rcp_f32 v181, v181\n"
"v_rcp_f32 v182, v182\n"
"v_rcp_f32 v183, v183\n"
"v_mul_f32 v36, v36, v44\n"
"v_mul_f32 v37, v37, v45\n"
"v_mul_f32 v38, v38, v46\n"
"v_mul_f32 v39, v39, v47\n"
"v_mul_f32 v36, v36, v180\n"
"v_mul_f32 v37, v37, v181\n"
"v_mul_f32 v38, v38, v182\n"
"v_mul_f32 v39, v39, v183\n"
"v_cvt_pk_bf16_f32 v160, v16, v17\n"
"v_cvt_pk_bf16_f32 v161, v18, v19\n"
"v_cvt_pk_bf16_f32 v162, v20, v21\n"
"v_cvt_pk_bf16_f32 v163, v22, v23\n"
"v_cvt_pk_bf16_f32 v164, v32, v33\n"
"v_cvt_pk_bf16_f32 v165, v34, v35\n"
"v_cvt_pk_bf16_f32 v166, v36, v37\n"
"v_cvt_pk_bf16_f32 v167, v38, v39\n"
"v_permlane32_swap_b32 v160, v162\n"
"v_permlane32_swap_b32 v161, v163\n"
"v_permlane32_swap_b32 v164, v166\n"
"v_permlane32_swap_b32 v165, v167\n"
"buffer_store_dwordx4 v[160:163], v11, s[40:43], s66 offen sc1\n"
"buffer_store_dwordx4 v[164:167], v12, s[40:43], s66 offen sc1\n"
"s_waitcnt vmcnt(2) lgkmcnt(0)\n"
"s_barrier\n"
"s_cmp_lt_u32 s62, s48\n"
"s_cbranch_scc0 Ln3silu36_%=\n"
"s_sub_u32 s67, 87808, 0\n"
"v_lshrrev_b32 v192, 2, v0\n"
"v_add_u32 v192, s67, v192\n"
"ds_read_b32 v193, v192\n"
"s_waitcnt lgkmcnt(0)\n"
"v_cmp_eq_u32_e64 s[80:81], s97, v193\n"
"s_nop 1\n"
"s_sub_u32 s68, s48, s62\n"
"s_min_u32 s68, s68, 2\n"
"s_mov_b32 s70, 0\n"
"Levnsilu36_%=:\n"
"s_cmp_ge_u32 s70, s68\n"
"s_cbranch_scc1 Lexnsilu36_%=\n"
"s_and_b32 s69, s80, 255\n"
"s_cmp_eq_u32 s69, 255\n"
"s_cbranch_scc0 Lexnsilu36_%=\n"
"s_lshr_b64 s[80:81], s[80:81], 8\n"
"s_add_u32 s70, s70, 1\n"
"s_branch Levnsilu36_%=\n"
"Lexnsilu36_%=:\n"
"s_add_u32 s58, s62, s70\n"
"s_cmp_lt_u32 s62, s58\n"
"s_cbranch_scc0 Ln2silu36_%=\n"
"v_readlane_b32 s67, v6, s62\n"
"s_nop 0\n"
"s_mul_i32 s67, s67, s50\n"
"s_add_u32 s65, s67, s49\n"
"s_sub_u32 s64, 36864, s77\n"
"s_lshl_b32 s68, %0, 10\n"
"s_add_u32 s64, s64, s68\n"
"s_add_u32 m0, s64, 0\n"
"s_add_u32 s70, s65, 0\n"
"buffer_load_dwordx4 v2, s[36:39], s70 offen sc1 lds\n"
"s_add_u32 m0, s64, 4096\n"
"s_add_u32 s70, s65, 512\n"
"buffer_load_dwordx4 v2, s[36:39], s70 offen sc1 lds\n"
"s_add_u32 m0, s64, 8192\n"
"s_add_u32 s70, s65, 1024\n"
"buffer_load_dwordx4 v2, s[36:39], s70 offen sc1 lds\n"
"s_add_u32 m0, s64, 12288\n"
"s_add_u32 s70, s65, 1536\n"
"buffer_load_dwordx4 v2, s[36:39], s70 offen sc1 lds\n"
"s_add_u32 m0, s64, 16384\n"
"s_add_u32 s70, s65, 2048\n"
"buffer_load_dwordx4 v2, s[36:39], s70 offen sc1 lds\n"
"s_add_u32 m0, s64, 20480\n"
"s_add_u32 s70, s65, 2560\n"
"buffer_load_dwordx4 v2, s[36:39], s70 offen sc1 lds\n"
"s_add_u32 m0, s64, 24576\n"
"s_add_u32 s70, s65, 3072\n"
"buffer_load_dwordx4 v2, s[36:39], s70 offen sc1 lds\n"
"s_add_u32 m0, s64, 28672\n"
"s_add_u32 s70, s65, 3584\n"
"buffer_load_dwordx4 v2, s[36:39], s70 offen sc1 lds\n"
"s_add_u32 m0, s64, 32768\n"
"s_add_u32 s70, s65, 4096\n"
"buffer_load_dwordx4 v2, s[36:39], s70 offen sc1 lds\n"
"s_waitcnt vmcnt(9) lgkmcnt(0)\n"
"s_barrier\n"
"s_cmp_lt_i32 s57, 0\n"
"s_cbranch_scc1 Lnsn1silu36_%=\n"
"v_readlane_b32 s67, v6, s57\n"
"s_nop 0\n"
"s_mul_i32 s67, s67, s54\n"
"s_add_u32 s67, s67, s53\n"
"buffer_store_dword v4, v3, s[44:47], s67 offen sc1\n"
"Lnsn1silu36_%=:\n"
"s_waitcnt vmcnt(1) lgkmcnt(0)\n"
"s_barrier\n"
"s_branch Ln4silu36_%=\n"
"Ln2silu36_%=:\n"
"s_lshl_b32 s67, s62, 2\n"
"s_add_u32 s67, s67, 86528\n"
"v_add_u32 v192, s67, v8\n"
"ds_read_b32 v192, v192\n"
"s_waitcnt lgkmcnt(0)\n"
"v_add_u32 v192, v192, v9\n"
"s_mov_b32 m0, 87296\n"
"s_and_b32 exec_lo, s78, 65535\n"
"s_mov_b32 exec_hi, 0\n"
"buffer_load_dword v192, s[44:47], 0 offen sc1 lds\n"
"s_mov_b64 exec, -1\n"
"s_waitcnt vmcnt(0) lgkmcnt(0)\n"
"s_barrier\n"
"s_cmp_lt_i32 s57, 0\n"
"s_cbranch_scc1 Lnsn2silu36_%=\n"
"v_readlane_b32 s67, v6, s57\n"
"s_nop 0\n"
"s_mul_i32 s67, s67, s54\n"
"s_add_u32 s67, s67, s53\n"
"buffer_store_dword v4, v3, s[44:47], s67 offen sc1\n"
"Lnsn2silu36_%=:\n"
"s_sub_u32 s67, 87296, 0\n"
"v_lshrrev_b32 v192, 2, v0\n"
"v_add_u32 v192, s67, v192\n"
"ds_read_b32 v193, v192\n"
"s_waitcnt lgkmcnt(0)\n"
"v_cmp_eq_u32_e64 s[80:81], s97, v193\n"
"s_nop 1\n"
"s_sub_u32 s68, s48, s62\n"
"s_min_u32 s68, s68, 2\n"
"s_mov_b32 s70, 0\n"
"Levn2silu36_%=:\n"
"s_cmp_ge_u32 s70, s68\n"
"s_cbranch_scc1 Lexn2silu36_%=\n"
"s_and_b32 s69, s80, 255\n"
"s_cmp_eq_u32 s69, 255\n"
"s_cbranch_scc0 Lexn2silu36_%=\n"
"s_lshr_b64 s[80:81], s[80:81], 8\n"
"s_add_u32 s70, s70, 1\n"
"s_branch Levn2silu36_%=\n"
"Lexn2silu36_%=:\n"
"s_add_u32 s58, s62, s70\n"
"s_cmp_lt_u32 s62, s58\n"
"s_cbranch_scc0 Ln5silu36_%=\n"
"v_readlane_b32 s67, v6, s62\n"
"s_nop 0\n"
"s_mul_i32 s67, s67, s50\n"
"s_add_u32 s65, s67, s49\n"
"s_sub_u32 s64, 36864, s77\n"
"s_lshl_b32 s68, %0, 10\n"
"s_add_u32 s64, s64, s68\n"
"s_add_u32 m0, s64, 0\n"
"s_add_u32 s70, s65, 0\n"
"buffer_load_dwordx4 v2, s[36:39], s70 offen sc1 lds\n"
"s_add_u32 m0, s64, 4096\n"
"s_add_u32 s70, s65, 512\n"
"buffer_load_dwordx4 v2, s[36:39], s70 offen sc1 lds\n"
"s_add_u32 m0, s64, 8192\n"
"s_add_u32 s70, s65, 1024\n"
"buffer_load_dwordx4 v2, s[36:39], s70 offen sc1 lds\n"
"s_add_u32 m0, s64, 12288\n"
"s_add_u32 s70, s65, 1536\n"
"buffer_load_dwordx4 v2, s[36:39], s70 offen sc1 lds\n"
"s_add_u32 m0, s64, 16384\n"
"s_add_u32 s70, s65, 2048\n"
"buffer_load_dwordx4 v2, s[36:39], s70 offen sc1 lds\n"
"s_add_u32 m0, s64, 20480\n"
"s_add_u32 s70, s65, 2560\n"
"buffer_load_dwordx4 v2, s[36:39], s70 offen sc1 lds\n"
"s_add_u32 m0, s64, 24576\n"
"s_add_u32 s70, s65, 3072\n"
"buffer_load_dwordx4 v2, s[36:39], s70 offen sc1 lds\n"
"s_add_u32 m0, s64, 28672\n"
"s_add_u32 s70, s65, 3584\n"
"buffer_load_dwordx4 v2, s[36:39], s70 offen sc1 lds\n"
"s_add_u32 m0, s64, 32768\n"
"s_add_u32 s70, s65, 4096\n"
"buffer_load_dwordx4 v2, s[36:39], s70 offen sc1 lds\n"
"s_waitcnt vmcnt(0) lgkmcnt(0)\n"
"s_barrier\n"
"s_branch Ln4silu36_%=\n"
"Ln5silu36_%=:\n"
"s_cmp_eq_u32 %0, 0\n"
"s_cbranch_scc0 Lbwnsilu36_%=\n"
"s_cmp_lg_u32 s75, 0\n"
"s_cbranch_scc1 Lbwnsilu36_%=\n"
"v_readlane_b32 s72, v7, s62\n"
"s_nop 3\n"
"s_mov_b32 s71, 0\n"
"s_cmp_lt_i32 s74, 0\n"
"s_cbranch_scc1 Lplnsilu36_%=\n"
"s_sub_u32 s67, s62, s74\n"
"s_cmp_gt_u32 s67, 3\n"
"s_cbranch_scc1 Lplnsilu36_%=\n"
"s_add_u32 s68, s62, 2\n"
"s_cmp_ge_u32 s68, s48\n"
"s_cbranch_scc1 Lplnsilu36_%=\n"
"s_mul_i32 s67, s67, 240\n"
"s_add_u32 s67, s67, s73\n"
"s_sub_u32 s67, s67, 40\n"
"s_mov_b32 s76, 0\n"
"Lslnsilu36_%=:\n"
"s_memrealtime s[84:85]\n"
"s_waitcnt lgkmcnt(0)\n"
"s_sub_u32 s68, s84, s67\n"
"s_cmp_ge_i32 s68, 0\n"
"s_cbranch_scc1 Lplnsilu36_%=\n"
"s_sleep 2\n"
"s_mov_b32 s71, 1\n"
"s_add_u32 s76, s76, 1\n"
"s_cmp_lt_u32 s76, 32768\n"
"s_cbranch_scc1 Lslnsilu36_%=\n"
"Lplnsilu36_%=:\n"
"s_mov_b32 s76, 0\n"
"Lppnsilu36_%=:\n"
"buffer_load_dword v192, v5, s[44:47], s72 offen sc1\n"
"s_waitcnt vmcnt(0)\n"
"v_cmp_ne_u32_e64 s[80:81], s97, v192\n"
"s_nop 1\n"
"s_and_b64 s[80:81], s[80:81], s[82:83]\n"
"s_cbranch_scc0 Lrdnsilu36_%=\n"
"s_sleep 3\n"
"s_add_u32 s76, s76, 1\n"
"s_cmp_lt_u32 s76, 32768\n"
"s_cbranch_scc1 Lppnsilu36_%=\n"
"s_mov_b32 s75, 1\n"
"v_mov_b32 v192, 16384\n"
"v_mov_b32 v193, 7000\n"
"buffer_store_dword v193, v192, s[44:47], 0 offen sc1\n"
"Lrdnsilu36_%=:\n"
"s_mov_b32 s74, -1\n"
"s_cmp_eq_u32 s76, 0\n"
"s_cbranch_scc1 Lbwnsilu36_%=\n"
"s_memrealtime s[84:85]\n"
"s_waitcnt lgkmcnt(0)\n"
"s_mov_b32 s73, s84\n"
"s_mov_b32 s74, s62\n"
"Lbwnsilu36_%=:\n"
"s_waitcnt lgkmcnt(0)\n"
"s_barrier\n"
"v_readlane_b32 s67, v6, s62\n"
"s_nop 0\n"
"s_mul_i32 s67, s67, s50\n"
"s_add_u32 s65, s67, s49\n"
"s_sub_u32 s64, 36864, s77\n"
"s_lshl_b32 s68, %0, 10\n"
"s_add_u32 s64, s64, s68\n"
"s_add_u32 m0, s64, 0\n"
"s_add_u32 s70, s65, 0\n"
"buffer_load_dwordx4 v2, s[36:39], s70 offen sc1 lds\n"
"s_add_u32 m0, s64, 4096\n"
"s_add_u32 s70, s65, 512\n"
"buffer_load_dwordx4 v2, s[36:39], s70 offen sc1 lds\n"
"s_add_u32 m0, s64, 8192\n"
"s_add_u32 s70, s65, 1024\n"
"buffer_load_dwordx4 v2, s[36:39], s70 offen sc1 lds\n"
"s_add_u32 m0, s64, 12288\n"
"s_add_u32 s70, s65, 1536\n"
"buffer_load_dwordx4 v2, s[36:39], s70 offen sc1 lds\n"
"s_add_u32 m0, s64, 16384\n"
"s_add_u32 s70, s65, 2048\n"
"buffer_load_dwordx4 v2, s[36:39], s70 offen sc1 lds\n"
"s_add_u32 m0, s64, 20480\n"
"s_add_u32 s70, s65, 2560\n"
"buffer_load_dwordx4 v2, s[36:39], s70 offen sc1 lds\n"
"s_add_u32 m0, s64, 24576\n"
"s_add_u32 s70, s65, 3072\n"
"buffer_load_dwordx4 v2, s[36:39], s70 offen sc1 lds\n"
"s_add_u32 m0, s64, 28672\n"
"s_add_u32 s70, s65, 3584\n"
"buffer_load_dwordx4 v2, s[36:39], s70 offen sc1 lds\n"
"s_add_u32 m0, s64, 32768\n"
"s_add_u32 s70, s65, 4096\n"
"buffer_load_dwordx4 v2, s[36:39], s70 offen sc1 lds\n"
"s_waitcnt vmcnt(0) lgkmcnt(0)\n"
"s_barrier\n"
"s_add_u32 s58, s62, 1\n"
"Ln4silu36_%=:\n"
"s_sub_u32 s77, 36864, s77\n"
"v_add_u32 v1, s77, v0\n"
"ds_read_b128 v[80:83], v1\n"
"ds_read_b128 v[84:87], v1 offset:1024\n"
"ds_read_b128 v[88:91], v1 offset:2048\n"
"ds_read_b128 v[92:95], v1 offset:3072\n"
"s_add_u32 s57, s57, 1\n"
"s_branch Ltopsilu36_%=\n"
"Ln3silu36_%=:\n"
"s_waitcnt vmcnt(0) lgkmcnt(0)\n"
"s_barrier\n"
"s_cmp_lt_i32 s57, 0\n"
"s_cbranch_scc1 Lnsn3silu36_%=\n"
"v_readlane_b32 s67, v6, s57\n"
"s_nop 0\n"
"s_mul_i32 s67, s67, s54\n"
"s_add_u32 s67, s67, s53\n"
"buffer_store_dword v4, v3, s[44:47], s67 offen sc1\n"
"Lnsn3silu36_%=:\n"
"s_waitcnt vmcnt(0) lgkmcnt(0)\n"
"s_barrier\n"
  GEMM_ASM_END
"v0","v1","v2","v3","v4","v5","v6","v7","v8","v9","v10","v11","v12","v13","v14","v15","v16","v17","v18","v19","v20","v21","v22","v23","v24","v25","v26","v27","v28","v29","v30","v31","v32","v33","v34","v35","v36","v37","v38","v39","v40","v41","v42","v43","v44","v45","v46","v47","v48","v49","v50","v51","v52","v53","v54","v55","v56","v57","v58","v59","v60","v61","v62","v63","v64","v65","v66","v67","v68","v69","v70","v71","v72","v73","v74","v75","v76","v77","v78","v79","v80","v81","v82","v83","v84","v85","v86","v87","v88","v89","v90","v91","v92","v93","v94","v95","v96","v97","v98","v99","v100","v101","v102","v103","v104","v105","v106","v107","v108","v109","v110","v111","v112","v113","v114","v115","v116","v117","v118","v119","v120","v121","v122","v123","v124","v125","v126","v128","v129","v130","v131","v132","v133","v134","v135","v136","v137","v138","v139","v140","v141","v142","v143","v144","v145","v146","v147","v148","v149","v150","v151","v152","v153","v154","v155","v156","v157","v158","v159","v160","v161","v162","v163","v164","v165","v166","v167","v168","v169","v170","v171","v172","v173","v174","v175","v176","v177","v178","v179","v180","v181","v182","v183","v184","v185","v186","v187","v188","v189","v190","v191","v192","v193","v194","v195","v196","v197","v198","v199","v200","v201","v202","v203","v204","v205","v206","v207","v208","v209","v210","v211","v212","v213","v214","v215","v216","v217","v218","v219","v220","v221","v222","v223","v224","v225","v226","v227","v228","v229","v230","v231","v232","v233","v234","v235","v236","v237","v238","v239","v240","v241","v242","v243","v244","v245","v246","v247","v248","v249","v250","v251","v252","v253","v254","v255","a0","a1","a2","a3","a4","a5","a6","a7","a8","a9","a10","a11","a12","a13","a14","a15","a16","a17","a18","a19","a20","a21","a22","a23","a24","a25","a26","a27","a28","a29","a30","a31","a32","a33","a34","a35","a36","a37","a38","a39","a40","a41","a42","a43","a44","a45","a46","a47","a48","a49","a50","a51","a52","a53","a54","a55","a56","a57","a58","a59","a60","a61","a62","a63","a64","a65","a66","a67","a68","a69","a70","a71","a72","a73","a74","a75","a76","a77","a78","a79","a80","a81","a82","a83","a84","a85","a86","a87","a88","a89","a90","a91","a92","a93","a94","a95","a96","a97","a98","a99","a100","a101","a102","a103","a104","a105","a106","a107","a108","a109","a110","a111","a112","a113","a114","a115","a116","a117","a118","a119","a120","a121","a122","a123","a124","a125","a126","a127","a128","a129","a130","a131","a132","a133","a134","a135","a136","a137","a138","a139","a140","a141","a142","a143","a144","a145","a146","a147","a148","a149","a150","a151","a152","a153","a154","a155","a156","a157","a158","a159","a160","a161","a162","a163","a164","a165","a166","a167","a168","a169","a170","a171","a172","a173","a174","a175","a176","a177","a178","a179","a180","a181","a182","a183","a184","a185","a186","a187","a188","a189","a190","a191","a192","a193","a194","a195","a196","a197","a198","a199","a200","a201","a202","a203","a204","a205","a206","a207","a208","a209","a210","a211","a212","a213","a214","a215","a216","a217","a218","a219","a220","a221","a222","a223","a224","a225","a226","a227","a228","a229","a230","a231","a232","a233","a234","a235","a236","a237","a238","a239","a240","a241","a242","a243","a244","a245","a246","a247","a248","a249","a250","a251","a252","a253","a254","a255","s36","s37","s38","s39","s40","s41","s42","s43","s44","s45","s46","s47","s48","s49","s50","s51","s52","s53","s54","s55","s56","s57","s58","s59","s60","s61","s62","s63","s64","s65","s66","s67","s68","s69","s70","s71","s72","s73","s74","s75","s76","s77","s78","s79","s80","s81","s82","s83","s84","s85","s86","s87","s88","s89","s90","s91","s92","s93","s94","s95","s96","s97","s98","s99","vcc","scc","memory"
  );
}
DEVI void role_f2(Ctx& c,CArgs& a,int kq,int ns){
  c.lane=fresh_lane();
  const int lane=c.lane, wave=c.wave;
  GP p; p.ri=a.ws+O_ACT; p.ri_n=SZ_ACT; p.ro=a.ws+O_ZP; p.ro_n=SZ_PART; part_params(p,lane,wave,kq,ns);
  p.inb=kq*36864; p.ins=294912; p.sigb=FL_Z+(kq*3+ns)*128; p.sigs=24*128; p.wstr=16*1152;
  const char* w=(const char*)a.wd+(size_t)(kq*576+w_row(lane,wave))*1152; const int ch=w_ch(lane,wave);
  p.p0=w+(ns*192+ch*8)*2; p.p1=w+clampcol(ns*192+128+ch*8)*2;
  p.tabv=FL_ACT+(lane*36+9*(kq>>1)+(kq&1)*4)*128; p.nf=5; p.pd=8;
  gemm_params(c,a,p);
  GEMM_ASM_BEGIN
"v_mbcnt_lo_u32_b32 v10, -1, 0\n"
"v_mbcnt_hi_u32_b32 v10, -1, v10\n"
"v_lshlrev_b32 v0, 4, v10\n"
"v_lshlrev_b32 v192, 2, v10\n"
"v_add_u32 v192, 87040, v192\n"
"ds_read_b32 v193, v192\n"
"s_lshl_b32 s67, %0, 12\n"
"s_add_u32 s67, s67, 90112\n"
"v_lshlrev_b32 v194, 6, v10\n"
"v_add_u32 v194, s67, v194\n"
"ds_read_b128 v[160:163], v194\n"
"ds_read_b128 v[164:167], v194 offset:16\n"
"ds_read_b128 v[168:171], v194 offset:32\n"
"ds_read_b128 v[172:175], v194 offset:48\n"
"s_waitcnt lgkmcnt(0)\n"
"v_readlane_b32 s36, v193, 0\n"
"v_readlane_b32 s37, v193, 1\n"
"v_readlane_b32 s38, v193, 2\n"
"v_readlane_b32 s40, v193, 3\n"
"v_readlane_b32 s41, v193, 4\n"
"v_readlane_b32 s42, v193, 5\n"
"v_readlane_b32 s44, v193, 6\n"
"v_readlane_b32 s45, v193, 7\n"
"v_readlane_b32 s46, v193, 8\n"
"v_readlane_b32 s48, v193, 9\n"
"v_readlane_b32 s49, v193, 10\n"
"v_readlane_b32 s50, v193, 11\n"
"v_readlane_b32 s51, v193, 12\n"
"v_readlane_b32 s52, v193, 13\n"
"v_readlane_b32 s53, v193, 14\n"
"v_readlane_b32 s54, v193, 15\n"
"v_readlane_b32 s55, v193, 16\n"
"v_readlane_b32 s56, v193, 17\n"
"v_readlane_b32 s97, v193, 18\n"
"v_readlane_b32 s98, v193, 19\n"
"s_nop 1\n"
"s_and_b32 s37, s37, 0xffff\n"
"s_mov_b32 s39, 0x20000\n"
"s_and_b32 s41, s41, 0xffff\n"
"s_mov_b32 s43, 0x20000\n"
"s_and_b32 s45, s45, 0xffff\n"
"s_mov_b32 s47, 0x20000\n"
"v_mov_b32 v176, v160\n"
"v_mov_b32 v177, v161\n"
"v_mov_b32 v178, v162\n"
"v_mov_b32 v179, v163\n"
"v_mov_b32 v2, v164\n"
"v_mov_b32 v11, v165\n"
"v_mov_b32 v12, v166\n"
"v_mov_b32 v13, v167\n"
"v_mov_b32 v14, v168\n"
"v_mov_b32 v3, v169\n"
"v_mov_b32 v8, v170\n"
"v_mov_b32 v9, v171\n"
"v_mov_b32 v180, v172\n"
"v_mov_b32 v181, v173\n"
"v_mov_b32 v6, v174\n"
"v_mov_b32 v7, v175\n"
"v_mov_b32 v4, 1\n"
"v_mov_b32 v15, 0x7ffffff0\n"
"v_lshlrev_b32 v5, 7, v10\n"
"v_cmp_gt_u32_e32 vcc, 5, v10\n"
"s_nop 1\n"
"v_cndmask_b32_e32 v5, v15, v5, vcc\n"
"s_mov_b64 s[82:83], 31\n"
"s_mov_b64 s[78:79], 0\n"
"s_cmp_eq_u32 %0, 2\n"
"s_cbranch_scc0 Lnxpack36_%=\n"
"s_mov_b32 s78, 0xffffffff\n"
"s_mov_b32 s79, 0xff\n"
"Lnxpack36_%=:\n"
"s_mov_b32 s75, 0\n"
"s_mov_b32 s74, -1\n"
"s_mov_b32 s73, 0\n"
"s_barrier\n"
"v_mov_b32 v200, 20480\n"
"buffer_load_dword v200, v200, s[44:47], 0 offen sc1\n"
"v_add_u32 v182, 0x10000, v180\n"
"v_add_u32 v183, 0x10000, v181\n"
"v_add_u32 v184, 0x20000, v180\n"
"v_add_u32 v185, 0x20000, v181\n"
"s_lshl_b32 s69, %0, 10\n"
"s_mov_b32 s96, 0\n"
"s_add_u32 m0, s69, 0\n"
"s_nop 0\n"
"global_load_lds_dwordx4 v[176:177], off\n"
"v_add_co_u32_e32 v176, vcc, s55, v176\n"
"v_addc_co_u32_e32 v177, vcc, 0, v177, vcc\n"
"s_add_u32 m0, s69, 4096\n"
"s_nop 0\n"
"global_load_lds_dwordx4 v[176:177], off\n"
"v_add_co_u32_e32 v176, vcc, s55, v176\n"
"v_addc_co_u32_e32 v177, vcc, 0, v177, vcc\n"
"s_add_u32 m0, s69, 8192\n"
"s_nop 0\n"
"global_load_lds_dwordx4 v[176:177], off\n"
"v_add_co_u32_e32 v176, vcc, s55, v176\n"
"v_addc_co_u32_e32 v177, vcc, 0, v177, vcc\n"
"s_add_u32 m0, s69, 12288\n"
"s_nop 0\n"
"global_load_lds_dwordx4 v[176:177], off\n"
"v_add_co_u32_e32 v176, vcc, s55, v176\n"
"v_addc_co_u32_e32 v177, vcc, 0, v177, vcc\n"
"s_add_u32 m0, s69, 16384\n"
"s_nop 0\n"
"global_load_lds_dwordx4 v[176:177], off\n"
"v_add_co_u32_e32 v176, vcc, s55, v176\n"
"v_addc_co_u32_e32 v177, vcc, 0, v177, vcc\n"
"s_add_u32 m0, s69, 20480\n"
"s_nop 0\n"
"global_load_lds_dwordx4 v[176:177], off\n"
"v_add_co_u32_e32 v176, vcc, s55, v176\n"
"v_addc_co_u32_e32 v177, vcc, 0, v177, vcc\n"
"s_add_u32 m0, s69, 24576\n"
"s_nop 0\n"
"global_load_lds_dwordx4 v[176:177], off\n"
"v_add_co_u32_e32 v176, vcc, s55, v176\n"
"v_addc_co_u32_e32 v177, vcc, 0, v177, vcc\n"
"s_add_u32 m0, s69, 28672\n"
"s_nop 0\n"
"global_load_lds_dwordx4 v[176:177], off\n"
"v_add_co_u32_e32 v176, vcc, s55, v176\n"
"v_addc_co_u32_e32 v177, vcc, 0, v177, vcc\n"
"s_add_u32 m0, s69, 32768\n"
"s_nop 0\n"
"global_load_lds_dwordx4 v[176:177], off\n"
"v_add_co_u32_e32 v176, vcc, s55, v176\n"
"v_addc_co_u32_e32 v177, vcc, 0, v177, vcc\n"
"s_add_u32 m0, s69, 36864\n"
"s_nop 0\n"
"global_load_lds_dwordx4 v[176:177], off\n"
"v_add_co_u32_e32 v176, vcc, s55, v176\n"
"v_addc_co_u32_e32 v177, vcc, 0, v177, vcc\n"
"s_add_u32 m0, s69, 40960\n"
"s_nop 0\n"
"global_load_lds_dwordx4 v[176:177], off\n"
"v_add_co_u32_e32 v176, vcc, s55, v176\n"
"v_addc_co_u32_e32 v177, vcc, 0, v177, vcc\n"
"s_add_u32 m0, s69, 45056\n"
"s_nop 0\n"
"global_load_lds_dwordx4 v[176:177], off\n"
"v_add_co_u32_e32 v176, vcc, s55, v176\n"
"v_addc_co_u32_e32 v177, vcc, 0, v177, vcc\n"
"s_add_u32 m0, s69, 49152\n"
"s_nop 0\n"
"global_load_lds_dwordx4 v[176:177], off\n"
"v_add_co_u32_e32 v176, vcc, s55, v176\n"
"v_addc_co_u32_e32 v177, vcc, 0, v177, vcc\n"
"s_add_u32 m0, s69, 53248\n"
"s_nop 0\n"
"global_load_lds_dwordx4 v[176:177], off\n"
"v_add_co_u32_e32 v176, vcc, s55, v176\n"
"v_addc_co_u32_e32 v177, vcc, 0, v177, vcc\n"
"s_add_u32 m0, s69, 57344\n"
"s_nop 0\n"
"global_load_lds_dwordx4 v[176:177], off\n"
"v_add_co_u32_e32 v176, vcc, s55, v176\n"
"v_addc_co_u32_e32 v177, vcc, 0, v177, vcc\n"
"s_add_u32 m0, s69, 61440\n"
"s_nop 0\n"
"global_load_lds_dwordx4 v[176:177], off\n"
"v_add_co_u32_e32 v176, vcc, s55, v176\n"
"v_addc_co_u32_e32 v177, vcc, 0, v177, vcc\n"
"s_add_u32 m0, s69, 65536\n"
"s_nop 0\n"
"global_load_lds_dwordx4 v[176:177], off\n"
"v_add_co_u32_e32 v176, vcc, s55, v176\n"
"v_addc_co_u32_e32 v177, vcc, 0, v177, vcc\n"
"s_add_u32 m0, s69, 69632\n"
"s_nop 0\n"
"global_load_lds_dwordx4 v[176:177], off\n"
"v_add_co_u32_e32 v176, vcc, s55, v176\n"
"v_addc_co_u32_e32 v177, vcc, 0, v177, vcc\n"
"s_add_u32 m0, s69, 73728\n"
"s_nop 0\n"
"global_load_lds_dwordx4 v[176:177], off\n"
"v_add_co_u32_e32 v176, vcc, s55, v176\n"
"v_addc_co_u32_e32 v177, vcc, 0, v177, vcc\n"
"s_add_u32 m0, s69, 77824\n"
"s_nop 0\n"
"global_load_lds_dwordx4 v[176:177], off\n"
"v_add_co_u32_e32 v176, vcc, s55, v176\n"
"v_addc_co_u32_e32 v177, vcc, 0, v177, vcc\n"
"s_add_u32 m0, s69, 81920\n"
"s_nop 0\n"
"global_load_lds_dwordx4 v[176:177], off\n"
"v_add_co_u32_e32 v176, vcc, s55, v176\n"
"v_addc_co_u32_e32 v177, vcc, 0, v177, vcc\n"
"s_add_u32 m0, s69, 86016\n"
"s_nop 0\n"
"global_load_lds_dwordx4 v[176:177], off\n"
"v_add_co_u32_e32 v176, vcc, s55, v176\n"
"v_addc_co_u32_e32 v177, vcc, 0, v177, vcc\n"
"s_add_u32 m0, s69, 90112\n"
"s_nop 0\n"
"global_load_lds_dwordx4 v[176:177], off\n"
"v_add_co_u32_e32 v176, vcc, s55, v176\n"
"v_addc_co_u32_e32 v177, vcc, 0, v177, vcc\n"
"s_add_u32 m0, s69, 94208\n"
"s_nop 0\n"
"global_load_lds_dwordx4 v[176:177], off\n"
"v_add_co_u32_e32 v176, vcc, s55, v176\n"
"v_addc_co_u32_e32 v177, vcc, 0, v177, vcc\n"
"s_add_u32 m0, s69, 98304\n"
"s_nop 0\n"
"global_load_lds_dwordx4 v[176:177], off\n"
"v_add_co_u32_e32 v176, vcc, s55, v176\n"
"v_addc_co_u32_e32 v177, vcc, 0, v177, vcc\n"
"s_add_u32 m0, s69, 102400\n"
"s_nop 0\n"
"global_load_lds_dwordx4 v[176:177], off\n"
"v_add_co_u32_e32 v176, vcc, s55, v176\n"
"v_addc_co_u32_e32 v177, vcc, 0, v177, vcc\n"
"s_add_u32 m0, s69, 106496\n"
"s_nop 0\n"
"global_load_lds_dwordx4 v[176:177], off\n"
"v_add_co_u32_e32 v176, vcc, s55, v176\n"
"v_addc_co_u32_e32 v177, vcc, 0, v177, vcc\n"
"s_add_u32 m0, s69, 110592\n"
"s_nop 0\n"
"global_load_lds_dwordx4 v[176:177], off\n"
"v_add_co_u32_e32 v176, vcc, s55, v176\n"
"v_addc_co_u32_e32 v177, vcc, 0, v177, vcc\n"
"s_add_u32 m0, s69, 114688\n"
"s_nop 0\n"
"global_load_lds_dwordx4 v[176:177], off\n"
"v_add_co_u32_e32 v176, vcc, s55, v176\n"
"v_addc_co_u32_e32 v177, vcc, 0, v177, vcc\n"
"s_add_u32 m0, s69, 118784\n"
"s_nop 0\n"
"global_load_lds_dwordx4 v[176:177], off\n"
"v_add_co_u32_e32 v176, vcc, s55, v176\n"
"v_addc_co_u32_e32 v177, vcc, 0, v177, vcc\n"
"s_add_u32 m0, s69, 122880\n"
"s_nop 0\n"
"global_load_lds_dwordx4 v[176:177], off\n"
"v_add_co_u32_e32 v176, vcc, s55, v176\n"
"v_addc_co_u32_e32 v177, vcc, 0, v177, vcc\n"
"s_add_u32 m0, s69, 126976\n"
"s_nop 0\n"
"global_load_lds_dwordx4 v[176:177], off\n"
"v_add_co_u32_e32 v176, vcc, s55, v176\n"
"v_addc_co_u32_e32 v177, vcc, 0, v177, vcc\n"
"s_add_u32 m0, s69, 131072\n"
"s_nop 0\n"
"global_load_lds_dwordx4 v[176:177], off\n"
"v_add_co_u32_e32 v176, vcc, s55, v176\n"
"v_addc_co_u32_e32 v177, vcc, 0, v177, vcc\n"
"s_add_u32 m0, s69, 135168\n"
"s_nop 0\n"
"global_load_lds_dwordx4 v[176:177], off\n"
"v_add_co_u32_e32 v176, vcc, s55, v176\n"
"v_addc_co_u32_e32 v177, vcc, 0, v177, vcc\n"
"s_add_u32 m0, s69, 139264\n"
"s_nop 0\n"
"global_load_lds_dwordx4 v[176:177], off\n"
"v_add_co_u32_e32 v176, vcc, s55, v176\n"
"v_addc_co_u32_e32 v177, vcc, 0, v177, vcc\n"
"s_add_u32 m0, s69, 143360\n"
"s_nop 0\n"
"global_load_lds_dwordx4 v[176:177], off\n"
"s_waitcnt vmcnt(27)\n"
"s_barrier\n"
"v_readfirstlane_b32 s90, v200\n"
"s_nop 3\n"
"s_add_u32 s90, s90, 1\n"
"s_cmp_eq_u32 s98, 1\n"
"s_cselect_b32 s97, s90, s97\n"
"v_mov_b32 v4, s97\n"
"v_mov_b32 v201, 163836\n"
"ds_write_b32 v201, v4\n"
"ds_read_b64_tr_b16 v[128:129], v180\n"
"ds_read_b64_tr_b16 v[130:131], v181\n"
"ds_read_b64_tr_b16 v[132:133], v180 offset:4096\n"
"ds_read_b64_tr_b16 v[134:135], v181 offset:4096\n"
"ds_read_b64_tr_b16 v[136:137], v180 offset:8192\n"
"ds_read_b64_tr_b16 v[138:139], v181 offset:8192\n"
"ds_read_b64_tr_b16 v[140:141], v180 offset:12288\n"
"ds_read_b64_tr_b16 v[142:143], v181 offset:12288\n"
"ds_read_b64_tr_b16 v[144:145], v180 offset:16384\n"
"ds_read_b64_tr_b16 v[146:147], v181 offset:16384\n"
"ds_read_b64_tr_b16 v[148:149], v180 offset:20480\n"
"ds_read_b64_tr_b16 v[150:151], v181 offset:20480\n"
"ds_read_b64_tr_b16 v[152:153], v180 offset:24576\n"
"ds_read_b64_tr_b16 v[154:155], v181 offset:24576\n"
"s_waitcnt lgkmcnt(8)\n"
"ds_read_b64_tr_b16 v[156:157], v180 offset:28672\n"
"ds_read_b64_tr_b16 v[158:159], v181 offset:28672\n"
"ds_read_b64_tr_b16 a[144:145], v180 offset:32768\n"
"ds_read_b64_tr_b16 a[146:147], v181 offset:32768\n"
"s_waitcnt lgkmcnt(0)\n"
"s_barrier\n"
"s_add_u32 m0, s69, 0\n"
"s_nop 0\n"
"global_load_lds_dwordx4 v[178:179], off\n"
"v_add_co_u32_e32 v178, vcc, s55, v178\n"
"v_addc_co_u32_e32 v179, vcc, 0, v179, vcc\n"
"s_add_u32 m0, s69, 4096\n"
"s_nop 0\n"
"global_load_lds_dwordx4 v[178:179], off\n"
"v_add_co_u32_e32 v178, vcc, s55, v178\n"
"v_addc_co_u32_e32 v179, vcc, 0, v179, vcc\n"
"s_add_u32 m0, s69, 8192\n"
"s_nop 0\n"
"global_load_lds_dwordx4 v[178:179], off\n"
"v_add_co_u32_e32 v178, vcc, s55, v178\n"
"v_addc_co_u32_e32 v179, vcc, 0, v179, vcc\n"
"s_add_u32 m0, s69, 12288\n"
"s_nop 0\n"
"global_load_lds_dwordx4 v[178:179], off\n"
"v_add_co_u32_e32 v178, vcc, s55, v178\n"
"v_addc_co_u32_e32 v179, vcc, 0, v179, vcc\n"
"s_add_u32 m0, s69, 16384\n"
"s_nop 0\n"
"global_load_lds_dwordx4 v[178:179], off\n"
"v_add_co_u32_e32 v178, vcc, s55, v178\n"
"v_addc_co_u32_e32 v179, vcc, 0, v179, vcc\n"
"s_add_u32 m0, s69, 20480\n"
"s_nop 0\n"
"global_load_lds_dwordx4 v[178:179], off\n"
"v_add_co_u32_e32 v178, vcc, s55, v178\n"
"v_addc_co_u32_e32 v179, vcc, 0, v179, vcc\n"
"s_add_u32 m0, s69, 24576\n"
"s_nop 0\n"
"global_load_lds_dwordx4 v[178:179], off\n"
"v_add_co_u32_e32 v178, vcc, s55, v178\n"
"v_addc_co_u32_e32 v179, vcc, 0, v179, vcc\n"
"s_add_u32 m0, s69, 28672\n"
"s_nop 0\n"
"global_load_lds_dwordx4 v[178:179], off\n"
"v_add_co_u32_e32 v178, vcc, s55, v178\n"
"v_addc_co_u32_e32 v179, vcc, 0, v179, vcc\n"
"s_add_u32 m0, s69, 32768\n"
"s_nop 0\n"
"global_load_lds_dwordx4 v[178:179], off\n"
"v_add_co_u32_e32 v178, vcc, s55, v178\n"
"v_addc_co_u32_e32 v179, vcc, 0, v179, vcc\n"
"s_waitcnt vmcnt(27)\n"
"s_barrier\n"
"ds_read_b64_tr_b16 a[148:149], v180 offset:36864\n"
"ds_read_b64_tr_b16 a[150:151], v181 offset:36864\n"
"ds_read_b64_tr_b16 a[152:153], v180 offset:40960\n"
"ds_read_b64_tr_b16 a[154:155], v181 offset:40960\n"
"ds_read_b64_tr_b16 a[156:157], v180 offset:45056\n"
"ds_read_b64_tr_b16 a[158:159], v181 offset:45056\n"
"ds_read_b64_tr_b16 a[160:161], v180 offset:49152\n"
"ds_read_b64_tr_b16 a[162:163], v181 offset:49152\n"
"ds_read_b64_tr_b16 a[164:165], v180 offset:53248\n"
"ds_read_b64_tr_b16 a[166:167], v181 offset:53248\n"
"ds_read_b64_tr_b16 a[168:169], v180 offset:57344\n"
"ds_read_b64_tr_b16 a[170:171], v181 offset:57344\n"
"ds_read_b64_tr_b16 a[172:173], v180 offset:61440\n"
"ds_read_b64_tr_b16 a[174:175], v181 offset:61440\n"
"ds_read_b64_tr_b16 a[176:177], v182\n"
"s_waitcnt lgkmcnt(8)\n"
"ds_read_b64_tr_b16 a[178:179], v183\n"
"ds_read_b64_tr_b16 a[180:181], v182 offset:4096\n"
"ds_read_b64_tr_b16 a[182:183], v183 offset:4096\n"
"s_waitcnt lgkmcnt(0)\n"
"s_barrier\n"
"s_add_u32 m0, s69, 36864\n"
"s_nop 0\n"
"global_load_lds_dwordx4 v[178:179], off\n"
"v_add_co_u32_e32 v178, vcc, s55, v178\n"
"v_addc_co_u32_e32 v179, vcc, 0, v179, vcc\n"
"s_add_u32 m0, s69, 40960\n"
"s_nop 0\n"
"global_load_lds_dwordx4 v[178:179], off\n"
"v_add_co_u32_e32 v178, vcc, s55, v178\n"
"v_addc_co_u32_e32 v179, vcc, 0, v179, vcc\n"
"s_add_u32 m0, s69, 45056\n"
"s_nop 0\n"
"global_load_lds_dwordx4 v[178:179], off\n"
"v_add_co_u32_e32 v178, vcc, s55, v178\n"
"v_addc_co_u32_e32 v179, vcc, 0, v179, vcc\n"
"s_add_u32 m0, s69, 49152\n"
"s_nop 0\n"
"global_load_lds_dwordx4 v[178:179], off\n"
"v_add_co_u32_e32 v178, vcc, s55, v178\n"
"v_addc_co_u32_e32 v179, vcc, 0, v179, vcc\n"
"s_add_u32 m0, s69, 53248\n"
"s_nop 0\n"
"global_load_lds_dwordx4 v[178:179], off\n"
"v_add_co_u32_e32 v178, vcc, s55, v178\n"
"v_addc_co_u32_e32 v179, vcc, 0, v179, vcc\n"
"s_add_u32 m0, s69, 57344\n"
"s_nop 0\n"
"global_load_lds_dwordx4 v[178:179], off\n"
"v_add_co_u32_e32 v178, vcc, s55, v178\n"
"v_addc_co_u32_e32 v179, vcc, 0, v179, vcc\n"
"s_add_u32 m0, s69, 61440\n"
"s_nop 0\n"
"global_load_lds_dwordx4 v[178:179], off\n"
"v_add_co_u32_e32 v178, vcc, s55, v178\n"
"v_addc_co_u32_e32 v179, vcc, 0, v179, vcc\n"
"s_add_u32 m0, s69, 65536\n"
"s_nop 0\n"
"global_load_lds_dwordx4 v[178:179], off\n"
"v_add_co_u32_e32 v178, vcc, s55, v178\n"
"v_addc_co_u32_e32 v179, vcc, 0, v179, vcc\n"
"s_add_u32 m0, s69, 69632\n"
"s_nop 0\n"
"global_load_lds_dwordx4 v[178:179], off\n"
"v_add_co_u32_e32 v178, vcc, s55, v178\n"
"v_addc_co_u32_e32 v179, vcc, 0, v179, vcc\n"
"s_waitcnt vmcnt(27)\n"
"s_barrier\n"
"ds_read_b64_tr_b16 a[184:185], v182 offset:8192\n"
"ds_read_b64_tr_b16 a[186:187], v183 offset:8192\n"
"ds_read_b64_tr_b16 a[188:189], v182 offset:12288\n"
"ds_read_b64_tr_b16 a[190:191], v183 offset:12288\n"
"ds_read_b64_tr_b16 a[192:193], v182 offset:16384\n"
"ds_read_b64_tr_b16 a[194:195], v183 offset:16384\n"
"ds_read_b64_tr_b16 a[196:197], v182 offset:20480\n"
"ds_read_b64_tr_b16 a[198:199], v183 offset:20480\n"
"ds_read_b64_tr_b16 a[200:201], v182 offset:24576\n"
"ds_read_b64_tr_b16 a[202:203], v183 offset:24576\n"
"ds_read_b64_tr_b16 a[204:205], v182 offset:28672\n"
"ds_read_b64_tr_b16 a[206:207], v183 offset:28672\n"
"ds_read_b64_tr_b16 a[208:209], v182 offset:32768\n"
"ds_read_b64_tr_b16 a[210:211], v183 offset:32768\n"
"ds_read_b64_tr_b16 a[212:213], v182 offset:36864\n"
"s_waitcnt lgkmcnt(8)\n"
"ds_read_b64_tr_b16 a[214:215], v183 offset:36864\n"
"ds_read_b64_tr_b16 a[216:217], v182 offset:40960\n"
"ds_read_b64_tr_b16 a[218:219], v183 offset:40960\n"
"s_waitcnt lgkmcnt(0)\n"
"s_barrier\n"
"s_add_u32 m0, s69, 73728\n"
"s_nop 0\n"
"global_load_lds_dwordx4 v[178:179], off\n"
"v_add_co_u32_e32 v178, vcc, s55, v178\n"
"v_addc_co_u32_e32 v179, vcc, 0, v179, vcc\n"
"s_add_u32 m0, s69, 77824\n"
"s_nop 0\n"
"global_load_lds_dwordx4 v[178:179], off\n"
"v_add_co_u32_e32 v178, vcc, s55, v178\n"
"v_addc_co_u32_e32 v179, vcc, 0, v179, vcc\n"
"s_add_u32 m0, s69, 81920\n"
"s_nop 0\n"
"global_load_lds_dwordx4 v[178:179], off\n"
"v_add_co_u32_e32 v178, vcc, s55, v178\n"
"v_addc_co_u32_e32 v179, vcc, 0, v179, vcc\n"
"s_add_u32 m0, s69, 86016\n"
"s_nop 0\n"
"global_load_lds_dwordx4 v[178:179], off\n"
"v_add_co_u32_e32 v178, vcc, s55, v178\n"
"v_addc_co_u32_e32 v179, vcc, 0, v179, vcc\n"
"s_add_u32 m0, s69, 90112\n"
"s_nop 0\n"
"global_load_lds_dwordx4 v[178:179], off\n"
"v_add_co_u32_e32 v178, vcc, s55, v178\n"
"v_addc_co_u32_e32 v179, vcc, 0, v179, vcc\n"
"s_add_u32 m0, s69, 94208\n"
"s_nop 0\n"
"global_load_lds_dwordx4 v[178:179], off\n"
"v_add_co_u32_e32 v178, vcc, s55, v178\n"
"v_addc_co_u32_e32 v179, vcc, 0, v179, vcc\n"
"s_add_u32 m0, s69, 98304\n"
"s_nop 0\n"
"global_load_lds_dwordx4 v[178:179], off\n"
"v_add_co_u32_e32 v178, vcc, s55, v178\n"
"v_addc_co_u32_e32 v179, vcc, 0, v179, vcc\n"
"s_add_u32 m0, s69, 102400\n"
"s_nop 0\n"
"global_load_lds_dwordx4 v[178:179], off\n"
"v_add_co_u32_e32 v178, vcc, s55, v178\n"
"v_addc_co_u32_e32 v179, vcc, 0, v179, vcc\n"
"s_add_u32 m0, s69, 106496\n"
"s_nop 0\n"
"global_load_lds_dwordx4 v[178:179], off\n"
"v_add_co_u32_e32 v178, vcc, s55, v178\n"
"v_addc_co_u32_e32 v179, vcc, 0, v179, vcc\n"
"s_waitcnt vmcnt(27)\n"
"s_barrier\n"
"ds_read_b64_tr_b16 a[220:221], v182 offset:45056\n"
"ds_read_b64_tr_b16 a[222:223], v183 offset:45056\n"
"ds_read_b64_tr_b16 a[224:225], v182 offset:49152\n"
"ds_read_b64_tr_b16 a[226:227], v183 offset:49152\n"
"ds_read_b64_tr_b16 a[228:229], v182 offset:53248\n"
"ds_read_b64_tr_b16 a[230:231], v183 offset:53248\n"
"ds_read_b64_tr_b16 a[232:233], v182 offset:57344\n"
"ds_read_b64_tr_b16 a[234:235], v183 offset:57344\n"
"ds_read_b64_tr_b16 a[236:237], v182 offset:61440\n"
"ds_read_b64_tr_b16 a[238:239], v183 offset:61440\n"
"ds_read_b64_tr_b16 a[240:241], v184\n"
"ds_read_b64_tr_b16 a[242:243], v185\n"
"ds_read_b64_tr_b16 a[244:245], v184 offset:4096\n"
"ds_read_b64_tr_b16 a[246:247], v185 offset:4096\n"
"ds_read_b64_tr_b16 a[248:249], v184 offset:8192\n"
"s_waitcnt lgkmcnt(8)\n"
"ds_read_b64_tr_b16 a[250:251], v185 offset:8192\n"
"ds_read_b64_tr_b16 a[252:253], v184 offset:12288\n"
"ds_read_b64_tr_b16 a[254:255], v185 offset:12288\n"
"s_waitcnt lgkmcnt(0)\n"
"s_barrier\n"
"s_waitcnt vmcnt(18)\n"
"s_barrier\n"
"ds_read_b64_tr_b16 a[0:1], v180\n"
"ds_read_b64_tr_b16 a[2:3], v181\n"
"ds_read_b64_tr_b16 a[4:5], v180 offset:4096\n"
"ds_read_b64_tr_b16 a[6:7], v181 offset:4096\n"
"ds_read_b64_tr_b16 a[8:9], v180 offset:8192\n"
"ds_read_b64_tr_b16 a[10:11], v181 offset:8192\n"
"ds_read_b64_tr_b16 a[12:13], v180 offset:12288\n"
"ds_read_b64_tr_b16 a[14:15], v181 offset:12288\n"
"ds_read_b64_tr_b16 a[16:17], v180 offset:16384\n"
"ds_read_b64_tr_b16 a[18:19], v181 offset:16384\n"
"ds_read_b64_tr_b16 a[20:21], v180 offset:20480\n"
"ds_read_b64_tr_b16 a[22:23], v181 offset:20480\n"
"ds_read_b64_tr_b16 a[24:25], v180 offset:24576\n"
"ds_read_b64_tr_b16 a[26:27], v181 offset:24576\n"
"ds_read_b64_tr_b16 a[28:29], v180 offset:28672\n"
"s_waitcnt lgkmcnt(8)\n"
"ds_read_b64_tr_b16 a[30:31], v181 offset:28672\n"
"ds_read_b64_tr_b16 a[32:33], v180 offset:32768\n"
"ds_read_b64_tr_b16 a[34:35], v181 offset:32768\n"
"s_waitcnt lgkmcnt(0)\n"
"s_barrier\n"
"s_waitcnt vmcnt(9)\n"
"s_barrier\n"
"ds_read_b64_tr_b16 a[36:37], v180 offset:36864\n"
"ds_read_b64_tr_b16 a[38:39], v181 offset:36864\n"
"ds_read_b64_tr_b16 a[40:41], v180 offset:40960\n"
"ds_read_b64_tr_b16 a[42:43], v181 offset:40960\n"
"ds_read_b64_tr_b16 a[44:45], v180 offset:45056\n"
"ds_read_b64_tr_b16 a[46:47], v181 offset:45056\n"
"ds_read_b64_tr_b16 a[48:49], v180 offset:49152\n"
"ds_read_b64_tr_b16 a[50:51], v181 offset:49152\n"
"ds_read_b64_tr_b16 a[52:53], v180 offset:53248\n"
"ds_read_b64_tr_b16 a[54:55], v181 offset:53248\n"
"ds_read_b64_tr_b16 a[56:57], v180 offset:57344\n"
"ds_read_b64_tr_b16 a[58:59], v181 offset:57344\n"
"ds_read_b64_tr_b16 a[60:61], v180 offset:61440\n"
"ds_read_b64_tr_b16 a[62:63], v181 offset:61440\n"
"ds_read_b64_tr_b16 a[64:65], v182\n"
"s_waitcnt lgkmcnt(8)\n"
"ds_read_b64_tr_b16 a[66:67], v183\n"
"ds_read_b64_tr_b16 a[68:69], v182 offset:4096\n"
"ds_read_b64_tr_b16 a[70:71], v183 offset:4096\n"
"s_waitcnt lgkmcnt(0)\n"
"s_barrier\n"
"v_readlane_b32 s72, v7, 0\n"
"s_nop 4\n"
"buffer_load_dword v198, v5, s[44:47], s72 offen sc1\n"
"s_add_u32 m0, s69, 110592\n"
"s_nop 0\n"
"global_load_lds_dwordx4 v[178:179], off\n"
"v_add_co_u32_e32 v178, vcc, s55, v178\n"
"v_addc_co_u32_e32 v179, vcc, 0, v179, vcc\n"
"s_add_u32 m0, s69, 114688\n"
"s_nop 0\n"
"global_load_lds_dwordx4 v[178:179], off\n"
"v_add_co_u32_e32 v178, vcc, s55, v178\n"
"v_addc_co_u32_e32 v179, vcc, 0, v179, vcc\n"
"s_add_u32 m0, s69, 118784\n"
"s_nop 0\n"
"global_load_lds_dwordx4 v[178:179], off\n"
"v_add_co_u32_e32 v178, vcc, s55, v178\n"
"v_addc_co_u32_e32 v179, vcc, 0, v179, vcc\n"
"s_add_u32 m0, s69, 122880\n"
"s_nop 0\n"
"global_load_lds_dwordx4 v[178:179], off\n"
"v_add_co_u32_e32 v178, vcc, s55, v178\n"
"v_addc_co_u32_e32 v179, vcc, 0, v179, vcc\n"
"s_add_u32 m0, s69, 126976\n"
"s_nop 0\n"
"global_load_lds_dwordx4 v[178:179], off\n"
"v_add_co_u32_e32 v178, vcc, s55, v178\n"
"v_addc_co_u32_e32 v179, vcc, 0, v179, vcc\n"
"s_add_u32 m0, s69, 131072\n"
"s_nop 0\n"
"global_load_lds_dwordx4 v[178:179], off\n"
"v_add_co_u32_e32 v178, vcc, s55, v178\n"
"v_addc_co_u32_e32 v179, vcc, 0, v179, vcc\n"
"s_add_u32 m0, s69, 135168\n"
"s_nop 0\n"
"global_load_lds_dwordx4 v[178:179], off\n"
"v_add_co_u32_e32 v178, vcc, s55, v178\n"
"v_addc_co_u32_e32 v179, vcc, 0, v179, vcc\n"
"s_add_u32 m0, s69, 139264\n"
"s_nop 0\n"
"global_load_lds_dwordx4 v[178:179], off\n"
"v_add_co_u32_e32 v178, vcc, s55, v178\n"
"v_addc_co_u32_e32 v179, vcc, 0, v179, vcc\n"
"s_add_u32 m0, s69, 143360\n"
"s_nop 0\n"
"global_load_lds_dwordx4 v[178:179], off\n"
"s_waitcnt vmcnt(9)\n"
"s_barrier\n"
"ds_read_b64_tr_b16 a[72:73], v182 offset:8192\n"
"ds_read_b64_tr_b16 a[74:75], v183 offset:8192\n"
"ds_read_b64_tr_b16 a[76:77], v182 offset:12288\n"
"ds_read_b64_tr_b16 a[78:79], v183 offset:12288\n"
"ds_read_b64_tr_b16 a[80:81], v182 offset:16384\n"
"ds_read_b64_tr_b16 a[82:83], v183 offset:16384\n"
"ds_read_b64_tr_b16 a[84:85], v182 offset:20480\n"
"ds_read_b64_tr_b16 a[86:87], v183 offset:20480\n"
"ds_read_b64_tr_b16 a[88:89], v182 offset:24576\n"
"ds_read_b64_tr_b16 a[90:91], v183 offset:24576\n"
"ds_read_b64_tr_b16 a[92:93], v182 offset:28672\n"
"ds_read_b64_tr_b16 a[94:95], v183 offset:28672\n"
"ds_read_b64_tr_b16 a[96:97], v182 offset:32768\n"
"ds_read_b64_tr_b16 a[98:99], v183 offset:32768\n"
"ds_read_b64_tr_b16 a[100:101], v182 offset:36864\n"
"s_waitcnt lgkmcnt(8)\n"
"ds_read_b64_tr_b16 a[102:103], v183 offset:36864\n"
"ds_read_b64_tr_b16 a[104:105], v182 offset:40960\n"
"ds_read_b64_tr_b16 a[106:107], v183 offset:40960\n"
"s_waitcnt lgkmcnt(0)\n"
"s_barrier\n"
"v_cmp_ne_u32_e64 s[80:81], s97, v198\n"
"s_nop 1\n"
"s_and_b64 s[80:81], s[80:81], s[82:83]\n"
"s_cbranch_scc1 Lx0npack36_%=\n"
"s_mov_b32 s96, 1\n"
"s_mov_b32 s57, 0\n"
"s_mov_b32 s77, 36864\n"
"v_readlane_b32 s67, v6, s57\n"
"s_nop 0\n"
"s_mul_i32 s67, s67, s50\n"
"s_add_u32 s65, s67, s49\n"
"s_sub_u32 s64, 36864, s77\n"
"s_lshl_b32 s68, %0, 10\n"
"s_add_u32 s64, s64, s68\n"
"s_add_u32 m0, s64, 0\n"
"s_add_u32 s70, s65, 0\n"
"buffer_load_dwordx4 v2, s[36:39], s70 offen sc1 lds\n"
"s_add_u32 m0, s64, 4096\n"
"s_add_u32 s70, s65, 4096\n"
"buffer_load_dwordx4 v2, s[36:39], s70 offen sc1 lds\n"
"s_add_u32 m0, s64, 8192\n"
"s_add_u32 s70, s65, 8192\n"
"buffer_load_dwordx4 v2, s[36:39], s70 offen sc1 lds\n"
"s_add_u32 m0, s64, 12288\n"
"s_add_u32 s70, s65, 12288\n"
"buffer_load_dwordx4 v2, s[36:39], s70 offen sc1 lds\n"
"s_add_u32 m0, s64, 16384\n"
"s_add_u32 s70, s65, 16384\n"
"buffer_load_dwordx4 v2, s[36:39], s70 offen sc1 lds\n"
"s_add_u32 m0, s64, 20480\n"
"s_add_u32 s70, s65, 20480\n"
"buffer_load_dwordx4 v2, s[36:39], s70 offen sc1 lds\n"
"s_add_u32 m0, s64, 24576\n"
"s_add_u32 s70, s65, 24576\n"
"buffer_load_dwordx4 v2, s[36:39], s70 offen sc1 lds\n"
"s_add_u32 m0, s64, 28672\n"
"s_add_u32 s70, s65, 28672\n"
"buffer_load_dwordx4 v2, s[36:39], s70 offen sc1 lds\n"
"s_add_u32 m0, s64, 32768\n"
"s_add_u32 s70, s65, 32768\n"
"buffer_load_dwordx4 v2, s[36:39], s70 offen sc1 lds\n"
"Lx0npack36_%=:\n"
"s_cmp_eq_u32 s96, 0\n"
"s_cbranch_scc1 Lw7apack36_%=\n"
"s_waitcnt vmcnt(9)\n"
"s_branch Lw7bpack36_%=\n"
"Lw7apack36_%=:\n"
"s_waitcnt vmcnt(0)\n"
"Lw7bpack36_%=:\n"
"s_barrier\n"
"ds_read_b64_tr_b16 a[108:109], v182 offset:45056\n"
"ds_read_b64_tr_b16 a[110:111], v183 offset:45056\n"
"ds_read_b64_tr_b16 a[112:113], v182 offset:49152\n"
"ds_read_b64_tr_b16 a[114:115], v183 offset:49152\n"
"ds_read_b64_tr_b16 a[116:117], v182 offset:53248\n"
"ds_read_b64_tr_b16 a[118:119], v183 offset:53248\n"
"ds_read_b64_tr_b16 a[120:121], v182 offset:57344\n"
"ds_read_b64_tr_b16 a[122:123], v183 offset:57344\n"
"ds_read_b64_tr_b16 a[124:125], v182 offset:61440\n"
"ds_read_b64_tr_b16 a[126:127], v183 offset:61440\n"
"ds_read_b64_tr_b16 a[128:129], v184\n"
"ds_read_b64_tr_b16 a[130:131], v185\n"
"ds_read_b64_tr_b16 a[132:133], v184 offset:4096\n"
"ds_read_b64_tr_b16 a[134:135], v185 offset:4096\n"
"ds_read_b64_tr_b16 a[136:137], v184 offset:8192\n"
"s_waitcnt lgkmcnt(8)\n"
"ds_read_b64_tr_b16 a[138:139], v185 offset:8192\n"
"ds_read_b64_tr_b16 a[140:141], v184 offset:12288\n"
"ds_read_b64_tr_b16 a[142:143], v185 offset:12288\n"
"s_waitcnt lgkmcnt(0)\n"
"s_barrier\n"
"s_lshl_b32 s55, %0, 10\n"
"s_mov_b32 s88, 0xffffffff\n"
"s_mov_b32 s89, 0xff\n"
"v_lshrrev_b32 v212, 2, v0\n"
"v_add_u32 v212, 86016, v212\n"
"v_lshlrev_b32 v192, 2, v10\n"
"v_add_u32 v192, 86528, v192\n"
"ds_write_b32 v192, v7\n"
"ds_write_b32 v192, v15 offset:256\n"
"s_mov_b32 s57, 0\n"
"s_mov_b32 s59, 0\n"
"s_mov_b32 s60, -1\n"
"s_mov_b32 s61, -1\n"
"s_mov_b32 s77, 36864\n"
"s_waitcnt lgkmcnt(0)\n"
"s_cmp_lg_u32 s96, 0\n"
"s_cbranch_scc1 Lbwipack36_%=\n"
"s_cmp_eq_u32 %0, 0\n"
"s_cbranch_scc0 Lbwipack36_%=\n"
"s_cmp_lg_u32 s75, 0\n"
"s_cbranch_scc1 Lbwipack36_%=\n"
"v_readlane_b32 s72, v7, s57\n"
"s_nop 3\n"
"s_mov_b32 s71, 0\n"
"s_cmp_lt_i32 s74, 0\n"
"s_cbranch_scc1 Lplipack36_%=\n"
"s_sub_u32 s67, s57, s74\n"
"s_cmp_gt_u32 s67, 3\n"
"s_cbranch_scc1 Lplipack36_%=\n"
"s_add_u32 s68, s57, 2\n"
"s_cmp_ge_u32 s68, s48\n"
"s_cbranch_scc1 Lplipack36_%=\n"
"s_mul_i32 s67, s67, 240\n"
"s_add_u32 s67, s67, s73\n"
"s_sub_u32 s67, s67, 40\n"
"s_mov_b32 s76, 0\n"
"Lslipack36_%=:\n"
"s_memrealtime s[84:85]\n"
"s_waitcnt lgkmcnt(0)\n"
"s_sub_u32 s68, s84, s67\n"
"s_cmp_ge_i32 s68, 0\n"
"s_cbranch_scc1 Lplipack36_%=\n"
"s_sleep 2\n"
"s_mov_b32 s71, 1\n"
"s_add_u32 s76, s76, 1\n"
"s_cmp_lt_u32 s76, 32768\n"
"s_cbranch_scc1 Lslipack36_%=\n"
"Lplipack36_%=:\n"
"s_mov_b32 s76, 0\n"
"Lppipack36_%=:\n"
"buffer_load_dword v192, v5, s[44:47], s72 offen sc1\n"
"s_waitcnt vmcnt(0)\n"
"v_cmp_ne_u32_e64 s[80:81], s97, v192\n"
"s_nop 1\n"
"s_and_b64 s[80:81], s[80:81], s[82:83]\n"
"s_cbranch_scc0 Lrdipack36_%=\n"
"s_sleep 12\n"
"s_add_u32 s76, s76, 1\n"
"s_cmp_lt_u32 s76, 32768\n"
"s_cbranch_scc1 Lppipack36_%=\n"
"s_mov_b32 s75, 1\n"
"v_mov_b32 v192, 16384\n"
"v_mov_b32 v193, 7000\n"
"buffer_store_dword v193, v192, s[44:47], 0 offen sc1\n"
"Lrdipack36_%=:\n"
"s_mov_b32 s74, -1\n"
"s_cmp_eq_u32 s76, 0\n"
"s_cbranch_scc1 Lbwipack36_%=\n"
"s_memrealtime s[84:85]\n"
"s_waitcnt lgkmcnt(0)\n"
"s_mov_b32 s73, s84\n"
"s_mov_b32 s74, s57\n"
"Lbwipack36_%=:\n"
"s_barrier\n"
"s_cmp_lg_u32 s96, 0\n"
"s_cbranch_scc1 Lx0spack36_%=\n"
"v_readlane_b32 s67, v6, s57\n"
"s_nop 0\n"
"s_mul_i32 s67, s67, s50\n"
"s_add_u32 s65, s67, s49\n"
"s_sub_u32 s64, 36864, s77\n"
"s_lshl_b32 s68, %0, 10\n"
"s_add_u32 s64, s64, s68\n"
"s_add_u32 m0, s64, 0\n"
"s_add_u32 s70, s65, 0\n"
"buffer_load_dwordx4 v2, s[36:39], s70 offen sc1 lds\n"
"s_add_u32 m0, s64, 4096\n"
"s_add_u32 s70, s65, 4096\n"
"buffer_load_dwordx4 v2, s[36:39], s70 offen sc1 lds\n"
"s_add_u32 m0, s64, 8192\n"
"s_add_u32 s70, s65, 8192\n"
"buffer_load_dwordx4 v2, s[36:39], s70 offen sc1 lds\n"
"s_add_u32 m0, s64, 12288\n"
"s_add_u32 s70, s65, 12288\n"
"buffer_load_dwordx4 v2, s[36:39], s70 offen sc1 lds\n"
"s_add_u32 m0, s64, 16384\n"
"s_add_u32 s70, s65, 16384\n"
"buffer_load_dwordx4 v2, s[36:39], s70 offen sc1 lds\n"
"s_add_u32 m0, s64, 20480\n"
"s_add_u32 s70, s65, 20480\n"
"buffer_load_dwordx4 v2, s[36:39], s70 offen sc1 lds\n"
"s_add_u32 m0, s64, 24576\n"
"s_add_u32 s70, s65, 24576\n"
"buffer_load_dwordx4 v2, s[36:39], s70 offen sc1 lds\n"
"s_add_u32 m0, s64, 28672\n"
"s_add_u32 s70, s65, 28672\n"
"buffer_load_dwordx4 v2, s[36:39], s70 offen sc1 lds\n"
"s_add_u32 m0, s64, 32768\n"
"s_add_u32 s70, s65, 32768\n"
"buffer_load_dwordx4 v2, s[36:39], s70 offen sc1 lds\n"
"Lx0spack36_%=:\n"
"s_waitcnt vmcnt(0) lgkmcnt(0)\n"
"s_barrier\n"
"s_sub_u32 s77, 36864, s77\n"
"v_add_u32 v1, s77, v0\n"
"ds_read_b128 v[80:83], v1\n"
"ds_read_b128 v[84:87], v1 offset:1024\n"
"ds_read_b128 v[88:91], v1 offset:2048\n"
"ds_read_b128 v[92:95], v1 offset:3072\n"
"s_mov_b32 s58, 1\n"
"Ltoppack36_%=:\n"
"s_cmp_eq_u32 s59, 1\n"
"s_cbranch_scc1 LPApack36_%=\n"
"s_cmp_eq_u32 s59, 2\n"
"s_cbranch_scc1 LPBpack36_%=\n"
"s_add_u32 s67, s57, 1\n"
"s_cmp_lt_u32 s67, s58\n"
"s_cbranch_scc0 Lnonpack36_%=\n"
"v_readlane_b32 s67, v6, s67\n"
"s_nop 0\n"
"s_mul_i32 s67, s67, s50\n"
"s_add_u32 s65, s67, s49\n"
"s_sub_u32 s64, 36864, s77\n"
"s_lshl_b32 s68, %0, 10\n"
"s_add_u32 s64, s64, s68\n"
"s_mov_b32 s62, -1\n"
"s_cmp_ge_u32 s58, s48\n"
"s_cbranch_scc1 Lq1pack36_%=\n"
"s_sub_u32 s68, s58, s57\n"
"s_cmp_le_u32 s68, 4\n"
"s_cselect_b32 s62, s58, -1\n"
"Lq1pack36_%=:\n"
"s_max_i32 s68, s60, s61\n"
"s_mov_b32 s63, 0\n"
"s_cmp_lt_i32 s68, 0\n"
"s_cbranch_scc1 Lq2pack36_%=\n"
"s_mov_b32 s63, 1\n"
"Lq2pack36_%=:\n"
"s_waitcnt lgkmcnt(3)\n"
"v_mfma_f32_32x32x16_bf16 v[16:31], v[128:131], v[80:83], 0\n"
"ds_read_b128 v[96:99], v1 offset:4096\n"
"v_mfma_f32_32x32x16_bf16 v[32:47], a[0:3], v[80:83], 0\n"
"s_waitcnt lgkmcnt(3)\n"
"v_mfma_f32_32x32x16_bf16 v[16:31], v[132:135], v[84:87], v[16:31]\n"
"ds_read_b128 v[100:103], v1 offset:5120\n"
"v_mfma_f32_32x32x16_bf16 v[32:47], a[4:7], v[84:87], v[32:47]\n"
"s_add_u32 m0, s64, 0\n"
"s_add_u32 s70, s65, 0\n"
"buffer_load_dwordx4 v2, s[36:39], s70 offen sc1 lds\n"
"s_waitcnt lgkmcnt(3)\n"
"v_mfma_f32_32x32x16_bf16 v[16:31], v[136:139], v[88:91], v[16:31]\n"
"ds_read_b128 v[80:83], v1 offset:6144\n"
"v_mfma_f32_32x32x16_bf16 v[32:47], a[8:11], v[88:91], v[32:47]\n"
"s_waitcnt lgkmcnt(3)\n"
"v_mfma_f32_32x32x16_bf16 v[16:31], v[140:143], v[92:95], v[16:31]\n"
"ds_read_b128 v[84:87], v1 offset:7168\n"
"v_mfma_f32_32x32x16_bf16 v[32:47], a[12:15], v[92:95], v[32:47]\n"
"s_add_u32 m0, s64, 4096\n"
"s_add_u32 s70, s65, 4096\n"
"buffer_load_dwordx4 v2, s[36:39], s70 offen sc1 lds\n"
"s_waitcnt lgkmcnt(3)\n"
"v_mfma_f32_32x32x16_bf16 v[16:31], v[144:147], v[96:99], v[16:31]\n"
"ds_read_b128 v[88:91], v1 offset:8192\n"
"v_mfma_f32_32x32x16_bf16 v[32:47], a[16:19], v[96:99], v[32:47]\n"
"s_waitcnt lgkmcnt(3)\n"
"v_mfma_f32_32x32x16_bf16 v[16:31], v[148:151], v[100:103], v[16:31]\n"
"ds_read_b128 v[92:95], v1 offset:9216\n"
"v_mfma_f32_32x32x16_bf16 v[32:47], a[20:23], v[100:103], v[32:47]\n"
"s_add_u32 m0, s64, 8192\n"
"s_add_u32 s70, s65, 8192\n"
"buffer_load_dwordx4 v2, s[36:39], s70 offen sc1 lds\n"
"s_waitcnt lgkmcnt(3)\n"
"v_mfma_f32_32x32x16_bf16 v[16:31], v[152:155], v[80:83], v[16:31]\n"
"ds_read_b128 v[96:99], v1 offset:10240\n"
"v_mfma_f32_32x32x16_bf16 v[32:47], a[24:27], v[80:83], v[32:47]\n"
"s_waitcnt lgkmcnt(3)\n"
"v_mfma_f32_32x32x16_bf16 v[16:31], v[156:159], v[84:87], v[16:31]\n"
"ds_read_b128 v[100:103], v1 offset:11264\n"
"v_mfma_f32_32x32x16_bf16 v[32:47], a[28:31], v[84:87], v[32:47]\n"
"s_add_u32 m0, s64, 12288\n"
"s_add_u32 s70, s65, 12288\n"
"buffer_load_dwordx4 v2, s[36:39], s70 offen sc1 lds\n"
"s_waitcnt lgkmcnt(3)\n"
"v_mfma_f32_32x32x16_bf16 v[16:31], a[144:147], v[88:91], v[16:31]\n"
"ds_read_b128 v[80:83], v1 offset:12288\n"
"v_mfma_f32_32x32x16_bf16 v[32:47], a[32:35], v[88:91], v[32:47]\n"
"s_waitcnt lgkmcnt(3)\n"
"v_mfma_f32_32x32x16_bf16 v[16:31], a[148:151], v[92:95], v[16:31]\n"
"ds_read_b128 v[84:87], v1 offset:13312\n"
"v_mfma_f32_32x32x16_bf16 v[32:47], a[36:39], v[92:95], v[32:47]\n"
"s_add_u32 m0, s64, 16384\n"
"s_add_u32 s70, s65, 16384\n"
"buffer_load_dwordx4 v2, s[36:39], s70 offen sc1 lds\n"
"s_waitcnt lgkmcnt(3)\n"
"v_mfma_f32_32x32x16_bf16 v[16:31], a[152:155], v[96:99], v[16:31]\n"
"ds_read_b128 v[88:91], v1 offset:14336\n"
"v_mfma_f32_32x32x16_bf16 v[32:47], a[40:43], v[96:99], v[32:47]\n"
"s_waitcnt lgkmcnt(3)\n"
"v_mfma_f32_32x32x16_bf16 v[16:31], a[156:159], v[100:103], v[16:31]\n"
"ds_read_b128 v[92:95], v1 offset:15360\n"
"v_mfma_f32_32x32x16_bf16 v[32:47], a[44:47], v[100:103], v[32:47]\n"
"s_add_u32 m0, s64, 20480\n"
"s_add_u32 s70, s65, 20480\n"
"buffer_load_dwordx4 v2, s[36:39], s70 offen sc1 lds\n"
"s_waitcnt lgkmcnt(3)\n"
"v_mfma_f32_32x32x16_bf16 v[16:31], a[160:163], v[80:83], v[16:31]\n"
"ds_read_b128 v[96:99], v1 offset:16384\n"
"v_mfma_f32_32x32x16_bf16 v[32:47], a[48:51], v[80:83], v[32:47]\n"
"s_waitcnt lgkmcnt(3)\n"
"v_mfma_f32_32x32x16_bf16 v[16:31], a[164:167], v[84:87], v[16:31]\n"
"ds_read_b128 v[100:103], v1 offset:17408\n"
"v_mfma_f32_32x32x16_bf16 v[32:47], a[52:55], v[84:87], v[32:47]\n"
"s_add_u32 m0, s64, 24576\n"
"s_add_u32 s70, s65, 24576\n"
"buffer_load_dwordx4 v2, s[36:39], s70 offen sc1 lds\n"
"s_waitcnt lgkmcnt(3)\n"
"v_mfma_f32_32x32x16_bf16 v[16:31], a[168:171], v[88:91], v[16:31]\n"
"ds_read_b128 v[80:83], v1 offset:18432\n"
"v_mfma_f32_32x32x16_bf16 v[32:47], a[56:59], v[88:91], v[32:47]\n"
"s_cmp_eq_u32 s63, 0\n"
"s_cbranch_scc1 LnmDpack36_%=\n"
"s_waitcnt vmcnt(7)\n"
"s_barrier\n"
"s_cmp_lt_i32 s60, 0\n"
"s_cbranch_scc1 LnsmaDpack36_%=\n"
"v_readlane_b32 s67, v6, s60\n"
"s_nop 0\n"
"s_mul_i32 s67, s67, s54\n"
"s_add_u32 s67, s67, s53\n"
"buffer_store_dword v4, v3, s[44:47], s67 offen sc1\n"
"LnsmaDpack36_%=:\n"
"s_cmp_lt_i32 s61, 0\n"
"s_cbranch_scc1 LnsmbDpack36_%=\n"
"v_readlane_b32 s67, v6, s61\n"
"s_nop 0\n"
"s_mul_i32 s67, s67, s54\n"
"s_add_u32 s67, s67, s53\n"
"buffer_store_dword v4, v3, s[44:47], s67 offen sc1\n"
"LnsmbDpack36_%=:\n"
"LnmDpack36_%=:\n"
"s_waitcnt lgkmcnt(3)\n"
"v_mfma_f32_32x32x16_bf16 v[16:31], a[172:175], v[92:95], v[16:31]\n"
"ds_read_b128 v[84:87], v1 offset:19456\n"
"v_mfma_f32_32x32x16_bf16 v[32:47], a[60:63], v[92:95], v[32:47]\n"
"s_add_u32 m0, s64, 28672\n"
"s_add_u32 s70, s65, 28672\n"
"buffer_load_dwordx4 v2, s[36:39], s70 offen sc1 lds\n"
"s_waitcnt lgkmcnt(3)\n"
"v_mfma_f32_32x32x16_bf16 v[16:31], a[176:179], v[96:99], v[16:31]\n"
"ds_read_b128 v[88:91], v1 offset:20480\n"
"v_mfma_f32_32x32x16_bf16 v[32:47], a[64:67], v[96:99], v[32:47]\n"
"s_waitcnt lgkmcnt(3)\n"
"v_mfma_f32_32x32x16_bf16 v[16:31], a[180:183], v[100:103], v[16:31]\n"
"ds_read_b128 v[92:95], v1 offset:21504\n"
"v_mfma_f32_32x32x16_bf16 v[32:47], a[68:71], v[100:103], v[32:47]\n"
"s_add_u32 m0, s64, 32768\n"
"s_add_u32 s70, s65, 32768\n"
"buffer_load_dwordx4 v2, s[36:39], s70 offen sc1 lds\n"
"s_waitcnt lgkmcnt(3)\n"
"v_mfma_f32_32x32x16_bf16 v[16:31], a[184:187], v[80:83], v[16:31]\n"
"ds_read_b128 v[96:99], v1 offset:22528\n"
"v_mfma_f32_32x32x16_bf16 v[32:47], a[72:75], v[80:83], v[32:47]\n"
"s_cmp_lt_i32 s62, 0\n"
"s_cbranch_scc1 LnpDpack36_%=\n"
"s_and_b32 s72, s57, 1\n"
"s_lshl_b32 s72, s72, 8\n"
"s_add_u32 s72, s72, 86016\n"
"s_waitcnt lgkmcnt(0)\n"
"s_lshl_b32 s67, s62, 2\n"
"s_add_u32 s67, s67, 86528\n"
"v_add_u32 v192, s67, v8\n"
"ds_read_b32 v192, v192\n"
"s_waitcnt lgkmcnt(0)\n"
"v_add_u32 v192, v192, v9\n"
"s_mov_b32 m0, s72\n"
"s_mov_b64 exec, s[78:79]\n"
"buffer_load_dword v192, s[44:47], 0 offen sc1 lds\n"
"s_mov_b64 exec, -1\n"
"s_waitcnt lgkmcnt(0)\n"
"LnpDpack36_%=:\n"
"s_waitcnt lgkmcnt(3)\n"
"v_mfma_f32_32x32x16_bf16 v[16:31], a[188:191], v[84:87], v[16:31]\n"
"ds_read_b128 v[100:103], v1 offset:23552\n"
"v_mfma_f32_32x32x16_bf16 v[32:47], a[76:79], v[84:87], v[32:47]\n"
"s_waitcnt lgkmcnt(3)\n"
"v_mfma_f32_32x32x16_bf16 v[16:31], a[192:195], v[88:91], v[16:31]\n"
"ds_read_b128 v[80:83], v1 offset:24576\n"
"v_mfma_f32_32x32x16_bf16 v[32:47], a[80:83], v[88:91], v[32:47]\n"
"s_waitcnt lgkmcnt(3)\n"
"v_mfma_f32_32x32x16_bf16 v[16:31], a[196:199], v[92:95], v[16:31]\n"
"ds_read_b128 v[84:87], v1 offset:25600\n"
"v_mfma_f32_32x32x16_bf16 v[32:47], a[84:87], v[92:95], v[32:47]\n"
"s_waitcnt lgkmcnt(3)\n"
"v_mfma_f32_32x32x16_bf16 v[16:31], a[200:203], v[96:99], v[16:31]\n"
"ds_read_b128 v[88:91], v1 offset:26624\n"
"v_mfma_f32_32x32x16_bf16 v[32:47], a[88:91], v[96:99], v[32:47]\n"
"s_waitcnt lgkmcnt(3)\n"
"v_mfma_f32_32x32x16_bf16 v[16:31], a[204:207], v[100:103], v[16:31]\n"
"ds_read_b128 v[92:95], v1 offset:27648\n"
"v_mfma_f32_32x32x16_bf16 v[32:47], a[92:95], v[100:103], v[32:47]\n"
"s_waitcnt lgkmcnt(3)\n"
"v_mfma_f32_32x32x16_bf16 v[16:31], a[208:211], v[80:83], v[16:31]\n"
"ds_read_b128 v[96:99], v1 offset:28672\n"
"v_mfma_f32_32x32x16_bf16 v[32:47], a[96:99], v[80:83], v[32:47]\n"
"s_waitcnt lgkmcnt(3)\n"
"v_mfma_f32_32x32x16_bf16 v[16:31], a[212:215], v[84:87], v[16:31]\n"
"ds_read_b128 v[100:103], v1 offset:29696\n"
"v_mfma_f32_32x32x16_bf16 v[32:47], a[100:103], v[84:87], v[32:47]\n"
"s_waitcnt lgkmcnt(3)\n"
"v_mfma_f32_32x32x16_bf16 v[16:31], a[216:219], v[88:91], v[16:31]\n"
"ds_read_b128 v[80:83], v1 offset:30720\n"
"v_mfma_f32_32x32x16_bf16 v[32:47], a[104:107], v[88:91], v[32:47]\n"
"s_waitcnt lgkmcnt(3)\n"
"v_mfma_f32_32x32x16_bf16 v[16:31], a[220:223], v[92:95], v[16:31]\n"
"ds_read_b128 v[84:87], v1 offset:31744\n"
"v_mfma_f32_32x32x16_bf16 v[32:47], a[108:111], v[92:95], v[32:47]\n"
"s_waitcnt lgkmcnt(3)\n"
"v_mfma_f32_32x32x16_bf16 v[16:31], a[224:227], v[96:99], v[16:31]\n"
"ds_read_b128 v[88:91], v1 offset:32768\n"
"v_mfma_f32_32x32x16_bf16 v[32:47], a[112:115], v[96:99], v[32:47]\n"
"s_waitcnt lgkmcnt(3)\n"
"v_mfma_f32_32x32x16_bf16 v[16:31], a[228:231], v[100:103], v[16:31]\n"
"ds_read_b128 v[92:95], v1 offset:33792\n"
"v_mfma_f32_32x32x16_bf16 v[32:47], a[116:119], v[100:103], v[32:47]\n"
"s_waitcnt lgkmcnt(3)\n"
"v_mfma_f32_32x32x16_bf16 v[16:31], a[232:235], v[80:83], v[16:31]\n"
"ds_read_b128 v[96:99], v1 offset:34816\n"
"v_mfma_f32_32x32x16_bf16 v[32:47], a[120:123], v[80:83], v[32:47]\n"
"s_waitcnt lgkmcnt(3)\n"
"v_mfma_f32_32x32x16_bf16 v[16:31], a[236:239], v[84:87], v[16:31]\n"
"ds_read_b128 v[100:103], v1 offset:35840\n"
"v_mfma_f32_32x32x16_bf16 v[32:47], a[124:127], v[84:87], v[32:47]\n"
"s_waitcnt lgkmcnt(3)\n"
"v_mfma_f32_32x32x16_bf16 v[16:31], a[240:243], v[88:91], v[16:31]\n"
"v_mfma_f32_32x32x16_bf16 v[32:47], a[128:131], v[88:91], v[32:47]\n"
"s_waitcnt lgkmcnt(2)\n"
"v_mfma_f32_32x32x16_bf16 v[16:31], a[244:247], v[92:95], v[16:31]\n"
"v_mfma_f32_32x32x16_bf16 v[32:47], a[132:135], v[92:95], v[32:47]\n"
"s_waitcnt lgkmcnt(1)\n"
"v_mfma_f32_32x32x16_bf16 v[16:31], a[248:251], v[96:99], v[16:31]\n"
"v_mfma_f32_32x32x16_bf16 v[32:47], a[136:139], v[96:99], v[32:47]\n"
"s_waitcnt lgkmcnt(0)\n"
"v_mfma_f32_32x32x16_bf16 v[16:31], a[252:255], v[100:103], v[16:31]\n"
"v_mfma_f32_32x32x16_bf16 v[32:47], a[140:143], v[100:103], v[32:47]\n"
"s_waitcnt vmcnt(0) lgkmcnt(0)\n"
"s_barrier\n"
"s_mov_b32 s99, 0\n"
"s_sub_u32 s77, 36864, s77\n"
"v_add_u32 v1, s77, v0\n"
"ds_read_b128 v[80:83], v1\n"
"ds_read_b128 v[84:87], v1 offset:1024\n"
"ds_read_b128 v[88:91], v1 offset:2048\n"
"ds_read_b128 v[92:95], v1 offset:3072\n"
"s_branch Le1pack36_%=\n"
"LPApack36_%=:\n"
"s_waitcnt lgkmcnt(3)\n"
"v_mfma_f32_32x32x16_bf16 v[48:63], v[128:131], v[80:83], 0\n"
"ds_read_b128 v[96:99], v1 offset:4096\n"
"s_add_u32 s92, s57, 1\n"
"v_readlane_b32 s92, v6, s92\n"
"s_sub_u32 s64, 36864, s77\n"
"v_mfma_f32_32x32x16_bf16 v[64:79], a[0:3], v[80:83], 0\n"
"s_add_u32 s64, s64, s55\n"
"s_mul_i32 s92, s92, s50\n"
"s_add_u32 s65, s92, s49\n"
"s_sub_u32 s90, s57, 1\n"
"s_and_b32 s90, s90, 1\n"
"s_waitcnt lgkmcnt(3)\n"
"v_mfma_f32_32x32x16_bf16 v[48:63], v[132:135], v[84:87], v[48:63]\n"
"ds_read_b128 v[100:103], v1 offset:5120\n"
"s_lshl_b32 s90, s90, 8\n"
"v_add_u32 v194, s90, v212\n"
"ds_read_b32 v195, v194\n"
"v_mfma_f32_32x32x16_bf16 v[64:79], a[4:7], v[84:87], v[64:79]\n"
"s_add_u32 m0, s64, 0\n"
"s_add_u32 s92, s65, 0\n"
"buffer_load_dwordx4 v2, s[36:39], s92 offen sc1 lds\n"
"s_sub_u32 s93, s57, 1\n"
"s_waitcnt lgkmcnt(4)\n"
"v_mfma_f32_32x32x16_bf16 v[48:63], v[136:139], v[88:91], v[48:63]\n"
"ds_read_b128 v[80:83], v1 offset:6144\n"
"v_readlane_b32 s93, v6, s93\n"
"v_mfma_f32_32x32x16_bf16 v[64:79], a[8:11], v[88:91], v[64:79]\n"
"s_add_u32 m0, s64, 4096\n"
"s_add_u32 s92, s65, 4096\n"
"buffer_load_dwordx4 v2, s[36:39], s92 offen sc1 lds\n"
"s_mul_i32 s93, s93, s52\n"
"s_waitcnt lgkmcnt(4)\n"
"v_mfma_f32_32x32x16_bf16 v[48:63], v[140:143], v[92:95], v[48:63]\n"
"ds_read_b128 v[84:87], v1 offset:7168\n"
"s_waitcnt lgkmcnt(2)\n"
"v_cmp_ne_u32_e64 s[80:81], s97, v195\n"
"s_add_u32 s66, s93, s51\n"
"v_readlane_b32 s93, v6, s60\n"
"v_mfma_f32_32x32x16_bf16 v[64:79], a[12:15], v[92:95], v[64:79]\n"
"s_add_u32 m0, s64, 8192\n"
"s_add_u32 s92, s65, 8192\n"
"buffer_load_dwordx4 v2, s[36:39], s92 offen sc1 lds\n"
"s_and_b64 s[80:81], s[80:81], s[88:89]\n"
"v_mfma_f32_32x32x16_bf16 v[48:63], v[144:147], v[96:99], v[48:63]\n"
"ds_read_b128 v[88:91], v1 offset:8192\n"
"s_ff1_i32_b64 s90, s[80:81]\n"
"s_cmp_lt_i32 s90, 0\n"
"s_cselect_b32 s90, 40, s90\n"
"s_mul_i32 s90, s90, 13108\n"
"v_mfma_f32_32x32x16_bf16 v[64:79], a[16:19], v[96:99], v[64:79]\n"
"s_add_u32 m0, s64, 12288\n"
"s_add_u32 s92, s65, 12288\n"
"buffer_load_dwordx4 v2, s[36:39], s92 offen sc1 lds\n"
"s_lshr_b32 s90, s90, 16\n"
"v_mfma_f32_32x32x16_bf16 v[48:63], v[148:151], v[100:103], v[48:63]\n"
"ds_read_b128 v[92:95], v1 offset:9216\n"
"s_sub_u32 s91, s48, s86\n"
"s_min_u32 s90, s90, s91\n"
"s_add_u32 s90, s90, s86\n"
"s_mul_i32 s93, s93, s54\n"
"v_mfma_f32_32x32x16_bf16 v[64:79], a[20:23], v[100:103], v[64:79]\n"
"s_add_u32 m0, s64, 16384\n"
"s_add_u32 s92, s65, 16384\n"
"buffer_load_dwordx4 v2, s[36:39], s92 offen sc1 lds\n"
"s_add_u32 s93, s93, s53\n"
"s_waitcnt lgkmcnt(3)\n"
"v_mfma_f32_32x32x16_bf16 v[48:63], v[152:155], v[80:83], v[48:63]\n"
"ds_read_b128 v[96:99], v1 offset:10240\n"
"s_cmp_lt_i32 s86, 0\n"
"s_cselect_b32 s58, s58, s90\n"
"v_mfma_f32_32x32x16_bf16 v[64:79], a[24:27], v[80:83], v[64:79]\n"
"s_add_u32 m0, s64, 20480\n"
"s_add_u32 s92, s65, 20480\n"
"buffer_load_dwordx4 v2, s[36:39], s92 offen sc1 lds\n"
"s_waitcnt lgkmcnt(3)\n"
"v_mfma_f32_32x32x16_bf16 v[48:63], v[156:159], v[84:87], v[48:63]\n"
"ds_read_b128 v[100:103], v1 offset:11264\n"
"s_cmp_lt_u32 s58, s48\n"
"s_cselect_b32 s62, s58, -1\n"
"s_sub_u32 s90, s58, s57\n"
"v_mfma_f32_32x32x16_bf16 v[64:79], a[28:31], v[84:87], v[64:79]\n"
"s_add_u32 m0, s64, 24576\n"
"s_add_u32 s92, s65, 24576\n"
"buffer_load_dwordx4 v2, s[36:39], s92 offen sc1 lds\n"
"s_waitcnt lgkmcnt(3)\n"
"v_mfma_f32_32x32x16_bf16 v[48:63], a[144:147], v[88:91], v[48:63]\n"
"ds_read_b128 v[80:83], v1 offset:12288\n"
"s_cmp_le_u32 s90, 4\n"
"s_cselect_b32 s62, s62, -1\n"
"s_max_i32 s90, s62, 0\n"
"v_mfma_f32_32x32x16_bf16 v[64:79], a[32:35], v[88:91], v[64:79]\n"
"s_add_u32 m0, s64, 28672\n"
"s_add_u32 s92, s65, 28672\n"
"buffer_load_dwordx4 v2, s[36:39], s92 offen sc1 lds\n"
"s_lshl_b32 s90, s90, 2\n"
"s_waitcnt lgkmcnt(3)\n"
"v_mfma_f32_32x32x16_bf16 v[48:63], a[148:151], v[92:95], v[48:63]\n"
"ds_read_b128 v[84:87], v1 offset:13312\n"
"s_add_u32 s90, s90, 86528\n"
"v_add_u32 v196, s90, v8\n"
"ds_read_b32 v196, v196\n"
"v_mfma_f32_32x32x16_bf16 v[64:79], a[36:39], v[92:95], v[64:79]\n"
"s_add_u32 m0, s64, 32768\n"
"s_add_u32 s92, s65, 32768\n"
"buffer_load_dwordx4 v2, s[36:39], s92 offen sc1 lds\n"
"s_waitcnt lgkmcnt(4)\n"
"v_mfma_f32_32x32x16_bf16 v[48:63], a[152:155], v[96:99], v[48:63]\n"
"ds_read_b128 v[88:91], v1 offset:14336\n"
"s_cmp_ge_i32 s62, 0\n"
"s_cselect_b64 s[94:95], s[78:79], 0\n"
"s_and_b32 s91, s57, 1\n"
"v_mfma_f32_32x32x16_bf16 v[64:79], a[40:43], v[96:99], v[64:79]\n"
"s_lshl_b32 s91, s91, 8\n"
"s_add_u32 s91, s91, 86016\n"
"s_add_u32 s90, s57, 2\n"
"s_cmp_lt_u32 s90, s58\n"
"s_cselect_b32 s87, 1, 0\n"
"s_waitcnt lgkmcnt(4)\n"
"v_mfma_f32_32x32x16_bf16 v[48:63], a[156:159], v[100:103], v[48:63]\n"
"ds_read_b128 v[92:95], v1 offset:15360\n"
"s_cmp_lt_i32 s60, 0\n"
"s_cselect_b32 s93, 0x7ffffff0, s93\n"
"v_add_u32 v197, s93, v3\n"
"v_mfma_f32_32x32x16_bf16 v[64:79], a[44:47], v[100:103], v[64:79]\n"
"v_cvt_pk_bf16_f32 v160, v16, v17\n"
"v_cvt_pk_bf16_f32 v161, v18, v19\n"
"v_cvt_pk_bf16_f32 v162, v20, v21\n"
"v_cvt_pk_bf16_f32 v163, v22, v23\n"
"v_cvt_pk_bf16_f32 v164, v24, v25\n"
"s_waitcnt lgkmcnt(4)\n"
"v_mfma_f32_32x32x16_bf16 v[48:63], a[160:163], v[80:83], v[48:63]\n"
"ds_read_b128 v[96:99], v1 offset:16384\n"
"s_waitcnt lgkmcnt(3)\n"
"v_add_u32 v196, v196, v9\n"
"v_cvt_pk_bf16_f32 v165, v26, v27\n"
"v_cvt_pk_bf16_f32 v166, v28, v29\n"
"v_mfma_f32_32x32x16_bf16 v[64:79], a[48:51], v[80:83], v[64:79]\n"
"s_mov_b32 m0, s91\n"
"s_mov_b64 exec, s[94:95]\n"
"buffer_load_dword v196, s[44:47], 0 offen sc1 lds\n"
"s_mov_b64 exec, -1\n"
"v_cvt_pk_bf16_f32 v167, v30, v31\n"
"v_mfma_f32_32x32x16_bf16 v[48:63], a[164:167], v[84:87], v[48:63]\n"
"ds_read_b128 v[100:103], v1 offset:17408\n"
"v_cvt_pk_bf16_f32 v168, v32, v33\n"
"v_cvt_pk_bf16_f32 v169, v34, v35\n"
"v_cvt_pk_bf16_f32 v170, v36, v37\n"
"v_cvt_pk_bf16_f32 v171, v38, v39\n"
"v_mfma_f32_32x32x16_bf16 v[64:79], a[52:55], v[84:87], v[64:79]\n"
"v_cvt_pk_bf16_f32 v172, v40, v41\n"
"v_cvt_pk_bf16_f32 v173, v42, v43\n"
"v_cvt_pk_bf16_f32 v174, v44, v45\n"
"v_cvt_pk_bf16_f32 v175, v46, v47\n"
"v_permlane32_swap_b32 v160, v162\n"
"s_waitcnt lgkmcnt(3)\n"
"v_mfma_f32_32x32x16_bf16 v[48:63], a[168:171], v[88:91], v[48:63]\n"
"ds_read_b128 v[80:83], v1 offset:18432\n"
"v_permlane32_swap_b32 v161, v163\n"
"v_permlane32_swap_b32 v164, v166\n"
"v_permlane32_swap_b32 v165, v167\n"
"v_mfma_f32_32x32x16_bf16 v[64:79], a[56:59], v[88:91], v[64:79]\n"
"v_permlane32_swap_b32 v168, v170\n"
"v_permlane32_swap_b32 v169, v171\n"
"v_permlane32_swap_b32 v172, v174\n"
"v_permlane32_swap_b32 v173, v175\n"
"s_waitcnt lgkmcnt(3)\n"
"v_mfma_f32_32x32x16_bf16 v[48:63], a[172:175], v[92:95], v[48:63]\n"
"ds_read_b128 v[84:87], v1 offset:19456\n"
"buffer_store_dwordx4 v[160:163], v11, s[40:43], s66 offen sc1\n"
"v_mfma_f32_32x32x16_bf16 v[64:79], a[60:63], v[92:95], v[64:79]\n"
"buffer_store_dwordx4 v[164:167], v12, s[40:43], s66 offen sc1\n"
"s_waitcnt lgkmcnt(3)\n"
"v_mfma_f32_32x32x16_bf16 v[48:63], a[176:179], v[96:99], v[48:63]\n"
"ds_read_b128 v[88:91], v1 offset:20480\n"
"buffer_store_dwordx4 v[168:171], v13, s[40:43], s66 offen sc1\n"
"v_mfma_f32_32x32x16_bf16 v[64:79], a[64:67], v[96:99], v[64:79]\n"
"buffer_store_dwordx4 v[172:175], v14, s[40:43], s66 offen sc1\n"
"s_waitcnt lgkmcnt(3)\n"
"v_mfma_f32_32x32x16_bf16 v[48:63], a[180:183], v[100:103], v[48:63]\n"
"ds_read_b128 v[92:95], v1 offset:21504\n"
"v_mfma_f32_32x32x16_bf16 v[64:79], a[68:71], v[100:103], v[64:79]\n"
"s_waitcnt lgkmcnt(3)\n"
"v_mfma_f32_32x32x16_bf16 v[48:63], a[184:187], v[80:83], v[48:63]\n"
"ds_read_b128 v[96:99], v1 offset:22528\n"
"v_mfma_f32_32x32x16_bf16 v[64:79], a[72:75], v[80:83], v[64:79]\n"
"s_waitcnt lgkmcnt(3)\n"
"v_mfma_f32_32x32x16_bf16 v[48:63], a[188:191], v[84:87], v[48:63]\n"
"ds_read_b128 v[100:103], v1 offset:23552\n"
"v_mfma_f32_32x32x16_bf16 v[64:79], a[76:79], v[84:87], v[64:79]\n"
"s_waitcnt lgkmcnt(3)\n"
"v_mfma_f32_32x32x16_bf16 v[48:63], a[192:195], v[88:91], v[48:63]\n"
"ds_read_b128 v[80:83], v1 offset:24576\n"
"v_mfma_f32_32x32x16_bf16 v[64:79], a[80:83], v[88:91], v[64:79]\n"
"s_waitcnt lgkmcnt(3)\n"
"v_mfma_f32_32x32x16_bf16 v[48:63], a[196:199], v[92:95], v[48:63]\n"
"ds_read_b128 v[84:87], v1 offset:25600\n"
"v_mfma_f32_32x32x16_bf16 v[64:79], a[84:87], v[92:95], v[64:79]\n"
"s_waitcnt lgkmcnt(3)\n"
"v_mfma_f32_32x32x16_bf16 v[48:63], a[200:203], v[96:99], v[48:63]\n"
"ds_read_b128 v[88:91], v1 offset:26624\n"
"v_mfma_f32_32x32x16_bf16 v[64:79], a[88:91], v[96:99], v[64:79]\n"
"s_waitcnt lgkmcnt(3)\n"
"v_mfma_f32_32x32x16_bf16 v[48:63], a[204:207], v[100:103], v[48:63]\n"
"ds_read_b128 v[92:95], v1 offset:27648\n"
"v_mfma_f32_32x32x16_bf16 v[64:79], a[92:95], v[100:103], v[64:79]\n"
"s_waitcnt lgkmcnt(3)\n"
"v_mfma_f32_32x32x16_bf16 v[48:63], a[208:211], v[80:83], v[48:63]\n"
"ds_read_b128 v[96:99], v1 offset:28672\n"
"v_mfma_f32_32x32x16_bf16 v[64:79], a[96:99], v[80:83], v[64:79]\n"
"s_waitcnt lgkmcnt(3)\n"
"v_mfma_f32_32x32x16_bf16 v[48:63], a[212:215], v[84:87], v[48:63]\n"
"ds_read_b128 v[100:103], v1 offset:29696\n"
"v_mfma_f32_32x32x16_bf16 v[64:79], a[100:103], v[84:87], v[64:79]\n"
"s_waitcnt lgkmcnt(3)\n"
"v_mfma_f32_32x32x16_bf16 v[48:63], a[216:219], v[88:91], v[48:63]\n"
"ds_read_b128 v[80:83], v1 offset:30720\n"
"v_mfma_f32_32x32x16_bf16 v[64:79], a[104:107], v[88:91], v[64:79]\n"
"s_waitcnt lgkmcnt(3)\n"
"v_mfma_f32_32x32x16_bf16 v[48:63], a[220:223], v[92:95], v[48:63]\n"
"ds_read_b128 v[84:87], v1 offset:31744\n"
"v_mfma_f32_32x32x16_bf16 v[64:79], a[108:111], v[92:95], v[64:79]\n"
"s_waitcnt lgkmcnt(3)\n"
"v_mfma_f32_32x32x16_bf16 v[48:63], a[224:227], v[96:99], v[48:63]\n"
"ds_read_b128 v[88:91], v1 offset:32768\n"
"v_mfma_f32_32x32x16_bf16 v[64:79], a[112:115], v[96:99], v[64:79]\n"
"s_waitcnt lgkmcnt(3)\n"
"v_mfma_f32_32x32x16_bf16 v[48:63], a[228:231], v[100:103], v[48:63]\n"
"ds_read_b128 v[92:95], v1 offset:33792\n"
"v_mfma_f32_32x32x16_bf16 v[64:79], a[116:119], v[100:103], v[64:79]\n"
"s_waitcnt lgkmcnt(3)\n"
"v_mfma_f32_32x32x16_bf16 v[48:63], a[232:235], v[80:83], v[48:63]\n"
"ds_read_b128 v[96:99], v1 offset:34816\n"
"v_mfma_f32_32x32x16_bf16 v[64:79], a[120:123], v[80:83], v[64:79]\n"
"s_waitcnt lgkmcnt(3)\n"
"v_mfma_f32_32x32x16_bf16 v[48:63], a[236:239], v[84:87], v[48:63]\n"
"ds_read_b128 v[100:103], v1 offset:35840\n"
"v_mfma_f32_32x32x16_bf16 v[64:79], a[124:127], v[84:87], v[64:79]\n"
"s_waitcnt lgkmcnt(3)\n"
"v_mfma_f32_32x32x16_bf16 v[48:63], a[240:243], v[88:91], v[48:63]\n"
"v_mfma_f32_32x32x16_bf16 v[64:79], a[128:131], v[88:91], v[64:79]\n"
"s_waitcnt vmcnt(4) lgkmcnt(0)\n"
"s_barrier\n"
"v_mfma_f32_32x32x16_bf16 v[48:63], a[244:247], v[92:95], v[48:63]\n"
"s_sub_u32 s77, 36864, s77\n"
"v_add_u32 v1, s77, v0\n"
"ds_read_b128 v[80:83], v1\n"
"ds_read_b128 v[84:87], v1 offset:1024\n"
"v_mfma_f32_32x32x16_bf16 v[64:79], a[132:135], v[92:95], v[64:79]\n"
"buffer_store_dword v4, v197, s[44:47], 0 offen sc1\n"
"v_mfma_f32_32x32x16_bf16 v[48:63], a[248:251], v[96:99], v[48:63]\n"
"ds_read_b128 v[88:91], v1 offset:2048\n"
"v_mfma_f32_32x32x16_bf16 v[64:79], a[136:139], v[96:99], v[64:79]\n"
"s_sub_u32 s60, s57, 1\n"
"s_mov_b32 s86, s62\n"
"s_add_u32 s57, s57, 1\n"
"s_cmp_eq_u32 s87, 0\n"
"v_mfma_f32_32x32x16_bf16 v[48:63], a[252:255], v[100:103], v[48:63]\n"
"ds_read_b128 v[92:95], v1 offset:3072\n"
"v_mfma_f32_32x32x16_bf16 v[64:79], a[140:143], v[100:103], v[64:79]\n"
"s_cbranch_scc1 LfxApack36_%=\n"
"LPBpack36_%=:\n"
"s_waitcnt lgkmcnt(3)\n"
"v_mfma_f32_32x32x16_bf16 v[16:31], v[128:131], v[80:83], 0\n"
"ds_read_b128 v[96:99], v1 offset:4096\n"
"s_add_u32 s92, s57, 1\n"
"v_readlane_b32 s92, v6, s92\n"
"s_sub_u32 s64, 36864, s77\n"
"v_mfma_f32_32x32x16_bf16 v[32:47], a[0:3], v[80:83], 0\n"
"s_add_u32 s64, s64, s55\n"
"s_mul_i32 s92, s92, s50\n"
"s_add_u32 s65, s92, s49\n"
"s_sub_u32 s90, s57, 1\n"
"s_and_b32 s90, s90, 1\n"
"s_waitcnt lgkmcnt(3)\n"
"v_mfma_f32_32x32x16_bf16 v[16:31], v[132:135], v[84:87], v[16:31]\n"
"ds_read_b128 v[100:103], v1 offset:5120\n"
"s_lshl_b32 s90, s90, 8\n"
"v_add_u32 v194, s90, v212\n"
"ds_read_b32 v195, v194\n"
"v_mfma_f32_32x32x16_bf16 v[32:47], a[4:7], v[84:87], v[32:47]\n"
"s_add_u32 m0, s64, 0\n"
"s_add_u32 s92, s65, 0\n"
"buffer_load_dwordx4 v2, s[36:39], s92 offen sc1 lds\n"
"s_sub_u32 s93, s57, 1\n"
"s_waitcnt lgkmcnt(4)\n"
"v_mfma_f32_32x32x16_bf16 v[16:31], v[136:139], v[88:91], v[16:31]\n"
"ds_read_b128 v[80:83], v1 offset:6144\n"
"v_readlane_b32 s93, v6, s93\n"
"v_mfma_f32_32x32x16_bf16 v[32:47], a[8:11], v[88:91], v[32:47]\n"
"s_add_u32 m0, s64, 4096\n"
"s_add_u32 s92, s65, 4096\n"
"buffer_load_dwordx4 v2, s[36:39], s92 offen sc1 lds\n"
"s_mul_i32 s93, s93, s52\n"
"s_waitcnt lgkmcnt(4)\n"
"v_mfma_f32_32x32x16_bf16 v[16:31], v[140:143], v[92:95], v[16:31]\n"
"ds_read_b128 v[84:87], v1 offset:7168\n"
"s_waitcnt lgkmcnt(2)\n"
"v_cmp_ne_u32_e64 s[80:81], s97, v195\n"
"s_add_u32 s66, s93, s51\n"
"v_readlane_b32 s93, v6, s60\n"
"v_mfma_f32_32x32x16_bf16 v[32:47], a[12:15], v[92:95], v[32:47]\n"
"s_add_u32 m0, s64, 8192\n"
"s_add_u32 s92, s65, 8192\n"
"buffer_load_dwordx4 v2, s[36:39], s92 offen sc1 lds\n"
"s_and_b64 s[80:81], s[80:81], s[88:89]\n"
"v_mfma_f32_32x32x16_bf16 v[16:31], v[144:147], v[96:99], v[16:31]\n"
"ds_read_b128 v[88:91], v1 offset:8192\n"
"s_ff1_i32_b64 s90, s[80:81]\n"
"s_cmp_lt_i32 s90, 0\n"
"s_cselect_b32 s90, 40, s90\n"
"s_mul_i32 s90, s90, 13108\n"
"v_mfma_f32_32x32x16_bf16 v[32:47], a[16:19], v[96:99], v[32:47]\n"
"s_add_u32 m0, s64, 12288\n"
"s_add_u32 s92, s65, 12288\n"
"buffer_load_dwordx4 v2, s[36:39], s92 offen sc1 lds\n"
"s_lshr_b32 s90, s90, 16\n"
"v_mfma_f32_32x32x16_bf16 v[16:31], v[148:151], v[100:103], v[16:31]\n"
"ds_read_b128 v[92:95], v1 offset:9216\n"
"s_sub_u32 s91, s48, s86\n"
"s_min_u32 s90, s90, s91\n"
"s_add_u32 s90, s90, s86\n"
"s_mul_i32 s93, s93, s54\n"
"v_mfma_f32_32x32x16_bf16 v[32:47], a[20:23], v[100:103], v[32:47]\n"
"s_add_u32 m0, s64, 16384\n"
"s_add_u32 s92, s65, 16384\n"
"buffer_load_dwordx4 v2, s[36:39], s92 offen sc1 lds\n"
"s_add_u32 s93, s93, s53\n"
"s_waitcnt lgkmcnt(3)\n"
"v_mfma_f32_32x32x16_bf16 v[16:31], v[152:155], v[80:83], v[16:31]\n"
"ds_read_b128 v[96:99], v1 offset:10240\n"
"s_cmp_lt_i32 s86, 0\n"
"s_cselect_b32 s58, s58, s90\n"
"v_mfma_f32_32x32x16_bf16 v[32:47], a[24:27], v[80:83], v[32:47]\n"
"s_add_u32 m0, s64, 20480\n"
"s_add_u32 s92, s65, 20480\n"
"buffer_load_dwordx4 v2, s[36:39], s92 offen sc1 lds\n"
"s_waitcnt lgkmcnt(3)\n"
"v_mfma_f32_32x32x16_bf16 v[16:31], v[156:159], v[84:87], v[16:31]\n"
"ds_read_b128 v[100:103], v1 offset:11264\n"
"s_cmp_lt_u32 s58, s48\n"
"s_cselect_b32 s62, s58, -1\n"
"s_sub_u32 s90, s58, s57\n"
"v_mfma_f32_32x32x16_bf16 v[32:47], a[28:31], v[84:87], v[32:47]\n"
"s_add_u32 m0, s64, 24576\n"
"s_add_u32 s92, s65, 24576\n"
"buffer_load_dwordx4 v2, s[36:39], s92 offen sc1 lds\n"
"s_waitcnt lgkmcnt(3)\n"
"v_mfma_f32_32x32x16_bf16 v[16:31], a[144:147], v[88:91], v[16:31]\n"
"ds_read_b128 v[80:83], v1 offset:12288\n"
"s_cmp_le_u32 s90, 4\n"
"s_cselect_b32 s62, s62, -1\n"
"s_max_i32 s90, s62, 0\n"
"v_mfma_f32_32x32x16_bf16 v[32:47], a[32:35], v[88:91], v[32:47]\n"
"s_add_u32 m0, s64, 28672\n"
"s_add_u32 s92, s65, 28672\n"
"buffer_load_dwordx4 v2, s[36:39], s92 offen sc1 lds\n"
"s_lshl_b32 s90, s90, 2\n"
"s_waitcnt lgkmcnt(3)\n"
"v_mfma_f32_32x32x16_bf16 v[16:31], a[148:151], v[92:95], v[16:31]\n"
"ds_read_b128 v[84:87], v1 offset:13312\n"
"s_add_u32 s90, s90, 86528\n"
"v_add_u32 v196, s90, v8\n"
"ds_read_b32 v196, v196\n"
"v_mfma_f32_32x32x16_bf16 v[32:47], a[36:39], v[92:95], v[32:47]\n"
"s_add_u32 m0, s64, 32768\n"
"s_add_u32 s92, s65, 32768\n"
"buffer_load_dwordx4 v2, s[36:39], s92 offen sc1 lds\n"
"s_waitcnt lgkmcnt(4)\n"
"v_mfma_f32_32x32x16_bf16 v[16:31], a[152:155], v[96:99], v[16:31]\n"
"ds_read_b128 v[88:91], v1 offset:14336\n"
"s_cmp_ge_i32 s62, 0\n"
"s_cselect_b64 s[94:95], s[78:79], 0\n"
"s_and_b32 s91, s57, 1\n"
"v_mfma_f32_32x32x16_bf16 v[32:47], a[40:43], v[96:99], v[32:47]\n"
"s_lshl_b32 s91, s91, 8\n"
"s_add_u32 s91, s91, 86016\n"
"s_add_u32 s90, s57, 2\n"
"s_cmp_lt_u32 s90, s58\n"
"s_cselect_b32 s87, 1, 0\n"
"s_waitcnt lgkmcnt(4)\n"
"v_mfma_f32_32x32x16_bf16 v[16:31], a[156:159], v[100:103], v[16:31]\n"
"ds_read_b128 v[92:95], v1 offset:15360\n"
"s_cmp_lt_i32 s60, 0\n"
"s_cselect_b32 s93, 0x7ffffff0, s93\n"
"v_add_u32 v197, s93, v3\n"
"v_mfma_f32_32x32x16_bf16 v[32:47], a[44:47], v[100:103], v[32:47]\n"
"v_cvt_pk_bf16_f32 v160, v48, v49\n"
"v_cvt_pk_bf16_f32 v161, v50, v51\n"
"v_cvt_pk_bf16_f32 v162, v52, v53\n"
"v_cvt_pk_bf16_f32 v163, v54, v55\n"
"v_cvt_pk_bf16_f32 v164, v56, v57\n"
"s_waitcnt lgkmcnt(4)\n"
"v_mfma_f32_32x32x16_bf16 v[16:31], a[160:163], v[80:83], v[16:31]\n"
"ds_read_b128 v[96:99], v1 offset:16384\n"
"s_waitcnt lgkmcnt(3)\n"
"v_add_u32 v196, v196, v9\n"
"v_cvt_pk_bf16_f32 v165, v58, v59\n"
"v_cvt_pk_bf16_f32 v166, v60, v61\n"
"v_mfma_f32_32x32x16_bf16 v[32:47], a[48:51], v[80:83], v[32:47]\n"
"s_mov_b32 m0, s91\n"
"s_mov_b64 exec, s[94:95]\n"
"buffer_load_dword v196, s[44:47], 0 offen sc1 lds\n"
"s_mov_b64 exec, -1\n"
"v_cvt_pk_bf16_f32 v167, v62, v63\n"
"v_mfma_f32_32x32x16_bf16 v[16:31], a[164:167], v[84:87], v[16:31]\n"
"ds_read_b128 v[100:103], v1 offset:17408\n"
"v_cvt_pk_bf16_f32 v168, v64, v65\n"
"v_cvt_pk_bf16_f32 v169, v66, v67\n"
"v_cvt_pk_bf16_f32 v170, v68, v69\n"
"v_cvt_pk_bf16_f32 v171, v70, v71\n"
"v_mfma_f32_32x32x16_bf16 v[32:47], a[52:55], v[84:87], v[32:47]\n"
"v_cvt_pk_bf16_f32 v172, v72, v73\n"
"v_cvt_pk_bf16_f32 v173, v74, v75\n"
"v_cvt_pk_bf16_f32 v174, v76, v77\n"
"v_cvt_pk_bf16_f32 v175, v78, v79\n"
"v_permlane32_swap_b32 v160, v162\n"
"s_waitcnt lgkmcnt(3)\n"
"v_mfma_f32_32x32x16_bf16 v[16:31], a[168:171], v[88:91], v[16:31]\n"
"ds_read_b128 v[80:83], v1 offset:18432\n"
"v_permlane32_swap_b32 v161, v163\n"
"v_permlane32_swap_b32 v164, v166\n"
"v_permlane32_swap_b32 v165, v167\n"
"v_mfma_f32_32x32x16_bf16 v[32:47], a[56:59], v[88:91], v[32:47]\n"
"v_permlane32_swap_b32 v168, v170\n"
"v_permlane32_swap_b32 v169, v171\n"
"v_permlane32_swap_b32 v172, v174\n"
"v_permlane32_swap_b32 v173, v175\n"
"s_waitcnt lgkmcnt(3)\n"
"v_mfma_f32_32x32x16_bf16 v[16:31], a[172:175], v[92:95], v[16:31]\n"
"ds_read_b128 v[84:87], v1 offset:19456\n"
"buffer_store_dwordx4 v[160:163], v11, s[40:43], s66 offen sc1\n"
"v_mfma_f32_32x32x16_bf16 v[32:47], a[60:63], v[92:95], v[32:47]\n"
"buffer_store_dwordx4 v[164:167], v12, s[40:43], s66 offen sc1\n"
"s_waitcnt lgkmcnt(3)\n"
"v_mfma_f32_32x32x16_bf16 v[16:31], a[176:179], v[96:99], v[16:31]\n"
"ds_read_b128 v[88:91], v1 offset:20480\n"
"buffer_store_dwordx4 v[168:171], v13, s[40:43], s66 offen sc1\n"
"v_mfma_f32_32x32x16_bf16 v[32:47], a[64:67], v[96:99], v[32:47]\n"
"buffer_store_dwordx4 v[172:175], v14, s[40:43], s66 offen sc1\n"
"s_waitcnt lgkmcnt(3)\n"
"v_mfma_f32_32x32x16_bf16 v[16:31], a[180:183], v[100:103], v[16:31]\n"
"ds_read_b128 v[92:95], v1 offset:21504\n"
"v_mfma_f32_32x32x16_bf16 v[32:47], a[68:71], v[100:103], v[32:47]\n"
"s_waitcnt lgkmcnt(3)\n"
"v_mfma_f32_32x32x16_bf16 v[16:31], a[184:187], v[80:83], v[16:31]\n"
"ds_read_b128 v[96:99], v1 offset:22528\n"
"v_mfma_f32_32x32x16_bf16 v[32:47], a[72:75], v[80:83], v[32:47]\n"
"s_waitcnt lgkmcnt(3)\n"
"v_mfma_f32_32x32x16_bf16 v[16:31], a[188:191], v[84:87], v[16:31]\n"
"ds_read_b128 v[100:103], v1 offset:23552\n"
"v_mfma_f32_32x32x16_bf16 v[32:47], a[76:79], v[84:87], v[32:47]\n"
"s_waitcnt lgkmcnt(3)\n"
"v_mfma_f32_32x32x16_bf16 v[16:31], a[192:195], v[88:91], v[16:31]\n"
"ds_read_b128 v[80:83], v1 offset:24576\n"
"v_mfma_f32_32x32x16_bf16 v[32:47], a[80:83], v[88:91], v[32:47]\n"
"s_waitcnt lgkmcnt(3)\n"
"v_mfma_f32_32x32x16_bf16 v[16:31], a[196:199], v[92:95], v[16:31]\n"
"ds_read_b128 v[84:87], v1 offset:25600\n"
"v_mfma_f32_32x32x16_bf16 v[32:47], a[84:87], v[92:95], v[32:47]\n"
"s_waitcnt lgkmcnt(3)\n"
"v_mfma_f32_32x32x16_bf16 v[16:31], a[200:203], v[96:99], v[16:31]\n"
"ds_read_b128 v[88:91], v1 offset:26624\n"
"v_mfma_f32_32x32x16_bf16 v[32:47], a[88:91], v[96:99], v[32:47]\n"
"s_waitcnt lgkmcnt(3)\n"
"v_mfma_f32_32x32x16_bf16 v[16:31], a[204:207], v[100:103], v[16:31]\n"
"ds_read_b128 v[92:95], v1 offset:27648\n"
"v_mfma_f32_32x32x16_bf16 v[32:47], a[92:95], v[100:103], v[32:47]\n"
"s_waitcnt lgkmcnt(3)\n"
"v_mfma_f32_32x32x16_bf16 v[16:31], a[208:211], v[80:83], v[16:31]\n"
"ds_read_b128 v[96:99], v1 offset:28672\n"
"v_mfma_f32_32x32x16_bf16 v[32:47], a[96:99], v[80:83], v[32:47]\n"
"s_waitcnt lgkmcnt(3)\n"
"v_mfma_f32_32x32x16_bf16 v[16:31], a[212:215], v[84:87], v[16:31]\n"
"ds_read_b128 v[100:103], v1 offset:29696\n"
"v_mfma_f32_32x32x16_bf16 v[32:47], a[100:103], v[84:87], v[32:47]\n"
"s_waitcnt lgkmcnt(3)\n"
"v_mfma_f32_32x32x16_bf16 v[16:31], a[216:219], v[88:91], v[16:31]\n"
"ds_read_b128 v[80:83], v1 offset:30720\n"
"v_mfma_f32_32x32x16_bf16 v[32:47], a[104:107], v[88:91], v[32:47]\n"
"s_waitcnt lgkmcnt(3)\n"
"v_mfma_f32_32x32x16_bf16 v[16:31], a[220:223], v[92:95], v[16:31]\n"
"ds_read_b128 v[84:87], v1 offset:31744\n"
"v_mfma_f32_32x32x16_bf16 v[32:47], a[108:111], v[92:95], v[32:47]\n"
"s_waitcnt lgkmcnt(3)\n"
"v_mfma_f32_32x32x16_bf16 v[16:31], a[224:227], v[96:99], v[16:31]\n"
"ds_read_b128 v[88:91], v1 offset:32768\n"
"v_mfma_f32_32x32x16_bf16 v[32:47], a[112:115], v[96:99], v[32:47]\n"
"s_waitcnt lgkmcnt(3)\n"
"v_mfma_f32_32x32x16_bf16 v[16:31], a[228:231], v[100:103], v[16:31]\n"
"ds_read_b128 v[92:95], v1 offset:33792\n"
"v_mfma_f32_32x32x16_bf16 v[32:47], a[116:119], v[100:103], v[32:47]\n"
"s_waitcnt lgkmcnt(3)\n"
"v_mfma_f32_32x32x16_bf16 v[16:31], a[232:235], v[80:83], v[16:31]\n"
"ds_read_b128 v[96:99], v1 offset:34816\n"
"v_mfma_f32_32x32x16_bf16 v[32:47], a[120:123], v[80:83], v[32:47]\n"
"s_waitcnt lgkmcnt(3)\n"
"v_mfma_f32_32x32x16_bf16 v[16:31], a[236:239], v[84:87], v[16:31]\n"
"ds_read_b128 v[100:103], v1 offset:35840\n"
"v_mfma_f32_32x32x16_bf16 v[32:47], a[124:127], v[84:87], v[32:47]\n"
"s_waitcnt lgkmcnt(3)\n"
"v_mfma_f32_32x32x16_bf16 v[16:31], a[240:243], v[88:91], v[16:31]\n"
"v_mfma_f32_32x32x16_bf16 v[32:47], a[128:131], v[88:91], v[32:47]\n"
"s_waitcnt vmcnt(4) lgkmcnt(0)\n"
"s_barrier\n"
"v_mfma_f32_32x32x16_bf16 v[16:31], a[244:247], v[92:95], v[16:31]\n"
"s_sub_u32 s77, 36864, s77\n"
"v_add_u32 v1, s77, v0\n"
"ds_read_b128 v[80:83], v1\n"
"ds_read_b128 v[84:87], v1 offset:1024\n"
"v_mfma_f32_32x32x16_bf16 v[32:47], a[132:135], v[92:95], v[32:47]\n"
"buffer_store_dword v4, v197, s[44:47], 0 offen sc1\n"
"v_mfma_f32_32x32x16_bf16 v[16:31], a[248:251], v[96:99], v[16:31]\n"
"ds_read_b128 v[88:91], v1 offset:2048\n"
"v_mfma_f32_32x32x16_bf16 v[32:47], a[136:139], v[96:99], v[32:47]\n"
"s_sub_u32 s60, s57, 1\n"
"s_mov_b32 s86, s62\n"
"s_add_u32 s57, s57, 1\n"
"s_cmp_eq_u32 s87, 0\n"
"v_mfma_f32_32x32x16_bf16 v[16:31], a[252:255], v[100:103], v[16:31]\n"
"ds_read_b128 v[92:95], v1 offset:3072\n"
"v_mfma_f32_32x32x16_bf16 v[32:47], a[140:143], v[100:103], v[32:47]\n"
"s_cbranch_scc0 LPApack36_%=\n"
"s_mov_b32 s99, 0\n"
"s_sub_u32 s57, s57, 1\n"
"s_branch Le1pack36_%=\n"
"LfxApack36_%=:\n"
"s_mov_b32 s99, 1\n"
"s_sub_u32 s57, s57, 1\n"
"Le1pack36_%=:\n"
"s_mov_b32 s60, -1\n"
"s_mov_b32 s61, -1\n"
"s_cmp_eq_u32 s59, 0\n"
"s_cbranch_scc1 Le2pack36_%=\n"
"s_sub_u32 s60, s57, 1\n"
"Le2pack36_%=:\n"
"s_cmp_lt_i32 s62, 0\n"
"s_cbranch_scc1 Le3pack36_%=\n"
"s_and_b32 s72, s57, 1\n"
"s_lshl_b32 s72, s72, 8\n"
"s_add_u32 s72, s72, 86016\n"
"s_sub_u32 s67, s72, 0\n"
"v_lshrrev_b32 v192, 2, v0\n"
"v_add_u32 v192, s67, v192\n"
"ds_read_b32 v193, v192\n"
"s_waitcnt lgkmcnt(0)\n"
"v_cmp_eq_u32_e64 s[80:81], s97, v193\n"
"s_nop 1\n"
"s_sub_u32 s68, s48, s62\n"
"s_min_u32 s68, s68, 8\n"
"s_mov_b32 s70, 0\n"
"Levepack36_%=:\n"
"s_cmp_ge_u32 s70, s68\n"
"s_cbranch_scc1 Lexepack36_%=\n"
"s_and_b32 s69, s80, 31\n"
"s_cmp_eq_u32 s69, 31\n"
"s_cbranch_scc0 Lexepack36_%=\n"
"s_lshr_b64 s[80:81], s[80:81], 5\n"
"s_add_u32 s70, s70, 1\n"
"s_branch Levepack36_%=\n"
"Lexepack36_%=:\n"
"s_add_u32 s58, s58, s70\n"
"s_waitcnt lgkmcnt(0)\n"
"Le3pack36_%=:\n"
"s_mov_b32 s59, 0\n"
"s_add_u32 s68, s57, 2\n"
"s_cmp_lt_u32 s68, s58\n"
"s_cbranch_scc0 Le4pack36_%=\n"
"s_cmp_ge_u32 s57, 3\n"
"s_cbranch_scc0 Le4pack36_%=\n"
"s_add_u32 s59, s99, 1\n"
"s_mov_b32 s86, -1\n"
"s_add_u32 s57, s57, 1\n"
"s_branch Ltoppack36_%=\n"
"Le4pack36_%=:\n"
"s_mov_b32 s61, s57\n"
"s_cmp_eq_u32 s99, 0\n"
"s_cbranch_scc0 Le5pack36_%=\n"
"s_nop 3\n"
"v_readlane_b32 s67, v6, s57\n"
"s_nop 0\n"
"s_mul_i32 s67, s67, s52\n"
"s_add_u32 s66, s67, s51\n"
"v_cvt_pk_bf16_f32 v160, v16, v17\n"
"v_cvt_pk_bf16_f32 v161, v18, v19\n"
"v_cvt_pk_bf16_f32 v162, v20, v21\n"
"v_cvt_pk_bf16_f32 v163, v22, v23\n"
"v_cvt_pk_bf16_f32 v164, v24, v25\n"
"v_cvt_pk_bf16_f32 v165, v26, v27\n"
"v_cvt_pk_bf16_f32 v166, v28, v29\n"
"v_cvt_pk_bf16_f32 v167, v30, v31\n"
"v_cvt_pk_bf16_f32 v168, v32, v33\n"
"v_cvt_pk_bf16_f32 v169, v34, v35\n"
"v_cvt_pk_bf16_f32 v170, v36, v37\n"
"v_cvt_pk_bf16_f32 v171, v38, v39\n"
"v_cvt_pk_bf16_f32 v172, v40, v41\n"
"v_cvt_pk_bf16_f32 v173, v42, v43\n"
"v_cvt_pk_bf16_f32 v174, v44, v45\n"
"v_cvt_pk_bf16_f32 v175, v46, v47\n"
"v_permlane32_swap_b32 v160, v162\n"
"v_permlane32_swap_b32 v161, v163\n"
"v_permlane32_swap_b32 v164, v166\n"
"v_permlane32_swap_b32 v165, v167\n"
"v_permlane32_swap_b32 v168, v170\n"
"v_permlane32_swap_b32 v169, v171\n"
"v_permlane32_swap_b32 v172, v174\n"
"v_permlane32_swap_b32 v173, v175\n"
"buffer_store_dwordx4 v[160:163], v11, s[40:43], s66 offen sc1\n"
"buffer_store_dwordx4 v[164:167], v12, s[40:43], s66 offen sc1\n"
"buffer_store_dwordx4 v[168:171], v13, s[40:43], s66 offen sc1\n"
"buffer_store_dwordx4 v[172:175], v14, s[40:43], s66 offen sc1\n"
"s_add_u32 s57, s57, 1\n"
"s_branch Ltoppack36_%=\n"
"Le5pack36_%=:\n"
"s_nop 3\n"
"v_readlane_b32 s67, v6, s57\n"
"s_nop 0\n"
"s_mul_i32 s67, s67, s52\n"
"s_add_u32 s66, s67, s51\n"
"v_cvt_pk_bf16_f32 v160, v48, v49\n"
"v_cvt_pk_bf16_f32 v161, v50, v51\n"
"v_cvt_pk_bf16_f32 v162, v52, v53\n"
"v_cvt_pk_bf16_f32 v163, v54, v55\n"
"v_cvt_pk_bf16_f32 v164, v56, v57\n"
"v_cvt_pk_bf16_f32 v165, v58, v59\n"
"v_cvt_pk_bf16_f32 v166, v60, v61\n"
"v_cvt_pk_bf16_f32 v167, v62, v63\n"
"v_cvt_pk_bf16_f32 v168, v64, v65\n"
"v_cvt_pk_bf16_f32 v169, v66, v67\n"
"v_cvt_pk_bf16_f32 v170, v68, v69\n"
"v_cvt_pk_bf16_f32 v171, v70, v71\n"
"v_cvt_pk_bf16_f32 v172, v72, v73\n"
"v_cvt_pk_bf16_f32 v173, v74, v75\n"
"v_cvt_pk_bf16_f32 v174, v76, v77\n"
"v_cvt_pk_bf16_f32 v175, v78, v79\n"
"v_permlane32_swap_b32 v160, v162\n"
"v_permlane32_swap_b32 v161, v163\n"
"v_permlane32_swap_b32 v164, v166\n"
"v_permlane32_swap_b32 v165, v167\n"
"v_permlane32_swap_b32 v168, v170\n"
"v_permlane32_swap_b32 v169, v171\n"
"v_permlane32_swap_b32 v172, v174\n"
"v_permlane32_swap_b32 v173, v175\n"
"buffer_store_dwordx4 v[160:163], v11, s[40:43], s66 offen sc1\n"
"buffer_store_dwordx4 v[164:167], v12, s[40:43], s66 offen sc1\n"
"buffer_store_dwordx4 v[168:171], v13, s[40:43], s66 offen sc1\n"
"buffer_store_dwordx4 v[172:175], v14, s[40:43], s66 offen sc1\n"
"s_add_u32 s57, s57, 1\n"
"s_branch Ltoppack36_%=\n"
"Lnonpack36_%=:\n"
"s_max_i32 s68, s60, s61\n"
"s_cmp_ge_i32 s68, 0\n"
"s_cselect_b32 s63, 1, 0\n"
"s_waitcnt lgkmcnt(3)\n"
"v_mfma_f32_32x32x16_bf16 v[16:31], v[128:131], v[80:83], 0\n"
"ds_read_b128 v[96:99], v1 offset:4096\n"
"v_mfma_f32_32x32x16_bf16 v[32:47], a[0:3], v[80:83], 0\n"
"s_add_u32 s90, s57, 1\n"
"s_cmp_lt_u32 s90, s48\n"
"s_cselect_b32 s62, s90, -1\n"
"s_lshl_b32 s90, s90, 2\n"
"s_add_u32 s90, s90, 86528\n"
"v_add_u32 v196, s90, v8\n"
"s_waitcnt lgkmcnt(3)\n"
"v_mfma_f32_32x32x16_bf16 v[16:31], v[132:135], v[84:87], v[16:31]\n"
"ds_read_b128 v[100:103], v1 offset:5120\n"
"ds_read_b32 v196, v196\n"
"s_cmp_ge_i32 s62, 0\n"
"s_cselect_b64 s[94:95], s[78:79], 0\n"
"v_mfma_f32_32x32x16_bf16 v[32:47], a[4:7], v[84:87], v[32:47]\n"
"s_and_b32 s94, s94, 1023\n"
"s_mov_b32 s95, 0\n"
"s_waitcnt lgkmcnt(4)\n"
"v_mfma_f32_32x32x16_bf16 v[16:31], v[136:139], v[88:91], v[16:31]\n"
"ds_read_b128 v[80:83], v1 offset:6144\n"
"v_mfma_f32_32x32x16_bf16 v[32:47], a[8:11], v[88:91], v[32:47]\n"
"s_waitcnt lgkmcnt(4)\n"
"v_mfma_f32_32x32x16_bf16 v[16:31], v[140:143], v[92:95], v[16:31]\n"
"ds_read_b128 v[84:87], v1 offset:7168\n"
"s_waitcnt lgkmcnt(2)\n"
"v_add_u32 v196, v196, v9\n"
"v_cndmask_b32_e64 v196, v15, v196, s[94:95]\n"
"v_mfma_f32_32x32x16_bf16 v[32:47], a[12:15], v[92:95], v[32:47]\n"
"s_mov_b32 s91, 87296\n"
"s_cmp_eq_u32 %0, 2\n"
"s_cselect_b32 m0, s91, 87040\n"
"s_nop 0\n"
"buffer_load_dword v196, s[44:47], 0 offen sc1 lds\n"
"v_mfma_f32_32x32x16_bf16 v[16:31], v[144:147], v[96:99], v[16:31]\n"
"ds_read_b128 v[88:91], v1 offset:8192\n"
"v_mfma_f32_32x32x16_bf16 v[32:47], a[16:19], v[96:99], v[32:47]\n"
"v_mfma_f32_32x32x16_bf16 v[16:31], v[148:151], v[100:103], v[16:31]\n"
"ds_read_b128 v[92:95], v1 offset:9216\n"
"v_mfma_f32_32x32x16_bf16 v[32:47], a[20:23], v[100:103], v[32:47]\n"
"s_waitcnt lgkmcnt(3)\n"
"v_mfma_f32_32x32x16_bf16 v[16:31], v[152:155], v[80:83], v[16:31]\n"
"ds_read_b128 v[96:99], v1 offset:10240\n"
"v_mfma_f32_32x32x16_bf16 v[32:47], a[24:27], v[80:83], v[32:47]\n"
"s_waitcnt lgkmcnt(3)\n"
"v_mfma_f32_32x32x16_bf16 v[16:31], v[156:159], v[84:87], v[16:31]\n"
"ds_read_b128 v[100:103], v1 offset:11264\n"
"v_mfma_f32_32x32x16_bf16 v[32:47], a[28:31], v[84:87], v[32:47]\n"
"s_waitcnt lgkmcnt(3)\n"
"v_mfma_f32_32x32x16_bf16 v[16:31], a[144:147], v[88:91], v[16:31]\n"
"ds_read_b128 v[80:83], v1 offset:12288\n"
"v_mfma_f32_32x32x16_bf16 v[32:47], a[32:35], v[88:91], v[32:47]\n"
"s_waitcnt lgkmcnt(3)\n"
"v_mfma_f32_32x32x16_bf16 v[16:31], a[148:151], v[92:95], v[16:31]\n"
"ds_read_b128 v[84:87], v1 offset:13312\n"
"v_mfma_f32_32x32x16_bf16 v[32:47], a[36:39], v[92:95], v[32:47]\n"
"s_waitcnt lgkmcnt(3)\n"
"v_mfma_f32_32x32x16_bf16 v[16:31], a[152:155], v[96:99], v[16:31]\n"
"ds_read_b128 v[88:91], v1 offset:14336\n"
"v_mfma_f32_32x32x16_bf16 v[32:47], a[40:43], v[96:99], v[32:47]\n"
"s_cmp_eq_u32 s63, 0\n"
"s_cbranch_scc1 LnmLpack36_%=\n"
"s_waitcnt vmcnt(1)\n"
"s_barrier\n"
"v_readlane_b32 s93, v6, s60\n"
"s_nop 0\n"
"s_mul_i32 s93, s93, s54\n"
"s_add_u32 s93, s93, s53\n"
"s_cmp_lt_i32 s60, 0\n"
"s_cselect_b32 s93, 0x7ffffff0, s93\n"
"v_add_u32 v197, s93, v3\n"
"buffer_store_dword v4, v197, s[44:47], 0 offen sc1\n"
"v_readlane_b32 s93, v6, s61\n"
"s_nop 0\n"
"s_mul_i32 s93, s93, s54\n"
"s_add_u32 s93, s93, s53\n"
"s_cmp_lt_i32 s61, 0\n"
"s_cselect_b32 s93, 0x7ffffff0, s93\n"
"v_add_u32 v199, s93, v3\n"
"buffer_store_dword v4, v199, s[44:47], 0 offen sc1\n"
"LnmLpack36_%=:\n"
"s_waitcnt lgkmcnt(3)\n"
"v_mfma_f32_32x32x16_bf16 v[16:31], a[156:159], v[100:103], v[16:31]\n"
"ds_read_b128 v[92:95], v1 offset:15360\n"
"v_mfma_f32_32x32x16_bf16 v[32:47], a[44:47], v[100:103], v[32:47]\n"
"s_waitcnt lgkmcnt(3)\n"
"v_mfma_f32_32x32x16_bf16 v[16:31], a[160:163], v[80:83], v[16:31]\n"
"ds_read_b128 v[96:99], v1 offset:16384\n"
"v_mfma_f32_32x32x16_bf16 v[32:47], a[48:51], v[80:83], v[32:47]\n"
"s_waitcnt lgkmcnt(3)\n"
"v_mfma_f32_32x32x16_bf16 v[16:31], a[164:167], v[84:87], v[16:31]\n"
"ds_read_b128 v[100:103], v1 offset:17408\n"
"v_mfma_f32_32x32x16_bf16 v[32:47], a[52:55], v[84:87], v[32:47]\n"
"s_waitcnt lgkmcnt(3)\n"
"v_mfma_f32_32x32x16_bf16 v[16:31], a[168:171], v[88:91], v[16:31]\n"
"ds_read_b128 v[80:83], v1 offset:18432\n"
"v_mfma_f32_32x32x16_bf16 v[32:47], a[56:59], v[88:91], v[32:47]\n"
"s_waitcnt lgkmcnt(3)\n"
"v_mfma_f32_32x32x16_bf16 v[16:31], a[172:175], v[92:95], v[16:31]\n"
"ds_read_b128 v[84:87], v1 offset:19456\n"
"v_mfma_f32_32x32x16_bf16 v[32:47], a[60:63], v[92:95], v[32:47]\n"
"s_mov_b32 s91, 87552\n"
"s_cmp_eq_u32 %0, 2\n"
"s_cselect_b32 m0, s91, 87040\n"
"s_nop 0\n"
"buffer_load_dword v196, s[44:47], 0 offen sc1 lds\n"
"s_waitcnt lgkmcnt(3)\n"
"v_mfma_f32_32x32x16_bf16 v[16:31], a[176:179], v[96:99], v[16:31]\n"
"ds_read_b128 v[88:91], v1 offset:20480\n"
"v_mfma_f32_32x32x16_bf16 v[32:47], a[64:67], v[96:99], v[32:47]\n"
"s_waitcnt lgkmcnt(3)\n"
"v_mfma_f32_32x32x16_bf16 v[16:31], a[180:183], v[100:103], v[16:31]\n"
"ds_read_b128 v[92:95], v1 offset:21504\n"
"v_mfma_f32_32x32x16_bf16 v[32:47], a[68:71], v[100:103], v[32:47]\n"
"s_waitcnt lgkmcnt(3)\n"
"v_mfma_f32_32x32x16_bf16 v[16:31], a[184:187], v[80:83], v[16:31]\n"
"ds_read_b128 v[96:99], v1 offset:22528\n"
"v_mfma_f32_32x32x16_bf16 v[32:47], a[72:75], v[80:83], v[32:47]\n"
"s_waitcnt lgkmcnt(3)\n"
"v_mfma_f32_32x32x16_bf16 v[16:31], a[188:191], v[84:87], v[16:31]\n"
"ds_read_b128 v[100:103], v1 offset:23552\n"
"v_mfma_f32_32x32x16_bf16 v[32:47], a[76:79], v[84:87], v[32:47]\n"
"s_waitcnt lgkmcnt(3)\n"
"v_mfma_f32_32x32x16_bf16 v[16:31], a[192:195], v[88:91], v[16:31]\n"
"ds_read_b128 v[80:83], v1 offset:24576\n"
"v_mfma_f32_32x32x16_bf16 v[32:47], a[80:83], v[88:91], v[32:47]\n"
"s_waitcnt lgkmcnt(3)\n"
"v_mfma_f32_32x32x16_bf16 v[16:31], a[196:199], v[92:95], v[16:31]\n"
"ds_read_b128 v[84:87], v1 offset:25600\n"
"v_mfma_f32_32x32x16_bf16 v[32:47], a[84:87], v[92:95], v[32:47]\n"
"s_waitcnt lgkmcnt(3)\n"
"v_mfma_f32_32x32x16_bf16 v[16:31], a[200:203], v[96:99], v[16:31]\n"
"ds_read_b128 v[88:91], v1 offset:26624\n"
"v_mfma_f32_32x32x16_bf16 v[32:47], a[88:91], v[96:99], v[32:47]\n"
"s_waitcnt lgkmcnt(3)\n"
"v_mfma_f32_32x32x16_bf16 v[16:31], a[204:207], v[100:103], v[16:31]\n"
"ds_read_b128 v[92:95], v1 offset:27648\n"
"v_mfma_f32_32x32x16_bf16 v[32:47], a[92:95], v[100:103], v[32:47]\n"
"s_cmp_eq_u32 s63, 0\n"
"s_cbranch_scc1 LpwLpack36_%=\n"
"s_waitcnt vmcnt(3)\n"
"s_branch LpxLpack36_%=\n"
"LpwLpack36_%=:\n"
"s_waitcnt vmcnt(1)\n"
"LpxLpack36_%=:\n"
"s_barrier\n"
"s_waitcnt lgkmcnt(3)\n"
"v_mfma_f32_32x32x16_bf16 v[16:31], a[208:211], v[80:83], v[16:31]\n"
"ds_read_b128 v[96:99], v1 offset:28672\n"
"ds_read_b32 v195, v212 offset:1280\n"
"s_add_u32 s92, s57, 1\n"
"v_readlane_b32 s92, v6, s92\n"
"s_sub_u32 s64, 36864, s77\n"
"v_mfma_f32_32x32x16_bf16 v[32:47], a[96:99], v[80:83], v[32:47]\n"
"s_add_u32 s64, s64, s55\n"
"s_mul_i32 s92, s92, s50\n"
"s_add_u32 s65, s92, s49\n"
"s_waitcnt lgkmcnt(4)\n"
"v_mfma_f32_32x32x16_bf16 v[16:31], a[212:215], v[84:87], v[16:31]\n"
"ds_read_b128 v[100:103], v1 offset:29696\n"
"v_mfma_f32_32x32x16_bf16 v[32:47], a[100:103], v[84:87], v[32:47]\n"
"s_waitcnt lgkmcnt(1)\n"
"v_cmp_ne_u32_e64 s[80:81], s97, v195\n"
"v_mfma_f32_32x32x16_bf16 v[16:31], a[216:219], v[88:91], v[16:31]\n"
"ds_read_b128 v[80:83], v1 offset:30720\n"
"s_and_b32 s80, s80, 1023\n"
"s_mov_b32 s81, 0\n"
"s_ff1_i32_b64 s90, s[80:81]\n"
"s_cmp_lt_i32 s90, 0\n"
"s_cselect_b32 s90, 10, s90\n"
"v_mfma_f32_32x32x16_bf16 v[32:47], a[104:107], v[88:91], v[32:47]\n"
"s_mul_i32 s90, s90, 13108\n"
"s_lshr_b32 s90, s90, 16\n"
"s_sub_u32 s91, s48, s62\n"
"s_min_u32 s90, s90, s91\n"
"s_add_u32 s90, s90, s62\n"
"v_mfma_f32_32x32x16_bf16 v[16:31], a[220:223], v[92:95], v[16:31]\n"
"ds_read_b128 v[84:87], v1 offset:31744\n"
"s_cmp_lt_i32 s62, 0\n"
"s_cselect_b32 s58, s58, s90\n"
"s_add_u32 s90, s57, 1\n"
"v_mfma_f32_32x32x16_bf16 v[32:47], a[108:111], v[92:95], v[32:47]\n"
"s_cmp_lt_u32 s90, s58\n"
"s_cselect_b64 s[94:95], -1, 0\n"
"s_cselect_b32 s87, 1, 0\n"
"v_cndmask_b32_e64 v198, v15, v2, s[94:95]\n"
"v_mfma_f32_32x32x16_bf16 v[16:31], a[224:227], v[96:99], v[16:31]\n"
"ds_read_b128 v[88:91], v1 offset:32768\n"
"s_add_u32 m0, s64, 0\n"
"s_add_u32 s92, s65, 0\n"
"buffer_load_dwordx4 v198, s[36:39], s92 offen sc1 lds\n"
"v_mfma_f32_32x32x16_bf16 v[32:47], a[112:115], v[96:99], v[32:47]\n"
"s_add_u32 m0, s64, 4096\n"
"s_add_u32 s92, s65, 4096\n"
"buffer_load_dwordx4 v198, s[36:39], s92 offen sc1 lds\n"
"s_waitcnt lgkmcnt(3)\n"
"v_mfma_f32_32x32x16_bf16 v[16:31], a[228:231], v[100:103], v[16:31]\n"
"ds_read_b128 v[92:95], v1 offset:33792\n"
"s_add_u32 m0, s64, 8192\n"
"s_add_u32 s92, s65, 8192\n"
"buffer_load_dwordx4 v198, s[36:39], s92 offen sc1 lds\n"
"v_mfma_f32_32x32x16_bf16 v[32:47], a[116:119], v[100:103], v[32:47]\n"
"s_add_u32 m0, s64, 12288\n"
"s_add_u32 s92, s65, 12288\n"
"buffer_load_dwordx4 v198, s[36:39], s92 offen sc1 lds\n"
"s_waitcnt lgkmcnt(3)\n"
"v_mfma_f32_32x32x16_bf16 v[16:31], a[232:235], v[80:83], v[16:31]\n"
"ds_read_b128 v[96:99], v1 offset:34816\n"
"s_add_u32 m0, s64, 16384\n"
"s_add_u32 s92, s65, 16384\n"
"buffer_load_dwordx4 v198, s[36:39], s92 offen sc1 lds\n"
"v_mfma_f32_32x32x16_bf16 v[32:47], a[120:123], v[80:83], v[32:47]\n"
"s_add_u32 m0, s64, 20480\n"
"s_add_u32 s92, s65, 20480\n"
"buffer_load_dwordx4 v198, s[36:39], s92 offen sc1 lds\n"
"s_waitcnt lgkmcnt(3)\n"
"v_mfma_f32_32x32x16_bf16 v[16:31], a[236:239], v[84:87], v[16:31]\n"
"ds_read_b128 v[100:103], v1 offset:35840\n"
"s_add_u32 m0, s64, 24576\n"
"s_add_u32 s92, s65, 24576\n"
"buffer_load_dwordx4 v198, s[36:39], s92 offen sc1 lds\n"
"v_mfma_f32_32x32x16_bf16 v[32:47], a[124:127], v[84:87], v[32:47]\n"
"s_add_u32 m0, s64, 28672\n"
"s_add_u32 s92, s65, 28672\n"
"buffer_load_dwordx4 v198, s[36:39], s92 offen sc1 lds\n"
"s_waitcnt lgkmcnt(3)\n"
"v_mfma_f32_32x32x16_bf16 v[16:31], a[240:243], v[88:91], v[16:31]\n"
"s_add_u32 m0, s64, 32768\n"
"s_add_u32 s92, s65, 32768\n"
"buffer_load_dwordx4 v198, s[36:39], s92 offen sc1 lds\n"
"v_mfma_f32_32x32x16_bf16 v[32:47], a[128:131], v[88:91], v[32:47]\n"
"s_waitcnt lgkmcnt(2)\n"
"v_mfma_f32_32x32x16_bf16 v[16:31], a[244:247], v[92:95], v[16:31]\n"
"v_mfma_f32_32x32x16_bf16 v[32:47], a[132:135], v[92:95], v[32:47]\n"
"s_waitcnt lgkmcnt(1)\n"
"v_mfma_f32_32x32x16_bf16 v[16:31], a[248:251], v[96:99], v[16:31]\n"
"v_mfma_f32_32x32x16_bf16 v[32:47], a[136:139], v[96:99], v[32:47]\n"
"s_waitcnt lgkmcnt(0)\n"
"v_mfma_f32_32x32x16_bf16 v[16:31], a[252:255], v[100:103], v[16:31]\n"
"v_mfma_f32_32x32x16_bf16 v[32:47], a[140:143], v[100:103], v[32:47]\n"
"s_mov_b32 s60, -1\n"
"s_mov_b32 s61, -1\n"
"s_cmp_eq_u32 s87, 0\n"
"s_cbranch_scc0 Lwaypack36_%=\n"
"s_cmp_lt_i32 s62, 0\n"
"s_cbranch_scc1 Lnoldpack36_%=\n"
"s_waitcnt vmcnt(9) lgkmcnt(0)\n"
"s_barrier\n"
"s_sub_u32 s67, 87552, 0\n"
"v_lshrrev_b32 v192, 2, v0\n"
"v_add_u32 v192, s67, v192\n"
"ds_read_b32 v193, v192\n"
"s_waitcnt lgkmcnt(0)\n"
"v_cmp_eq_u32_e64 s[80:81], s97, v193\n"
"s_nop 1\n"
"s_sub_u32 s68, s48, s62\n"
"s_min_u32 s68, s68, 2\n"
"s_mov_b32 s70, 0\n"
"Levs2pack36_%=:\n"
"s_cmp_ge_u32 s70, s68\n"
"s_cbranch_scc1 Lexs2pack36_%=\n"
"s_and_b32 s69, s80, 31\n"
"s_cmp_eq_u32 s69, 31\n"
"s_cbranch_scc0 Lexs2pack36_%=\n"
"s_lshr_b64 s[80:81], s[80:81], 5\n"
"s_add_u32 s70, s70, 1\n"
"s_branch Levs2pack36_%=\n"
"Lexs2pack36_%=:\n"
"s_add_u32 s58, s62, s70\n"
"s_cmp_lt_u32 s62, s58\n"
"s_cbranch_scc0 Lnoldpack36_%=\n"
"v_readlane_b32 s67, v6, s62\n"
"s_nop 0\n"
"s_mul_i32 s67, s67, s50\n"
"s_add_u32 s65, s67, s49\n"
"s_sub_u32 s64, 36864, s77\n"
"s_lshl_b32 s68, %0, 10\n"
"s_add_u32 s64, s64, s68\n"
"s_add_u32 m0, s64, 0\n"
"s_add_u32 s70, s65, 0\n"
"buffer_load_dwordx4 v2, s[36:39], s70 offen sc1 lds\n"
"s_add_u32 m0, s64, 4096\n"
"s_add_u32 s70, s65, 4096\n"
"buffer_load_dwordx4 v2, s[36:39], s70 offen sc1 lds\n"
"s_add_u32 m0, s64, 8192\n"
"s_add_u32 s70, s65, 8192\n"
"buffer_load_dwordx4 v2, s[36:39], s70 offen sc1 lds\n"
"s_add_u32 m0, s64, 12288\n"
"s_add_u32 s70, s65, 12288\n"
"buffer_load_dwordx4 v2, s[36:39], s70 offen sc1 lds\n"
"s_add_u32 m0, s64, 16384\n"
"s_add_u32 s70, s65, 16384\n"
"buffer_load_dwordx4 v2, s[36:39], s70 offen sc1 lds\n"
"s_add_u32 m0, s64, 20480\n"
"s_add_u32 s70, s65, 20480\n"
"buffer_load_dwordx4 v2, s[36:39], s70 offen sc1 lds\n"
"s_add_u32 m0, s64, 24576\n"
"s_add_u32 s70, s65, 24576\n"
"buffer_load_dwordx4 v2, s[36:39], s70 offen sc1 lds\n"
"s_add_u32 m0, s64, 28672\n"
"s_add_u32 s70, s65, 28672\n"
"buffer_load_dwordx4 v2, s[36:39], s70 offen sc1 lds\n"
"s_add_u32 m0, s64, 32768\n"
"s_add_u32 s70, s65, 32768\n"
"buffer_load_dwordx4 v2, s[36:39], s70 offen sc1 lds\n"
"Lwaypack36_%=:\n"
"s_waitcnt lgkmcnt(0)\n"
"s_nop 7\n"
"v_readlane_b32 s67, v6, s57\n"
"s_nop 0\n"
"s_mul_i32 s67, s67, s52\n"
"s_add_u32 s66, s67, s51\n"
"v_cvt_pk_bf16_f32 v160, v16, v17\n"
"v_cvt_pk_bf16_f32 v161, v18, v19\n"
"v_cvt_pk_bf16_f32 v162, v20, v21\n"
"v_cvt_pk_bf16_f32 v163, v22, v23\n"
"v_cvt_pk_bf16_f32 v164, v24, v25\n"
"v_cvt_pk_bf16_f32 v165, v26, v27\n"
"v_cvt_pk_bf16_f32 v166, v28, v29\n"
"v_cvt_pk_bf16_f32 v167, v30, v31\n"
"v_cvt_pk_bf16_f32 v168, v32, v33\n"
"v_cvt_pk_bf16_f32 v169, v34, v35\n"
"v_cvt_pk_bf16_f32 v170, v36, v37\n"
"v_cvt_pk_bf16_f32 v171, v38, v39\n"
"v_cvt_pk_bf16_f32 v172, v40, v41\n"
"v_cvt_pk_bf16_f32 v173, v42, v43\n"
"v_cvt_pk_bf16_f32 v174, v44, v45\n"
"v_cvt_pk_bf16_f32 v175, v46, v47\n"
"v_permlane32_swap_b32 v160, v162\n"
"v_permlane32_swap_b32 v161, v163\n"
"v_permlane32_swap_b32 v164, v166\n"
"v_permlane32_swap_b32 v165, v167\n"
"v_permlane32_swap_b32 v168, v170\n"
"v_permlane32_swap_b32 v169, v171\n"
"v_permlane32_swap_b32 v172, v174\n"
"v_permlane32_swap_b32 v173, v175\n"
"buffer_store_dwordx4 v[160:163], v11, s[40:43], s66 offen sc1\n"
"buffer_store_dwordx4 v[164:167], v12, s[40:43], s66 offen sc1\n"
"buffer_store_dwordx4 v[168:171], v13, s[40:43], s66 offen sc1\n"
"buffer_store_dwordx4 v[172:175], v14, s[40:43], s66 offen sc1\n"
"s_waitcnt vmcnt(4) lgkmcnt(0)\n"
"s_barrier\n"
"s_mov_b32 s61, s57\n"
"s_add_u32 s62, s57, 1\n"
"s_sub_u32 s77, 36864, s77\n"
"v_add_u32 v1, s77, v0\n"
"ds_read_b128 v[80:83], v1\n"
"ds_read_b128 v[84:87], v1 offset:1024\n"
"ds_read_b128 v[88:91], v1 offset:2048\n"
"ds_read_b128 v[92:95], v1 offset:3072\n"
"s_add_u32 s57, s57, 1\n"
"s_branch Ltoppack36_%=\n"
"Lnoldpack36_%=:\n"
"s_add_u32 s62, s57, 1\n"
"s_waitcnt lgkmcnt(0)\n"
"s_cmp_lt_u32 s62, s48\n"
"s_cbranch_scc0 Ln1pack36_%=\n"
"s_lshl_b32 s67, s62, 2\n"
"s_add_u32 s67, s67, 86528\n"
"v_add_u32 v192, s67, v8\n"
"ds_read_b32 v192, v192\n"
"s_waitcnt lgkmcnt(0)\n"
"v_add_u32 v192, v192, v9\n"
"s_mov_b32 m0, 87808\n"
"s_and_b32 exec_lo, s78, 1023\n"
"s_mov_b32 exec_hi, 0\n"
"buffer_load_dword v192, s[44:47], 0 offen sc1 lds\n"
"s_mov_b64 exec, -1\n"
"s_waitcnt lgkmcnt(0)\n"
"Ln1pack36_%=:\n"
"s_nop 7\n"
"v_readlane_b32 s67, v6, s57\n"
"s_nop 0\n"
"s_mul_i32 s67, s67, s52\n"
"s_add_u32 s66, s67, s51\n"
"v_cvt_pk_bf16_f32 v160, v16, v17\n"
"v_cvt_pk_bf16_f32 v161, v18, v19\n"
"v_cvt_pk_bf16_f32 v162, v20, v21\n"
"v_cvt_pk_bf16_f32 v163, v22, v23\n"
"v_cvt_pk_bf16_f32 v164, v24, v25\n"
"v_cvt_pk_bf16_f32 v165, v26, v27\n"
"v_cvt_pk_bf16_f32 v166, v28, v29\n"
"v_cvt_pk_bf16_f32 v167, v30, v31\n"
"v_cvt_pk_bf16_f32 v168, v32, v33\n"
"v_cvt_pk_bf16_f32 v169, v34, v35\n"
"v_cvt_pk_bf16_f32 v170, v36, v37\n"
"v_cvt_pk_bf16_f32 v171, v38, v39\n"
"v_cvt_pk_bf16_f32 v172, v40, v41\n"
"v_cvt_pk_bf16_f32 v173, v42, v43\n"
"v_cvt_pk_bf16_f32 v174, v44, v45\n"
"v_cvt_pk_bf16_f32 v175, v46, v47\n"
"v_permlane32_swap_b32 v160, v162\n"
"v_permlane32_swap_b32 v161, v163\n"
"v_permlane32_swap_b32 v164, v166\n"
"v_permlane32_swap_b32 v165, v167\n"
"v_permlane32_swap_b32 v168, v170\n"
"v_permlane32_swap_b32 v169, v171\n"
"v_permlane32_swap_b32 v172, v174\n"
"v_permlane32_swap_b32 v173, v175\n"
"buffer_store_dwordx4 v[160:163], v11, s[40:43], s66 offen sc1\n"
"buffer_store_dwordx4 v[164:167], v12, s[40:43], s66 offen sc1\n"
"buffer_store_dwordx4 v[168:171], v13, s[40:43], s66 offen sc1\n"
"buffer_store_dwordx4 v[172:175], v14, s[40:43], s66 offen sc1\n"
"s_waitcnt vmcnt(4) lgkmcnt(0)\n"
"s_barrier\n"
"s_cmp_lt_u32 s62, s48\n"
"s_cbranch_scc0 Ln3pack36_%=\n"
"s_sub_u32 s67, 87808, 0\n"
"v_lshrrev_b32 v192, 2, v0\n"
"v_add_u32 v192, s67, v192\n"
"ds_read_b32 v193, v192\n"
"s_waitcnt lgkmcnt(0)\n"
"v_cmp_eq_u32_e64 s[80:81], s97, v193\n"
"s_nop 1\n"
"s_sub_u32 s68, s48, s62\n"
"s_min_u32 s68, s68, 2\n"
"s_mov_b32 s70, 0\n"
"Levnpack36_%=:\n"
"s_cmp_ge_u32 s70, s68\n"
"s_cbranch_scc1 Lexnpack36_%=\n"
"s_and_b32 s69, s80, 31\n"
"s_cmp_eq_u32 s69, 31\n"
"s_cbranch_scc0 Lexnpack36_%=\n"
"s_lshr_b64 s[80:81], s[80:81], 5\n"
"s_add_u32 s70, s70, 1\n"
"s_branch Levnpack36_%=\n"
"Lexnpack36_%=:\n"
"s_add_u32 s58, s62, s70\n"
"s_cmp_lt_u32 s62, s58\n"
"s_cbranch_scc0 Ln2pack36_%=\n"
"v_readlane_b32 s67, v6, s62\n"
"s_nop 0\n"
"s_mul_i32 s67, s67, s50\n"
"s_add_u32 s65, s67, s49\n"
"s_sub_u32 s64, 36864, s77\n"
"s_lshl_b32 s68, %0, 10\n"
"s_add_u32 s64, s64, s68\n"
"s_add_u32 m0, s64, 0\n"
"s_add_u32 s70, s65, 0\n"
"buffer_load_dwordx4 v2, s[36:39], s70 offen sc1 lds\n"
"s_add_u32 m0, s64, 4096\n"
"s_add_u32 s70, s65, 4096\n"
"buffer_load_dwordx4 v2, s[36:39], s70 offen sc1 lds\n"
"s_add_u32 m0, s64, 8192\n"
"s_add_u32 s70, s65, 8192\n"
"buffer_load_dwordx4 v2, s[36:39], s70 offen sc1 lds\n"
"s_add_u32 m0, s64, 12288\n"
"s_add_u32 s70, s65, 12288\n"
"buffer_load_dwordx4 v2, s[36:39], s70 offen sc1 lds\n"
"s_add_u32 m0, s64, 16384\n"
"s_add_u32 s70, s65, 16384\n"
"buffer_load_dwordx4 v2, s[36:39], s70 offen sc1 lds\n"
"s_add_u32 m0, s64, 20480\n"
"s_add_u32 s70, s65, 20480\n"
"buffer_load_dwordx4 v2, s[36:39], s70 offen sc1 lds\n"
"s_add_u32 m0, s64, 24576\n"
"s_add_u32 s70, s65, 24576\n"
"buffer_load_dwordx4 v2, s[36:39], s70 offen sc1 lds\n"
"s_add_u32 m0, s64, 28672\n"
"s_add_u32 s70, s65, 28672\n"
"buffer_load_dwordx4 v2, s[36:39], s70 offen sc1 lds\n"
"s_add_u32 m0, s64, 32768\n"
"s_add_u32 s70, s65, 32768\n"
"buffer_load_dwordx4 v2, s[36:39], s70 offen sc1 lds\n"
"s_waitcnt vmcnt(9) lgkmcnt(0)\n"
"s_barrier\n"
"s_cmp_lt_i32 s57, 0\n"
"s_cbranch_scc1 Lnsn1pack36_%=\n"
"v_readlane_b32 s67, v6, s57\n"
"s_nop 0\n"
"s_mul_i32 s67, s67, s54\n"
"s_add_u32 s67, s67, s53\n"
"buffer_store_dword v4, v3, s[44:47], s67 offen sc1\n"
"Lnsn1pack36_%=:\n"
"s_waitcnt vmcnt(1) lgkmcnt(0)\n"
"s_barrier\n"
"s_branch Ln4pack36_%=\n"
"Ln2pack36_%=:\n"
"s_lshl_b32 s67, s62, 2\n"
"s_add_u32 s67, s67, 86528\n"
"v_add_u32 v192, s67, v8\n"
"ds_read_b32 v192, v192\n"
"s_waitcnt lgkmcnt(0)\n"
"v_add_u32 v192, v192, v9\n"
"s_mov_b32 m0, 87296\n"
"s_and_b32 exec_lo, s78, 1023\n"
"s_mov_b32 exec_hi, 0\n"
"buffer_load_dword v192, s[44:47], 0 offen sc1 lds\n"
"s_mov_b64 exec, -1\n"
"s_waitcnt vmcnt(0) lgkmcnt(0)\n"
"s_barrier\n"
"s_cmp_lt_i32 s57, 0\n"
"s_cbranch_scc1 Lnsn2pack36_%=\n"
"v_readlane_b32 s67, v6, s57\n"
"s_nop 0\n"
"s_mul_i32 s67, s67, s54\n"
"s_add_u32 s67, s67, s53\n"
"buffer_store_dword v4, v3, s[44:47], s67 offen sc1\n"
"Lnsn2pack36_%=:\n"
"s_sub_u32 s67, 87296, 0\n"
"v_lshrrev_b32 v192, 2, v0\n"
"v_add_u32 v192, s67, v192\n"
"ds_read_b32 v193, v192\n"
"s_waitcnt lgkmcnt(0)\n"
"v_cmp_eq_u32_e64 s[80:81], s97, v193\n"
"s_nop 1\n"
"s_sub_u32 s68, s48, s62\n"
"s_min_u32 s68, s68, 2\n"
"s_mov_b32 s70, 0\n"
"Levn2pack36_%=:\n"
"s_cmp_ge_u32 s70, s68\n"
"s_cbranch_scc1 Lexn2pack36_%=\n"
"s_and_b32 s69, s80, 31\n"
"s_cmp_eq_u32 s69, 31\n"
"s_cbranch_scc0 Lexn2pack36_%=\n"
"s_lshr_b64 s[80:81], s[80:81], 5\n"
"s_add_u32 s70, s70, 1\n"
"s_branch Levn2pack36_%=\n"
"Lexn2pack36_%=:\n"
"s_add_u32 s58, s62, s70\n"
"s_cmp_lt_u32 s62, s58\n"
"s_cbranch_scc0 Ln5pack36_%=\n"
"v_readlane_b32 s67, v6, s62\n"
"s_nop 0\n"
"s_mul_i32 s67, s67, s50\n"
"s_add_u32 s65, s67, s49\n"
"s_sub_u32 s64, 36864, s77\n"
"s_lshl_b32 s68, %0, 10\n"
"s_add_u32 s64, s64, s68\n"
"s_add_u32 m0, s64, 0\n"
"s_add_u32 s70, s65, 0\n"
"buffer_load_dwordx4 v2, s[36:39], s70 offen sc1 lds\n"
"s_add_u32 m0, s64, 4096\n"
"s_add_u32 s70, s65, 4096\n"
"buffer_load_dwordx4 v2, s[36:39], s70 offen sc1 lds\n"
"s_add_u32 m0, s64, 8192\n"
"s_add_u32 s70, s65, 8192\n"
"buffer_load_dwordx4 v2, s[36:39], s70 offen sc1 lds\n"
"s_add_u32 m0, s64, 12288\n"
"s_add_u32 s70, s65, 12288\n"
"buffer_load_dwordx4 v2, s[36:39], s70 offen sc1 lds\n"
"s_add_u32 m0, s64, 16384\n"
"s_add_u32 s70, s65, 16384\n"
"buffer_load_dwordx4 v2, s[36:39], s70 offen sc1 lds\n"
"s_add_u32 m0, s64, 20480\n"
"s_add_u32 s70, s65, 20480\n"
"buffer_load_dwordx4 v2, s[36:39], s70 offen sc1 lds\n"
"s_add_u32 m0, s64, 24576\n"
"s_add_u32 s70, s65, 24576\n"
"buffer_load_dwordx4 v2, s[36:39], s70 offen sc1 lds\n"
"s_add_u32 m0, s64, 28672\n"
"s_add_u32 s70, s65, 28672\n"
"buffer_load_dwordx4 v2, s[36:39], s70 offen sc1 lds\n"
"s_add_u32 m0, s64, 32768\n"
"s_add_u32 s70, s65, 32768\n"
"buffer_load_dwordx4 v2, s[36:39], s70 offen sc1 lds\n"
"s_waitcnt vmcnt(0) lgkmcnt(0)\n"
"s_barrier\n"
"s_branch Ln4pack36_%=\n"
"Ln5pack36_%=:\n"
"s_cmp_eq_u32 %0, 0\n"
"s_cbranch_scc0 Lbwnpack36_%=\n"
"s_cmp_lg_u32 s75, 0\n"
"s_cbranch_scc1 Lbwnpack36_%=\n"
"v_readlane_b32 s72, v7, s62\n"
"s_nop 3\n"
"s_mov_b32 s71, 0\n"
"s_cmp_lt_i32 s74, 0\n"
"s_cbranch_scc1 Lplnpack36_%=\n"
"s_sub_u32 s67, s62, s74\n"
"s_cmp_gt_u32 s67, 3\n"
"s_cbranch_scc1 Lplnpack36_%=\n"
"s_add_u32 s68, s62, 2\n"
"s_cmp_ge_u32 s68, s48\n"
"s_cbranch_scc1 Lplnpack36_%=\n"
"s_mul_i32 s67, s67, 240\n"
"s_add_u32 s67, s67, s73\n"
"s_sub_u32 s67, s67, 40\n"
"s_mov_b32 s76, 0\n"
"Lslnpack36_%=:\n"
"s_memrealtime s[84:85]\n"
"s_waitcnt lgkmcnt(0)\n"
"s_sub_u32 s68, s84, s67\n"
"s_cmp_ge_i32 s68, 0\n"
"s_cbranch_scc1 Lplnpack36_%=\n"
"s_sleep 2\n"
"s_mov_b32 s71, 1\n"
"s_add_u32 s76, s76, 1\n"
"s_cmp_lt_u32 s76, 32768\n"
"s_cbranch_scc1 Lslnpack36_%=\n"
"Lplnpack36_%=:\n"
"s_mov_b32 s76, 0\n"
"Lppnpack36_%=:\n"
"buffer_load_dword v192, v5, s[44:47], s72 offen sc1\n"
"s_waitcnt vmcnt(0)\n"
"v_cmp_ne_u32_e64 s[80:81], s97, v192\n"
"s_nop 1\n"
"s_and_b64 s[80:81], s[80:81], s[82:83]\n"
"s_cbranch_scc0 Lrdnpack36_%=\n"
"s_sleep 3\n"
"s_add_u32 s76, s76, 1\n"
"s_cmp_lt_u32 s76, 32768\n"
"s_cbranch_scc1 Lppnpack36_%=\n"
"s_mov_b32 s75, 1\n"
"v_mov_b32 v192, 16384\n"
"v_mov_b32 v193, 7000\n"
"buffer_store_dword v193, v192, s[44:47], 0 offen sc1\n"
"Lrdnpack36_%=:\n"
"s_mov_b32 s74, -1\n"
"s_cmp_eq_u32 s76, 0\n"
"s_cbranch_scc1 Lbwnpack36_%=\n"
"s_memrealtime s[84:85]\n"
"s_waitcnt lgkmcnt(0)\n"
"s_mov_b32 s73, s84\n"
"s_mov_b32 s74, s62\n"
"Lbwnpack36_%=:\n"
"s_waitcnt lgkmcnt(0)\n"
"s_barrier\n"
"v_readlane_b32 s67, v6, s62\n"
"s_nop 0\n"
"s_mul_i32 s67, s67, s50\n"
"s_add_u32 s65, s67, s49\n"
"s_sub_u32 s64, 36864, s77\n"
"s_lshl_b32 s68, %0, 10\n"
"s_add_u32 s64, s64, s68\n"
"s_add_u32 m0, s64, 0\n"
"s_add_u32 s70, s65, 0\n"
"buffer_load_dwordx4 v2, s[36:39], s70 offen sc1 lds\n"
"s_add_u32 m0, s64, 4096\n"
"s_add_u32 s70, s65, 4096\n"
"buffer_load_dwordx4 v2, s[36:39], s70 offen sc1 lds\n"
"s_add_u32 m0, s64, 8192\n"
"s_add_u32 s70, s65, 8192\n"
"buffer_load_dwordx4 v2, s[36:39], s70 offen sc1 lds\n"
"s_add_u32 m0, s64, 12288\n"
"s_add_u32 s70, s65, 12288\n"
"buffer_load_dwordx4 v2, s[36:39], s70 offen sc1 lds\n"
"s_add_u32 m0, s64, 16384\n"
"s_add_u32 s70, s65, 16384\n"
"buffer_load_dwordx4 v2, s[36:39], s70 offen sc1 lds\n"
"s_add_u32 m0, s64, 20480\n"
"s_add_u32 s70, s65, 20480\n"
"buffer_load_dwordx4 v2, s[36:39], s70 offen sc1 lds\n"
"s_add_u32 m0, s64, 24576\n"
"s_add_u32 s70, s65, 24576\n"
"buffer_load_dwordx4 v2, s[36:39], s70 offen sc1 lds\n"
"s_add_u32 m0, s64, 28672\n"
"s_add_u32 s70, s65, 28672\n"
"buffer_load_dwordx4 v2, s[36:39], s70 offen sc1 lds\n"
"s_add_u32 m0, s64, 32768\n"
"s_add_u32 s70, s65, 32768\n"
"buffer_load_dwordx4 v2, s[36:39], s70 offen sc1 lds\n"
"s_waitcnt vmcnt(0) lgkmcnt(0)\n"
"s_barrier\n"
"s_add_u32 s58, s62, 1\n"
"Ln4pack36_%=:\n"
"s_sub_u32 s77, 36864, s77\n"
"v_add_u32 v1, s77, v0\n"
"ds_read_b128 v[80:83], v1\n"
"ds_read_b128 v[84:87], v1 offset:1024\n"
"ds_read_b128 v[88:91], v1 offset:2048\n"
"ds_read_b128 v[92:95], v1 offset:3072\n"
"s_add_u32 s57, s57, 1\n"
"s_branch Ltoppack36_%=\n"
"Ln3pack36_%=:\n"
"s_waitcnt vmcnt(0) lgkmcnt(0)\n"
"s_barrier\n"
"s_cmp_lt_i32 s57, 0\n"
"s_cbranch_scc1 Lnsn3pack36_%=\n"
"v_readlane_b32 s67, v6, s57\n"
"s_nop 0\n"
"s_mul_i32 s67, s67, s54\n"
"s_add_u32 s67, s67, s53\n"
"buffer_store_dword v4, v3, s[44:47], s67 offen sc1\n"
"Lnsn3pack36_%=:\n"
"s_waitcnt vmcnt(0) lgkmcnt(0)\n"
"s_barrier\n"
  GEMM_ASM_END
"v0","v1","v2","v3","v4","v5","v6","v7","v8","v9","v10","v11","v12","v13","v14","v15","v16","v17","v18","v19","v20","v21","v22","v23","v24","v25","v26","v27","v28","v29","v30","v31","v32","v33","v34","v35","v36","v37","v38","v39","v40","v41","v42","v43","v44","v45","v46","v47","v48","v49","v50","v51","v52","v53","v54","v55","v56","v57","v58","v59","v60","v61","v62","v63","v64","v65","v66","v67","v68","v69","v70","v71","v72","v73","v74","v75","v76","v77","v78","v79","v80","v81","v82","v83","v84","v85","v86","v87","v88","v89","v90","v91","v92","v93","v94","v95","v96","v97","v98","v99","v100","v101","v102","v103","v104","v105","v106","v107","v108","v109","v110","v111","v112","v113","v114","v115","v116","v117","v118","v119","v120","v121","v122","v123","v124","v125","v126","v128","v129","v130","v131","v132","v133","v134","v135","v136","v137","v138","v139","v140","v141","v142","v143","v144","v145","v146","v147","v148","v149","v150","v151","v152","v153","v154","v155","v156","v157","v158","v159","v160","v161","v162","v163","v164","v165","v166","v167","v168","v169","v170","v171","v172","v173","v174","v175","v176","v177","v178","v179","v180","v181","v182","v183","v184","v185","v186","v187","v188","v189","v190","v191","v192","v193","v194","v195","v196","v197","v198","v199","v200","v201","v202","v203","v204","v205","v206","v207","v208","v209","v210","v211","v212","v213","v214","v215","v216","v217","v218","v219","v220","v221","v222","v223","v224","v225","v226","v227","v228","v229","v230","v231","v232","v233","v234","v235","v236","v237","v238","v239","v240","v241","v242","v243","v244","v245","v246","v247","v248","v249","v250","v251","v252","v253","v254","v255","a0","a1","a2","a3","a4","a5","a6","a7","a8","a9","a10","a11","a12","a13","a14","a15","a16","a17","a18","a19","a20","a21","a22","a23","a24","a25","a26","a27","a28","a29","a30","a31","a32","a33","a34","a35","a36","a37","a38","a39","a40","a41","a42","a43","a44","a45","a46","a47","a48","a49","a50","a51","a52","a53","a54","a55","a56","a57","a58","a59","a60","a61","a62","a63","a64","a65","a66","a67","a68","a69","a70","a71","a72","a73","a74","a75","a76","a77","a78","a79","a80","a81","a82","a83","a84","a85","a86","a87","a88","a89","a90","a91","a92","a93","a94","a95","a96","a97","a98","a99","a100","a101","a102","a103","a104","a105","a106","a107","a108","a109","a110","a111","a112","a113","a114","a115","a116","a117","a118","a119","a120","a121","a122","a123","a124","a125","a126","a127","a128","a129","a130","a131","a132","a133","a134","a135","a136","a137","a138","a139","a140","a141","a142","a143","a144","a145","a146","a147","a148","a149","a150","a151","a152","a153","a154","a155","a156","a157","a158","a159","a160","a161","a162","a163","a164","a165","a166","a167","a168","a169","a170","a171","a172","a173","a174","a175","a176","a177","a178","a179","a180","a181","a182","a183","a184","a185","a186","a187","a188","a189","a190","a191","a192","a193","a194","a195","a196","a197","a198","a199","a200","a201","a202","a203","a204","a205","a206","a207","a208","a209","a210","a211","a212","a213","a214","a215","a216","a217","a218","a219","a220","a221","a222","a223","a224","a225","a226","a227","a228","a229","a230","a231","a232","a233","a234","a235","a236","a237","a238","a239","a240","a241","a242","a243","a244","a245","a246","a247","a248","a249","a250","a251","a252","a253","a254","a255","s36","s37","s38","s39","s40","s41","s42","s43","s44","s45","s46","s47","s48","s49","s50","s51","s52","s53","s54","s55","s56","s57","s58","s59","s60","s61","s62","s63","s64","s65","s66","s67","s68","s69","s70","s71","s72","s73","s74","s75","s76","s77","s78","s79","s80","s81","s82","s83","s84","s85","s86","s87","s88","s89","s90","s91","s92","s93","s94","s95","s96","s97","s98","s99","vcc","scc","memory"
  );
}
DEVI f4 mm32(v4u a,v4u b,f4 c){ return __builtin_amdgcn_mfma_f32_16x16x32_bf16(asbf8(a),asbf8(b),c,0,0,0); }
DEVI float max4(f4 v){ return fmaxf(fmaxf(v[0],v[1]),fmaxf(v[2],v[3])); }
DEVI float colmax(float m){
  v2u t=swap16(__float_as_uint(m),__float_as_uint(m)); m=fmaxf(__uint_as_float(t.x),__uint_as_float(t.y));
  t=swap32(__float_as_uint(m),__float_as_uint(m)); return fmaxf(__uint_as_float(t.x),__uint_as_float(t.y)); }
template<int I> DEVI void att_dma(rsrc_t r,unsigned lds,unsigned vo,unsigned so){ dma16c<I*1024,I*1024>(r,lds,vo,so); }
DEVI void role_att(Ctx& c,CArgs& a,int grp,int wv){
  c.lane=fresh_lane();
  rsrc_t rq=mkr(a.ws+O_Q,SZ_QKV), rk=mkr(a.ws+O_K,SZ_QKV), rv=mkr(a.ws+O_V,SZ_QKV), rkm=mkr(a.ws+O_KM,SZ_KM), rvm=mkr(a.ws+O_VM,SZ_KM), ro=mkr(a.ws+O_O,SZ_O);
  const int l=c.lane, g=l>>4, n16=l&15;
  const float SCALE=0.08838834764831845f, L2E=1.4426950408889634f;
  const f4 z4={0.f,0.f,0.f,0.f};
  const v4u one4={0x3f803f80u,0x3f803f80u,0x3f803f80u,0x3f803f80u};
  const int head=grp*8+(wv&7);
  Ctx cw{mkr(a.win,2u<<20),c.lane,c.wave,false,false,c.tag};
  const unsigned LB=c.wave*28672u;
  Pred pr{0u,-1};
  unsigned long long rdy=0, mrdy=0;
  auto need_qkv=[&](int s){
    if(!((rdy>>s)&1ull) && !c.dead){
      unsigned sp=0;
      for(;;){
        asm volatile("" ::: "memory");
        const unsigned fq=ld4c(c.fl,FL_Q+head*256+l*4,0), fk=ld4c(c.fl,FL_K+head*256+l*4,0), fv=ld4c(c.fl,FL_V+head*256+l*4,0);
        rdy=__ballot(fq==c.tag && fk==c.tag && fv==c.tag);
        if((rdy>>s)&1ull) break;
        __builtin_amdgcn_s_sleep(16);
        if(++sp>SPIN_MAX){ give_up(c,FL_Q); break; }
      }
      __builtin_amdgcn_s_waitcnt(0x0F70);
    } };
  auto fetch=[&](int s,bool mem){
    const unsigned item=(unsigned)(s*32+head);
    sfor<8>([&](auto I){ att_dma<I>(rq,LB,l*16,item*8192u); });
    sfor<8>([&](auto I){ att_dma<I>(rk,LB+8192,l*16,item*8192u); });
    sfor<8>([&](auto I){ att_dma<I>(rv,LB+16384,l*16,item*8192u); });
    if(mem){ sfor<2>([&](auto I){ att_dma<I>(rkm,LB+24576,l*16,item*2048u); }); sfor<2>([&](auto I){ att_dma<I>(rvm,LB+26624,l*16,item*2048u); }); }
  };
  int n=0; bool back=true, havemem=false;
  need_qkv(wv>>3);
  fetch(wv>>3,false);
  for(int s=wv>>3;s<64;s+=3,n++){
    const unsigned item=(unsigned)(s*32+head);
    FST(0);
    asm volatile("s_waitcnt vmcnt(0)" ::: "memory");
    v4u Q[2][4],K[2][4],V[8],KM[4]; v2u VM[8];
    #pragma unroll
    for(int t=0;t<2;t++) for(int ks=0;ks<4;ks++){ Q[t][ks]=*(const LDSP v4u*)(uintptr_t)(LB+l*16+(t*4+ks)*1024); K[t][ks]=*(const LDSP v4u*)(uintptr_t)(LB+8192+l*16+(t*4+ks)*1024); }
    #pragma unroll
    for(int ft=0;ft<8;ft++) V[ft]=*(const LDSP v4u*)(uintptr_t)(LB+16384+l*16+ft*1024);
    const bool mem=havemem;
    if(mem){
      #pragma unroll
      for(int ks=0;ks<4;ks++) KM[ks]=*(const LDSP v4u*)(uintptr_t)(LB+24576+ks*512+g*128+(l&7)*16);
      #pragma unroll
      for(int ft=0;ft<8;ft++) VM[ft]=*(const LDSP v2u*)(uintptr_t)(LB+26624+(16*ft+n16)*16+(g&1)*8);
    }
    const int sn=s+3;
    if(sn<64){
      need_qkv(sn);
      if(back && !((mrdy>>sn)&1ull)){ asm volatile("" ::: "memory"); mrdy=__ballot(ld4c(cw.fl,500*4096+(head*65+l+1)*8,0)==c.tag); __builtin_amdgcn_s_waitcnt(0x0F70); }
      havemem=(mrdy>>sn)&1ull;
      asm volatile("s_waitcnt lgkmcnt(0)" : "+v"(Q[0][0]),"+v"(Q[1][3]),"+v"(K[0][0]),"+v"(K[1][3]),"+v"(V[0]),"+v"(V[7]) :: "memory");
      __builtin_amdgcn_s_waitcnt(0xC07F);
      fetch(sn,havemem);
    }
    FST(1);
    f4 S00=z4,S01=z4,S11=z4;
    #pragma unroll
    for(int ks=0;ks<4;ks++){ S00=mm32(K[0][ks],Q[0][ks],S00); S01=mm32(K[0][ks],Q[1][ks],S01); S11=mm32(K[1][ks],Q[1][ks],S11); }
    #pragma unroll
    for(int r=0;r<4;r++){ const bool ok=(4*g+r)<=n16; S00[r]=ok? S00[r]*SCALE : -1e9f; S11[r]=ok? S11[r]*SCALE : -1e9f; S01[r]*=SCALE; }
    const float t0=colmax(max4(S00)), t1=colmax(fmaxf(max4(S01),max4(S11)));
    float e00[4],e01[4],e11[4];
    #pragma unroll
    for(int r=0;r<4;r++){ e00[r]=__builtin_amdgcn_exp2f((S00[r]-t0)*L2E); e01[r]=__builtin_amdgcn_exp2f((S01[r]-t1)*L2E); e11[r]=__builtin_amdgcn_exp2f((S11[r]-t1)*L2E); }
    const v4u P0={pk2(e00[0],e00[1]),pk2(e00[2],e00[3]),0u,0u}, P1={pk2(e01[0],e01[1]),pk2(e01[2],e01[3]),pk2(e11[0],e11[1]),pk2(e11[2],e11[3])};
    const float st0=mm32(one4,P0,z4)[0], st1=mm32(one4,P1,z4)[0];
    f4 O0[8],O1[8];
    #pragma unroll
    for(int ft=0;ft<8;ft++){ O0[ft]=mm32(V[ft],P0,z4); O1[ft]=mm32(V[ft],P1,z4); }
    FST(2);
    if(!mem){
      bool got=false;
      if(s>=SENT0 && !c.dead){
        if(pr.s>=0 && s-pr.s<=3){ const unsigned target=pr.t+(unsigned)(s-pr.s)*PERIOD-SMARG; unsigned gq=0; while((int)(now_t()-target)<0 && ++gq<SPIN_MAX) __builtin_amdgcn_s_sleep(2); }
        for(int it=0;it<SENTN;it++){
          asm volatile("" ::: "memory");
          unsigned mx=0;
          #pragma unroll
          for(int ks=0;ks<4;ks++){ KM[ks]=ld16c(rkm,ks*512+g*128+(l&7)*16,item*2048u); }
          #pragma unroll
          for(int ft=0;ft<8;ft++){ VM[ft]=ld8c(rvm,(16*ft+n16)*16+(g&1)*8,item*2048u); }
          #pragma unroll
          for(int ks=0;ks<4;ks++) mx=max(max(mx,max(KM[ks].x,KM[ks].y)),max(KM[ks].z,KM[ks].w));
          #pragma unroll
          for(int ft=0;ft<8;ft++) mx=max(mx,max(VM[ft].x,VM[ft].y));
          if(__ballot(mx==0xffffffffu)==0ull){ got=true; pr.t=now_t(); pr.s=s; break; }
        }
        back=false;
      }
      if(!got){
        back=wait_pred(cw,pr,s,500*4096+(head*65+s+1)*8,1,c.tag); c.dead=c.dead||cw.dead;
        FST(3);
        #pragma unroll
        for(int ks=0;ks<4;ks++) KM[ks]=ld16c(rkm,ks*512+g*128+(l&7)*16,item*2048u);
        #pragma unroll
        for(int ft=0;ft<8;ft++) VM[ft]=ld8c(rvm,(16*ft+n16)*16+(g&1)*8,item*2048u);
      }
    }
    f4 Sm0=z4,Sm1=z4;
    #pragma unroll
    for(int ks=0;ks<4;ks++){ Sm0=mm32(KM[ks],Q[0][ks],Sm0); Sm1=mm32(KM[ks],Q[1][ks],Sm1); }
    Sm0*=SCALE; Sm1*=SCALE;
    const float m0=fmaxf(t0,colmax(max4(Sm0))), m1=fmaxf(t1,colmax(max4(Sm1)));
    const float a0=__builtin_amdgcn_exp2f((t0-m0)*L2E), a1=__builtin_amdgcn_exp2f((t1-m1)*L2E);
    float f0[4],f1[4];
    #pragma unroll
    for(int r=0;r<4;r++){ f0[r]=(g<2)? __builtin_amdgcn_exp2f((Sm0[r]-m0)*L2E) : 0.f; f1[r]=(g<2)? __builtin_amdgcn_exp2f((Sm1[r]-m1)*L2E) : 0.f; }
    const float r0=__builtin_amdgcn_rcpf(a0*st0+rows_sum((f0[0]+f0[1])+(f0[2]+f0[3]))), r1=__builtin_amdgcn_rcpf(a1*st1+rows_sum((f1[0]+f1[1])+(f1[2]+f1[3])));
    const v4u M0={pk2(f0[0]*r0,f0[1]*r0),pk2(f0[2]*r0,f0[3]*r0),0u,0u}, M1={pk2(f1[0]*r1,f1[1]*r1),pk2(f1[2]*r1,f1[3]*r1),0u,0u};
    const float c0=a0*r0, c1=a1*r1;
    FST(4);
    #pragma unroll
    for(int ft=0;ft<8;ft++){
      const v4u vm4={VM[ft].x,VM[ft].y,0u,0u};
      const f4 o0=mm32(vm4,M0,O0[ft]*c0), o1=mm32(vm4,M1,O1[ft]*c1);
      v2u x=swap16(pk2(o0[0],o0[1]),pk2(o1[0],o1[1])), y=swap16(pk2(o0[2],o0[3]),pk2(o1[2],o1[3]));
      v4u o={x.x,y.x,x.y,y.y};
      st16c(o,ro,l*16+ft*1024,(unsigned)s*262144u+head*8192);
    }
    FST(5);
    wait_vm0();
    FST(6);
    flag_set(c,FL_O+(s*32+head)*128);
    STAMP(c,64+n);
  }
}
struct RowMap { v4u sel1,sel2; unsigned voA,voB; int c1,c2,c3,h,tk; bool v3; };
DEVI RowMap row_map(int l,int w){
  RowMap m; const int g=l>>4, n=l&15;
  unsigned e[4];
  #pragma unroll
  for(int d=0;d<4;d++) e[d]=((n&7)==2*d)? 0x3f80u : ((n&7)==2*d+1)? 0x3f800000u : 0u;
  const bool s1=(g==(n>>3)), s2=(g==2+(n>>3));
  m.sel1=(v4u){s1?e[0]:0u,s1?e[1]:0u,s1?e[2]:0u,s1?e[3]:0u}; m.sel2=(v4u){s2?e[0]:0u,s2?e[1]:0u,s2?e[2]:0u,s2?e[3]:0u};
  m.voA=18*w*64+l*16; m.voB=(l<8)? (18*w+16)*64+l*16 : 0x7ffffff0u;
  m.c1=18*w+4*(g>>1)+(n>>2); m.c2=m.c1+8; m.v3=(g<2 && n<8); m.c3=m.v3? 18*w+16+(n>>2) : m.c1; m.h=g&1; m.tk=n&3;
  return m;
}
constexpr unsigned L_ZERO=110592;
DEVI void part_sum(unsigned buf,const RowMap& m,f4& y1,f4& y2,f4& y3){
  y1=(f4){0.f,0.f,0.f,0.f}; y2=y1; y3=y1;
  v4u pa[8],pb[8];
  const unsigned aA=buf+m.voA, aB=(m.voB!=0x7ffffff0u)? buf+m.voB : L_ZERO;
  #pragma unroll
  for(int q=0;q<8;q++){ pa[q]=*(const LDSP v4u*)(uintptr_t)(aA+q*4608); pb[q]=*(const LDSP v4u*)(uintptr_t)(aB+q*4608); }
  __builtin_amdgcn_sched_barrier(0);
  #pragma unroll
  for(int q=0;q<8;q++){ y1=mm32(m.sel1,pa[q],y1); y2=mm32(m.sel2,pa[q],y2); y3=mm32(m.sel1,pb[q],y3); }
}
DEVI void zero_spots(Ctx& c){ if(c.wave==0 && c.lane<8) *(LDSP v4u*)(uintptr_t)(L_ZERO+c.lane*4608)=(v4u){0u,0u,0u,0u}; }
DEVI f4 up4(v2u v){ return (f4){blo(v.x),bhi(v.x),blo(v.y),bhi(v.y)}; }
DEVI v2u pk4(f4 v){ return (v2u){pk2(v[0],v[1]),pk2(v[2],v[3])}; }
DEVI float hsum4(f4 v){ return (v[0]+v[1])+(v[2]+v[3]); }
DEVI v2u ld8(rsrc_t r,unsigned vo){ return __builtin_amdgcn_raw_buffer_load_b64(r,vo,0,0); }
DEVI void st8c(v2u v,rsrc_t r,unsigned vo){ __builtin_amdgcn_raw_buffer_store_b64(v,r,vo,0,16); }
template<bool COH> DEVI void side_dma(Ctx& c,rsrc_t r,unsigned gbase,unsigned lds){
  const unsigned vo=c.lane*16, so=gbase+c.wave*1024, l0=lds+c.wave*1024;
  if(COH) asm volatile("s_mov_b32 m0, %0\n\ts_nop 0\n\tbuffer_load_dwordx4 %1, %2, %3 offen sc1 lds" :: "s"(l0),"v"(vo),"s"(r),"s"(so) : "memory");
  else asm volatile("s_mov_b32 m0, %0\n\ts_nop 0\n\tbuffer_load_dwordx4 %1, %2, %3 offen lds" :: "s"(l0),"v"(vo),"s"(r),"s"(so) : "memory");
  if(c.wave==0 && c.lane<32){
    const unsigned s2=gbase+4096, l2=lds+4096;
    if(COH) asm volatile("s_mov_b32 m0, %0\n\ts_nop 0\n\tbuffer_load_dwordx4 %1, %2, %3 offen sc1 lds" :: "s"(l2),"v"(vo),"s"(r),"s"(s2) : "memory");
    else asm volatile("s_mov_b32 m0, %0\n\ts_nop 0\n\tbuffer_load_dwordx4 %1, %2, %3 offen lds" :: "s"(l2),"v"(vo),"s"(r),"s"(s2) : "memory");
  }
}
DEVI v2u lds8(unsigned off){ return *(const LDSP v2u*)(uintptr_t)off; }
struct RoleLn2 { Ctx& c; rsrc_t ri,rx,rh,r1; int tg; RowMap m; int o1,o2,o3; f4 ga1,ga2,ga3,cb1,cb2,cb3,sb1,sb2,sb3;
  typedef TileSrc Src; static constexpr int NF=24, NST=6, GS=4096, NDMA=9; static constexpr unsigned TILE=41472;
  MEMF int nseg(){ return 64; } MEMF int seg(int k){ return k; } MEMF int sdiv(){ return 1; }
  MEMF unsigned in_off(int s){ return FL_Y+s*24*128; }
  MEMF Src src(int s,unsigned lds){ return src_linear((unsigned)s*294912u+tg*36864,lds,c.lane,c.wave); }
  MEMF void extra(int s,unsigned lds){ side_dma<false>(c,rx,(unsigned)(s*32+tg*4)*1152u,lds+36864); }
  template<bool PRE> MEMF void work(int s,unsigned buf,int ns,unsigned nbuf,int psig,int polloff){
    const unsigned OOB=0x7ffffff0u; const int l=c.lane, w=c.wave;
    if(PRE){ Src q=src(ns,nbuf); sfor<NDMA>([&](auto I){ dma16c<I*4096,I*GS>(ri,q.lds,q.vo,q.so); }); extra(ns,nbuf); }
    const unsigned xb=buf+36864+m.tk*1152;
    const f4 X1=up4(lds8(xb+o1*2)), X2=up4(lds8(xb+o2*2)), X3=m.v3? up4(lds8(xb+o3*2)) : (f4){0.f,0.f,0.f,0.f};
    WST(3);
    f4 A1,A2,A3;
    part_sum(buf,m,A1,A2,A3);
    A1=X1+ga1*A1; A2=X2+ga2*A2; A3=X3+ga3*A3;
    const float mw=wsum4(hsum4(A1)+hsum4(A2)+hsum4(A3))*(1.f/144.f);
    const f4 d1=A1-mw, d2=A2-mw, d3=A3-mw;
    float q=hsum4(d1*d1)+hsum4(d2*d2)+(m.v3? hsum4(d3*d3) : 0.f);
    q=wsum4(q);
    LDSP float* sp=(LDSP float*)(uintptr_t)(L_CTL+1024)+(s&1)*32;
    if(l<4){ sp[w*8+l*2]=mw; sp[w*8+l*2+1]=q; }
    WST(4);
    asm volatile("s_waitcnt vmcnt(%0) lgkmcnt(0)\n\ts_barrier" :: "n"(PRE? 10 : 0) : "memory");
    WST(5);
    if(psig>=0 && w==1) signal(psig);
    float mm[4],M2=0.f,mean=0.f;
    #pragma unroll
    for(int i=0;i<4;i++){ mm[i]=sp[i*8+m.tk*2]; M2+=sp[i*8+m.tk*2+1]; mean+=mm[i]; }
    mean*=0.25f;
    #pragma unroll
    for(int i=0;i<4;i++){ const float d=mm[i]-mean; M2+=144.f*d*d; }
    const float rs=__builtin_amdgcn_rsqf(M2*(1.f/576.f)+1e-5f);
    WST(6);
    if(!PRE) apoll_issue<NF>(c,polloff>=0,(unsigned)polloff,2);
    const unsigned so=(unsigned)s*36864u+tg*4608+m.tk*16+m.h*8;
    st8c(pk4((A1-mean)*rs*cb1+sb1),rh,so+m.c1*64); st8c(pk4((A2-mean)*rs*cb2+sb2),rh,so+m.c2*64); st8c(pk4((A3-mean)*rs*cb3+sb3),rh,m.v3? so+m.c3*64 : OOB);
    st8c(pk4(A1),r1,so+m.c1*64); st8c(pk4(A2),r1,so+m.c2*64); st8c(pk4(A3),r1,m.v3? so+m.c3*64 : OOB);
  }
  MEMF void signal(int s){ flag_set(c,FL_H2+(s*8+tg)*128); }
  MEMF void dma(int ns,unsigned nbuf){ Src q=src(ns,nbuf); sfor<NDMA>([&](auto I){ dma16c<I*4096,I*GS>(ri,q.lds,q.vo,q.so); }); extra(ns,nbuf); }
  MEMF void poll2(int s1,int row){
    if(c.wave==2 && c.lane<48){ const int sg=s1+(c.lane>=24? 1 : 0); if(sg<64) poll_lds_at(c,in_off(sg)+(c.lane-(c.lane>=24? 24 : 0))*128,L_CTL+row*256); }
  }
  MEMF int eval2(int s1,int row){
    const unsigned fv=*(LDSP volatile unsigned*)(uintptr_t)(L_CTL+row*256+c.lane*4);
    const unsigned long long m=__builtin_amdgcn_ballot_w64(fv==c.tag);
    const bool r1=s1<64 && (m&0xffffffull)==0xffffffull, r2=r1 && s1+1<64 && ((m>>24)&0xffffffull)==0xffffffull;
    return r2? 2 : r1? 1 : 0;
  }
  MEMF void loop(){
    const unsigned OOB=0x7ffffff0u; const int l=c.lane, w=c.wave;
    Pred pr{0u,-1};
    __builtin_amdgcn_s_waitcnt(0);
    if(w==0) wait_pred(c,pr,0,in_off(0),NF,c.tag);
    bar_lds();
    dma(0,0);
    bool early=false;
    int q0=-1, q1=-1;
    int kn=1, fp=-1;
    for(int s=0;s<64;s++){
      const int k=s; (void)k;
      const unsigned buf=(s&1)*TILE, nbuf=TILE-buf;
      RST(0);
      if(early) asm volatile("s_waitcnt vmcnt(%0) lgkmcnt(0)\n\ts_barrier" :: "n"(NST) : "memory"); else bar_all();
      RST(1);
      STAMP(c,136+k);
      if(q0>=0){ if(w==1) signal(q0); STAMP(c,64+q0); }
      q0=q1; q1=-1;
      if(fp>=0){ kn=max(kn,fp+eval2(fp,(s-1)&1)); fp=-1; }
      const bool fast=s+1<kn;
      if(fast){ poll2(s+2,s&1); fp=s+2; dma(s+1,nbuf); }
      else poll2(s+1,s&1);
      const unsigned xb=buf+36864+m.tk*1152;
      const f4 X1=up4(lds8(xb+o1*2)), X2=up4(lds8(xb+o2*2)), X3=m.v3? up4(lds8(xb+o3*2)) : (f4){0.f,0.f,0.f,0.f};
      f4 A1,A2,A3;
      part_sum(buf,m,A1,A2,A3);
      A1=X1+ga1*A1; A2=X2+ga2*A2; A3=X3+ga3*A3;
      const float mw=wsum4(hsum4(A1)+hsum4(A2)+hsum4(A3))*(1.f/144.f);
      const f4 d1=A1-mw, d2=A2-mw, d3=A3-mw;
      float q=hsum4(d1*d1)+hsum4(d2*d2)+(m.v3? hsum4(d3*d3) : 0.f);
      q=wsum4(q);
      LDSP float* sp=(LDSP float*)(uintptr_t)(L_CTL+1024)+(s&1)*32;
      if(l<4){ sp[w*8+l*2]=mw; sp[w*8+l*2+1]=q; }
      WST(4);
      bool go=fast;
      if(fast) bar_lds();
      else {
        bar_all();
        if(q0>=0){ if(w==1) signal(q0); STAMP(c,64+q0); q0=-1; }
        kn=max(kn,s+1+eval2(s+1,s&1));
        go=s+1<kn;
        if(go) dma(s+1,nbuf); else apoll_issue<NF>(c,s+1<64,in_off(s+1),2);
      }
      WST(5);
      float mm[4],M2=0.f,mean=0.f;
      #pragma unroll
      for(int i=0;i<4;i++){ mm[i]=sp[i*8+m.tk*2]; M2+=sp[i*8+m.tk*2+1]; mean+=mm[i]; }
      mean*=0.25f;
      #pragma unroll
      for(int i=0;i<4;i++){ const float d=mm[i]-mean; M2+=144.f*d*d; }
      const float rs=__builtin_amdgcn_rsqf(M2*(1.f/576.f)+1e-5f);
      WST(6);
      const unsigned so=(unsigned)s*36864u+tg*4608+m.tk*16+m.h*8;
      st8c(pk4((A1-mean)*rs*cb1+sb1),rh,so+m.c1*64); st8c(pk4((A2-mean)*rs*cb2+sb2),rh,so+m.c2*64); st8c(pk4((A3-mean)*rs*cb3+sb3),rh,m.v3? so+m.c3*64 : OOB);
      st8c(pk4(A1),r1,so+m.c1*64); st8c(pk4(A2),r1,so+m.c2*64); st8c(pk4(A3),r1,m.v3? so+m.c3*64 : OOB);
      early=go;
      if(go) q1=s;
      else {
        asm volatile("s_waitcnt vmcnt(%0) lgkmcnt(0)\n\ts_barrier" :: "n"(NST) : "memory");
        if(apoll_eval<NF>(c,2)){
          dma(s+1,nbuf);
          asm volatile("s_waitcnt vmcnt(%0) lgkmcnt(0)\n\ts_barrier" :: "n"(NDMA+1) : "memory");
          if(w==1) signal(s);
          STAMP(c,64+s);
        } else {
          apoll_issue<NF>(c,s+1<64,in_off(s+1),3);
          bar_all();
          RST(8);
          if(w==1) signal(s);
          STAMP(c,64+s);
          if(s+1<64){
            if(!apoll_eval<NF>(c,3)){ if(w==0) wait_pred(c,pr,s+1,in_off(s+1),NF,c.tag); bar_lds(); }
            RST(9);
            dma(s+1,nbuf);
          }
        }
        kn=max(kn,s+2);
      }
    }
  }
};
DEVI void role_ln2(Ctx& c,CArgs& a,int tg){
  c.lane=fresh_lane();
  for(int i=0;i<LN2SL;i++) __builtin_amdgcn_s_sleep(32);
  mod_fetch(c,a,2*576,3*576);
  const RowMap m=row_map(c.lane,c.wave);
  const int o1=m.c1*8+m.h*4, o2=m.c2*8+m.h*4, o3=m.c3*8+m.h*4;
  RoleLn2 r{c,mkr(a.ws+O_YP,SZ_PART),mkr(a.x,(size_t)2048*1152),mkr(a.ws+O_H2,SZ_T576),mkr(a.ws+O_X1,SZ_T576),tg,m,o1,o2,o3,
    ldmod(o1),ldmod(o2),ldmod(o3),
    ldmod(2*576+o1)+1.f,ldmod(2*576+o2)+1.f,ldmod(2*576+o3)+1.f,
    ldmod(576+o1),ldmod(576+o2),ldmod(576+o3)};
  bar_all();
  zero_spots(c);
  STAMP(c,63);
  r.loop();
}
struct RoleFin { Ctx& c; rsrc_t ri,r1,ro; int tg; RowMap m; int o1,o2,o3; f4 gb1,gb2,gb3;
  typedef TileSrc Src; static constexpr int NF=24, NST=3, GS=4096, NDMA=9; static constexpr unsigned TILE=41472;
  MEMF int nseg(){ return 64; } MEMF int seg(int k){ return k; } MEMF int sdiv(){ return 1; }
  MEMF unsigned in_off(int s){ return FL_Z+s*24*128; }
  MEMF Src src(int s,unsigned lds){ return src_linear((unsigned)s*294912u+tg*36864,lds,c.lane,c.wave); }
  MEMF void extra(int s,unsigned lds){ side_dma<true>(c,r1,(unsigned)s*36864u+tg*4608,lds+36864); }
  template<bool PRE> MEMF void work(int s,unsigned buf,int ns,unsigned nbuf,int,int polloff){
    if(!PRE) apoll_issue<NF>(c,polloff>=0,(unsigned)polloff,2);
    if(PRE){ Src q=src(ns,nbuf); sfor<NDMA>([&](auto I){ dma16c<I*4096,I*GS>(ri,q.lds,q.vo,q.so); }); extra(ns,nbuf); }
    const unsigned xb=buf+36864+m.tk*16+m.h*8;
    const f4 X1=up4(lds8(xb+m.c1*64)), X2=up4(lds8(xb+m.c2*64)), X3=up4(lds8(xb+m.c3*64));
    f4 A1,A2,A3;
    part_sum(buf,m,A1,A2,A3);
    const unsigned tokoff=(unsigned)(s*32+tg*4+m.tk)*1152u;
    __builtin_amdgcn_raw_buffer_store_b64(pk4(X1+gb1*A1),ro,tokoff+o1*2,0,0);
    __builtin_amdgcn_raw_buffer_store_b64(pk4(X2+gb2*A2),ro,tokoff+o2*2,0,0);
    __builtin_amdgcn_raw_buffer_store_b64(pk4(X3+gb3*A3),ro,m.v3? tokoff+o3*2 : 0x7ffffff0u,0,0);
  }
  MEMF void signal(int){ }
};
DEVI void role_final(Ctx& c,CArgs& a,int tg){
  c.lane=fresh_lane();
  mod_fetch(c,a,5*576,576);
  const RowMap m=row_map(c.lane,c.wave);
  const int o1=m.c1*8+m.h*4, o2=m.c2*8+m.h*4, o3=m.c3*8+m.h*4;
  RoleFin r{c,mkr(a.ws+O_ZP,SZ_PART),mkr(a.ws+O_X1,SZ_T576),mkr(a.out,(size_t)2048*1152),tg,m,o1,o2,o3,
    ldmod(o1),ldmod(o2),ldmod(o3)};
  bar_all();
  zero_spots(c);
  tile_loop(c,r,64);
  if(tg==0 && c.wave==0 && fresh_lane()==0) st4c(c.tag,c.fl,FL_EPOCH,0);
}

__global__ void __launch_bounds__(256,1) k_main(Args a_){
  const int b=blockIdx.x, xcd=b&7, idx=b>>3;
  int wave=ufl(threadIdx.x>>6); asm volatile("" : "+s"(wave));
  if(xcd<4 && idx<16){
    const void* kp=(const void*)__builtin_amdgcn_kernarg_segment_ptr();
    asm volatile(
"s_load_dwordx16 s[8:23], %0, 0\n"
"s_load_dwordx8 s[24:31], %0, 64\n"
"s_load_dword s32, %0, 96\n"
"v_mbcnt_lo_u32_b32 v20, -1, 0\n"
"v_mbcnt_hi_u32_b32 v20, -1, v20\n"
"s_and_b32 s86, %2, 3\n"
"s_lshr_b32 s87, %2, 3\n"
"s_waitcnt lgkmcnt(0)\n"
"s_mov_b32 s40, s24\n"
"s_and_b32 s41, s25, 0xffff\n"
"s_mov_b32 s42, 2097152\n"
"s_mov_b32 s43, 0x20000\n"
"s_mov_b32 s44, s16\n"
"s_and_b32 s45, s17, 0xffff\n"
"s_mov_b32 s46, 16777216\n"
"s_mov_b32 s47, 0x20000\n"
"s_mov_b32 s48, s18\n"
"s_and_b32 s49, s19, 0xffff\n"
"s_mov_b32 s50, 16777216\n"
"s_mov_b32 s51, 0x20000\n"
"s_mov_b32 s52, s20\n"
"s_and_b32 s53, s21, 0xffff\n"
"s_mov_b32 s54, 4194304\n"
"s_mov_b32 s55, 0x20000\n"
"s_mov_b32 s56, s22\n"
"s_and_b32 s57, s23, 0xffff\n"
"s_mov_b32 s58, 4194304\n"
"s_mov_b32 s59, 0x20000\n"
"s_mov_b32 s60, s26\n"
"s_and_b32 s61, s27, 0xffff\n"
"s_mov_b32 s62, 32768\n"
"s_mov_b32 s63, 0x20000\n"
"s_mov_b32 s35, %1\n"
"v_mov_b32 v119, 20480\n"
"buffer_load_dword v119, v119, s[60:63], 0 offen sc1\n"
"s_lshr_b32 s94, s87, 1\n"
"s_lshl_b32 s36, s86, 3\n"
"s_add_u32 s36, s36, s94\n"
"s_and_b32 s37, s87, 1\n"
"s_mov_b32 s39, 0\n"
"s_mov_b32 s33, 0x3e0293ee\n"
"v_lshrrev_b32 v21, 4, v20\n"
"v_and_b32 v22, 15, v20\n"
"v_lshrrev_b32 v23, 2, v22\n"
"v_and_b32 v24, 3, v20\n"
"v_lshlrev_b32 v0, 4, v20\n"
"v_and_b32 v27, 1, v21\n"
"v_lshrrev_b32 v28, 1, v21\n"
"v_and_b32 v29, 7, v20\n"
"s_mul_i32 s94, s35, 32768\n"
"s_add_u32 s94, s94, 32768\n"
"v_add_u32 v1, s94, v0\n"
"s_lshl_b32 s94, s35, 10\n"
"v_add_u32 v7, s94, v0\n"
"s_add_u32 s94, s35, 1\n"
"s_and_b32 s94, s94, 3\n"
"s_lshl_b32 s94, s94, 10\n"
"v_add_u32 v8, s94, v0\n"
"s_add_u32 s94, s35, 2\n"
"s_and_b32 s94, s94, 3\n"
"s_lshl_b32 s94, s94, 10\n"
"v_add_u32 v9, s94, v0\n"
"s_add_u32 s94, s35, 3\n"
"s_and_b32 s94, s94, 3\n"
"s_lshl_b32 s94, s94, 10\n"
"v_add_u32 v10, s94, v0\n"
"v_cmp_gt_u32_e64 s[96:97], 32, v20\n"
"v_cmp_lt_u32_e64 s[98:99], 7, v22\n"
"v_cmp_gt_u32_e64 s[16:17], 8, v22\n"
"v_mov_b32 v25, 0x4444\n"
"v_mov_b32 v26, 0xeeee\n"
"s_nop 1\n"
"v_cndmask_b32_e64 v2, v25, v26, s[98:99]\n"
"v_mov_b32 v25, 0\n"
"v_mov_b32 v26, 0x3f803f80\n"
"v_cndmask_b32_e64 v12, v25, v26, s[96:97]\n"
"v_mov_b32 v13, v12\n"
"v_mov_b32 v14, v26\n"
"v_mov_b32 v15, v26\n"
"v_mov_b32 v16, v26\n"
"v_mov_b32 v17, v26\n"
"v_lshl_add_u32 v25, v21, 3, v23\n"
"v_lshlrev_b32 v25, 4, v25\n"
"v_and_b32 v26, 1, v24\n"
"v_lshl_add_u32 v25, v26, 3, v25\n"
"v_add_u32 v3, 9216, v25\n"
"v_lshl_add_u32 v25, v28, 4, v22\n"
"s_lshl_b32 s94, s35, 5\n"
"v_add_u32 v25, s94, v25\n"
"v_lshlrev_b32 v4, 4, v25\n"
"v_lshl_add_u32 v4, v27, 3, v4\n"
"v_add_u32 v4, 9216, v4\n"
"v_lshrrev_b32 v26, 3, v22\n"
"s_lshl_b32 s94, s35, 1\n"
"v_add_u32 v25, s94, v26\n"
"v_lshl_add_u32 v25, v25, 1, v28\n"
"v_lshlrev_b32 v5, 7, v25\n"
"v_lshl_add_u32 v5, v29, 4, v5\n"
"v_lshl_add_u32 v5, v27, 3, v5\n"
"v_add_u32 v5, 13312, v5\n"
"v_lshlrev_b32 v6, 14, v28\n"
"v_lshl_add_u32 v6, v26, 12, v6\n"
"v_lshl_add_u32 v6, v29, 4, v6\n"
"v_lshl_add_u32 v6, v27, 3, v6\n"
"v_mov_b32 v11, 0x7ffffff0\n"
"v_add_u32 v126, 13312, v0\n"
"s_cmp_eq_u32 s35, 3\n"
"s_cbranch_scc0 Lnv2_%=\n"
"v_mov_b32 v11, v0\n"
"v_add_u32 v126, 8192, v0\n"
"Lnv2_%=:\n"
"s_lshl_b32 s94, s86, 4\n"
"s_add_u32 s94, s94, s87\n"
"s_lshl_b32 s95, s86, 1\n"
"s_add_u32 s95, s95, s87\n"
"s_add_u32 s95, s95, 64\n"
"s_cmp_eq_u32 s35, 0\n"
"s_cselect_b32 s27, s94, s95\n"
"s_cselect_b32 s38, 1, 0\n"
"s_cmp_eq_u32 s35, 1\n"
"s_cselect_b32 s95, 1, 0\n"
"s_cmp_lt_u32 s87, 2\n"
"s_cselect_b32 s95, s95, 0\n"
"s_or_b32 s38, s38, s95\n"
"s_lshl_b32 s30, s35, 2\n"
"s_mul_i32 s94, s35, 5\n"
"s_sub_u32 s94, s94, 2\n"
"s_cmp_lt_u32 s35, 2\n"
"s_cselect_b32 s30, s30, s94\n"
"s_lshl_b32 s25, s36, 8\n"
"s_lshl_b32 s84, s35, 9\n"
"v_and_b32 v105, 15, v20\n"
"v_lshrrev_b32 v106, 3, v105\n"
"v_lshrrev_b32 v107, 5, v20\n"
"v_lshl_add_u32 v106, v107, 1, v106\n"
"s_cmp_ge_u32 s27, 64\n"
"s_cselect_b32 s94, 10, 14\n"
"v_lshlrev_b32 v106, s94, v106\n"
"v_and_b32 v105, 7, v20\n"
"v_lshl_add_u32 v106, v105, 4, v106\n"
"v_lshrrev_b32 v107, 4, v20\n"
"v_and_b32 v107, 1, v107\n"
"v_lshl_add_u32 v127, v107, 3, v106\n"
"s_mov_b32 s85, -1\n"
"s_cmp_eq_u32 s38, 0\n"
"s_cbranch_scc0 Lisred_%=\n"
"v_mov_b32 v127, 0x7ffffff0\n"
"s_cmp_eq_u32 s35, 2\n"
"s_cbranch_scc0 Lisred_%=\n"
"s_cmp_eq_u32 s37, 0\n"
"s_cbranch_scc0 Lisred_%=\n"
"s_mov_b32 s85, 1\n"
"v_cmp_eq_u32_e32 vcc, 0, v20\n"
"v_mov_b32 v105, 0\n"
"s_nop 1\n"
"v_cndmask_b32_e32 v127, v127, v105, vcc\n"
"Lisred_%=:\n"
"s_mul_i32 s26, s36, 520\n"
"s_add_u32 s26, s26, 2048000\n"
"v_mov_b32 v30, 0\n"
"v_mov_b32 v31, 0x3f80\n"
"v_cmp_eq_u32_e32 vcc, 0, v29\n"
"s_nop 1\n"
"v_cndmask_b32_e32 v120, v30, v31, vcc\n"
"v_mov_b32 v31, 0x3f800000\n"
"v_cmp_eq_u32_e32 vcc, 1, v29\n"
"s_nop 1\n"
"v_cndmask_b32_e32 v120, v120, v31, vcc\n"
"v_mov_b32 v30, 0\n"
"v_mov_b32 v31, 0x3f80\n"
"v_cmp_eq_u32_e32 vcc, 2, v29\n"
"s_nop 1\n"
"v_cndmask_b32_e32 v121, v30, v31, vcc\n"
"v_mov_b32 v31, 0x3f800000\n"
"v_cmp_eq_u32_e32 vcc, 3, v29\n"
"s_nop 1\n"
"v_cndmask_b32_e32 v121, v121, v31, vcc\n"
"v_mov_b32 v30, 0\n"
"v_mov_b32 v31, 0x3f80\n"
"v_cmp_eq_u32_e32 vcc, 4, v29\n"
"s_nop 1\n"
"v_cndmask_b32_e32 v122, v30, v31, vcc\n"
"v_mov_b32 v31, 0x3f800000\n"
"v_cmp_eq_u32_e32 vcc, 5, v29\n"
"s_nop 1\n"
"v_cndmask_b32_e32 v122, v122, v31, vcc\n"
"v_mov_b32 v30, 0\n"
"v_mov_b32 v31, 0x3f80\n"
"v_cmp_eq_u32_e32 vcc, 6, v29\n"
"s_nop 1\n"
"v_cndmask_b32_e32 v123, v30, v31, vcc\n"
"v_mov_b32 v31, 0x3f800000\n"
"v_cmp_eq_u32_e32 vcc, 7, v29\n"
"s_nop 1\n"
"v_cndmask_b32_e32 v123, v123, v31, vcc\n"
"s_mov_b32 s20, s14\n"
"s_and_b32 s21, s15, 0xffff\n"
"s_mov_b32 s22, 4718592\n"
"s_mov_b32 s23, 0x20000\n"
"s_mul_i32 s94, s36, 147456\n"
"s_mul_i32 s95, s37, 576\n"
"s_add_u32 s94, s94, s95\n"
"s_mul_i32 s95, s35, 1152\n"
"v_add_u32 v30, s95, v20\n"
"s_mul_i32 s24, s35, 18432\n"
"s_add_u32 s24, s24, 32768\n"
"v_add_u32 v31, 0, v30\n"
"v_mul_u32_u24 v32, 0xe38f, v31\n"
"v_lshrrev_b32 v32, 21, v32\n"
"v_mul_u32_u24 v33, 36, v32\n"
"v_sub_u32 v33, v31, v33\n"
"v_mul_u32_u24 v32, 0x480, v32\n"
"v_lshl_add_u32 v32, v33, 4, v32\n"
"s_add_u32 m0, s24, 0\n"
"s_nop 0\n"
"buffer_load_dwordx4 v32, s[20:23], s94 offen lds\n"
"v_add_u32 v31, 64, v30\n"
"v_mul_u32_u24 v32, 0xe38f, v31\n"
"v_lshrrev_b32 v32, 21, v32\n"
"v_mul_u32_u24 v33, 36, v32\n"
"v_sub_u32 v33, v31, v33\n"
"v_mul_u32_u24 v32, 0x480, v32\n"
"v_lshl_add_u32 v32, v33, 4, v32\n"
"s_add_u32 m0, s24, 1024\n"
"s_nop 0\n"
"buffer_load_dwordx4 v32, s[20:23], s94 offen lds\n"
"v_add_u32 v31, 128, v30\n"
"v_mul_u32_u24 v32, 0xe38f, v31\n"
"v_lshrrev_b32 v32, 21, v32\n"
"v_mul_u32_u24 v33, 36, v32\n"
"v_sub_u32 v33, v31, v33\n"
"v_mul_u32_u24 v32, 0x480, v32\n"
"v_lshl_add_u32 v32, v33, 4, v32\n"
"s_add_u32 m0, s24, 2048\n"
"s_nop 0\n"
"buffer_load_dwordx4 v32, s[20:23], s94 offen lds\n"
"v_add_u32 v31, 192, v30\n"
"v_mul_u32_u24 v32, 0xe38f, v31\n"
"v_lshrrev_b32 v32, 21, v32\n"
"v_mul_u32_u24 v33, 36, v32\n"
"v_sub_u32 v33, v31, v33\n"
"v_mul_u32_u24 v32, 0x480, v32\n"
"v_lshl_add_u32 v32, v33, 4, v32\n"
"s_add_u32 m0, s24, 3072\n"
"s_nop 0\n"
"buffer_load_dwordx4 v32, s[20:23], s94 offen lds\n"
"v_add_u32 v31, 256, v30\n"
"v_mul_u32_u24 v32, 0xe38f, v31\n"
"v_lshrrev_b32 v32, 21, v32\n"
"v_mul_u32_u24 v33, 36, v32\n"
"v_sub_u32 v33, v31, v33\n"
"v_mul_u32_u24 v32, 0x480, v32\n"
"v_lshl_add_u32 v32, v33, 4, v32\n"
"s_add_u32 m0, s24, 4096\n"
"s_nop 0\n"
"buffer_load_dwordx4 v32, s[20:23], s94 offen lds\n"
"v_add_u32 v31, 320, v30\n"
"v_mul_u32_u24 v32, 0xe38f, v31\n"
"v_lshrrev_b32 v32, 21, v32\n"
"v_mul_u32_u24 v33, 36, v32\n"
"v_sub_u32 v33, v31, v33\n"
"v_mul_u32_u24 v32, 0x480, v32\n"
"v_lshl_add_u32 v32, v33, 4, v32\n"
"s_add_u32 m0, s24, 5120\n"
"s_nop 0\n"
"buffer_load_dwordx4 v32, s[20:23], s94 offen lds\n"
"v_add_u32 v31, 384, v30\n"
"v_mul_u32_u24 v32, 0xe38f, v31\n"
"v_lshrrev_b32 v32, 21, v32\n"
"v_mul_u32_u24 v33, 36, v32\n"
"v_sub_u32 v33, v31, v33\n"
"v_mul_u32_u24 v32, 0x480, v32\n"
"v_lshl_add_u32 v32, v33, 4, v32\n"
"s_add_u32 m0, s24, 6144\n"
"s_nop 0\n"
"buffer_load_dwordx4 v32, s[20:23], s94 offen lds\n"
"v_add_u32 v31, 448, v30\n"
"v_mul_u32_u24 v32, 0xe38f, v31\n"
"v_lshrrev_b32 v32, 21, v32\n"
"v_mul_u32_u24 v33, 36, v32\n"
"v_sub_u32 v33, v31, v33\n"
"v_mul_u32_u24 v32, 0x480, v32\n"
"v_lshl_add_u32 v32, v33, 4, v32\n"
"s_add_u32 m0, s24, 7168\n"
"s_nop 0\n"
"buffer_load_dwordx4 v32, s[20:23], s94 offen lds\n"
"v_add_u32 v31, 512, v30\n"
"v_mul_u32_u24 v32, 0xe38f, v31\n"
"v_lshrrev_b32 v32, 21, v32\n"
"v_mul_u32_u24 v33, 36, v32\n"
"v_sub_u32 v33, v31, v33\n"
"v_mul_u32_u24 v32, 0x480, v32\n"
"v_lshl_add_u32 v32, v33, 4, v32\n"
"s_add_u32 m0, s24, 8192\n"
"s_nop 0\n"
"buffer_load_dwordx4 v32, s[20:23], s94 offen lds\n"
"v_add_u32 v31, 576, v30\n"
"v_mul_u32_u24 v32, 0xe38f, v31\n"
"v_lshrrev_b32 v32, 21, v32\n"
"v_mul_u32_u24 v33, 36, v32\n"
"v_sub_u32 v33, v31, v33\n"
"v_mul_u32_u24 v32, 0x480, v32\n"
"v_lshl_add_u32 v32, v33, 4, v32\n"
"s_add_u32 m0, s24, 9216\n"
"s_nop 0\n"
"buffer_load_dwordx4 v32, s[20:23], s94 offen lds\n"
"v_add_u32 v31, 640, v30\n"
"v_mul_u32_u24 v32, 0xe38f, v31\n"
"v_lshrrev_b32 v32, 21, v32\n"
"v_mul_u32_u24 v33, 36, v32\n"
"v_sub_u32 v33, v31, v33\n"
"v_mul_u32_u24 v32, 0x480, v32\n"
"v_lshl_add_u32 v32, v33, 4, v32\n"
"s_add_u32 m0, s24, 10240\n"
"s_nop 0\n"
"buffer_load_dwordx4 v32, s[20:23], s94 offen lds\n"
"v_add_u32 v31, 704, v30\n"
"v_mul_u32_u24 v32, 0xe38f, v31\n"
"v_lshrrev_b32 v32, 21, v32\n"
"v_mul_u32_u24 v33, 36, v32\n"
"v_sub_u32 v33, v31, v33\n"
"v_mul_u32_u24 v32, 0x480, v32\n"
"v_lshl_add_u32 v32, v33, 4, v32\n"
"s_add_u32 m0, s24, 11264\n"
"s_nop 0\n"
"buffer_load_dwordx4 v32, s[20:23], s94 offen lds\n"
"v_add_u32 v31, 768, v30\n"
"v_mul_u32_u24 v32, 0xe38f, v31\n"
"v_lshrrev_b32 v32, 21, v32\n"
"v_mul_u32_u24 v33, 36, v32\n"
"v_sub_u32 v33, v31, v33\n"
"v_mul_u32_u24 v32, 0x480, v32\n"
"v_lshl_add_u32 v32, v33, 4, v32\n"
"s_add_u32 m0, s24, 12288\n"
"s_nop 0\n"
"buffer_load_dwordx4 v32, s[20:23], s94 offen lds\n"
"v_add_u32 v31, 832, v30\n"
"v_mul_u32_u24 v32, 0xe38f, v31\n"
"v_lshrrev_b32 v32, 21, v32\n"
"v_mul_u32_u24 v33, 36, v32\n"
"v_sub_u32 v33, v31, v33\n"
"v_mul_u32_u24 v32, 0x480, v32\n"
"v_lshl_add_u32 v32, v33, 4, v32\n"
"s_add_u32 m0, s24, 13312\n"
"s_nop 0\n"
"buffer_load_dwordx4 v32, s[20:23], s94 offen lds\n"
"v_add_u32 v31, 896, v30\n"
"v_mul_u32_u24 v32, 0xe38f, v31\n"
"v_lshrrev_b32 v32, 21, v32\n"
"v_mul_u32_u24 v33, 36, v32\n"
"v_sub_u32 v33, v31, v33\n"
"v_mul_u32_u24 v32, 0x480, v32\n"
"v_lshl_add_u32 v32, v33, 4, v32\n"
"s_add_u32 m0, s24, 14336\n"
"s_nop 0\n"
"buffer_load_dwordx4 v32, s[20:23], s94 offen lds\n"
"v_add_u32 v31, 960, v30\n"
"v_mul_u32_u24 v32, 0xe38f, v31\n"
"v_lshrrev_b32 v32, 21, v32\n"
"v_mul_u32_u24 v33, 36, v32\n"
"v_sub_u32 v33, v31, v33\n"
"v_mul_u32_u24 v32, 0x480, v32\n"
"v_lshl_add_u32 v32, v33, 4, v32\n"
"s_add_u32 m0, s24, 15360\n"
"s_nop 0\n"
"buffer_load_dwordx4 v32, s[20:23], s94 offen lds\n"
"v_add_u32 v31, 1024, v30\n"
"v_mul_u32_u24 v32, 0xe38f, v31\n"
"v_lshrrev_b32 v32, 21, v32\n"
"v_mul_u32_u24 v33, 36, v32\n"
"v_sub_u32 v33, v31, v33\n"
"v_mul_u32_u24 v32, 0x480, v32\n"
"v_lshl_add_u32 v32, v33, 4, v32\n"
"s_add_u32 m0, s24, 16384\n"
"s_nop 0\n"
"buffer_load_dwordx4 v32, s[20:23], s94 offen lds\n"
"v_add_u32 v31, 1088, v30\n"
"v_mul_u32_u24 v32, 0xe38f, v31\n"
"v_lshrrev_b32 v32, 21, v32\n"
"v_mul_u32_u24 v33, 36, v32\n"
"v_sub_u32 v33, v31, v33\n"
"v_mul_u32_u24 v32, 0x480, v32\n"
"v_lshl_add_u32 v32, v33, 4, v32\n"
"s_add_u32 m0, s24, 17408\n"
"s_nop 0\n"
"buffer_load_dwordx4 v32, s[20:23], s94 offen lds\n"
"s_lshl_b32 s95, s35, 3\n"
"s_add_u32 s95, s95, 0\n"
"v_add_u32 v31, s95, v21\n"
"v_lshlrev_b32 v31, 13, v31\n"
"s_lshl_b32 s95, s35, 1\n"
"s_add_u32 s95, s95, 0\n"
"s_and_b32 s95, s95, 3\n"
"s_lshl_b32 s95, s95, 2\n"
"v_lshlrev_b32 v32, 1, v27\n"
"v_add_u32 v32, s95, v32\n"
"v_xor_b32 v32, v32, v22\n"
"v_lshl_add_u32 v34, v32, 4, v31\n"
"s_lshl_b32 s95, s35, 3\n"
"s_add_u32 s95, s95, 4\n"
"v_add_u32 v31, s95, v21\n"
"v_lshlrev_b32 v31, 13, v31\n"
"s_lshl_b32 s95, s35, 1\n"
"s_add_u32 s95, s95, 1\n"
"s_and_b32 s95, s95, 3\n"
"s_lshl_b32 s95, s95, 2\n"
"v_lshlrev_b32 v32, 1, v27\n"
"v_add_u32 v32, s95, v32\n"
"v_xor_b32 v32, v32, v22\n"
"v_lshl_add_u32 v35, v32, 4, v31\n"
"s_lshl_b32 s24, s35, 11\n"
"s_mov_b32 s20, s8\n"
"s_and_b32 s21, s9, 0xffff\n"
"s_mov_b32 s22, 4718592\n"
"s_mov_b32 s23, 0x20000\n"
"s_lshl_b32 s94, s36, 8\n"
"s_add_u32 s94, s94, 0\n"
"s_add_u32 m0, s24, 118784\n"
"s_nop 0\n"
"buffer_load_dwordx4 v34, s[20:23], s94 offen lds\n"
"s_add_u32 m0, s24, 119808\n"
"s_nop 0\n"
"buffer_load_dwordx4 v35, s[20:23], s94 offen lds\n"
"s_lshl_b32 s94, s36, 8\n"
"s_add_u32 s94, s94, 262144\n"
"s_add_u32 m0, s24, 151552\n"
"s_nop 0\n"
"buffer_load_dwordx4 v34, s[20:23], s94 offen lds\n"
"s_add_u32 m0, s24, 152576\n"
"s_nop 0\n"
"buffer_load_dwordx4 v35, s[20:23], s94 offen lds\n"
"s_lshl_b32 s94, s36, 8\n"
"s_add_u32 s94, s94, 524288\n"
"s_add_u32 m0, s24, 0\n"
"s_nop 0\n"
"buffer_load_dwordx4 v34, s[20:23], s94 offen lds\n"
"s_add_u32 m0, s24, 1024\n"
"s_nop 0\n"
"buffer_load_dwordx4 v35, s[20:23], s94 offen lds\n"
"s_lshl_b32 s94, s36, 8\n"
"s_add_u32 s94, s94, 786432\n"
"s_add_u32 m0, s24, 8192\n"
"s_nop 0\n"
"buffer_load_dwordx4 v34, s[20:23], s94 offen lds\n"
"s_add_u32 m0, s24, 9216\n"
"s_nop 0\n"
"buffer_load_dwordx4 v35, s[20:23], s94 offen lds\n"
"s_lshl_b32 s94, s36, 8\n"
"s_add_u32 s94, s94, 1048576\n"
"s_add_u32 m0, s24, 16384\n"
"s_nop 0\n"
"buffer_load_dwordx4 v34, s[20:23], s94 offen lds\n"
"s_add_u32 m0, s24, 17408\n"
"s_nop 0\n"
"buffer_load_dwordx4 v35, s[20:23], s94 offen lds\n"
"s_lshl_b32 s94, s36, 8\n"
"s_add_u32 s94, s94, 1310720\n"
"s_add_u32 m0, s24, 24576\n"
"s_nop 0\n"
"buffer_load_dwordx4 v34, s[20:23], s94 offen lds\n"
"s_add_u32 m0, s24, 25600\n"
"s_nop 0\n"
"buffer_load_dwordx4 v35, s[20:23], s94 offen lds\n"
"s_waitcnt vmcnt(12)\n"
"s_barrier\n"
"v_lshlrev_b32 v38, 4, v28\n"
"v_lshl_add_u32 v38, v27, 2, v38\n"
"v_and_b32 v31, 1, v23\n"
"v_add_u32 v38, v38, v31\n"
"v_lshrrev_b32 v32, 1, v23\n"
"v_lshl_add_u32 v38, v32, 3, v38\n"
"s_lshl_b32 s94, s30, 5\n"
"s_add_u32 s94, s94, 32768\n"
"v_mul_u32_u24 v39, 0x240, v38\n"
"v_lshl_add_u32 v39, v24, 3, v39\n"
"v_add_u32 v39, s94, v39\n"
"v_add_u32 v40, 36864, v39\n"
"ds_read_b64_tr_b16 v[192:193], v39\n"
"ds_read_b64_tr_b16 v[194:195], v39 offset:1152\n"
"ds_read_b64_tr_b16 v[196:197], v39 offset:18432\n"
"ds_read_b64_tr_b16 v[198:199], v39 offset:19584\n"
"ds_read_b64_tr_b16 v[208:209], v39 offset:32\n"
"ds_read_b64_tr_b16 v[210:211], v39 offset:1184\n"
"ds_read_b64_tr_b16 v[212:213], v39 offset:18464\n"
"ds_read_b64_tr_b16 v[214:215], v39 offset:19616\n"
"ds_read_b64_tr_b16 v[224:225], v39 offset:64\n"
"ds_read_b64_tr_b16 v[226:227], v39 offset:1216\n"
"ds_read_b64_tr_b16 v[228:229], v39 offset:18496\n"
"ds_read_b64_tr_b16 v[230:231], v39 offset:19648\n"
"ds_read_b64_tr_b16 v[240:241], v39 offset:96\n"
"ds_read_b64_tr_b16 v[242:243], v39 offset:1248\n"
"ds_read_b64_tr_b16 v[244:245], v39 offset:18528\n"
"s_waitcnt lgkmcnt(8)\n"
"ds_read_b64_tr_b16 v[246:247], v39 offset:19680\n"
"ds_read_b64_tr_b16 v[48:49], v39 offset:128\n"
"ds_read_b64_tr_b16 v[50:51], v39 offset:1280\n"
"ds_read_b64_tr_b16 v[52:53], v39 offset:18560\n"
"ds_read_b64_tr_b16 v[54:55], v39 offset:19712\n"
"s_waitcnt lgkmcnt(0)\n"
"ds_read_b64_tr_b16 v[200:201], v40\n"
"ds_read_b64_tr_b16 v[202:203], v40 offset:1152\n"
"ds_read_b64_tr_b16 v[204:205], v40 offset:18432\n"
"ds_read_b64_tr_b16 v[206:207], v40 offset:19584\n"
"ds_read_b64_tr_b16 v[216:217], v40 offset:32\n"
"ds_read_b64_tr_b16 v[218:219], v40 offset:1184\n"
"ds_read_b64_tr_b16 v[220:221], v40 offset:18464\n"
"ds_read_b64_tr_b16 v[222:223], v40 offset:19616\n"
"ds_read_b64_tr_b16 v[232:233], v40 offset:64\n"
"ds_read_b64_tr_b16 v[234:235], v40 offset:1216\n"
"ds_read_b64_tr_b16 v[236:237], v40 offset:18496\n"
"ds_read_b64_tr_b16 v[238:239], v40 offset:19648\n"
"ds_read_b64_tr_b16 v[248:249], v40 offset:96\n"
"ds_read_b64_tr_b16 v[250:251], v40 offset:1248\n"
"ds_read_b64_tr_b16 v[252:253], v40 offset:18528\n"
"s_waitcnt lgkmcnt(8)\n"
"ds_read_b64_tr_b16 v[254:255], v40 offset:19680\n"
"ds_read_b64_tr_b16 v[56:57], v40 offset:128\n"
"ds_read_b64_tr_b16 v[58:59], v40 offset:1280\n"
"ds_read_b64_tr_b16 v[60:61], v40 offset:18560\n"
"ds_read_b64_tr_b16 v[62:63], v40 offset:19712\n"
"s_waitcnt lgkmcnt(0)\n"
"v_lshl_add_u32 v32, v32, 1, v27\n"
"v_lshl_add_u32 v31, v32, 1, v31\n"
"v_lshlrev_b32 v31, 1, v31\n"
"v_lshrrev_b32 v32, 1, v24\n"
"s_lshl_b32 s94, s35, 2\n"
"v_add_u32 v32, s94, v32\n"
"v_and_b32 v33, 1, v24\n"
"v_add_u32 v43, 0, v32\n"
"v_xor_b32 v43, v43, v31\n"
"v_lshlrev_b32 v43, 4, v43\n"
"v_lshl_add_u32 v43, v33, 3, v43\n"
"v_lshl_add_u32 v41, v38, 8, v43\n"
"v_add_u32 v43, 2, v32\n"
"v_xor_b32 v43, v43, v31\n"
"v_lshlrev_b32 v43, 4, v43\n"
"v_lshl_add_u32 v43, v33, 3, v43\n"
"v_lshl_add_u32 v42, v38, 8, v43\n"
"v_add_u32 v44, 0x10000, v41\n"
"v_add_u32 v45, 0x10000, v42\n"
"v_add_u32 v46, 0x20000, v41\n"
"v_add_u32 v47, 0x20000, v42\n"
"s_waitcnt vmcnt(10)\n"
"s_waitcnt lgkmcnt(0)\n"
"s_barrier\n"
"s_lshl_b32 s94, s36, 8\n"
"s_add_u32 s94, s94, 1572864\n"
"s_add_u32 m0, s24, 53248\n"
"s_nop 0\n"
"buffer_load_dwordx4 v34, s[20:23], s94 offen lds\n"
"s_add_u32 m0, s24, 54272\n"
"s_nop 0\n"
"buffer_load_dwordx4 v35, s[20:23], s94 offen lds\n"
"s_lshl_b32 s94, s36, 8\n"
"s_add_u32 s94, s94, 1835008\n"
"s_add_u32 m0, s24, 86016\n"
"s_nop 0\n"
"buffer_load_dwordx4 v34, s[20:23], s94 offen lds\n"
"s_add_u32 m0, s24, 87040\n"
"s_nop 0\n"
"buffer_load_dwordx4 v35, s[20:23], s94 offen lds\n"
"s_cmp_eq_u32 s35, 0\n"
"s_cbranch_scc0 Lp0_0_%=\n"
"ds_read_b64_tr_b16 a[0:1], v44 offset:53248\n"
"ds_read_b64_tr_b16 a[2:3], v44 offset:53760\n"
"ds_read_b64_tr_b16 v[64:65], v45 offset:53248\n"
"ds_read_b64_tr_b16 v[66:67], v45 offset:53760\n"
"s_waitcnt lgkmcnt(0)\n"
"ds_write_b128 v1, v[64:67]\n"
"s_branch Lq0_%=\n"
"Lp0_0_%=:\n"
"s_cmp_eq_u32 s35, 1\n"
"s_cbranch_scc0 Lp0_1_%=\n"
"ds_read_b64_tr_b16 a[32:33], v44 offset:53248\n"
"ds_read_b64_tr_b16 a[34:35], v44 offset:53760\n"
"ds_read_b64_tr_b16 v[64:65], v45 offset:53248\n"
"ds_read_b64_tr_b16 v[66:67], v45 offset:53760\n"
"s_waitcnt lgkmcnt(0)\n"
"ds_write_b128 v1, v[64:67] offset:4096\n"
"s_branch Lq0_%=\n"
"Lp0_1_%=:\n"
"s_cmp_eq_u32 s35, 2\n"
"s_cbranch_scc0 Lp0_2_%=\n"
"ds_read_b64_tr_b16 a[24:25], v44 offset:53248\n"
"ds_read_b64_tr_b16 a[26:27], v44 offset:53760\n"
"ds_read_b64_tr_b16 a[224:225], v45 offset:53248\n"
"ds_read_b64_tr_b16 a[226:227], v45 offset:53760\n"
"s_branch Lq0_%=\n"
"Lp0_2_%=:\n"
"ds_read_b64_tr_b16 a[16:17], v44 offset:53248\n"
"ds_read_b64_tr_b16 a[18:19], v44 offset:53760\n"
"ds_read_b64_tr_b16 v[64:65], v45 offset:53248\n"
"ds_read_b64_tr_b16 v[66:67], v45 offset:53760\n"
"s_waitcnt lgkmcnt(0)\n"
"ds_write_b128 v1, v[64:67] offset:2048\n"
"Lq0_%=:\n"
"s_waitcnt vmcnt(12)\n"
"s_waitcnt lgkmcnt(0)\n"
"s_barrier\n"
"s_lshl_b32 s94, s36, 8\n"
"s_add_u32 s94, s94, 2097152\n"
"s_add_u32 m0, s24, 118784\n"
"s_nop 0\n"
"buffer_load_dwordx4 v34, s[20:23], s94 offen lds\n"
"s_add_u32 m0, s24, 119808\n"
"s_nop 0\n"
"buffer_load_dwordx4 v35, s[20:23], s94 offen lds\n"
"s_cmp_eq_u32 s35, 0\n"
"s_cbranch_scc0 Lp1_0_%=\n"
"ds_read_b64_tr_b16 a[4:5], v46 offset:20480\n"
"ds_read_b64_tr_b16 a[6:7], v46 offset:20992\n"
"ds_read_b64_tr_b16 v[68:69], v47 offset:20480\n"
"ds_read_b64_tr_b16 v[70:71], v47 offset:20992\n"
"s_waitcnt lgkmcnt(0)\n"
"ds_write_b128 v1, v[68:71] offset:1024\n"
"s_branch Lq1_%=\n"
"Lp1_0_%=:\n"
"s_cmp_eq_u32 s35, 1\n"
"s_cbranch_scc0 Lp1_1_%=\n"
"ds_read_b64_tr_b16 a[36:37], v46 offset:20480\n"
"ds_read_b64_tr_b16 a[38:39], v46 offset:20992\n"
"ds_read_b64_tr_b16 v[68:69], v47 offset:20480\n"
"ds_read_b64_tr_b16 v[70:71], v47 offset:20992\n"
"s_waitcnt lgkmcnt(0)\n"
"ds_write_b128 v1, v[68:71] offset:5120\n"
"s_branch Lq1_%=\n"
"Lp1_1_%=:\n"
"s_cmp_eq_u32 s35, 2\n"
"s_cbranch_scc0 Lp1_2_%=\n"
"ds_read_b64_tr_b16 a[28:29], v46 offset:20480\n"
"ds_read_b64_tr_b16 a[30:31], v46 offset:20992\n"
"ds_read_b64_tr_b16 a[228:229], v47 offset:20480\n"
"ds_read_b64_tr_b16 a[230:231], v47 offset:20992\n"
"s_branch Lq1_%=\n"
"Lp1_2_%=:\n"
"ds_read_b64_tr_b16 a[20:21], v46 offset:20480\n"
"ds_read_b64_tr_b16 a[22:23], v46 offset:20992\n"
"ds_read_b64_tr_b16 v[68:69], v47 offset:20480\n"
"ds_read_b64_tr_b16 v[70:71], v47 offset:20992\n"
"s_waitcnt lgkmcnt(0)\n"
"ds_write_b128 v1, v[68:71] offset:3072\n"
"Lq1_%=:\n"
"s_waitcnt vmcnt(12)\n"
"s_waitcnt lgkmcnt(0)\n"
"s_barrier\n"
"s_lshl_b32 s94, s36, 8\n"
"s_add_u32 s94, s94, 2359296\n"
"s_add_u32 m0, s24, 151552\n"
"s_nop 0\n"
"buffer_load_dwordx4 v34, s[20:23], s94 offen lds\n"
"s_add_u32 m0, s24, 152576\n"
"s_nop 0\n"
"buffer_load_dwordx4 v35, s[20:23], s94 offen lds\n"
"s_cmp_eq_u32 s35, 0\n"
"s_cbranch_scc0 Lp2_0_%=\n"
"ds_read_b64_tr_b16 a[16:17], v41\n"
"ds_read_b64_tr_b16 a[18:19], v41 offset:512\n"
"ds_read_b64_tr_b16 v[64:65], v42\n"
"ds_read_b64_tr_b16 v[66:67], v42 offset:512\n"
"s_waitcnt lgkmcnt(0)\n"
"ds_write_b128 v1, v[64:67] offset:2048\n"
"s_branch Lq2_%=\n"
"Lp2_0_%=:\n"
"s_cmp_eq_u32 s35, 1\n"
"s_cbranch_scc0 Lp2_1_%=\n"
"ds_read_b64_tr_b16 a[0:1], v41\n"
"ds_read_b64_tr_b16 a[2:3], v41 offset:512\n"
"ds_read_b64_tr_b16 v[64:65], v42\n"
"ds_read_b64_tr_b16 v[66:67], v42 offset:512\n"
"s_waitcnt lgkmcnt(0)\n"
"ds_write_b128 v1, v[64:67]\n"
"s_branch Lq2_%=\n"
"Lp2_1_%=:\n"
"s_cmp_eq_u32 s35, 2\n"
"s_cbranch_scc0 Lp2_2_%=\n"
"ds_read_b64_tr_b16 a[32:33], v41\n"
"ds_read_b64_tr_b16 a[34:35], v41 offset:512\n"
"ds_read_b64_tr_b16 v[64:65], v42\n"
"ds_read_b64_tr_b16 v[66:67], v42 offset:512\n"
"s_waitcnt lgkmcnt(0)\n"
"ds_write_b128 v1, v[64:67] offset:4096\n"
"s_branch Lq2_%=\n"
"Lp2_2_%=:\n"
"ds_read_b64_tr_b16 a[24:25], v41\n"
"ds_read_b64_tr_b16 a[26:27], v41 offset:512\n"
"ds_read_b64_tr_b16 a[224:225], v42\n"
"ds_read_b64_tr_b16 a[226:227], v42 offset:512\n"
"Lq2_%=:\n"
"s_waitcnt vmcnt(12)\n"
"s_waitcnt lgkmcnt(0)\n"
"s_barrier\n"
"s_lshl_b32 s94, s36, 8\n"
"s_add_u32 s94, s94, 2621440\n"
"s_add_u32 m0, s24, 0\n"
"s_nop 0\n"
"buffer_load_dwordx4 v34, s[20:23], s94 offen lds\n"
"s_add_u32 m0, s24, 1024\n"
"s_nop 0\n"
"buffer_load_dwordx4 v35, s[20:23], s94 offen lds\n"
"s_cmp_eq_u32 s35, 0\n"
"s_cbranch_scc0 Lp3_0_%=\n"
"ds_read_b64_tr_b16 a[20:21], v41 offset:8192\n"
"ds_read_b64_tr_b16 a[22:23], v41 offset:8704\n"
"ds_read_b64_tr_b16 v[68:69], v42 offset:8192\n"
"ds_read_b64_tr_b16 v[70:71], v42 offset:8704\n"
"s_waitcnt lgkmcnt(0)\n"
"ds_write_b128 v1, v[68:71] offset:3072\n"
"s_branch Lq3_%=\n"
"Lp3_0_%=:\n"
"s_cmp_eq_u32 s35, 1\n"
"s_cbranch_scc0 Lp3_1_%=\n"
"ds_read_b64_tr_b16 a[4:5], v41 offset:8192\n"
"ds_read_b64_tr_b16 a[6:7], v41 offset:8704\n"
"ds_read_b64_tr_b16 v[68:69], v42 offset:8192\n"
"ds_read_b64_tr_b16 v[70:71], v42 offset:8704\n"
"s_waitcnt lgkmcnt(0)\n"
"ds_write_b128 v1, v[68:71] offset:1024\n"
"s_branch Lq3_%=\n"
"Lp3_1_%=:\n"
"s_cmp_eq_u32 s35, 2\n"
"s_cbranch_scc0 Lp3_2_%=\n"
"ds_read_b64_tr_b16 a[36:37], v41 offset:8192\n"
"ds_read_b64_tr_b16 a[38:39], v41 offset:8704\n"
"ds_read_b64_tr_b16 v[68:69], v42 offset:8192\n"
"ds_read_b64_tr_b16 v[70:71], v42 offset:8704\n"
"s_waitcnt lgkmcnt(0)\n"
"ds_write_b128 v1, v[68:71] offset:5120\n"
"s_branch Lq3_%=\n"
"Lp3_2_%=:\n"
"ds_read_b64_tr_b16 a[28:29], v41 offset:8192\n"
"ds_read_b64_tr_b16 a[30:31], v41 offset:8704\n"
"ds_read_b64_tr_b16 a[228:229], v42 offset:8192\n"
"ds_read_b64_tr_b16 a[230:231], v42 offset:8704\n"
"Lq3_%=:\n"
"s_waitcnt vmcnt(12)\n"
"s_waitcnt lgkmcnt(0)\n"
"s_barrier\n"
"s_lshl_b32 s94, s36, 8\n"
"s_add_u32 s94, s94, 2883584\n"
"s_add_u32 m0, s24, 8192\n"
"s_nop 0\n"
"buffer_load_dwordx4 v34, s[20:23], s94 offen lds\n"
"s_add_u32 m0, s24, 9216\n"
"s_nop 0\n"
"buffer_load_dwordx4 v35, s[20:23], s94 offen lds\n"
"s_cmp_eq_u32 s35, 0\n"
"s_cbranch_scc0 Lp4_0_%=\n"
"ds_read_b64_tr_b16 a[24:25], v41 offset:16384\n"
"ds_read_b64_tr_b16 a[26:27], v41 offset:16896\n"
"ds_read_b64_tr_b16 a[224:225], v42 offset:16384\n"
"ds_read_b64_tr_b16 a[226:227], v42 offset:16896\n"
"s_branch Lq4_%=\n"
"Lp4_0_%=:\n"
"s_cmp_eq_u32 s35, 1\n"
"s_cbranch_scc0 Lp4_1_%=\n"
"ds_read_b64_tr_b16 a[16:17], v41 offset:16384\n"
"ds_read_b64_tr_b16 a[18:19], v41 offset:16896\n"
"ds_read_b64_tr_b16 v[64:65], v42 offset:16384\n"
"ds_read_b64_tr_b16 v[66:67], v42 offset:16896\n"
"s_waitcnt lgkmcnt(0)\n"
"ds_write_b128 v1, v[64:67] offset:2048\n"
"s_branch Lq4_%=\n"
"Lp4_1_%=:\n"
"s_cmp_eq_u32 s35, 2\n"
"s_cbranch_scc0 Lp4_2_%=\n"
"ds_read_b64_tr_b16 a[0:1], v41 offset:16384\n"
"ds_read_b64_tr_b16 a[2:3], v41 offset:16896\n"
"ds_read_b64_tr_b16 v[64:65], v42 offset:16384\n"
"ds_read_b64_tr_b16 v[66:67], v42 offset:16896\n"
"s_waitcnt lgkmcnt(0)\n"
"ds_write_b128 v1, v[64:67]\n"
"s_branch Lq4_%=\n"
"Lp4_2_%=:\n"
"ds_read_b64_tr_b16 a[32:33], v41 offset:16384\n"
"ds_read_b64_tr_b16 a[34:35], v41 offset:16896\n"
"ds_read_b64_tr_b16 v[64:65], v42 offset:16384\n"
"ds_read_b64_tr_b16 v[66:67], v42 offset:16896\n"
"s_waitcnt lgkmcnt(0)\n"
"ds_write_b128 v1, v[64:67] offset:4096\n"
"Lq4_%=:\n"
"s_waitcnt vmcnt(12)\n"
"s_waitcnt lgkmcnt(0)\n"
"s_barrier\n"
"s_lshl_b32 s94, s36, 8\n"
"s_add_u32 s94, s94, 3145728\n"
"s_add_u32 m0, s24, 16384\n"
"s_nop 0\n"
"buffer_load_dwordx4 v34, s[20:23], s94 offen lds\n"
"s_add_u32 m0, s24, 17408\n"
"s_nop 0\n"
"buffer_load_dwordx4 v35, s[20:23], s94 offen lds\n"
"s_cmp_eq_u32 s35, 0\n"
"s_cbranch_scc0 Lp5_0_%=\n"
"ds_read_b64_tr_b16 a[28:29], v41 offset:24576\n"
"ds_read_b64_tr_b16 a[30:31], v41 offset:25088\n"
"ds_read_b64_tr_b16 a[228:229], v42 offset:24576\n"
"ds_read_b64_tr_b16 a[230:231], v42 offset:25088\n"
"s_branch Lq5_%=\n"
"Lp5_0_%=:\n"
"s_cmp_eq_u32 s35, 1\n"
"s_cbranch_scc0 Lp5_1_%=\n"
"ds_read_b64_tr_b16 a[20:21], v41 offset:24576\n"
"ds_read_b64_tr_b16 a[22:23], v41 offset:25088\n"
"ds_read_b64_tr_b16 v[68:69], v42 offset:24576\n"
"ds_read_b64_tr_b16 v[70:71], v42 offset:25088\n"
"s_waitcnt lgkmcnt(0)\n"
"ds_write_b128 v1, v[68:71] offset:3072\n"
"s_branch Lq5_%=\n"
"Lp5_1_%=:\n"
"s_cmp_eq_u32 s35, 2\n"
"s_cbranch_scc0 Lp5_2_%=\n"
"ds_read_b64_tr_b16 a[4:5], v41 offset:24576\n"
"ds_read_b64_tr_b16 a[6:7], v41 offset:25088\n"
"ds_read_b64_tr_b16 v[68:69], v42 offset:24576\n"
"ds_read_b64_tr_b16 v[70:71], v42 offset:25088\n"
"s_waitcnt lgkmcnt(0)\n"
"ds_write_b128 v1, v[68:71] offset:1024\n"
"s_branch Lq5_%=\n"
"Lp5_2_%=:\n"
"ds_read_b64_tr_b16 a[36:37], v41 offset:24576\n"
"ds_read_b64_tr_b16 a[38:39], v41 offset:25088\n"
"ds_read_b64_tr_b16 v[68:69], v42 offset:24576\n"
"ds_read_b64_tr_b16 v[70:71], v42 offset:25088\n"
"s_waitcnt lgkmcnt(0)\n"
"ds_write_b128 v1, v[68:71] offset:5120\n"
"Lq5_%=:\n"
"s_waitcnt vmcnt(12)\n"
"s_waitcnt lgkmcnt(0)\n"
"s_barrier\n"
"s_lshl_b32 s94, s36, 8\n"
"s_add_u32 s94, s94, 3407872\n"
"s_add_u32 m0, s24, 24576\n"
"s_nop 0\n"
"buffer_load_dwordx4 v34, s[20:23], s94 offen lds\n"
"s_add_u32 m0, s24, 25600\n"
"s_nop 0\n"
"buffer_load_dwordx4 v35, s[20:23], s94 offen lds\n"
"s_cmp_eq_u32 s35, 0\n"
"s_cbranch_scc0 Lp6_0_%=\n"
"ds_read_b64_tr_b16 a[32:33], v41 offset:53248\n"
"ds_read_b64_tr_b16 a[34:35], v41 offset:53760\n"
"ds_read_b64_tr_b16 v[64:65], v42 offset:53248\n"
"ds_read_b64_tr_b16 v[66:67], v42 offset:53760\n"
"s_waitcnt lgkmcnt(0)\n"
"ds_write_b128 v1, v[64:67] offset:4096\n"
"s_branch Lq6_%=\n"
"Lp6_0_%=:\n"
"s_cmp_eq_u32 s35, 1\n"
"s_cbranch_scc0 Lp6_1_%=\n"
"ds_read_b64_tr_b16 a[24:25], v41 offset:53248\n"
"ds_read_b64_tr_b16 a[26:27], v41 offset:53760\n"
"ds_read_b64_tr_b16 a[224:225], v42 offset:53248\n"
"ds_read_b64_tr_b16 a[226:227], v42 offset:53760\n"
"s_branch Lq6_%=\n"
"Lp6_1_%=:\n"
"s_cmp_eq_u32 s35, 2\n"
"s_cbranch_scc0 Lp6_2_%=\n"
"ds_read_b64_tr_b16 a[16:17], v41 offset:53248\n"
"ds_read_b64_tr_b16 a[18:19], v41 offset:53760\n"
"ds_read_b64_tr_b16 v[64:65], v42 offset:53248\n"
"ds_read_b64_tr_b16 v[66:67], v42 offset:53760\n"
"s_waitcnt lgkmcnt(0)\n"
"ds_write_b128 v1, v[64:67] offset:2048\n"
"s_branch Lq6_%=\n"
"Lp6_2_%=:\n"
"ds_read_b64_tr_b16 a[0:1], v41 offset:53248\n"
"ds_read_b64_tr_b16 a[2:3], v41 offset:53760\n"
"ds_read_b64_tr_b16 v[64:65], v42 offset:53248\n"
"ds_read_b64_tr_b16 v[66:67], v42 offset:53760\n"
"s_waitcnt lgkmcnt(0)\n"
"ds_write_b128 v1, v[64:67]\n"
"Lq6_%=:\n"
"s_waitcnt vmcnt(12)\n"
"s_waitcnt lgkmcnt(0)\n"
"s_barrier\n"
"s_lshl_b32 s94, s36, 8\n"
"s_add_u32 s94, s94, 3670016\n"
"s_add_u32 m0, s24, 53248\n"
"s_nop 0\n"
"buffer_load_dwordx4 v34, s[20:23], s94 offen lds\n"
"s_add_u32 m0, s24, 54272\n"
"s_nop 0\n"
"buffer_load_dwordx4 v35, s[20:23], s94 offen lds\n"
"s_cmp_eq_u32 s35, 0\n"
"s_cbranch_scc0 Lp7_0_%=\n"
"ds_read_b64_tr_b16 a[36:37], v44 offset:20480\n"
"ds_read_b64_tr_b16 a[38:39], v44 offset:20992\n"
"ds_read_b64_tr_b16 v[68:69], v45 offset:20480\n"
"ds_read_b64_tr_b16 v[70:71], v45 offset:20992\n"
"s_waitcnt lgkmcnt(0)\n"
"ds_write_b128 v1, v[68:71] offset:5120\n"
"s_branch Lq7_%=\n"
"Lp7_0_%=:\n"
"s_cmp_eq_u32 s35, 1\n"
"s_cbranch_scc0 Lp7_1_%=\n"
"ds_read_b64_tr_b16 a[28:29], v44 offset:20480\n"
"ds_read_b64_tr_b16 a[30:31], v44 offset:20992\n"
"ds_read_b64_tr_b16 a[228:229], v45 offset:20480\n"
"ds_read_b64_tr_b16 a[230:231], v45 offset:20992\n"
"s_branch Lq7_%=\n"
"Lp7_1_%=:\n"
"s_cmp_eq_u32 s35, 2\n"
"s_cbranch_scc0 Lp7_2_%=\n"
"ds_read_b64_tr_b16 a[20:21], v44 offset:20480\n"
"ds_read_b64_tr_b16 a[22:23], v44 offset:20992\n"
"ds_read_b64_tr_b16 v[68:69], v45 offset:20480\n"
"ds_read_b64_tr_b16 v[70:71], v45 offset:20992\n"
"s_waitcnt lgkmcnt(0)\n"
"ds_write_b128 v1, v[68:71] offset:3072\n"
"s_branch Lq7_%=\n"
"Lp7_2_%=:\n"
"ds_read_b64_tr_b16 a[4:5], v44 offset:20480\n"
"ds_read_b64_tr_b16 a[6:7], v44 offset:20992\n"
"ds_read_b64_tr_b16 v[68:69], v45 offset:20480\n"
"ds_read_b64_tr_b16 v[70:71], v45 offset:20992\n"
"s_waitcnt lgkmcnt(0)\n"
"ds_write_b128 v1, v[68:71] offset:1024\n"
"Lq7_%=:\n"
"s_waitcnt vmcnt(12)\n"
"s_waitcnt lgkmcnt(0)\n"
"s_barrier\n"
"s_lshl_b32 s94, s36, 8\n"
"s_add_u32 s94, s94, 3932160\n"
"s_add_u32 m0, s24, 86016\n"
"s_nop 0\n"
"buffer_load_dwordx4 v34, s[20:23], s94 offen lds\n"
"s_add_u32 m0, s24, 87040\n"
"s_nop 0\n"
"buffer_load_dwordx4 v35, s[20:23], s94 offen lds\n"
"s_cmp_eq_u32 s35, 0\n"
"s_cbranch_scc0 Lp8_0_%=\n"
"ds_read_b64_tr_b16 a[8:9], v44 offset:53248\n"
"ds_read_b64_tr_b16 a[10:11], v44 offset:53760\n"
"ds_read_b64_tr_b16 a[216:217], v45 offset:53248\n"
"ds_read_b64_tr_b16 a[218:219], v45 offset:53760\n"
"s_branch Lq8_%=\n"
"Lp8_0_%=:\n"
"s_cmp_eq_u32 s35, 1\n"
"s_cbranch_scc0 Lp8_1_%=\n"
"ds_read_b64_tr_b16 a[56:57], v44 offset:53248\n"
"ds_read_b64_tr_b16 a[58:59], v44 offset:53760\n"
"ds_read_b64_tr_b16 a[240:241], v45 offset:53248\n"
"ds_read_b64_tr_b16 a[242:243], v45 offset:53760\n"
"s_branch Lq8_%=\n"
"Lp8_1_%=:\n"
"s_cmp_eq_u32 s35, 2\n"
"s_cbranch_scc0 Lp8_2_%=\n"
"ds_read_b64_tr_b16 a[48:49], v44 offset:53248\n"
"ds_read_b64_tr_b16 a[50:51], v44 offset:53760\n"
"ds_read_b64_tr_b16 v[64:65], v45 offset:53248\n"
"ds_read_b64_tr_b16 v[66:67], v45 offset:53760\n"
"s_waitcnt lgkmcnt(0)\n"
"ds_write_b128 v1, v[64:67] offset:6144\n"
"s_branch Lq8_%=\n"
"Lp8_2_%=:\n"
"ds_read_b64_tr_b16 a[40:41], v44 offset:53248\n"
"ds_read_b64_tr_b16 a[42:43], v44 offset:53760\n"
"ds_read_b64_tr_b16 a[232:233], v45 offset:53248\n"
"ds_read_b64_tr_b16 a[234:235], v45 offset:53760\n"
"Lq8_%=:\n"
"s_waitcnt vmcnt(12)\n"
"s_waitcnt lgkmcnt(0)\n"
"s_barrier\n"
"s_lshl_b32 s94, s36, 8\n"
"s_add_u32 s94, s94, 4194304\n"
"s_add_u32 m0, s24, 118784\n"
"s_nop 0\n"
"buffer_load_dwordx4 v34, s[20:23], s94 offen lds\n"
"s_add_u32 m0, s24, 119808\n"
"s_nop 0\n"
"buffer_load_dwordx4 v35, s[20:23], s94 offen lds\n"
"s_cmp_eq_u32 s35, 0\n"
"s_cbranch_scc0 Lp9_0_%=\n"
"ds_read_b64_tr_b16 a[12:13], v46 offset:20480\n"
"ds_read_b64_tr_b16 a[14:15], v46 offset:20992\n"
"ds_read_b64_tr_b16 a[220:221], v47 offset:20480\n"
"ds_read_b64_tr_b16 a[222:223], v47 offset:20992\n"
"s_branch Lq9_%=\n"
"Lp9_0_%=:\n"
"s_cmp_eq_u32 s35, 1\n"
"s_cbranch_scc0 Lp9_1_%=\n"
"ds_read_b64_tr_b16 a[60:61], v46 offset:20480\n"
"ds_read_b64_tr_b16 a[62:63], v46 offset:20992\n"
"ds_read_b64_tr_b16 a[244:245], v47 offset:20480\n"
"ds_read_b64_tr_b16 a[246:247], v47 offset:20992\n"
"s_branch Lq9_%=\n"
"Lp9_1_%=:\n"
"s_cmp_eq_u32 s35, 2\n"
"s_cbranch_scc0 Lp9_2_%=\n"
"ds_read_b64_tr_b16 a[52:53], v46 offset:20480\n"
"ds_read_b64_tr_b16 a[54:55], v46 offset:20992\n"
"ds_read_b64_tr_b16 v[68:69], v47 offset:20480\n"
"ds_read_b64_tr_b16 v[70:71], v47 offset:20992\n"
"s_waitcnt lgkmcnt(0)\n"
"ds_write_b128 v1, v[68:71] offset:7168\n"
"s_branch Lq9_%=\n"
"Lp9_2_%=:\n"
"ds_read_b64_tr_b16 a[44:45], v46 offset:20480\n"
"ds_read_b64_tr_b16 a[46:47], v46 offset:20992\n"
"ds_read_b64_tr_b16 a[236:237], v47 offset:20480\n"
"ds_read_b64_tr_b16 a[238:239], v47 offset:20992\n"
"Lq9_%=:\n"
"s_waitcnt vmcnt(12)\n"
"s_waitcnt lgkmcnt(0)\n"
"s_barrier\n"
"s_lshl_b32 s94, s36, 8\n"
"s_add_u32 s94, s94, 4456448\n"
"s_add_u32 m0, s24, 151552\n"
"s_nop 0\n"
"buffer_load_dwordx4 v34, s[20:23], s94 offen lds\n"
"s_add_u32 m0, s24, 152576\n"
"s_nop 0\n"
"buffer_load_dwordx4 v35, s[20:23], s94 offen lds\n"
"s_cmp_eq_u32 s35, 0\n"
"s_cbranch_scc0 Lp10_0_%=\n"
"ds_read_b64_tr_b16 a[40:41], v41\n"
"ds_read_b64_tr_b16 a[42:43], v41 offset:512\n"
"ds_read_b64_tr_b16 a[232:233], v42\n"
"ds_read_b64_tr_b16 a[234:235], v42 offset:512\n"
"s_branch Lq10_%=\n"
"Lp10_0_%=:\n"
"s_cmp_eq_u32 s35, 1\n"
"s_cbranch_scc0 Lp10_1_%=\n"
"ds_read_b64_tr_b16 a[8:9], v41\n"
"ds_read_b64_tr_b16 a[10:11], v41 offset:512\n"
"ds_read_b64_tr_b16 a[216:217], v42\n"
"ds_read_b64_tr_b16 a[218:219], v42 offset:512\n"
"s_branch Lq10_%=\n"
"Lp10_1_%=:\n"
"s_cmp_eq_u32 s35, 2\n"
"s_cbranch_scc0 Lp10_2_%=\n"
"ds_read_b64_tr_b16 a[56:57], v41\n"
"ds_read_b64_tr_b16 a[58:59], v41 offset:512\n"
"ds_read_b64_tr_b16 a[240:241], v42\n"
"ds_read_b64_tr_b16 a[242:243], v42 offset:512\n"
"s_branch Lq10_%=\n"
"Lp10_2_%=:\n"
"ds_read_b64_tr_b16 a[48:49], v41\n"
"ds_read_b64_tr_b16 a[50:51], v41 offset:512\n"
"ds_read_b64_tr_b16 v[64:65], v42\n"
"ds_read_b64_tr_b16 v[66:67], v42 offset:512\n"
"s_waitcnt lgkmcnt(0)\n"
"ds_write_b128 v1, v[64:67] offset:6144\n"
"Lq10_%=:\n"
"s_waitcnt vmcnt(12)\n"
"s_waitcnt lgkmcnt(0)\n"
"s_barrier\n"
"s_mov_b32 s20, s10\n"
"s_and_b32 s21, s11, 0xffff\n"
"s_mov_b32 s22, 4718592\n"
"s_mov_b32 s23, 0x20000\n"
"s_lshl_b32 s94, s36, 8\n"
"s_add_u32 s94, s94, 0\n"
"s_add_u32 m0, s24, 0\n"
"s_nop 0\n"
"buffer_load_dwordx4 v34, s[20:23], s94 offen lds\n"
"s_add_u32 m0, s24, 1024\n"
"s_nop 0\n"
"buffer_load_dwordx4 v35, s[20:23], s94 offen lds\n"
"s_cmp_eq_u32 s35, 0\n"
"s_cbranch_scc0 Lp11_0_%=\n"
"ds_read_b64_tr_b16 a[44:45], v41 offset:8192\n"
"ds_read_b64_tr_b16 a[46:47], v41 offset:8704\n"
"ds_read_b64_tr_b16 a[236:237], v42 offset:8192\n"
"ds_read_b64_tr_b16 a[238:239], v42 offset:8704\n"
"s_branch Lq11_%=\n"
"Lp11_0_%=:\n"
"s_cmp_eq_u32 s35, 1\n"
"s_cbranch_scc0 Lp11_1_%=\n"
"ds_read_b64_tr_b16 a[12:13], v41 offset:8192\n"
"ds_read_b64_tr_b16 a[14:15], v41 offset:8704\n"
"ds_read_b64_tr_b16 a[220:221], v42 offset:8192\n"
"ds_read_b64_tr_b16 a[222:223], v42 offset:8704\n"
"s_branch Lq11_%=\n"
"Lp11_1_%=:\n"
"s_cmp_eq_u32 s35, 2\n"
"s_cbranch_scc0 Lp11_2_%=\n"
"ds_read_b64_tr_b16 a[60:61], v41 offset:8192\n"
"ds_read_b64_tr_b16 a[62:63], v41 offset:8704\n"
"ds_read_b64_tr_b16 a[244:245], v42 offset:8192\n"
"ds_read_b64_tr_b16 a[246:247], v42 offset:8704\n"
"s_branch Lq11_%=\n"
"Lp11_2_%=:\n"
"ds_read_b64_tr_b16 a[52:53], v41 offset:8192\n"
"ds_read_b64_tr_b16 a[54:55], v41 offset:8704\n"
"ds_read_b64_tr_b16 v[68:69], v42 offset:8192\n"
"ds_read_b64_tr_b16 v[70:71], v42 offset:8704\n"
"s_waitcnt lgkmcnt(0)\n"
"ds_write_b128 v1, v[68:71] offset:7168\n"
"Lq11_%=:\n"
"s_waitcnt vmcnt(12)\n"
"s_waitcnt lgkmcnt(0)\n"
"s_barrier\n"
"s_lshl_b32 s94, s36, 8\n"
"s_add_u32 s94, s94, 262144\n"
"s_add_u32 m0, s24, 8192\n"
"s_nop 0\n"
"buffer_load_dwordx4 v34, s[20:23], s94 offen lds\n"
"s_add_u32 m0, s24, 9216\n"
"s_nop 0\n"
"buffer_load_dwordx4 v35, s[20:23], s94 offen lds\n"
"s_cmp_eq_u32 s35, 0\n"
"s_cbranch_scc0 Lp12_0_%=\n"
"ds_read_b64_tr_b16 a[48:49], v41 offset:16384\n"
"ds_read_b64_tr_b16 a[50:51], v41 offset:16896\n"
"ds_read_b64_tr_b16 v[64:65], v42 offset:16384\n"
"ds_read_b64_tr_b16 v[66:67], v42 offset:16896\n"
"s_waitcnt lgkmcnt(0)\n"
"ds_write_b128 v1, v[64:67] offset:6144\n"
"s_branch Lq12_%=\n"
"Lp12_0_%=:\n"
"s_cmp_eq_u32 s35, 1\n"
"s_cbranch_scc0 Lp12_1_%=\n"
"ds_read_b64_tr_b16 a[40:41], v41 offset:16384\n"
"ds_read_b64_tr_b16 a[42:43], v41 offset:16896\n"
"ds_read_b64_tr_b16 a[232:233], v42 offset:16384\n"
"ds_read_b64_tr_b16 a[234:235], v42 offset:16896\n"
"s_branch Lq12_%=\n"
"Lp12_1_%=:\n"
"s_cmp_eq_u32 s35, 2\n"
"s_cbranch_scc0 Lp12_2_%=\n"
"ds_read_b64_tr_b16 a[8:9], v41 offset:16384\n"
"ds_read_b64_tr_b16 a[10:11], v41 offset:16896\n"
"ds_read_b64_tr_b16 a[216:217], v42 offset:16384\n"
"ds_read_b64_tr_b16 a[218:219], v42 offset:16896\n"
"s_branch Lq12_%=\n"
"Lp12_2_%=:\n"
"ds_read_b64_tr_b16 a[56:57], v41 offset:16384\n"
"ds_read_b64_tr_b16 a[58:59], v41 offset:16896\n"
"ds_read_b64_tr_b16 a[240:241], v42 offset:16384\n"
"ds_read_b64_tr_b16 a[242:243], v42 offset:16896\n"
"Lq12_%=:\n"
"s_waitcnt vmcnt(12)\n"
"s_waitcnt lgkmcnt(0)\n"
"s_barrier\n"
"s_lshl_b32 s94, s36, 8\n"
"s_add_u32 s94, s94, 524288\n"
"s_add_u32 m0, s24, 16384\n"
"s_nop 0\n"
"buffer_load_dwordx4 v34, s[20:23], s94 offen lds\n"
"s_add_u32 m0, s24, 17408\n"
"s_nop 0\n"
"buffer_load_dwordx4 v35, s[20:23], s94 offen lds\n"
"s_cmp_eq_u32 s35, 0\n"
"s_cbranch_scc0 Lp13_0_%=\n"
"ds_read_b64_tr_b16 a[52:53], v41 offset:24576\n"
"ds_read_b64_tr_b16 a[54:55], v41 offset:25088\n"
"ds_read_b64_tr_b16 v[68:69], v42 offset:24576\n"
"ds_read_b64_tr_b16 v[70:71], v42 offset:25088\n"
"s_waitcnt lgkmcnt(0)\n"
"ds_write_b128 v1, v[68:71] offset:7168\n"
"s_branch Lq13_%=\n"
"Lp13_0_%=:\n"
"s_cmp_eq_u32 s35, 1\n"
"s_cbranch_scc0 Lp13_1_%=\n"
"ds_read_b64_tr_b16 a[44:45], v41 offset:24576\n"
"ds_read_b64_tr_b16 a[46:47], v41 offset:25088\n"
"ds_read_b64_tr_b16 a[236:237], v42 offset:24576\n"
"ds_read_b64_tr_b16 a[238:239], v42 offset:25088\n"
"s_branch Lq13_%=\n"
"Lp13_1_%=:\n"
"s_cmp_eq_u32 s35, 2\n"
"s_cbranch_scc0 Lp13_2_%=\n"
"ds_read_b64_tr_b16 a[12:13], v41 offset:24576\n"
"ds_read_b64_tr_b16 a[14:15], v41 offset:25088\n"
"ds_read_b64_tr_b16 a[220:221], v42 offset:24576\n"
"ds_read_b64_tr_b16 a[222:223], v42 offset:25088\n"
"s_branch Lq13_%=\n"
"Lp13_2_%=:\n"
"ds_read_b64_tr_b16 a[60:61], v41 offset:24576\n"
"ds_read_b64_tr_b16 a[62:63], v41 offset:25088\n"
"ds_read_b64_tr_b16 a[244:245], v42 offset:24576\n"
"ds_read_b64_tr_b16 a[246:247], v42 offset:25088\n"
"Lq13_%=:\n"
"s_waitcnt vmcnt(12)\n"
"s_waitcnt lgkmcnt(0)\n"
"s_barrier\n"
"s_lshl_b32 s94, s36, 8\n"
"s_add_u32 s94, s94, 786432\n"
"s_add_u32 m0, s24, 24576\n"
"s_nop 0\n"
"buffer_load_dwordx4 v34, s[20:23], s94 offen lds\n"
"s_add_u32 m0, s24, 25600\n"
"s_nop 0\n"
"buffer_load_dwordx4 v35, s[20:23], s94 offen lds\n"
"s_cmp_eq_u32 s35, 0\n"
"s_cbranch_scc0 Lp14_0_%=\n"
"ds_read_b64_tr_b16 a[56:57], v41 offset:53248\n"
"ds_read_b64_tr_b16 a[58:59], v41 offset:53760\n"
"ds_read_b64_tr_b16 a[240:241], v42 offset:53248\n"
"ds_read_b64_tr_b16 a[242:243], v42 offset:53760\n"
"s_branch Lq14_%=\n"
"Lp14_0_%=:\n"
"s_cmp_eq_u32 s35, 1\n"
"s_cbranch_scc0 Lp14_1_%=\n"
"ds_read_b64_tr_b16 a[48:49], v41 offset:53248\n"
"ds_read_b64_tr_b16 a[50:51], v41 offset:53760\n"
"ds_read_b64_tr_b16 v[64:65], v42 offset:53248\n"
"ds_read_b64_tr_b16 v[66:67], v42 offset:53760\n"
"s_waitcnt lgkmcnt(0)\n"
"ds_write_b128 v1, v[64:67] offset:6144\n"
"s_branch Lq14_%=\n"
"Lp14_1_%=:\n"
"s_cmp_eq_u32 s35, 2\n"
"s_cbranch_scc0 Lp14_2_%=\n"
"ds_read_b64_tr_b16 a[40:41], v41 offset:53248\n"
"ds_read_b64_tr_b16 a[42:43], v41 offset:53760\n"
"ds_read_b64_tr_b16 a[232:233], v42 offset:53248\n"
"ds_read_b64_tr_b16 a[234:235], v42 offset:53760\n"
"s_branch Lq14_%=\n"
"Lp14_2_%=:\n"
"ds_read_b64_tr_b16 a[8:9], v41 offset:53248\n"
"ds_read_b64_tr_b16 a[10:11], v41 offset:53760\n"
"ds_read_b64_tr_b16 a[216:217], v42 offset:53248\n"
"ds_read_b64_tr_b16 a[218:219], v42 offset:53760\n"
"Lq14_%=:\n"
"s_waitcnt vmcnt(12)\n"
"s_waitcnt lgkmcnt(0)\n"
"s_barrier\n"
"s_lshl_b32 s94, s36, 8\n"
"s_add_u32 s94, s94, 1048576\n"
"s_add_u32 m0, s24, 53248\n"
"s_nop 0\n"
"buffer_load_dwordx4 v34, s[20:23], s94 offen lds\n"
"s_add_u32 m0, s24, 54272\n"
"s_nop 0\n"
"buffer_load_dwordx4 v35, s[20:23], s94 offen lds\n"
"s_cmp_eq_u32 s35, 0\n"
"s_cbranch_scc0 Lp15_0_%=\n"
"ds_read_b64_tr_b16 a[60:61], v44 offset:20480\n"
"ds_read_b64_tr_b16 a[62:63], v44 offset:20992\n"
"ds_read_b64_tr_b16 a[244:245], v45 offset:20480\n"
"ds_read_b64_tr_b16 a[246:247], v45 offset:20992\n"
"s_branch Lq15_%=\n"
"Lp15_0_%=:\n"
"s_cmp_eq_u32 s35, 1\n"
"s_cbranch_scc0 Lp15_1_%=\n"
"ds_read_b64_tr_b16 a[52:53], v44 offset:20480\n"
"ds_read_b64_tr_b16 a[54:55], v44 offset:20992\n"
"ds_read_b64_tr_b16 v[68:69], v45 offset:20480\n"
"ds_read_b64_tr_b16 v[70:71], v45 offset:20992\n"
"s_waitcnt lgkmcnt(0)\n"
"ds_write_b128 v1, v[68:71] offset:7168\n"
"s_branch Lq15_%=\n"
"Lp15_1_%=:\n"
"s_cmp_eq_u32 s35, 2\n"
"s_cbranch_scc0 Lp15_2_%=\n"
"ds_read_b64_tr_b16 a[44:45], v44 offset:20480\n"
"ds_read_b64_tr_b16 a[46:47], v44 offset:20992\n"
"ds_read_b64_tr_b16 a[236:237], v45 offset:20480\n"
"ds_read_b64_tr_b16 a[238:239], v45 offset:20992\n"
"s_branch Lq15_%=\n"
"Lp15_2_%=:\n"
"ds_read_b64_tr_b16 a[12:13], v44 offset:20480\n"
"ds_read_b64_tr_b16 a[14:15], v44 offset:20992\n"
"ds_read_b64_tr_b16 a[220:221], v45 offset:20480\n"
"ds_read_b64_tr_b16 a[222:223], v45 offset:20992\n"
"Lq15_%=:\n"
"s_waitcnt vmcnt(12)\n"
"s_waitcnt lgkmcnt(0)\n"
"s_barrier\n"
"s_lshl_b32 s94, s36, 8\n"
"s_add_u32 s94, s94, 1310720\n"
"s_add_u32 m0, s24, 86016\n"
"s_nop 0\n"
"buffer_load_dwordx4 v34, s[20:23], s94 offen lds\n"
"s_add_u32 m0, s24, 87040\n"
"s_nop 0\n"
"buffer_load_dwordx4 v35, s[20:23], s94 offen lds\n"
"ds_read_b64_tr_b16 a[64:65], v44 offset:53248\n"
"ds_read_b64_tr_b16 a[66:67], v44 offset:53760\n"
"ds_read_b64_tr_b16 v[64:65], v45 offset:53248\n"
"ds_read_b64_tr_b16 v[66:67], v45 offset:53760\n"
"s_waitcnt lgkmcnt(0)\n"
"ds_write_b128 v1, v[64:67] offset:8192\n"
"Lq16_%=:\n"
"s_waitcnt vmcnt(12)\n"
"s_waitcnt lgkmcnt(0)\n"
"s_barrier\n"
"s_lshl_b32 s94, s36, 8\n"
"s_add_u32 s94, s94, 1572864\n"
"s_add_u32 m0, s24, 118784\n"
"s_nop 0\n"
"buffer_load_dwordx4 v34, s[20:23], s94 offen lds\n"
"s_add_u32 m0, s24, 119808\n"
"s_nop 0\n"
"buffer_load_dwordx4 v35, s[20:23], s94 offen lds\n"
"ds_read_b64_tr_b16 a[68:69], v46 offset:20480\n"
"ds_read_b64_tr_b16 a[70:71], v46 offset:20992\n"
"ds_read_b64_tr_b16 v[68:69], v47 offset:20480\n"
"ds_read_b64_tr_b16 v[70:71], v47 offset:20992\n"
"s_waitcnt lgkmcnt(0)\n"
"ds_write_b128 v1, v[68:71] offset:9216\n"
"Lq17_%=:\n"
"s_waitcnt vmcnt(12)\n"
"s_waitcnt lgkmcnt(0)\n"
"s_barrier\n"
"s_lshl_b32 s94, s36, 8\n"
"s_add_u32 s94, s94, 1835008\n"
"s_add_u32 m0, s24, 151552\n"
"s_nop 0\n"
"buffer_load_dwordx4 v34, s[20:23], s94 offen lds\n"
"s_add_u32 m0, s24, 152576\n"
"s_nop 0\n"
"buffer_load_dwordx4 v35, s[20:23], s94 offen lds\n"
"s_cmp_eq_u32 s35, 0\n"
"s_cbranch_scc0 Lp18_0_%=\n"
"ds_read_b64_tr_b16 a[72:73], v41\n"
"ds_read_b64_tr_b16 a[74:75], v41 offset:512\n"
"ds_read_b64_tr_b16 a[248:249], v42\n"
"ds_read_b64_tr_b16 a[250:251], v42 offset:512\n"
"s_branch Lq18_%=\n"
"Lp18_0_%=:\n"
"s_cmp_eq_u32 s35, 1\n"
"s_cbranch_scc0 Lp18_1_%=\n"
"ds_read_b64_tr_b16 a[104:105], v41\n"
"ds_read_b64_tr_b16 a[106:107], v41 offset:512\n"
"ds_read_b64_tr_b16 v[136:137], v42\n"
"ds_read_b64_tr_b16 v[138:139], v42 offset:512\n"
"s_branch Lq18_%=\n"
"Lp18_1_%=:\n"
"s_cmp_eq_u32 s35, 2\n"
"s_cbranch_scc0 Lp18_2_%=\n"
"ds_read_b64_tr_b16 a[96:97], v41\n"
"ds_read_b64_tr_b16 a[98:99], v41 offset:512\n"
"ds_read_b64_tr_b16 v[64:65], v42\n"
"ds_read_b64_tr_b16 v[66:67], v42 offset:512\n"
"s_waitcnt lgkmcnt(0)\n"
"ds_write_b128 v1, v[64:67] offset:12288\n"
"s_branch Lq18_%=\n"
"Lp18_2_%=:\n"
"ds_read_b64_tr_b16 a[88:89], v41\n"
"ds_read_b64_tr_b16 a[90:91], v41 offset:512\n"
"ds_read_b64_tr_b16 v[128:129], v42\n"
"ds_read_b64_tr_b16 v[130:131], v42 offset:512\n"
"Lq18_%=:\n"
"s_waitcnt vmcnt(12)\n"
"s_waitcnt lgkmcnt(0)\n"
"s_barrier\n"
"s_lshl_b32 s94, s36, 8\n"
"s_add_u32 s94, s94, 2097152\n"
"s_add_u32 m0, s24, 0\n"
"s_nop 0\n"
"buffer_load_dwordx4 v34, s[20:23], s94 offen lds\n"
"s_add_u32 m0, s24, 1024\n"
"s_nop 0\n"
"buffer_load_dwordx4 v35, s[20:23], s94 offen lds\n"
"s_cmp_eq_u32 s35, 0\n"
"s_cbranch_scc0 Lp19_0_%=\n"
"ds_read_b64_tr_b16 a[76:77], v41 offset:8192\n"
"ds_read_b64_tr_b16 a[78:79], v41 offset:8704\n"
"ds_read_b64_tr_b16 a[252:253], v42 offset:8192\n"
"ds_read_b64_tr_b16 a[254:255], v42 offset:8704\n"
"s_branch Lq19_%=\n"
"Lp19_0_%=:\n"
"s_cmp_eq_u32 s35, 1\n"
"s_cbranch_scc0 Lp19_1_%=\n"
"ds_read_b64_tr_b16 a[108:109], v41 offset:8192\n"
"ds_read_b64_tr_b16 a[110:111], v41 offset:8704\n"
"ds_read_b64_tr_b16 v[140:141], v42 offset:8192\n"
"ds_read_b64_tr_b16 v[142:143], v42 offset:8704\n"
"s_branch Lq19_%=\n"
"Lp19_1_%=:\n"
"s_cmp_eq_u32 s35, 2\n"
"s_cbranch_scc0 Lp19_2_%=\n"
"ds_read_b64_tr_b16 a[100:101], v41 offset:8192\n"
"ds_read_b64_tr_b16 a[102:103], v41 offset:8704\n"
"ds_read_b64_tr_b16 v[68:69], v42 offset:8192\n"
"ds_read_b64_tr_b16 v[70:71], v42 offset:8704\n"
"s_waitcnt lgkmcnt(0)\n"
"ds_write_b128 v1, v[68:71] offset:13312\n"
"s_branch Lq19_%=\n"
"Lp19_2_%=:\n"
"ds_read_b64_tr_b16 a[92:93], v41 offset:8192\n"
"ds_read_b64_tr_b16 a[94:95], v41 offset:8704\n"
"ds_read_b64_tr_b16 v[132:133], v42 offset:8192\n"
"ds_read_b64_tr_b16 v[134:135], v42 offset:8704\n"
"Lq19_%=:\n"
"s_waitcnt vmcnt(12)\n"
"s_waitcnt lgkmcnt(0)\n"
"s_barrier\n"
"s_lshl_b32 s94, s36, 8\n"
"s_add_u32 s94, s94, 2359296\n"
"s_add_u32 m0, s24, 8192\n"
"s_nop 0\n"
"buffer_load_dwordx4 v34, s[20:23], s94 offen lds\n"
"s_add_u32 m0, s24, 9216\n"
"s_nop 0\n"
"buffer_load_dwordx4 v35, s[20:23], s94 offen lds\n"
"s_cmp_eq_u32 s35, 0\n"
"s_cbranch_scc0 Lp20_0_%=\n"
"ds_read_b64_tr_b16 a[88:89], v41 offset:16384\n"
"ds_read_b64_tr_b16 a[90:91], v41 offset:16896\n"
"ds_read_b64_tr_b16 v[128:129], v42 offset:16384\n"
"ds_read_b64_tr_b16 v[130:131], v42 offset:16896\n"
"s_branch Lq20_%=\n"
"Lp20_0_%=:\n"
"s_cmp_eq_u32 s35, 1\n"
"s_cbranch_scc0 Lp20_1_%=\n"
"ds_read_b64_tr_b16 a[72:73], v41 offset:16384\n"
"ds_read_b64_tr_b16 a[74:75], v41 offset:16896\n"
"ds_read_b64_tr_b16 a[248:249], v42 offset:16384\n"
"ds_read_b64_tr_b16 a[250:251], v42 offset:16896\n"
"s_branch Lq20_%=\n"
"Lp20_1_%=:\n"
"s_cmp_eq_u32 s35, 2\n"
"s_cbranch_scc0 Lp20_2_%=\n"
"ds_read_b64_tr_b16 a[104:105], v41 offset:16384\n"
"ds_read_b64_tr_b16 a[106:107], v41 offset:16896\n"
"ds_read_b64_tr_b16 v[136:137], v42 offset:16384\n"
"ds_read_b64_tr_b16 v[138:139], v42 offset:16896\n"
"s_branch Lq20_%=\n"
"Lp20_2_%=:\n"
"ds_read_b64_tr_b16 a[96:97], v41 offset:16384\n"
"ds_read_b64_tr_b16 a[98:99], v41 offset:16896\n"
"ds_read_b64_tr_b16 v[64:65], v42 offset:16384\n"
"ds_read_b64_tr_b16 v[66:67], v42 offset:16896\n"
"s_waitcnt lgkmcnt(0)\n"
"ds_write_b128 v1, v[64:67] offset:12288\n"
"Lq20_%=:\n"
"s_waitcnt vmcnt(12)\n"
"s_waitcnt lgkmcnt(0)\n"
"s_barrier\n"
"s_lshl_b32 s94, s36, 8\n"
"s_add_u32 s94, s94, 2621440\n"
"s_add_u32 m0, s24, 16384\n"
"s_nop 0\n"
"buffer_load_dwordx4 v34, s[20:23], s94 offen lds\n"
"s_add_u32 m0, s24, 17408\n"
"s_nop 0\n"
"buffer_load_dwordx4 v35, s[20:23], s94 offen lds\n"
"s_cmp_eq_u32 s35, 0\n"
"s_cbranch_scc0 Lp21_0_%=\n"
"ds_read_b64_tr_b16 a[92:93], v41 offset:24576\n"
"ds_read_b64_tr_b16 a[94:95], v41 offset:25088\n"
"ds_read_b64_tr_b16 v[132:133], v42 offset:24576\n"
"ds_read_b64_tr_b16 v[134:135], v42 offset:25088\n"
"s_branch Lq21_%=\n"
"Lp21_0_%=:\n"
"s_cmp_eq_u32 s35, 1\n"
"s_cbranch_scc0 Lp21_1_%=\n"
"ds_read_b64_tr_b16 a[76:77], v41 offset:24576\n"
"ds_read_b64_tr_b16 a[78:79], v41 offset:25088\n"
"ds_read_b64_tr_b16 a[252:253], v42 offset:24576\n"
"ds_read_b64_tr_b16 a[254:255], v42 offset:25088\n"
"s_branch Lq21_%=\n"
"Lp21_1_%=:\n"
"s_cmp_eq_u32 s35, 2\n"
"s_cbranch_scc0 Lp21_2_%=\n"
"ds_read_b64_tr_b16 a[108:109], v41 offset:24576\n"
"ds_read_b64_tr_b16 a[110:111], v41 offset:25088\n"
"ds_read_b64_tr_b16 v[140:141], v42 offset:24576\n"
"ds_read_b64_tr_b16 v[142:143], v42 offset:25088\n"
"s_branch Lq21_%=\n"
"Lp21_2_%=:\n"
"ds_read_b64_tr_b16 a[100:101], v41 offset:24576\n"
"ds_read_b64_tr_b16 a[102:103], v41 offset:25088\n"
"ds_read_b64_tr_b16 v[68:69], v42 offset:24576\n"
"ds_read_b64_tr_b16 v[70:71], v42 offset:25088\n"
"s_waitcnt lgkmcnt(0)\n"
"ds_write_b128 v1, v[68:71] offset:13312\n"
"Lq21_%=:\n"
"s_waitcnt vmcnt(12)\n"
"s_waitcnt lgkmcnt(0)\n"
"s_barrier\n"
"s_lshl_b32 s94, s36, 8\n"
"s_add_u32 s94, s94, 2883584\n"
"s_add_u32 m0, s24, 24576\n"
"s_nop 0\n"
"buffer_load_dwordx4 v34, s[20:23], s94 offen lds\n"
"s_add_u32 m0, s24, 25600\n"
"s_nop 0\n"
"buffer_load_dwordx4 v35, s[20:23], s94 offen lds\n"
"s_cmp_eq_u32 s35, 0\n"
"s_cbranch_scc0 Lp22_0_%=\n"
"ds_read_b64_tr_b16 a[96:97], v41 offset:53248\n"
"ds_read_b64_tr_b16 a[98:99], v41 offset:53760\n"
"ds_read_b64_tr_b16 v[64:65], v42 offset:53248\n"
"ds_read_b64_tr_b16 v[66:67], v42 offset:53760\n"
"s_waitcnt lgkmcnt(0)\n"
"ds_write_b128 v1, v[64:67] offset:12288\n"
"s_branch Lq22_%=\n"
"Lp22_0_%=:\n"
"s_cmp_eq_u32 s35, 1\n"
"s_cbranch_scc0 Lp22_1_%=\n"
"ds_read_b64_tr_b16 a[88:89], v41 offset:53248\n"
"ds_read_b64_tr_b16 a[90:91], v41 offset:53760\n"
"ds_read_b64_tr_b16 v[128:129], v42 offset:53248\n"
"ds_read_b64_tr_b16 v[130:131], v42 offset:53760\n"
"s_branch Lq22_%=\n"
"Lp22_1_%=:\n"
"s_cmp_eq_u32 s35, 2\n"
"s_cbranch_scc0 Lp22_2_%=\n"
"ds_read_b64_tr_b16 a[72:73], v41 offset:53248\n"
"ds_read_b64_tr_b16 a[74:75], v41 offset:53760\n"
"ds_read_b64_tr_b16 a[248:249], v42 offset:53248\n"
"ds_read_b64_tr_b16 a[250:251], v42 offset:53760\n"
"s_branch Lq22_%=\n"
"Lp22_2_%=:\n"
"ds_read_b64_tr_b16 a[104:105], v41 offset:53248\n"
"ds_read_b64_tr_b16 a[106:107], v41 offset:53760\n"
"ds_read_b64_tr_b16 v[136:137], v42 offset:53248\n"
"ds_read_b64_tr_b16 v[138:139], v42 offset:53760\n"
"Lq22_%=:\n"
"s_waitcnt vmcnt(12)\n"
"s_waitcnt lgkmcnt(0)\n"
"s_barrier\n"
"s_lshl_b32 s94, s36, 8\n"
"s_add_u32 s94, s94, 3145728\n"
"s_add_u32 m0, s24, 53248\n"
"s_nop 0\n"
"buffer_load_dwordx4 v34, s[20:23], s94 offen lds\n"
"s_add_u32 m0, s24, 54272\n"
"s_nop 0\n"
"buffer_load_dwordx4 v35, s[20:23], s94 offen lds\n"
"s_cmp_eq_u32 s35, 0\n"
"s_cbranch_scc0 Lp23_0_%=\n"
"ds_read_b64_tr_b16 a[100:101], v44 offset:20480\n"
"ds_read_b64_tr_b16 a[102:103], v44 offset:20992\n"
"ds_read_b64_tr_b16 v[68:69], v45 offset:20480\n"
"ds_read_b64_tr_b16 v[70:71], v45 offset:20992\n"
"s_waitcnt lgkmcnt(0)\n"
"ds_write_b128 v1, v[68:71] offset:13312\n"
"s_branch Lq23_%=\n"
"Lp23_0_%=:\n"
"s_cmp_eq_u32 s35, 1\n"
"s_cbranch_scc0 Lp23_1_%=\n"
"ds_read_b64_tr_b16 a[92:93], v44 offset:20480\n"
"ds_read_b64_tr_b16 a[94:95], v44 offset:20992\n"
"ds_read_b64_tr_b16 v[132:133], v45 offset:20480\n"
"ds_read_b64_tr_b16 v[134:135], v45 offset:20992\n"
"s_branch Lq23_%=\n"
"Lp23_1_%=:\n"
"s_cmp_eq_u32 s35, 2\n"
"s_cbranch_scc0 Lp23_2_%=\n"
"ds_read_b64_tr_b16 a[76:77], v44 offset:20480\n"
"ds_read_b64_tr_b16 a[78:79], v44 offset:20992\n"
"ds_read_b64_tr_b16 a[252:253], v45 offset:20480\n"
"ds_read_b64_tr_b16 a[254:255], v45 offset:20992\n"
"s_branch Lq23_%=\n"
"Lp23_2_%=:\n"
"ds_read_b64_tr_b16 a[108:109], v44 offset:20480\n"
"ds_read_b64_tr_b16 a[110:111], v44 offset:20992\n"
"ds_read_b64_tr_b16 v[140:141], v45 offset:20480\n"
"ds_read_b64_tr_b16 v[142:143], v45 offset:20992\n"
"Lq23_%=:\n"
"s_waitcnt vmcnt(12)\n"
"s_waitcnt lgkmcnt(0)\n"
"s_barrier\n"
"s_lshl_b32 s94, s36, 8\n"
"s_add_u32 s94, s94, 3407872\n"
"s_add_u32 m0, s24, 86016\n"
"s_nop 0\n"
"buffer_load_dwordx4 v34, s[20:23], s94 offen lds\n"
"s_add_u32 m0, s24, 87040\n"
"s_nop 0\n"
"buffer_load_dwordx4 v35, s[20:23], s94 offen lds\n"
"s_cmp_eq_u32 s35, 0\n"
"s_cbranch_scc0 Lp24_0_%=\n"
"ds_read_b64_tr_b16 a[104:105], v44 offset:53248\n"
"ds_read_b64_tr_b16 a[106:107], v44 offset:53760\n"
"ds_read_b64_tr_b16 v[136:137], v45 offset:53248\n"
"ds_read_b64_tr_b16 v[138:139], v45 offset:53760\n"
"s_branch Lq24_%=\n"
"Lp24_0_%=:\n"
"s_cmp_eq_u32 s35, 1\n"
"s_cbranch_scc0 Lp24_1_%=\n"
"ds_read_b64_tr_b16 a[96:97], v44 offset:53248\n"
"ds_read_b64_tr_b16 a[98:99], v44 offset:53760\n"
"ds_read_b64_tr_b16 v[64:65], v45 offset:53248\n"
"ds_read_b64_tr_b16 v[66:67], v45 offset:53760\n"
"s_waitcnt lgkmcnt(0)\n"
"ds_write_b128 v1, v[64:67] offset:12288\n"
"s_branch Lq24_%=\n"
"Lp24_1_%=:\n"
"s_cmp_eq_u32 s35, 2\n"
"s_cbranch_scc0 Lp24_2_%=\n"
"ds_read_b64_tr_b16 a[88:89], v44 offset:53248\n"
"ds_read_b64_tr_b16 a[90:91], v44 offset:53760\n"
"ds_read_b64_tr_b16 v[128:129], v45 offset:53248\n"
"ds_read_b64_tr_b16 v[130:131], v45 offset:53760\n"
"s_branch Lq24_%=\n"
"Lp24_2_%=:\n"
"ds_read_b64_tr_b16 a[72:73], v44 offset:53248\n"
"ds_read_b64_tr_b16 a[74:75], v44 offset:53760\n"
"ds_read_b64_tr_b16 a[248:249], v45 offset:53248\n"
"ds_read_b64_tr_b16 a[250:251], v45 offset:53760\n"
"Lq24_%=:\n"
"s_waitcnt vmcnt(12)\n"
"s_waitcnt lgkmcnt(0)\n"
"s_barrier\n"
"s_lshl_b32 s94, s36, 8\n"
"s_add_u32 s94, s94, 3670016\n"
"s_add_u32 m0, s24, 118784\n"
"s_nop 0\n"
"buffer_load_dwordx4 v34, s[20:23], s94 offen lds\n"
"s_add_u32 m0, s24, 119808\n"
"s_nop 0\n"
"buffer_load_dwordx4 v35, s[20:23], s94 offen lds\n"
"s_cmp_eq_u32 s35, 0\n"
"s_cbranch_scc0 Lp25_0_%=\n"
"ds_read_b64_tr_b16 a[108:109], v46 offset:20480\n"
"ds_read_b64_tr_b16 a[110:111], v46 offset:20992\n"
"ds_read_b64_tr_b16 v[140:141], v47 offset:20480\n"
"ds_read_b64_tr_b16 v[142:143], v47 offset:20992\n"
"s_branch Lq25_%=\n"
"Lp25_0_%=:\n"
"s_cmp_eq_u32 s35, 1\n"
"s_cbranch_scc0 Lp25_1_%=\n"
"ds_read_b64_tr_b16 a[100:101], v46 offset:20480\n"
"ds_read_b64_tr_b16 a[102:103], v46 offset:20992\n"
"ds_read_b64_tr_b16 v[68:69], v47 offset:20480\n"
"ds_read_b64_tr_b16 v[70:71], v47 offset:20992\n"
"s_waitcnt lgkmcnt(0)\n"
"ds_write_b128 v1, v[68:71] offset:13312\n"
"s_branch Lq25_%=\n"
"Lp25_1_%=:\n"
"s_cmp_eq_u32 s35, 2\n"
"s_cbranch_scc0 Lp25_2_%=\n"
"ds_read_b64_tr_b16 a[92:93], v46 offset:20480\n"
"ds_read_b64_tr_b16 a[94:95], v46 offset:20992\n"
"ds_read_b64_tr_b16 v[132:133], v47 offset:20480\n"
"ds_read_b64_tr_b16 v[134:135], v47 offset:20992\n"
"s_branch Lq25_%=\n"
"Lp25_2_%=:\n"
"ds_read_b64_tr_b16 a[76:77], v46 offset:20480\n"
"ds_read_b64_tr_b16 a[78:79], v46 offset:20992\n"
"ds_read_b64_tr_b16 a[252:253], v47 offset:20480\n"
"ds_read_b64_tr_b16 a[254:255], v47 offset:20992\n"
"Lq25_%=:\n"
"s_waitcnt vmcnt(12)\n"
"s_waitcnt lgkmcnt(0)\n"
"s_barrier\n"
"s_lshl_b32 s94, s36, 8\n"
"s_add_u32 s94, s94, 3932160\n"
"s_add_u32 m0, s24, 151552\n"
"s_nop 0\n"
"buffer_load_dwordx4 v34, s[20:23], s94 offen lds\n"
"s_add_u32 m0, s24, 152576\n"
"s_nop 0\n"
"buffer_load_dwordx4 v35, s[20:23], s94 offen lds\n"
"s_cmp_eq_u32 s35, 0\n"
"s_cbranch_scc0 Lp26_0_%=\n"
"ds_read_b64_tr_b16 a[80:81], v41\n"
"ds_read_b64_tr_b16 a[82:83], v41 offset:512\n"
"ds_read_b64_tr_b16 v[64:65], v42\n"
"ds_read_b64_tr_b16 v[66:67], v42 offset:512\n"
"s_waitcnt lgkmcnt(0)\n"
"ds_write_b128 v1, v[64:67] offset:10240\n"
"s_branch Lq26_%=\n"
"Lp26_0_%=:\n"
"s_cmp_eq_u32 s35, 1\n"
"s_cbranch_scc0 Lp26_1_%=\n"
"ds_read_b64_tr_b16 a[128:129], v41\n"
"ds_read_b64_tr_b16 a[130:131], v41 offset:512\n"
"ds_read_b64_tr_b16 v[64:65], v42\n"
"ds_read_b64_tr_b16 v[66:67], v42 offset:512\n"
"s_waitcnt lgkmcnt(0)\n"
"ds_write_b128 v1, v[64:67] offset:16384\n"
"s_branch Lq26_%=\n"
"Lp26_1_%=:\n"
"s_cmp_eq_u32 s35, 2\n"
"s_cbranch_scc0 Lp26_2_%=\n"
"ds_read_b64_tr_b16 a[120:121], v41\n"
"ds_read_b64_tr_b16 a[122:123], v41 offset:512\n"
"ds_read_b64_tr_b16 v[144:145], v42\n"
"ds_read_b64_tr_b16 v[146:147], v42 offset:512\n"
"s_branch Lq26_%=\n"
"Lp26_2_%=:\n"
"ds_read_b64_tr_b16 a[112:113], v41\n"
"ds_read_b64_tr_b16 a[114:115], v41 offset:512\n"
"ds_read_b64_tr_b16 v[64:65], v42\n"
"ds_read_b64_tr_b16 v[66:67], v42 offset:512\n"
"s_waitcnt lgkmcnt(0)\n"
"ds_write_b128 v1, v[64:67] offset:14336\n"
"Lq26_%=:\n"
"s_waitcnt vmcnt(12)\n"
"s_waitcnt lgkmcnt(0)\n"
"s_barrier\n"
"s_lshl_b32 s94, s36, 8\n"
"s_add_u32 s94, s94, 4194304\n"
"s_add_u32 m0, s24, 0\n"
"s_nop 0\n"
"buffer_load_dwordx4 v34, s[20:23], s94 offen lds\n"
"s_add_u32 m0, s24, 1024\n"
"s_nop 0\n"
"buffer_load_dwordx4 v35, s[20:23], s94 offen lds\n"
"s_cmp_eq_u32 s35, 0\n"
"s_cbranch_scc0 Lp27_0_%=\n"
"ds_read_b64_tr_b16 a[84:85], v41 offset:8192\n"
"ds_read_b64_tr_b16 a[86:87], v41 offset:8704\n"
"ds_read_b64_tr_b16 v[68:69], v42 offset:8192\n"
"ds_read_b64_tr_b16 v[70:71], v42 offset:8704\n"
"s_waitcnt lgkmcnt(0)\n"
"ds_write_b128 v1, v[68:71] offset:11264\n"
"s_branch Lq27_%=\n"
"Lp27_0_%=:\n"
"s_cmp_eq_u32 s35, 1\n"
"s_cbranch_scc0 Lp27_1_%=\n"
"ds_read_b64_tr_b16 a[132:133], v41 offset:8192\n"
"ds_read_b64_tr_b16 a[134:135], v41 offset:8704\n"
"ds_read_b64_tr_b16 v[68:69], v42 offset:8192\n"
"ds_read_b64_tr_b16 v[70:71], v42 offset:8704\n"
"s_waitcnt lgkmcnt(0)\n"
"ds_write_b128 v1, v[68:71] offset:17408\n"
"s_branch Lq27_%=\n"
"Lp27_1_%=:\n"
"s_cmp_eq_u32 s35, 2\n"
"s_cbranch_scc0 Lp27_2_%=\n"
"ds_read_b64_tr_b16 a[124:125], v41 offset:8192\n"
"ds_read_b64_tr_b16 a[126:127], v41 offset:8704\n"
"ds_read_b64_tr_b16 v[148:149], v42 offset:8192\n"
"ds_read_b64_tr_b16 v[150:151], v42 offset:8704\n"
"s_branch Lq27_%=\n"
"Lp27_2_%=:\n"
"ds_read_b64_tr_b16 a[116:117], v41 offset:8192\n"
"ds_read_b64_tr_b16 a[118:119], v41 offset:8704\n"
"ds_read_b64_tr_b16 v[68:69], v42 offset:8192\n"
"ds_read_b64_tr_b16 v[70:71], v42 offset:8704\n"
"s_waitcnt lgkmcnt(0)\n"
"ds_write_b128 v1, v[68:71] offset:15360\n"
"Lq27_%=:\n"
"s_waitcnt vmcnt(12)\n"
"s_waitcnt lgkmcnt(0)\n"
"s_barrier\n"
"s_lshl_b32 s94, s36, 8\n"
"s_add_u32 s94, s94, 4456448\n"
"s_add_u32 m0, s24, 8192\n"
"s_nop 0\n"
"buffer_load_dwordx4 v34, s[20:23], s94 offen lds\n"
"s_add_u32 m0, s24, 9216\n"
"s_nop 0\n"
"buffer_load_dwordx4 v35, s[20:23], s94 offen lds\n"
"s_cmp_eq_u32 s35, 0\n"
"s_cbranch_scc0 Lp28_0_%=\n"
"ds_read_b64_tr_b16 a[112:113], v41 offset:16384\n"
"ds_read_b64_tr_b16 a[114:115], v41 offset:16896\n"
"ds_read_b64_tr_b16 v[64:65], v42 offset:16384\n"
"ds_read_b64_tr_b16 v[66:67], v42 offset:16896\n"
"s_waitcnt lgkmcnt(0)\n"
"ds_write_b128 v1, v[64:67] offset:14336\n"
"s_branch Lq28_%=\n"
"Lp28_0_%=:\n"
"s_cmp_eq_u32 s35, 1\n"
"s_cbranch_scc0 Lp28_1_%=\n"
"ds_read_b64_tr_b16 a[80:81], v41 offset:16384\n"
"ds_read_b64_tr_b16 a[82:83], v41 offset:16896\n"
"ds_read_b64_tr_b16 v[64:65], v42 offset:16384\n"
"ds_read_b64_tr_b16 v[66:67], v42 offset:16896\n"
"s_waitcnt lgkmcnt(0)\n"
"ds_write_b128 v1, v[64:67] offset:10240\n"
"s_branch Lq28_%=\n"
"Lp28_1_%=:\n"
"s_cmp_eq_u32 s35, 2\n"
"s_cbranch_scc0 Lp28_2_%=\n"
"ds_read_b64_tr_b16 a[128:129], v41 offset:16384\n"
"ds_read_b64_tr_b16 a[130:131], v41 offset:16896\n"
"ds_read_b64_tr_b16 v[64:65], v42 offset:16384\n"
"ds_read_b64_tr_b16 v[66:67], v42 offset:16896\n"
"s_waitcnt lgkmcnt(0)\n"
"ds_write_b128 v1, v[64:67] offset:16384\n"
"s_branch Lq28_%=\n"
"Lp28_2_%=:\n"
"ds_read_b64_tr_b16 a[120:121], v41 offset:16384\n"
"ds_read_b64_tr_b16 a[122:123], v41 offset:16896\n"
"ds_read_b64_tr_b16 v[144:145], v42 offset:16384\n"
"ds_read_b64_tr_b16 v[146:147], v42 offset:16896\n"
"Lq28_%=:\n"
"s_waitcnt vmcnt(12)\n"
"s_waitcnt lgkmcnt(0)\n"
"s_barrier\n"
"s_mov_b32 s20, s12\n"
"s_and_b32 s21, s13, 0xffff\n"
"s_mov_b32 s22, 4718592\n"
"s_mov_b32 s23, 0x20000\n"
"s_lshl_b32 s94, s36, 8\n"
"s_add_u32 s94, s94, 0\n"
"s_add_u32 m0, s24, 16384\n"
"s_nop 0\n"
"buffer_load_dwordx4 v34, s[20:23], s94 offen lds\n"
"s_add_u32 m0, s24, 17408\n"
"s_nop 0\n"
"buffer_load_dwordx4 v35, s[20:23], s94 offen lds\n"
"s_cmp_eq_u32 s35, 0\n"
"s_cbranch_scc0 Lp29_0_%=\n"
"ds_read_b64_tr_b16 a[116:117], v41 offset:24576\n"
"ds_read_b64_tr_b16 a[118:119], v41 offset:25088\n"
"ds_read_b64_tr_b16 v[68:69], v42 offset:24576\n"
"ds_read_b64_tr_b16 v[70:71], v42 offset:25088\n"
"s_waitcnt lgkmcnt(0)\n"
"ds_write_b128 v1, v[68:71] offset:15360\n"
"s_branch Lq29_%=\n"
"Lp29_0_%=:\n"
"s_cmp_eq_u32 s35, 1\n"
"s_cbranch_scc0 Lp29_1_%=\n"
"ds_read_b64_tr_b16 a[84:85], v41 offset:24576\n"
"ds_read_b64_tr_b16 a[86:87], v41 offset:25088\n"
"ds_read_b64_tr_b16 v[68:69], v42 offset:24576\n"
"ds_read_b64_tr_b16 v[70:71], v42 offset:25088\n"
"s_waitcnt lgkmcnt(0)\n"
"ds_write_b128 v1, v[68:71] offset:11264\n"
"s_branch Lq29_%=\n"
"Lp29_1_%=:\n"
"s_cmp_eq_u32 s35, 2\n"
"s_cbranch_scc0 Lp29_2_%=\n"
"ds_read_b64_tr_b16 a[132:133], v41 offset:24576\n"
"ds_read_b64_tr_b16 a[134:135], v41 offset:25088\n"
"ds_read_b64_tr_b16 v[68:69], v42 offset:24576\n"
"ds_read_b64_tr_b16 v[70:71], v42 offset:25088\n"
"s_waitcnt lgkmcnt(0)\n"
"ds_write_b128 v1, v[68:71] offset:17408\n"
"s_branch Lq29_%=\n"
"Lp29_2_%=:\n"
"ds_read_b64_tr_b16 a[124:125], v41 offset:24576\n"
"ds_read_b64_tr_b16 a[126:127], v41 offset:25088\n"
"ds_read_b64_tr_b16 v[148:149], v42 offset:24576\n"
"ds_read_b64_tr_b16 v[150:151], v42 offset:25088\n"
"Lq29_%=:\n"
"s_waitcnt vmcnt(12)\n"
"s_waitcnt lgkmcnt(0)\n"
"s_barrier\n"
"s_lshl_b32 s94, s36, 8\n"
"s_add_u32 s94, s94, 262144\n"
"s_add_u32 m0, s24, 24576\n"
"s_nop 0\n"
"buffer_load_dwordx4 v34, s[20:23], s94 offen lds\n"
"s_add_u32 m0, s24, 25600\n"
"s_nop 0\n"
"buffer_load_dwordx4 v35, s[20:23], s94 offen lds\n"
"s_cmp_eq_u32 s35, 0\n"
"s_cbranch_scc0 Lp30_0_%=\n"
"ds_read_b64_tr_b16 a[120:121], v41 offset:53248\n"
"ds_read_b64_tr_b16 a[122:123], v41 offset:53760\n"
"ds_read_b64_tr_b16 v[144:145], v42 offset:53248\n"
"ds_read_b64_tr_b16 v[146:147], v42 offset:53760\n"
"s_branch Lq30_%=\n"
"Lp30_0_%=:\n"
"s_cmp_eq_u32 s35, 1\n"
"s_cbranch_scc0 Lp30_1_%=\n"
"ds_read_b64_tr_b16 a[112:113], v41 offset:53248\n"
"ds_read_b64_tr_b16 a[114:115], v41 offset:53760\n"
"ds_read_b64_tr_b16 v[64:65], v42 offset:53248\n"
"ds_read_b64_tr_b16 v[66:67], v42 offset:53760\n"
"s_waitcnt lgkmcnt(0)\n"
"ds_write_b128 v1, v[64:67] offset:14336\n"
"s_branch Lq30_%=\n"
"Lp30_1_%=:\n"
"s_cmp_eq_u32 s35, 2\n"
"s_cbranch_scc0 Lp30_2_%=\n"
"ds_read_b64_tr_b16 a[80:81], v41 offset:53248\n"
"ds_read_b64_tr_b16 a[82:83], v41 offset:53760\n"
"ds_read_b64_tr_b16 v[64:65], v42 offset:53248\n"
"ds_read_b64_tr_b16 v[66:67], v42 offset:53760\n"
"s_waitcnt lgkmcnt(0)\n"
"ds_write_b128 v1, v[64:67] offset:10240\n"
"s_branch Lq30_%=\n"
"Lp30_2_%=:\n"
"ds_read_b64_tr_b16 a[128:129], v41 offset:53248\n"
"ds_read_b64_tr_b16 a[130:131], v41 offset:53760\n"
"ds_read_b64_tr_b16 v[64:65], v42 offset:53248\n"
"ds_read_b64_tr_b16 v[66:67], v42 offset:53760\n"
"s_waitcnt lgkmcnt(0)\n"
"ds_write_b128 v1, v[64:67] offset:16384\n"
"Lq30_%=:\n"
"s_waitcnt vmcnt(12)\n"
"s_waitcnt lgkmcnt(0)\n"
"s_barrier\n"
"s_cmp_eq_u32 s35, 0\n"
"s_cbranch_scc0 Lp31_0_%=\n"
"ds_read_b64_tr_b16 a[124:125], v44 offset:20480\n"
"ds_read_b64_tr_b16 a[126:127], v44 offset:20992\n"
"ds_read_b64_tr_b16 v[148:149], v45 offset:20480\n"
"ds_read_b64_tr_b16 v[150:151], v45 offset:20992\n"
"s_branch Lq31_%=\n"
"Lp31_0_%=:\n"
"s_cmp_eq_u32 s35, 1\n"
"s_cbranch_scc0 Lp31_1_%=\n"
"ds_read_b64_tr_b16 a[116:117], v44 offset:20480\n"
"ds_read_b64_tr_b16 a[118:119], v44 offset:20992\n"
"ds_read_b64_tr_b16 v[68:69], v45 offset:20480\n"
"ds_read_b64_tr_b16 v[70:71], v45 offset:20992\n"
"s_waitcnt lgkmcnt(0)\n"
"ds_write_b128 v1, v[68:71] offset:15360\n"
"s_branch Lq31_%=\n"
"Lp31_1_%=:\n"
"s_cmp_eq_u32 s35, 2\n"
"s_cbranch_scc0 Lp31_2_%=\n"
"ds_read_b64_tr_b16 a[84:85], v44 offset:20480\n"
"ds_read_b64_tr_b16 a[86:87], v44 offset:20992\n"
"ds_read_b64_tr_b16 v[68:69], v45 offset:20480\n"
"ds_read_b64_tr_b16 v[70:71], v45 offset:20992\n"
"s_waitcnt lgkmcnt(0)\n"
"ds_write_b128 v1, v[68:71] offset:11264\n"
"s_branch Lq31_%=\n"
"Lp31_2_%=:\n"
"ds_read_b64_tr_b16 a[132:133], v44 offset:20480\n"
"ds_read_b64_tr_b16 a[134:135], v44 offset:20992\n"
"ds_read_b64_tr_b16 v[68:69], v45 offset:20480\n"
"ds_read_b64_tr_b16 v[70:71], v45 offset:20992\n"
"s_waitcnt lgkmcnt(0)\n"
"ds_write_b128 v1, v[68:71] offset:17408\n"
"Lq31_%=:\n"
"s_waitcnt vmcnt(10)\n"
"s_waitcnt lgkmcnt(0)\n"
"s_barrier\n"
"s_cmp_eq_u32 s35, 0\n"
"s_cbranch_scc0 Lp32_0_%=\n"
"ds_read_b64_tr_b16 a[128:129], v44 offset:53248\n"
"ds_read_b64_tr_b16 a[130:131], v44 offset:53760\n"
"ds_read_b64_tr_b16 v[64:65], v45 offset:53248\n"
"ds_read_b64_tr_b16 v[66:67], v45 offset:53760\n"
"s_waitcnt lgkmcnt(0)\n"
"ds_write_b128 v1, v[64:67] offset:16384\n"
"s_branch Lq32_%=\n"
"Lp32_0_%=:\n"
"s_cmp_eq_u32 s35, 1\n"
"s_cbranch_scc0 Lp32_1_%=\n"
"ds_read_b64_tr_b16 a[120:121], v44 offset:53248\n"
"ds_read_b64_tr_b16 a[122:123], v44 offset:53760\n"
"ds_read_b64_tr_b16 v[144:145], v45 offset:53248\n"
"ds_read_b64_tr_b16 v[146:147], v45 offset:53760\n"
"s_branch Lq32_%=\n"
"Lp32_1_%=:\n"
"s_cmp_eq_u32 s35, 2\n"
"s_cbranch_scc0 Lp32_2_%=\n"
"ds_read_b64_tr_b16 a[112:113], v44 offset:53248\n"
"ds_read_b64_tr_b16 a[114:115], v44 offset:53760\n"
"ds_read_b64_tr_b16 v[64:65], v45 offset:53248\n"
"ds_read_b64_tr_b16 v[66:67], v45 offset:53760\n"
"s_waitcnt lgkmcnt(0)\n"
"ds_write_b128 v1, v[64:67] offset:14336\n"
"s_branch Lq32_%=\n"
"Lp32_2_%=:\n"
"ds_read_b64_tr_b16 a[80:81], v44 offset:53248\n"
"ds_read_b64_tr_b16 a[82:83], v44 offset:53760\n"
"ds_read_b64_tr_b16 v[64:65], v45 offset:53248\n"
"ds_read_b64_tr_b16 v[66:67], v45 offset:53760\n"
"s_waitcnt lgkmcnt(0)\n"
"ds_write_b128 v1, v[64:67] offset:10240\n"
"Lq32_%=:\n"
"s_waitcnt vmcnt(8)\n"
"s_waitcnt lgkmcnt(0)\n"
"s_barrier\n"
"s_cmp_eq_u32 s35, 0\n"
"s_cbranch_scc0 Lp33_0_%=\n"
"ds_read_b64_tr_b16 a[132:133], v46 offset:20480\n"
"ds_read_b64_tr_b16 a[134:135], v46 offset:20992\n"
"ds_read_b64_tr_b16 v[68:69], v47 offset:20480\n"
"ds_read_b64_tr_b16 v[70:71], v47 offset:20992\n"
"s_waitcnt lgkmcnt(0)\n"
"ds_write_b128 v1, v[68:71] offset:17408\n"
"s_branch Lq33_%=\n"
"Lp33_0_%=:\n"
"s_cmp_eq_u32 s35, 1\n"
"s_cbranch_scc0 Lp33_1_%=\n"
"ds_read_b64_tr_b16 a[124:125], v46 offset:20480\n"
"ds_read_b64_tr_b16 a[126:127], v46 offset:20992\n"
"ds_read_b64_tr_b16 v[148:149], v47 offset:20480\n"
"ds_read_b64_tr_b16 v[150:151], v47 offset:20992\n"
"s_branch Lq33_%=\n"
"Lp33_1_%=:\n"
"s_cmp_eq_u32 s35, 2\n"
"s_cbranch_scc0 Lp33_2_%=\n"
"ds_read_b64_tr_b16 a[116:117], v46 offset:20480\n"
"ds_read_b64_tr_b16 a[118:119], v46 offset:20992\n"
"ds_read_b64_tr_b16 v[68:69], v47 offset:20480\n"
"ds_read_b64_tr_b16 v[70:71], v47 offset:20992\n"
"s_waitcnt lgkmcnt(0)\n"
"ds_write_b128 v1, v[68:71] offset:15360\n"
"s_branch Lq33_%=\n"
"Lp33_2_%=:\n"
"ds_read_b64_tr_b16 a[84:85], v46 offset:20480\n"
"ds_read_b64_tr_b16 a[86:87], v46 offset:20992\n"
"ds_read_b64_tr_b16 v[68:69], v47 offset:20480\n"
"ds_read_b64_tr_b16 v[70:71], v47 offset:20992\n"
"s_waitcnt lgkmcnt(0)\n"
"ds_write_b128 v1, v[68:71] offset:11264\n"
"Lq33_%=:\n"
"s_waitcnt vmcnt(6)\n"
"s_waitcnt lgkmcnt(0)\n"
"s_barrier\n"
"ds_read_b64_tr_b16 a[136:137], v41\n"
"ds_read_b64_tr_b16 a[138:139], v41 offset:512\n"
"ds_read_b64_tr_b16 v[64:65], v42\n"
"ds_read_b64_tr_b16 v[66:67], v42 offset:512\n"
"s_waitcnt lgkmcnt(0)\n"
"ds_write_b128 v1, v[64:67] offset:18432\n"
"Lq34_%=:\n"
"s_waitcnt vmcnt(4)\n"
"s_waitcnt lgkmcnt(0)\n"
"s_barrier\n"
"s_lshl_b32 s94, s36, 8\n"
"s_add_u32 s94, s94, 524288\n"
"s_add_u32 m0, s24, 0\n"
"s_nop 0\n"
"buffer_load_dwordx4 v34, s[20:23], s94 offen lds\n"
"s_add_u32 m0, s24, 1024\n"
"s_nop 0\n"
"buffer_load_dwordx4 v35, s[20:23], s94 offen lds\n"
"ds_read_b64_tr_b16 a[140:141], v41 offset:8192\n"
"ds_read_b64_tr_b16 a[142:143], v41 offset:8704\n"
"ds_read_b64_tr_b16 v[68:69], v42 offset:8192\n"
"ds_read_b64_tr_b16 v[70:71], v42 offset:8704\n"
"s_waitcnt lgkmcnt(0)\n"
"ds_write_b128 v1, v[68:71] offset:19456\n"
"Lq35_%=:\n"
"s_waitcnt vmcnt(4)\n"
"s_waitcnt lgkmcnt(0)\n"
"s_barrier\n"
"s_lshl_b32 s94, s36, 8\n"
"s_add_u32 s94, s94, 786432\n"
"s_add_u32 m0, s24, 8192\n"
"s_nop 0\n"
"buffer_load_dwordx4 v34, s[20:23], s94 offen lds\n"
"s_add_u32 m0, s24, 9216\n"
"s_nop 0\n"
"buffer_load_dwordx4 v35, s[20:23], s94 offen lds\n"
"s_cmp_eq_u32 s35, 0\n"
"s_cbranch_scc0 Lp36_0_%=\n"
"ds_read_b64_tr_b16 a[144:145], v41 offset:16384\n"
"ds_read_b64_tr_b16 a[146:147], v41 offset:16896\n"
"ds_read_b64_tr_b16 v[152:153], v42 offset:16384\n"
"ds_read_b64_tr_b16 v[154:155], v42 offset:16896\n"
"s_branch Lq36_%=\n"
"Lp36_0_%=:\n"
"s_cmp_eq_u32 s35, 1\n"
"s_cbranch_scc0 Lp36_1_%=\n"
"ds_read_b64_tr_b16 a[176:177], v41 offset:16384\n"
"ds_read_b64_tr_b16 a[178:179], v41 offset:16896\n"
"ds_read_b64_tr_b16 v[64:65], v42 offset:16384\n"
"ds_read_b64_tr_b16 v[66:67], v42 offset:16896\n"
"s_waitcnt lgkmcnt(0)\n"
"ds_write_b128 v1, v[64:67] offset:22528\n"
"s_branch Lq36_%=\n"
"Lp36_1_%=:\n"
"s_cmp_eq_u32 s35, 2\n"
"s_cbranch_scc0 Lp36_2_%=\n"
"ds_read_b64_tr_b16 a[168:169], v41 offset:16384\n"
"ds_read_b64_tr_b16 a[170:171], v41 offset:16896\n"
"ds_read_b64_tr_b16 v[168:169], v42 offset:16384\n"
"ds_read_b64_tr_b16 v[170:171], v42 offset:16896\n"
"s_branch Lq36_%=\n"
"Lp36_2_%=:\n"
"ds_read_b64_tr_b16 a[160:161], v41 offset:16384\n"
"ds_read_b64_tr_b16 a[162:163], v41 offset:16896\n"
"ds_read_b64_tr_b16 v[160:161], v42 offset:16384\n"
"ds_read_b64_tr_b16 v[162:163], v42 offset:16896\n"
"Lq36_%=:\n"
"s_waitcnt vmcnt(4)\n"
"s_waitcnt lgkmcnt(0)\n"
"s_barrier\n"
"s_lshl_b32 s94, s36, 8\n"
"s_add_u32 s94, s94, 1048576\n"
"s_add_u32 m0, s24, 16384\n"
"s_nop 0\n"
"buffer_load_dwordx4 v34, s[20:23], s94 offen lds\n"
"s_add_u32 m0, s24, 17408\n"
"s_nop 0\n"
"buffer_load_dwordx4 v35, s[20:23], s94 offen lds\n"
"s_cmp_eq_u32 s35, 0\n"
"s_cbranch_scc0 Lp37_0_%=\n"
"ds_read_b64_tr_b16 a[148:149], v41 offset:24576\n"
"ds_read_b64_tr_b16 a[150:151], v41 offset:25088\n"
"ds_read_b64_tr_b16 v[156:157], v42 offset:24576\n"
"ds_read_b64_tr_b16 v[158:159], v42 offset:25088\n"
"s_branch Lq37_%=\n"
"Lp37_0_%=:\n"
"s_cmp_eq_u32 s35, 1\n"
"s_cbranch_scc0 Lp37_1_%=\n"
"ds_read_b64_tr_b16 a[180:181], v41 offset:24576\n"
"ds_read_b64_tr_b16 a[182:183], v41 offset:25088\n"
"ds_read_b64_tr_b16 v[68:69], v42 offset:24576\n"
"ds_read_b64_tr_b16 v[70:71], v42 offset:25088\n"
"s_waitcnt lgkmcnt(0)\n"
"ds_write_b128 v1, v[68:71] offset:23552\n"
"s_branch Lq37_%=\n"
"Lp37_1_%=:\n"
"s_cmp_eq_u32 s35, 2\n"
"s_cbranch_scc0 Lp37_2_%=\n"
"ds_read_b64_tr_b16 a[172:173], v41 offset:24576\n"
"ds_read_b64_tr_b16 a[174:175], v41 offset:25088\n"
"ds_read_b64_tr_b16 v[172:173], v42 offset:24576\n"
"ds_read_b64_tr_b16 v[174:175], v42 offset:25088\n"
"s_branch Lq37_%=\n"
"Lp37_2_%=:\n"
"ds_read_b64_tr_b16 a[164:165], v41 offset:24576\n"
"ds_read_b64_tr_b16 a[166:167], v41 offset:25088\n"
"ds_read_b64_tr_b16 v[164:165], v42 offset:24576\n"
"ds_read_b64_tr_b16 v[166:167], v42 offset:25088\n"
"Lq37_%=:\n"
"s_waitcnt vmcnt(4)\n"
"s_waitcnt lgkmcnt(0)\n"
"s_barrier\n"
"s_lshl_b32 s94, s36, 8\n"
"s_add_u32 s94, s94, 1310720\n"
"s_add_u32 m0, s24, 24576\n"
"s_nop 0\n"
"buffer_load_dwordx4 v34, s[20:23], s94 offen lds\n"
"s_add_u32 m0, s24, 25600\n"
"s_nop 0\n"
"buffer_load_dwordx4 v35, s[20:23], s94 offen lds\n"
"s_cmp_eq_u32 s35, 0\n"
"s_cbranch_scc0 Lp38_0_%=\n"
"ds_read_b64_tr_b16 a[160:161], v41\n"
"ds_read_b64_tr_b16 a[162:163], v41 offset:512\n"
"ds_read_b64_tr_b16 v[160:161], v42\n"
"ds_read_b64_tr_b16 v[162:163], v42 offset:512\n"
"s_branch Lq38_%=\n"
"Lp38_0_%=:\n"
"s_cmp_eq_u32 s35, 1\n"
"s_cbranch_scc0 Lp38_1_%=\n"
"ds_read_b64_tr_b16 a[144:145], v41\n"
"ds_read_b64_tr_b16 a[146:147], v41 offset:512\n"
"ds_read_b64_tr_b16 v[152:153], v42\n"
"ds_read_b64_tr_b16 v[154:155], v42 offset:512\n"
"s_branch Lq38_%=\n"
"Lp38_1_%=:\n"
"s_cmp_eq_u32 s35, 2\n"
"s_cbranch_scc0 Lp38_2_%=\n"
"ds_read_b64_tr_b16 a[176:177], v41\n"
"ds_read_b64_tr_b16 a[178:179], v41 offset:512\n"
"ds_read_b64_tr_b16 v[64:65], v42\n"
"ds_read_b64_tr_b16 v[66:67], v42 offset:512\n"
"s_waitcnt lgkmcnt(0)\n"
"ds_write_b128 v1, v[64:67] offset:22528\n"
"s_branch Lq38_%=\n"
"Lp38_2_%=:\n"
"ds_read_b64_tr_b16 a[168:169], v41\n"
"ds_read_b64_tr_b16 a[170:171], v41 offset:512\n"
"ds_read_b64_tr_b16 v[168:169], v42\n"
"ds_read_b64_tr_b16 v[170:171], v42 offset:512\n"
"Lq38_%=:\n"
"s_waitcnt vmcnt(4)\n"
"s_waitcnt lgkmcnt(0)\n"
"s_barrier\n"
"s_lshl_b32 s94, s36, 8\n"
"s_add_u32 s94, s94, 1572864\n"
"s_add_u32 m0, s24, 0\n"
"s_nop 0\n"
"buffer_load_dwordx4 v34, s[20:23], s94 offen lds\n"
"s_add_u32 m0, s24, 1024\n"
"s_nop 0\n"
"buffer_load_dwordx4 v35, s[20:23], s94 offen lds\n"
"s_cmp_eq_u32 s35, 0\n"
"s_cbranch_scc0 Lp39_0_%=\n"
"ds_read_b64_tr_b16 a[164:165], v41 offset:8192\n"
"ds_read_b64_tr_b16 a[166:167], v41 offset:8704\n"
"ds_read_b64_tr_b16 v[164:165], v42 offset:8192\n"
"ds_read_b64_tr_b16 v[166:167], v42 offset:8704\n"
"s_branch Lq39_%=\n"
"Lp39_0_%=:\n"
"s_cmp_eq_u32 s35, 1\n"
"s_cbranch_scc0 Lp39_1_%=\n"
"ds_read_b64_tr_b16 a[148:149], v41 offset:8192\n"
"ds_read_b64_tr_b16 a[150:151], v41 offset:8704\n"
"ds_read_b64_tr_b16 v[156:157], v42 offset:8192\n"
"ds_read_b64_tr_b16 v[158:159], v42 offset:8704\n"
"s_branch Lq39_%=\n"
"Lp39_1_%=:\n"
"s_cmp_eq_u32 s35, 2\n"
"s_cbranch_scc0 Lp39_2_%=\n"
"ds_read_b64_tr_b16 a[180:181], v41 offset:8192\n"
"ds_read_b64_tr_b16 a[182:183], v41 offset:8704\n"
"ds_read_b64_tr_b16 v[68:69], v42 offset:8192\n"
"ds_read_b64_tr_b16 v[70:71], v42 offset:8704\n"
"s_waitcnt lgkmcnt(0)\n"
"ds_write_b128 v1, v[68:71] offset:23552\n"
"s_branch Lq39_%=\n"
"Lp39_2_%=:\n"
"ds_read_b64_tr_b16 a[172:173], v41 offset:8192\n"
"ds_read_b64_tr_b16 a[174:175], v41 offset:8704\n"
"ds_read_b64_tr_b16 v[172:173], v42 offset:8192\n"
"ds_read_b64_tr_b16 v[174:175], v42 offset:8704\n"
"Lq39_%=:\n"
"s_waitcnt vmcnt(4)\n"
"s_waitcnt lgkmcnt(0)\n"
"s_barrier\n"
"s_lshl_b32 s94, s36, 8\n"
"s_add_u32 s94, s94, 1835008\n"
"s_add_u32 m0, s24, 8192\n"
"s_nop 0\n"
"buffer_load_dwordx4 v34, s[20:23], s94 offen lds\n"
"s_add_u32 m0, s24, 9216\n"
"s_nop 0\n"
"buffer_load_dwordx4 v35, s[20:23], s94 offen lds\n"
"s_cmp_eq_u32 s35, 0\n"
"s_cbranch_scc0 Lp40_0_%=\n"
"ds_read_b64_tr_b16 a[168:169], v41 offset:16384\n"
"ds_read_b64_tr_b16 a[170:171], v41 offset:16896\n"
"ds_read_b64_tr_b16 v[168:169], v42 offset:16384\n"
"ds_read_b64_tr_b16 v[170:171], v42 offset:16896\n"
"s_branch Lq40_%=\n"
"Lp40_0_%=:\n"
"s_cmp_eq_u32 s35, 1\n"
"s_cbranch_scc0 Lp40_1_%=\n"
"ds_read_b64_tr_b16 a[160:161], v41 offset:16384\n"
"ds_read_b64_tr_b16 a[162:163], v41 offset:16896\n"
"ds_read_b64_tr_b16 v[160:161], v42 offset:16384\n"
"ds_read_b64_tr_b16 v[162:163], v42 offset:16896\n"
"s_branch Lq40_%=\n"
"Lp40_1_%=:\n"
"s_cmp_eq_u32 s35, 2\n"
"s_cbranch_scc0 Lp40_2_%=\n"
"ds_read_b64_tr_b16 a[144:145], v41 offset:16384\n"
"ds_read_b64_tr_b16 a[146:147], v41 offset:16896\n"
"ds_read_b64_tr_b16 v[152:153], v42 offset:16384\n"
"ds_read_b64_tr_b16 v[154:155], v42 offset:16896\n"
"s_branch Lq40_%=\n"
"Lp40_2_%=:\n"
"ds_read_b64_tr_b16 a[176:177], v41 offset:16384\n"
"ds_read_b64_tr_b16 a[178:179], v41 offset:16896\n"
"ds_read_b64_tr_b16 v[64:65], v42 offset:16384\n"
"ds_read_b64_tr_b16 v[66:67], v42 offset:16896\n"
"s_waitcnt lgkmcnt(0)\n"
"ds_write_b128 v1, v[64:67] offset:22528\n"
"Lq40_%=:\n"
"s_waitcnt vmcnt(4)\n"
"s_waitcnt lgkmcnt(0)\n"
"s_barrier\n"
"s_lshl_b32 s94, s36, 8\n"
"s_add_u32 s94, s94, 2097152\n"
"s_add_u32 m0, s24, 16384\n"
"s_nop 0\n"
"buffer_load_dwordx4 v34, s[20:23], s94 offen lds\n"
"s_add_u32 m0, s24, 17408\n"
"s_nop 0\n"
"buffer_load_dwordx4 v35, s[20:23], s94 offen lds\n"
"s_cmp_eq_u32 s35, 0\n"
"s_cbranch_scc0 Lp41_0_%=\n"
"ds_read_b64_tr_b16 a[172:173], v41 offset:24576\n"
"ds_read_b64_tr_b16 a[174:175], v41 offset:25088\n"
"ds_read_b64_tr_b16 v[172:173], v42 offset:24576\n"
"ds_read_b64_tr_b16 v[174:175], v42 offset:25088\n"
"s_branch Lq41_%=\n"
"Lp41_0_%=:\n"
"s_cmp_eq_u32 s35, 1\n"
"s_cbranch_scc0 Lp41_1_%=\n"
"ds_read_b64_tr_b16 a[164:165], v41 offset:24576\n"
"ds_read_b64_tr_b16 a[166:167], v41 offset:25088\n"
"ds_read_b64_tr_b16 v[164:165], v42 offset:24576\n"
"ds_read_b64_tr_b16 v[166:167], v42 offset:25088\n"
"s_branch Lq41_%=\n"
"Lp41_1_%=:\n"
"s_cmp_eq_u32 s35, 2\n"
"s_cbranch_scc0 Lp41_2_%=\n"
"ds_read_b64_tr_b16 a[148:149], v41 offset:24576\n"
"ds_read_b64_tr_b16 a[150:151], v41 offset:25088\n"
"ds_read_b64_tr_b16 v[156:157], v42 offset:24576\n"
"ds_read_b64_tr_b16 v[158:159], v42 offset:25088\n"
"s_branch Lq41_%=\n"
"Lp41_2_%=:\n"
"ds_read_b64_tr_b16 a[180:181], v41 offset:24576\n"
"ds_read_b64_tr_b16 a[182:183], v41 offset:25088\n"
"ds_read_b64_tr_b16 v[68:69], v42 offset:24576\n"
"ds_read_b64_tr_b16 v[70:71], v42 offset:25088\n"
"s_waitcnt lgkmcnt(0)\n"
"ds_write_b128 v1, v[68:71] offset:23552\n"
"Lq41_%=:\n"
"s_waitcnt vmcnt(4)\n"
"s_waitcnt lgkmcnt(0)\n"
"s_barrier\n"
"s_lshl_b32 s94, s36, 8\n"
"s_add_u32 s94, s94, 2359296\n"
"s_add_u32 m0, s24, 24576\n"
"s_nop 0\n"
"buffer_load_dwordx4 v34, s[20:23], s94 offen lds\n"
"s_add_u32 m0, s24, 25600\n"
"s_nop 0\n"
"buffer_load_dwordx4 v35, s[20:23], s94 offen lds\n"
"s_cmp_eq_u32 s35, 0\n"
"s_cbranch_scc0 Lp42_0_%=\n"
"ds_read_b64_tr_b16 a[176:177], v41\n"
"ds_read_b64_tr_b16 a[178:179], v41 offset:512\n"
"ds_read_b64_tr_b16 v[64:65], v42\n"
"ds_read_b64_tr_b16 v[66:67], v42 offset:512\n"
"s_waitcnt lgkmcnt(0)\n"
"ds_write_b128 v1, v[64:67] offset:22528\n"
"s_branch Lq42_%=\n"
"Lp42_0_%=:\n"
"s_cmp_eq_u32 s35, 1\n"
"s_cbranch_scc0 Lp42_1_%=\n"
"ds_read_b64_tr_b16 a[168:169], v41\n"
"ds_read_b64_tr_b16 a[170:171], v41 offset:512\n"
"ds_read_b64_tr_b16 v[168:169], v42\n"
"ds_read_b64_tr_b16 v[170:171], v42 offset:512\n"
"s_branch Lq42_%=\n"
"Lp42_1_%=:\n"
"s_cmp_eq_u32 s35, 2\n"
"s_cbranch_scc0 Lp42_2_%=\n"
"ds_read_b64_tr_b16 a[160:161], v41\n"
"ds_read_b64_tr_b16 a[162:163], v41 offset:512\n"
"ds_read_b64_tr_b16 v[160:161], v42\n"
"ds_read_b64_tr_b16 v[162:163], v42 offset:512\n"
"s_branch Lq42_%=\n"
"Lp42_2_%=:\n"
"ds_read_b64_tr_b16 a[144:145], v41\n"
"ds_read_b64_tr_b16 a[146:147], v41 offset:512\n"
"ds_read_b64_tr_b16 v[152:153], v42\n"
"ds_read_b64_tr_b16 v[154:155], v42 offset:512\n"
"Lq42_%=:\n"
"s_waitcnt vmcnt(4)\n"
"s_waitcnt lgkmcnt(0)\n"
"s_barrier\n"
"s_lshl_b32 s94, s36, 8\n"
"s_add_u32 s94, s94, 2621440\n"
"s_add_u32 m0, s24, 0\n"
"s_nop 0\n"
"buffer_load_dwordx4 v34, s[20:23], s94 offen lds\n"
"s_add_u32 m0, s24, 1024\n"
"s_nop 0\n"
"buffer_load_dwordx4 v35, s[20:23], s94 offen lds\n"
"s_cmp_eq_u32 s35, 0\n"
"s_cbranch_scc0 Lp43_0_%=\n"
"ds_read_b64_tr_b16 a[180:181], v41 offset:8192\n"
"ds_read_b64_tr_b16 a[182:183], v41 offset:8704\n"
"ds_read_b64_tr_b16 v[68:69], v42 offset:8192\n"
"ds_read_b64_tr_b16 v[70:71], v42 offset:8704\n"
"s_waitcnt lgkmcnt(0)\n"
"ds_write_b128 v1, v[68:71] offset:23552\n"
"s_branch Lq43_%=\n"
"Lp43_0_%=:\n"
"s_cmp_eq_u32 s35, 1\n"
"s_cbranch_scc0 Lp43_1_%=\n"
"ds_read_b64_tr_b16 a[172:173], v41 offset:8192\n"
"ds_read_b64_tr_b16 a[174:175], v41 offset:8704\n"
"ds_read_b64_tr_b16 v[172:173], v42 offset:8192\n"
"ds_read_b64_tr_b16 v[174:175], v42 offset:8704\n"
"s_branch Lq43_%=\n"
"Lp43_1_%=:\n"
"s_cmp_eq_u32 s35, 2\n"
"s_cbranch_scc0 Lp43_2_%=\n"
"ds_read_b64_tr_b16 a[164:165], v41 offset:8192\n"
"ds_read_b64_tr_b16 a[166:167], v41 offset:8704\n"
"ds_read_b64_tr_b16 v[164:165], v42 offset:8192\n"
"ds_read_b64_tr_b16 v[166:167], v42 offset:8704\n"
"s_branch Lq43_%=\n"
"Lp43_2_%=:\n"
"ds_read_b64_tr_b16 a[148:149], v41 offset:8192\n"
"ds_read_b64_tr_b16 a[150:151], v41 offset:8704\n"
"ds_read_b64_tr_b16 v[156:157], v42 offset:8192\n"
"ds_read_b64_tr_b16 v[158:159], v42 offset:8704\n"
"Lq43_%=:\n"
"s_waitcnt vmcnt(4)\n"
"s_waitcnt lgkmcnt(0)\n"
"s_barrier\n"
"s_lshl_b32 s94, s36, 8\n"
"s_add_u32 s94, s94, 2883584\n"
"s_add_u32 m0, s24, 8192\n"
"s_nop 0\n"
"buffer_load_dwordx4 v34, s[20:23], s94 offen lds\n"
"s_add_u32 m0, s24, 9216\n"
"s_nop 0\n"
"buffer_load_dwordx4 v35, s[20:23], s94 offen lds\n"
"s_cmp_eq_u32 s35, 0\n"
"s_cbranch_scc0 Lp44_0_%=\n"
"ds_read_b64_tr_b16 a[152:153], v41 offset:16384\n"
"ds_read_b64_tr_b16 a[154:155], v41 offset:16896\n"
"ds_read_b64_tr_b16 v[64:65], v42 offset:16384\n"
"ds_read_b64_tr_b16 v[66:67], v42 offset:16896\n"
"s_waitcnt lgkmcnt(0)\n"
"ds_write_b128 v1, v[64:67] offset:20480\n"
"s_branch Lq44_%=\n"
"Lp44_0_%=:\n"
"s_cmp_eq_u32 s35, 1\n"
"s_cbranch_scc0 Lp44_1_%=\n"
"ds_read_b64_tr_b16 a[200:201], v41 offset:16384\n"
"ds_read_b64_tr_b16 a[202:203], v41 offset:16896\n"
"ds_read_b64_tr_b16 v[184:185], v42 offset:16384\n"
"ds_read_b64_tr_b16 v[186:187], v42 offset:16896\n"
"s_branch Lq44_%=\n"
"Lp44_1_%=:\n"
"s_cmp_eq_u32 s35, 2\n"
"s_cbranch_scc0 Lp44_2_%=\n"
"ds_read_b64_tr_b16 a[192:193], v41 offset:16384\n"
"ds_read_b64_tr_b16 a[194:195], v41 offset:16896\n"
"ds_read_b64_tr_b16 v[64:65], v42 offset:16384\n"
"ds_read_b64_tr_b16 v[66:67], v42 offset:16896\n"
"s_waitcnt lgkmcnt(0)\n"
"ds_write_b128 v1, v[64:67] offset:24576\n"
"s_branch Lq44_%=\n"
"Lp44_2_%=:\n"
"ds_read_b64_tr_b16 a[184:185], v41 offset:16384\n"
"ds_read_b64_tr_b16 a[186:187], v41 offset:16896\n"
"ds_read_b64_tr_b16 v[176:177], v42 offset:16384\n"
"ds_read_b64_tr_b16 v[178:179], v42 offset:16896\n"
"Lq44_%=:\n"
"s_waitcnt vmcnt(4)\n"
"s_waitcnt lgkmcnt(0)\n"
"s_barrier\n"
"s_lshl_b32 s94, s36, 8\n"
"s_add_u32 s94, s94, 3145728\n"
"s_add_u32 m0, s24, 16384\n"
"s_nop 0\n"
"buffer_load_dwordx4 v34, s[20:23], s94 offen lds\n"
"s_add_u32 m0, s24, 17408\n"
"s_nop 0\n"
"buffer_load_dwordx4 v35, s[20:23], s94 offen lds\n"
"s_cmp_eq_u32 s35, 0\n"
"s_cbranch_scc0 Lp45_0_%=\n"
"ds_read_b64_tr_b16 a[156:157], v41 offset:24576\n"
"ds_read_b64_tr_b16 a[158:159], v41 offset:25088\n"
"ds_read_b64_tr_b16 v[68:69], v42 offset:24576\n"
"ds_read_b64_tr_b16 v[70:71], v42 offset:25088\n"
"s_waitcnt lgkmcnt(0)\n"
"ds_write_b128 v1, v[68:71] offset:21504\n"
"s_branch Lq45_%=\n"
"Lp45_0_%=:\n"
"s_cmp_eq_u32 s35, 1\n"
"s_cbranch_scc0 Lp45_1_%=\n"
"ds_read_b64_tr_b16 a[204:205], v41 offset:24576\n"
"ds_read_b64_tr_b16 a[206:207], v41 offset:25088\n"
"ds_read_b64_tr_b16 v[188:189], v42 offset:24576\n"
"ds_read_b64_tr_b16 v[190:191], v42 offset:25088\n"
"s_branch Lq45_%=\n"
"Lp45_1_%=:\n"
"s_cmp_eq_u32 s35, 2\n"
"s_cbranch_scc0 Lp45_2_%=\n"
"ds_read_b64_tr_b16 a[196:197], v41 offset:24576\n"
"ds_read_b64_tr_b16 a[198:199], v41 offset:25088\n"
"ds_read_b64_tr_b16 v[68:69], v42 offset:24576\n"
"ds_read_b64_tr_b16 v[70:71], v42 offset:25088\n"
"s_waitcnt lgkmcnt(0)\n"
"ds_write_b128 v1, v[68:71] offset:25600\n"
"s_branch Lq45_%=\n"
"Lp45_2_%=:\n"
"ds_read_b64_tr_b16 a[188:189], v41 offset:24576\n"
"ds_read_b64_tr_b16 a[190:191], v41 offset:25088\n"
"ds_read_b64_tr_b16 v[180:181], v42 offset:24576\n"
"ds_read_b64_tr_b16 v[182:183], v42 offset:25088\n"
"Lq45_%=:\n"
"s_waitcnt vmcnt(4)\n"
"s_waitcnt lgkmcnt(0)\n"
"s_barrier\n"
"s_lshl_b32 s94, s36, 8\n"
"s_add_u32 s94, s94, 3407872\n"
"s_add_u32 m0, s24, 24576\n"
"s_nop 0\n"
"buffer_load_dwordx4 v34, s[20:23], s94 offen lds\n"
"s_add_u32 m0, s24, 25600\n"
"s_nop 0\n"
"buffer_load_dwordx4 v35, s[20:23], s94 offen lds\n"
"s_cmp_eq_u32 s35, 0\n"
"s_cbranch_scc0 Lp46_0_%=\n"
"ds_read_b64_tr_b16 a[184:185], v41\n"
"ds_read_b64_tr_b16 a[186:187], v41 offset:512\n"
"ds_read_b64_tr_b16 v[176:177], v42\n"
"ds_read_b64_tr_b16 v[178:179], v42 offset:512\n"
"s_branch Lq46_%=\n"
"Lp46_0_%=:\n"
"s_cmp_eq_u32 s35, 1\n"
"s_cbranch_scc0 Lp46_1_%=\n"
"ds_read_b64_tr_b16 a[152:153], v41\n"
"ds_read_b64_tr_b16 a[154:155], v41 offset:512\n"
"ds_read_b64_tr_b16 v[64:65], v42\n"
"ds_read_b64_tr_b16 v[66:67], v42 offset:512\n"
"s_waitcnt lgkmcnt(0)\n"
"ds_write_b128 v1, v[64:67] offset:20480\n"
"s_branch Lq46_%=\n"
"Lp46_1_%=:\n"
"s_cmp_eq_u32 s35, 2\n"
"s_cbranch_scc0 Lp46_2_%=\n"
"ds_read_b64_tr_b16 a[200:201], v41\n"
"ds_read_b64_tr_b16 a[202:203], v41 offset:512\n"
"ds_read_b64_tr_b16 v[184:185], v42\n"
"ds_read_b64_tr_b16 v[186:187], v42 offset:512\n"
"s_branch Lq46_%=\n"
"Lp46_2_%=:\n"
"ds_read_b64_tr_b16 a[192:193], v41\n"
"ds_read_b64_tr_b16 a[194:195], v41 offset:512\n"
"ds_read_b64_tr_b16 v[64:65], v42\n"
"ds_read_b64_tr_b16 v[66:67], v42 offset:512\n"
"s_waitcnt lgkmcnt(0)\n"
"ds_write_b128 v1, v[64:67] offset:24576\n"
"Lq46_%=:\n"
"s_waitcnt vmcnt(4)\n"
"s_waitcnt lgkmcnt(0)\n"
"s_barrier\n"
"s_lshl_b32 s94, s36, 8\n"
"s_add_u32 s94, s94, 3670016\n"
"s_add_u32 m0, s24, 0\n"
"s_nop 0\n"
"buffer_load_dwordx4 v34, s[20:23], s94 offen lds\n"
"s_add_u32 m0, s24, 1024\n"
"s_nop 0\n"
"buffer_load_dwordx4 v35, s[20:23], s94 offen lds\n"
"s_cmp_eq_u32 s35, 0\n"
"s_cbranch_scc0 Lp47_0_%=\n"
"ds_read_b64_tr_b16 a[188:189], v41 offset:8192\n"
"ds_read_b64_tr_b16 a[190:191], v41 offset:8704\n"
"ds_read_b64_tr_b16 v[180:181], v42 offset:8192\n"
"ds_read_b64_tr_b16 v[182:183], v42 offset:8704\n"
"s_branch Lq47_%=\n"
"Lp47_0_%=:\n"
"s_cmp_eq_u32 s35, 1\n"
"s_cbranch_scc0 Lp47_1_%=\n"
"ds_read_b64_tr_b16 a[156:157], v41 offset:8192\n"
"ds_read_b64_tr_b16 a[158:159], v41 offset:8704\n"
"ds_read_b64_tr_b16 v[68:69], v42 offset:8192\n"
"ds_read_b64_tr_b16 v[70:71], v42 offset:8704\n"
"s_waitcnt lgkmcnt(0)\n"
"ds_write_b128 v1, v[68:71] offset:21504\n"
"s_branch Lq47_%=\n"
"Lp47_1_%=:\n"
"s_cmp_eq_u32 s35, 2\n"
"s_cbranch_scc0 Lp47_2_%=\n"
"ds_read_b64_tr_b16 a[204:205], v41 offset:8192\n"
"ds_read_b64_tr_b16 a[206:207], v41 offset:8704\n"
"ds_read_b64_tr_b16 v[188:189], v42 offset:8192\n"
"ds_read_b64_tr_b16 v[190:191], v42 offset:8704\n"
"s_branch Lq47_%=\n"
"Lp47_2_%=:\n"
"ds_read_b64_tr_b16 a[196:197], v41 offset:8192\n"
"ds_read_b64_tr_b16 a[198:199], v41 offset:8704\n"
"ds_read_b64_tr_b16 v[68:69], v42 offset:8192\n"
"ds_read_b64_tr_b16 v[70:71], v42 offset:8704\n"
"s_waitcnt lgkmcnt(0)\n"
"ds_write_b128 v1, v[68:71] offset:25600\n"
"Lq47_%=:\n"
"s_waitcnt vmcnt(4)\n"
"s_waitcnt lgkmcnt(0)\n"
"s_barrier\n"
"s_lshl_b32 s94, s36, 8\n"
"s_add_u32 s94, s94, 3932160\n"
"s_add_u32 m0, s24, 8192\n"
"s_nop 0\n"
"buffer_load_dwordx4 v34, s[20:23], s94 offen lds\n"
"s_add_u32 m0, s24, 9216\n"
"s_nop 0\n"
"buffer_load_dwordx4 v35, s[20:23], s94 offen lds\n"
"s_cmp_eq_u32 s35, 0\n"
"s_cbranch_scc0 Lp48_0_%=\n"
"ds_read_b64_tr_b16 a[192:193], v41 offset:16384\n"
"ds_read_b64_tr_b16 a[194:195], v41 offset:16896\n"
"ds_read_b64_tr_b16 v[64:65], v42 offset:16384\n"
"ds_read_b64_tr_b16 v[66:67], v42 offset:16896\n"
"s_waitcnt lgkmcnt(0)\n"
"ds_write_b128 v1, v[64:67] offset:24576\n"
"s_branch Lq48_%=\n"
"Lp48_0_%=:\n"
"s_cmp_eq_u32 s35, 1\n"
"s_cbranch_scc0 Lp48_1_%=\n"
"ds_read_b64_tr_b16 a[184:185], v41 offset:16384\n"
"ds_read_b64_tr_b16 a[186:187], v41 offset:16896\n"
"ds_read_b64_tr_b16 v[176:177], v42 offset:16384\n"
"ds_read_b64_tr_b16 v[178:179], v42 offset:16896\n"
"s_branch Lq48_%=\n"
"Lp48_1_%=:\n"
"s_cmp_eq_u32 s35, 2\n"
"s_cbranch_scc0 Lp48_2_%=\n"
"ds_read_b64_tr_b16 a[152:153], v41 offset:16384\n"
"ds_read_b64_tr_b16 a[154:155], v41 offset:16896\n"
"ds_read_b64_tr_b16 v[64:65], v42 offset:16384\n"
"ds_read_b64_tr_b16 v[66:67], v42 offset:16896\n"
"s_waitcnt lgkmcnt(0)\n"
"ds_write_b128 v1, v[64:67] offset:20480\n"
"s_branch Lq48_%=\n"
"Lp48_2_%=:\n"
"ds_read_b64_tr_b16 a[200:201], v41 offset:16384\n"
"ds_read_b64_tr_b16 a[202:203], v41 offset:16896\n"
"ds_read_b64_tr_b16 v[184:185], v42 offset:16384\n"
"ds_read_b64_tr_b16 v[186:187], v42 offset:16896\n"
"Lq48_%=:\n"
"s_waitcnt vmcnt(4)\n"
"s_waitcnt lgkmcnt(0)\n"
"s_barrier\n"
"s_lshl_b32 s94, s36, 8\n"
"s_add_u32 s94, s94, 4194304\n"
"s_add_u32 m0, s24, 16384\n"
"s_nop 0\n"
"buffer_load_dwordx4 v34, s[20:23], s94 offen lds\n"
"s_add_u32 m0, s24, 17408\n"
"s_nop 0\n"
"buffer_load_dwordx4 v35, s[20:23], s94 offen lds\n"
"s_cmp_eq_u32 s35, 0\n"
"s_cbranch_scc0 Lp49_0_%=\n"
"ds_read_b64_tr_b16 a[196:197], v41 offset:24576\n"
"ds_read_b64_tr_b16 a[198:199], v41 offset:25088\n"
"ds_read_b64_tr_b16 v[68:69], v42 offset:24576\n"
"ds_read_b64_tr_b16 v[70:71], v42 offset:25088\n"
"s_waitcnt lgkmcnt(0)\n"
"ds_write_b128 v1, v[68:71] offset:25600\n"
"s_branch Lq49_%=\n"
"Lp49_0_%=:\n"
"s_cmp_eq_u32 s35, 1\n"
"s_cbranch_scc0 Lp49_1_%=\n"
"ds_read_b64_tr_b16 a[188:189], v41 offset:24576\n"
"ds_read_b64_tr_b16 a[190:191], v41 offset:25088\n"
"ds_read_b64_tr_b16 v[180:181], v42 offset:24576\n"
"ds_read_b64_tr_b16 v[182:183], v42 offset:25088\n"
"s_branch Lq49_%=\n"
"Lp49_1_%=:\n"
"s_cmp_eq_u32 s35, 2\n"
"s_cbranch_scc0 Lp49_2_%=\n"
"ds_read_b64_tr_b16 a[156:157], v41 offset:24576\n"
"ds_read_b64_tr_b16 a[158:159], v41 offset:25088\n"
"ds_read_b64_tr_b16 v[68:69], v42 offset:24576\n"
"ds_read_b64_tr_b16 v[70:71], v42 offset:25088\n"
"s_waitcnt lgkmcnt(0)\n"
"ds_write_b128 v1, v[68:71] offset:21504\n"
"s_branch Lq49_%=\n"
"Lp49_2_%=:\n"
"ds_read_b64_tr_b16 a[204:205], v41 offset:24576\n"
"ds_read_b64_tr_b16 a[206:207], v41 offset:25088\n"
"ds_read_b64_tr_b16 v[188:189], v42 offset:24576\n"
"ds_read_b64_tr_b16 v[190:191], v42 offset:25088\n"
"Lq49_%=:\n"
"s_waitcnt vmcnt(4)\n"
"s_waitcnt lgkmcnt(0)\n"
"s_barrier\n"
"s_lshl_b32 s94, s36, 8\n"
"s_add_u32 s94, s94, 4456448\n"
"s_add_u32 m0, s24, 24576\n"
"s_nop 0\n"
"buffer_load_dwordx4 v34, s[20:23], s94 offen lds\n"
"s_add_u32 m0, s24, 25600\n"
"s_nop 0\n"
"buffer_load_dwordx4 v35, s[20:23], s94 offen lds\n"
"s_cmp_eq_u32 s35, 0\n"
"s_cbranch_scc0 Lp50_0_%=\n"
"ds_read_b64_tr_b16 a[200:201], v41\n"
"ds_read_b64_tr_b16 a[202:203], v41 offset:512\n"
"ds_read_b64_tr_b16 v[184:185], v42\n"
"ds_read_b64_tr_b16 v[186:187], v42 offset:512\n"
"s_branch Lq50_%=\n"
"Lp50_0_%=:\n"
"s_cmp_eq_u32 s35, 1\n"
"s_cbranch_scc0 Lp50_1_%=\n"
"ds_read_b64_tr_b16 a[192:193], v41\n"
"ds_read_b64_tr_b16 a[194:195], v41 offset:512\n"
"ds_read_b64_tr_b16 v[64:65], v42\n"
"ds_read_b64_tr_b16 v[66:67], v42 offset:512\n"
"s_waitcnt lgkmcnt(0)\n"
"ds_write_b128 v1, v[64:67] offset:24576\n"
"s_branch Lq50_%=\n"
"Lp50_1_%=:\n"
"s_cmp_eq_u32 s35, 2\n"
"s_cbranch_scc0 Lp50_2_%=\n"
"ds_read_b64_tr_b16 a[184:185], v41\n"
"ds_read_b64_tr_b16 a[186:187], v41 offset:512\n"
"ds_read_b64_tr_b16 v[176:177], v42\n"
"ds_read_b64_tr_b16 v[178:179], v42 offset:512\n"
"s_branch Lq50_%=\n"
"Lp50_2_%=:\n"
"ds_read_b64_tr_b16 a[152:153], v41\n"
"ds_read_b64_tr_b16 a[154:155], v41 offset:512\n"
"ds_read_b64_tr_b16 v[64:65], v42\n"
"ds_read_b64_tr_b16 v[66:67], v42 offset:512\n"
"s_waitcnt lgkmcnt(0)\n"
"ds_write_b128 v1, v[64:67] offset:20480\n"
"Lq50_%=:\n"
"s_waitcnt vmcnt(4)\n"
"s_waitcnt lgkmcnt(0)\n"
"s_barrier\n"
"s_cmp_eq_u32 s35, 0\n"
"s_cbranch_scc0 Lp51_0_%=\n"
"ds_read_b64_tr_b16 a[204:205], v41 offset:8192\n"
"ds_read_b64_tr_b16 a[206:207], v41 offset:8704\n"
"ds_read_b64_tr_b16 v[188:189], v42 offset:8192\n"
"ds_read_b64_tr_b16 v[190:191], v42 offset:8704\n"
"s_branch Lq51_%=\n"
"Lp51_0_%=:\n"
"s_cmp_eq_u32 s35, 1\n"
"s_cbranch_scc0 Lp51_1_%=\n"
"ds_read_b64_tr_b16 a[196:197], v41 offset:8192\n"
"ds_read_b64_tr_b16 a[198:199], v41 offset:8704\n"
"ds_read_b64_tr_b16 v[68:69], v42 offset:8192\n"
"ds_read_b64_tr_b16 v[70:71], v42 offset:8704\n"
"s_waitcnt lgkmcnt(0)\n"
"ds_write_b128 v1, v[68:71] offset:25600\n"
"s_branch Lq51_%=\n"
"Lp51_1_%=:\n"
"s_cmp_eq_u32 s35, 2\n"
"s_cbranch_scc0 Lp51_2_%=\n"
"ds_read_b64_tr_b16 a[188:189], v41 offset:8192\n"
"ds_read_b64_tr_b16 a[190:191], v41 offset:8704\n"
"ds_read_b64_tr_b16 v[180:181], v42 offset:8192\n"
"ds_read_b64_tr_b16 v[182:183], v42 offset:8704\n"
"s_branch Lq51_%=\n"
"Lp51_2_%=:\n"
"ds_read_b64_tr_b16 a[156:157], v41 offset:8192\n"
"ds_read_b64_tr_b16 a[158:159], v41 offset:8704\n"
"ds_read_b64_tr_b16 v[68:69], v42 offset:8192\n"
"ds_read_b64_tr_b16 v[70:71], v42 offset:8704\n"
"s_waitcnt lgkmcnt(0)\n"
"ds_write_b128 v1, v[68:71] offset:21504\n"
"Lq51_%=:\n"
"s_waitcnt vmcnt(2)\n"
"s_waitcnt lgkmcnt(0)\n"
"s_barrier\n"
"ds_read_b64_tr_b16 a[208:209], v41 offset:16384\n"
"ds_read_b64_tr_b16 a[210:211], v41 offset:16896\n"
"ds_read_b64_tr_b16 v[64:65], v42 offset:16384\n"
"ds_read_b64_tr_b16 v[66:67], v42 offset:16896\n"
"s_waitcnt lgkmcnt(0)\n"
"ds_write_b128 v1, v[64:67] offset:26624\n"
"Lq52_%=:\n"
"s_waitcnt vmcnt(0)\n"
"s_waitcnt lgkmcnt(0)\n"
"s_barrier\n"
"ds_read_b64_tr_b16 a[212:213], v41 offset:24576\n"
"ds_read_b64_tr_b16 a[214:215], v41 offset:25088\n"
"ds_read_b64_tr_b16 v[68:69], v42 offset:24576\n"
"ds_read_b64_tr_b16 v[70:71], v42 offset:25088\n"
"s_waitcnt lgkmcnt(0)\n"
"ds_write_b128 v1, v[68:71] offset:27648\n"
"Lq53_%=:\n"
"s_waitcnt lgkmcnt(0)\n"
"s_barrier\n"
"ds_write_b128 v1, v[48:51] offset:28672\n"
"ds_write_b128 v1, v[52:55] offset:29696\n"
"ds_write_b128 v1, v[56:59] offset:30720\n"
"ds_write_b128 v1, v[60:63] offset:31744\n"
"ds_write_b128 v0, v[120:123] offset:31744\n"
"s_waitcnt vmcnt(0) lgkmcnt(0)\n"
"v_readfirstlane_b32 s14, v119\n"
"s_nop 3\n"
"s_add_u32 s14, s14, 1\n"
"s_mov_b32 s24, 0\n"
"v_lshrrev_b32 v125, 2, v0\n"
"v_add_u32 v124, 20608, v125\n"
"v_lshrrev_b32 v123, 1, v0\n"
"v_and_b32 v123, 0xf8, v123\n"
"v_add_u32 v123, 20864, v123\n"
"Lwin_%=:\n"
"buffer_load_dword v118, v124, s[60:63], 0 offen sc1\n"
"buffer_load_dwordx2 v[116:117], v123, s[60:63], 0 offen sc1\n"
"s_waitcnt vmcnt(0)\n"
"v_cmp_ne_u32_e32 vcc, s14, v118\n"
"v_cmp_ne_u32_e64 s[100:101], s14, v117\n"
"s_nop 1\n"
"s_and_b32 vcc_lo, vcc_lo, 268435455\n"
"s_mov_b32 vcc_hi, 0\n"
"s_or_b64 vcc, vcc, s[100:101]\n"
"s_cbranch_scc0 Lwok_%=\n"
"s_sleep 4\n"
"s_add_u32 s24, s24, 1\n"
"s_cmp_lt_u32 s24, 65536\n"
"s_cbranch_scc1 Lwin_%=\n"
"s_mov_b32 s39, 1\n"
"v_mov_b32 v124, 6000\n"
"v_add_u32 v124, s88, v124\n"
"v_mov_b32 v125, 16384\n"
"buffer_store_dword v124, v125, s[60:63], 0 offen sc1\n"
"Lwok_%=:\n"
"s_mov_b32 s9, -1\n"
"s_mov_b32 s10, -1\n"
"v_readlane_b32 s11, v116, 0\n"
"v_readlane_b32 s12, v116, 1\n"
"s_nop 0\n"
"s_min_u32 s9, s9, s11\n"
"s_min_u32 s10, s10, s12\n"
"v_readlane_b32 s11, v116, 2\n"
"v_readlane_b32 s12, v116, 3\n"
"s_nop 0\n"
"s_min_u32 s9, s9, s11\n"
"s_min_u32 s10, s10, s12\n"
"v_readlane_b32 s11, v116, 4\n"
"v_readlane_b32 s12, v116, 5\n"
"s_nop 0\n"
"s_min_u32 s9, s9, s11\n"
"s_min_u32 s10, s10, s12\n"
"v_readlane_b32 s11, v116, 6\n"
"v_readlane_b32 s12, v116, 7\n"
"s_nop 0\n"
"s_min_u32 s9, s9, s11\n"
"s_min_u32 s10, s10, s12\n"
"v_readlane_b32 s11, v116, 8\n"
"v_readlane_b32 s12, v116, 9\n"
"s_nop 0\n"
"s_min_u32 s9, s9, s11\n"
"s_min_u32 s10, s10, s12\n"
"v_readlane_b32 s11, v116, 10\n"
"v_readlane_b32 s12, v116, 11\n"
"s_nop 0\n"
"s_min_u32 s9, s9, s11\n"
"s_min_u32 s10, s10, s12\n"
"v_readlane_b32 s11, v116, 12\n"
"v_readlane_b32 s12, v116, 13\n"
"s_nop 0\n"
"s_min_u32 s9, s9, s11\n"
"s_min_u32 s10, s10, s12\n"
"v_readlane_b32 s11, v116, 14\n"
"v_readlane_b32 s12, v116, 15\n"
"s_nop 0\n"
"s_min_u32 s9, s9, s11\n"
"s_min_u32 s10, s10, s12\n"
"v_readlane_b32 s11, v116, 16\n"
"v_readlane_b32 s12, v116, 17\n"
"s_nop 0\n"
"s_min_u32 s9, s9, s11\n"
"s_min_u32 s10, s10, s12\n"
"v_readlane_b32 s11, v116, 18\n"
"v_readlane_b32 s12, v116, 19\n"
"s_nop 0\n"
"s_min_u32 s9, s9, s11\n"
"s_min_u32 s10, s10, s12\n"
"v_readlane_b32 s11, v116, 20\n"
"v_readlane_b32 s12, v116, 21\n"
"s_nop 0\n"
"s_min_u32 s9, s9, s11\n"
"s_min_u32 s10, s10, s12\n"
"v_readlane_b32 s11, v116, 22\n"
"v_readlane_b32 s12, v116, 23\n"
"s_nop 0\n"
"s_min_u32 s9, s9, s11\n"
"s_min_u32 s10, s10, s12\n"
"v_readlane_b32 s11, v116, 24\n"
"v_readlane_b32 s12, v116, 25\n"
"s_nop 0\n"
"s_min_u32 s9, s9, s11\n"
"s_min_u32 s10, s10, s12\n"
"v_readlane_b32 s11, v116, 26\n"
"v_readlane_b32 s12, v116, 27\n"
"s_nop 0\n"
"s_min_u32 s9, s9, s11\n"
"s_min_u32 s10, s10, s12\n"
"v_readlane_b32 s11, v116, 28\n"
"v_readlane_b32 s12, v116, 29\n"
"s_nop 0\n"
"s_min_u32 s9, s9, s11\n"
"s_min_u32 s10, s10, s12\n"
"v_readlane_b32 s11, v116, 30\n"
"v_readlane_b32 s12, v116, 31\n"
"s_nop 0\n"
"s_min_u32 s9, s9, s11\n"
"s_min_u32 s10, s10, s12\n"
"s_cmp_le_u32 s9, s10\n"
"s_cselect_b32 s34, 0, 8192\n"
"s_and_b32 s86, %2, 3\n"
"s_lshr_b32 s87, %2, 3\n"
"s_lshl_b32 s11, s36, 7\n"
"s_add_u32 s94, s86, 108\n"
"s_lshl_b32 s94, s94, 14\n"
"s_add_u32 s94, s94, s34\n"
"s_lshl_b32 s95, s35, 10\n"
"s_add_u32 s64, s94, s95\n"
"s_lshl_b32 s95, s86, 10\n"
"s_add_u32 s95, s95, 1966080\n"
"s_add_u32 s65, s95, s34\n"
"s_mul_i32 s94, s37, 36\n"
"s_lshl_b32 s95, s30, 1\n"
"s_add_u32 s95, s95, s94\n"
"s_lshl_b32 s95, s95, 13\n"
"s_add_u32 s95, s95, s34\n"
"s_add_u32 s66, s95, s11\n"
"s_lshl_b32 s95, s94, 13\n"
"s_add_u32 s95, s95, s34\n"
"s_add_u32 s95, s95, s11\n"
"s_add_u32 s68, s95, 262144\n"
"s_lshl_b32 s94, s35, 16\n"
"s_add_u32 s67, s95, s94\n"
"s_lshr_b32 s95, s27, 1\n"
"s_lshl_b32 s95, s95, 14\n"
"s_and_b32 s94, s27, 1\n"
"s_lshl_b32 s94, s94, 12\n"
"s_add_u32 s95, s95, s94\n"
"s_add_u32 s69, s95, s34\n"
"s_lshr_b32 s94, s27, 3\n"
"s_lshr_b32 s95, s94, 2\n"
"s_lshl_b32 s95, s95, 12\n"
"s_and_b32 s94, s94, 3\n"
"s_lshl_b32 s94, s94, 10\n"
"s_add_u32 s95, s95, s94\n"
"s_add_u32 s95, s95, 1769472\n"
"s_cmp_ge_u32 s27, 64\n"
"s_cselect_b32 s95, 1966080, s95\n"
"s_cselect_b32 s71, 1, 4\n"
"s_lshl_b32 s71, s71, 14\n"
"s_add_u32 s95, s95, s34\n"
"s_and_b32 s94, s27, 7\n"
"s_lshl_b32 s94, s94, 7\n"
"s_add_u32 s70, s95, s94\n"
"s_cmp_eq_u32 s85, 1\n"
"s_cselect_b32 s85, s14, s85\n"
"s_cselect_b32 s15, 1, 0\n"
"s_load_dword s86, %0, 100\n"
"s_waitcnt lgkmcnt(0)\n"
"s_mov_b64 s[92:93], 0\n"
"s_mov_b32 s88, 0\n"
"s_mov_b32 s83, 0\n"
"s_bitcmp1_b64 s[92:93], s88\n"
"s_cbranch_scc1 Lkvok0_%=\n"
"s_mov_b32 s24, 0\n"
"Lkvp0_%=:\n"
"v_lshrrev_b32 v125, 2, v0\n"
"buffer_load_dword v124, v125, s[60:63], s25 offen sc1\n"
"v_add_u32 v125, 8192, v125\n"
"buffer_load_dword v125, v125, s[60:63], s25 offen sc1\n"
"s_waitcnt vmcnt(0)\n"
"v_cmp_eq_u32_e32 vcc, s14, v124\n"
"v_cmp_eq_u32_e64 s[92:93], s14, v125\n"
"s_nop 1\n"
"s_and_b64 s[92:93], s[92:93], vcc\n"
"s_bitcmp1_b64 s[92:93], s88\n"
"s_cbranch_scc1 Lkvok0_%=\n"
"s_cmp_lg_u32 s39, 0\n"
"s_cbranch_scc1 Lkvok0_%=\n"
"s_sleep 2\n"
"s_add_u32 s24, s24, 1\n"
"s_cmp_lt_u32 s24, 65536\n"
"s_cbranch_scc1 Lkvp0_%=\n"
"s_mov_b32 s39, 1\n"
"v_mov_b32 v124, 3000\n"
"v_add_u32 v124, s88, v124\n"
"v_mov_b32 v125, 16384\n"
"buffer_store_dword v124, v125, s[60:63], 0 offen sc1\n"
"Lkvok0_%=:\n"
"s_lshl_b32 s89, s36, 13\n"
"s_add_u32 s90, s89, 4096\n"
"buffer_load_dwordx4 v[32:35], v7, s[44:47], s89 offen sc1\n"
"buffer_load_dwordx4 v[36:39], v7, s[44:47], s90 offen sc1\n"
"buffer_load_dwordx4 v[40:43], v7, s[48:51], s89 offen sc1\n"
"buffer_load_dwordx4 v[44:47], v7, s[48:51], s90 offen sc1\n"
"s_waitcnt vmcnt(3)\n"
"ds_write_b128 v7, v[32:35] offset:15360\n"
"s_waitcnt vmcnt(2)\n"
"ds_write_b128 v7, v[36:39] offset:19456\n"
"s_waitcnt vmcnt(1)\n"
"ds_write_b128 v7, v[40:43] offset:23552\n"
"s_waitcnt vmcnt(0)\n"
"ds_write_b128 v7, v[44:47] offset:27648\n"
"s_lshl_b32 s91, s36, 11\n"
"s_waitcnt vmcnt(0) lgkmcnt(0)\n"
"s_barrier\n"
"s_lshl_b32 s94, s83, 16\n"
"s_add_u32 s72, s64, s94\n"
"s_add_u32 s73, s72, 4096\n"
"s_lshl_b32 s94, s83, 14\n"
"s_add_u32 s74, s65, s94\n"
"s_mul_i32 s94, s83, 589824\n"
"s_add_u32 s75, s66, s94\n"
"s_add_u32 s76, s75, 32768\n"
"s_add_u32 s77, s75, 65536\n"
"s_add_u32 s80, s69, s94\n"
"s_add_u32 s95, s83, 1\n"
"s_cmp_eq_u32 s95, 3\n"
"s_cselect_b32 s95, 0, s95\n"
"s_mul_i32 s94, s95, s71\n"
"s_add_u32 s81, s70, s94\n"
"s_add_u32 s95, s83, 2\n"
"s_cmp_ge_u32 s95, 3\n"
"s_cselect_b32 s94, 3, 0\n"
"s_sub_u32 s95, s95, s94\n"
"s_mul_i32 s94, s95, s71\n"
"s_add_u32 s82, s70, s94\n"
"s_lshl_b32 s94, s88, 3\n"
"s_add_u32 s94, s94, s26\n"
"s_cmp_eq_u32 s15, 1\n"
"s_cselect_b32 s82, s94, s82\n"
"s_mul_i32 s94, s95, 589824\n"
"s_add_u32 s78, s67, s94\n"
"s_add_u32 s79, s68, s94\n"
"v_mov_b64 v[54:55], 0\n"
"v_mov_b64 v[56:57], 0\n"
"v_mov_b64 v[58:59], 0\n"
"v_mov_b64 v[60:61], 0\n"
"v_mov_b64 v[62:63], 0\n"
"v_mov_b64 v[64:65], 0\n"
"v_mov_b64 v[66:67], 0\n"
"v_mov_b64 v[68:69], 0\n"
"v_mov_b64 v[70:71], 0\n"
"v_mov_b64 v[72:73], 0\n"
"v_mov_b64 v[74:75], 0\n"
"v_mov_b64 v[76:77], 0\n"
"ds_read_b128 v[78:81], v1\n"
"ds_read_b128 v[82:85], v1 offset:1024\n"
"ds_read_b128 v[86:89], v1 offset:10240\n"
"ds_read_b128 v[90:93], v1 offset:11264\n"
"ds_read_b128 v[94:97], v1 offset:20480\n"
"ds_read_b128 v[98:101], v1 offset:21504\n"
"s_waitcnt lgkmcnt(0)\n"
"Lstep_%=:\n"
"s_mov_b32 s24, 0\n"
"buffer_load_dwordx4 v[18:21], v0, s[40:43], s72 offen sc1\n"
"buffer_load_dwordx4 v[22:25], v0, s[40:43], s73 offen sc1\n"
"buffer_load_dwordx4 v[50:53], v11, s[40:43], s74 offen sc1\n"
"s_sleep 3\n"
"buffer_load_dwordx4 v[104:107], v0, s[40:43], s72 offen sc1\n"
"buffer_load_dwordx4 v[108:111], v0, s[40:43], s73 offen sc1\n"
"buffer_load_dwordx4 v[112:115], v11, s[40:43], s74 offen sc1\n"
"LblkA_%=:\n"
"s_waitcnt vmcnt(5)\n"
"ds_write_b128 v7, v[18:21]\n"
"v_smfmac_f32_16x16x64_bf16 v[54:57], v[18:21], a[0:7], v2\n"
"v_max3_u32 v102, v18, v19, v20\n"
"v_smfmac_f32_16x16x64_bf16 v[62:65], v[18:21], a[72:79], v2\n"
"s_waitcnt vmcnt(4)\n"
"ds_write_b128 v7, v[22:25] offset:4096\n"
"v_smfmac_f32_16x16x64_bf16 v[70:73], v[18:21], a[144:151], v2\n"
"v_max3_u32 v103, v21, v22, v23\n"
"v_smfmac_f32_16x16x64_bf16 v[58:61], v[18:21], v[78:85], v2\n"
"v_max3_u32 v102, v102, v24, v25\n"
"s_waitcnt vmcnt(3)\n"
"v_max3_u32 v103, v103, v50, v51\n"
"v_smfmac_f32_16x16x64_bf16 v[66:69], v[18:21], a[248:255], v2\n"
"v_max3_u32 v102, v102, v52, v53\n"
"v_max_u32 v102, v102, v103\n"
"v_cmp_eq_u32_e32 vcc, -1, v102\n"
"v_smfmac_f32_16x16x64_bf16 v[74:77], v[18:21], v[152:159], v2\n"
"s_cbranch_vccnz LretryA_%=\n"
"LokA_%=:\n"
"s_waitcnt lgkmcnt(0)\n"
"s_barrier\n"
"v_smfmac_f32_16x16x64_bf16 v[54:57], v[22:25], a[8:15], v2\n"
"ds_read_b128 v[26:29], v8\n"
"ds_read_b128 v[78:81], v1 offset:2048\n"
"v_smfmac_f32_16x16x64_bf16 v[62:65], v[22:25], a[80:87], v2\n"
"ds_read_b128 v[82:85], v1 offset:3072\n"
"ds_read_b128 v[30:33], v9\n"
"v_smfmac_f32_16x16x64_bf16 v[70:73], v[22:25], a[152:159], v2\n"
"ds_read_b128 v[34:37], v10\n"
"ds_read_b128 v[38:41], v8 offset:4096\n"
"v_smfmac_f32_16x16x64_bf16 v[58:61], v[22:25], a[216:223], v2\n"
"ds_read_b128 v[42:45], v9 offset:4096\n"
"ds_read_b128 v[46:49], v10 offset:4096\n"
"v_smfmac_f32_16x16x64_bf16 v[66:69], v[22:25], v[86:93], v2\n"
"v_smfmac_f32_16x16x64_bf16 v[74:77], v[22:25], v[94:101], v2\n"
"v_mov_b32 v124, s85\n"
"v_mov_b32 v125, s85\n"
"buffer_store_dwordx2 v[124:125], v127, s[40:43], s82 offen sc1\n"
"s_waitcnt lgkmcnt(7)\n"
"v_smfmac_f32_16x16x64_bf16 v[54:57], v[26:29], a[16:23], v2\n"
"ds_read_b128 v[86:89], v1 offset:4096\n"
"ds_read_b128 v[90:93], v1 offset:5120\n"
"s_waitcnt lgkmcnt(7)\n"
"v_smfmac_f32_16x16x64_bf16 v[58:61], v[26:29], v[78:85], v2\n"
"s_waitcnt lgkmcnt(6)\n"
"v_smfmac_f32_16x16x64_bf16 v[54:57], v[30:33], a[24:31], v2\n"
"ds_read_b128 v[94:97], v1 offset:6144\n"
"ds_read_b128 v[98:101], v1 offset:7168\n"
"v_smfmac_f32_16x16x64_bf16 v[58:61], v[30:33], a[224:231], v2\n"
"ds_write_b128 v126, v[50:53]\n"
"s_waitcnt lgkmcnt(8)\n"
"v_smfmac_f32_16x16x64_bf16 v[54:57], v[34:37], a[32:39], v2\n"
"ds_read_b128 v[78:81], v1 offset:8192\n"
"ds_read_b128 v[82:85], v1 offset:9216\n"
"s_waitcnt lgkmcnt(5)\n"
"v_smfmac_f32_16x16x64_bf16 v[58:61], v[34:37], v[86:93], v2\n"
"v_smfmac_f32_16x16x64_bf16 v[54:57], v[38:41], a[40:47], v2\n"
"s_waitcnt lgkmcnt(2)\n"
"s_barrier\n"
"ds_read_b128 v[50:53], v0 offset:8192\n"
"v_smfmac_f32_16x16x64_bf16 v[58:61], v[38:41], a[232:239], v2\n"
"ds_read_b128 v[86:89], v1 offset:12288\n"
"ds_read_b128 v[90:93], v1 offset:13312\n"
"v_smfmac_f32_16x16x64_bf16 v[54:57], v[42:45], a[48:55], v2\n"
"v_smfmac_f32_16x16x64_bf16 v[58:61], v[42:45], v[94:101], v2\n"
"ds_read_b128 v[94:97], v1 offset:14336\n"
"ds_read_b128 v[98:101], v1 offset:15360\n"
"v_smfmac_f32_16x16x64_bf16 v[54:57], v[46:49], a[56:63], v2\n"
"v_smfmac_f32_16x16x64_bf16 v[58:61], v[46:49], a[240:247], v2\n"
"s_waitcnt lgkmcnt(4)\n"
"v_smfmac_f32_16x16x64_bf16 v[54:57], v[50:53], a[64:71], v2\n"
"v_smfmac_f32_16x16x64_bf16 v[58:61], v[50:53], v[78:85], v2\n"
"v_smfmac_f32_16x16x64_bf16 v[62:65], v[30:33], a[96:103], v2\n"
"ds_read_b128 v[78:81], v1 offset:16384\n"
"ds_read_b128 v[82:85], v1 offset:17408\n"
"s_waitcnt lgkmcnt(4)\n"
"v_smfmac_f32_16x16x64_bf16 v[66:69], v[30:33], v[86:93], v2\n"
"v_smfmac_f32_16x16x64_bf16 v[62:65], v[38:41], a[112:119], v2\n"
"ds_read_b128 v[86:89], v1 offset:18432\n"
"ds_read_b128 v[90:93], v1 offset:19456\n"
"s_waitcnt lgkmcnt(4)\n"
"v_smfmac_f32_16x16x64_bf16 v[66:69], v[38:41], v[94:101], v2\n"
"v_permlane32_swap_b32 v54, v58\n"
"v_permlane32_swap_b32 v55, v59\n"
"v_smfmac_f32_16x16x64_bf16 v[62:65], v[46:49], a[128:135], v2\n"
"v_permlane32_swap_b32 v56, v60\n"
"v_permlane32_swap_b32 v57, v61\n"
"s_waitcnt lgkmcnt(2)\n"
"v_smfmac_f32_16x16x64_bf16 v[66:69], v[46:49], v[78:85], v2\n"
"v_add_f32 v54, v54, v58\n"
"v_add_f32 v55, v55, v59\n"
"v_smfmac_f32_16x16x64_bf16 v[62:65], v[50:53], a[136:143], v2\n"
"v_add_f32 v56, v56, v60\n"
"v_add_f32 v57, v57, v61\n"
"s_waitcnt lgkmcnt(0)\n"
"v_smfmac_f32_16x16x64_bf16 v[66:69], v[50:53], v[86:93], v2\n"
"v_cvt_pk_bf16_f32 v18, v54, v55\n"
"v_cvt_pk_bf16_f32 v19, v56, v57\n"
"ds_write_b64 v4, v[18:19]\n"
"v_smfmac_f32_16x16x64_bf16 v[62:65], v[26:29], a[88:95], v2\n"
"ds_read_b128 v[94:97], v0 offset:15360\n"
"ds_read_b128 v[98:101], v0 offset:19456\n"
"v_smfmac_f32_16x16x64_bf16 v[66:69], v[26:29], v[128:135], v2\n"
"ds_read_b128 v[54:57], v0 offset:16384\n"
"ds_read_b128 v[58:61], v0 offset:20480\n"
"v_smfmac_f32_16x16x64_bf16 v[62:65], v[34:37], a[104:111], v2\n"
"ds_read_b128 v[78:81], v0 offset:17408\n"
"ds_read_b128 v[82:85], v0 offset:21504\n"
"s_waitcnt lgkmcnt(6)\n"
"s_barrier\n"
"s_waitcnt vmcnt(1)\n"
"v_smfmac_f32_16x16x64_bf16 v[66:69], v[34:37], v[136:143], v2\n"
"ds_read_b64_tr_b16 v[102:103], v3\n"
"ds_read_b64_tr_b16 v[104:105], v3 offset:64\n"
"v_smfmac_f32_16x16x64_bf16 v[62:65], v[42:45], a[120:127], v2\n"
"ds_read_b64_tr_b16 v[106:107], v3 offset:512\n"
"ds_read_b64_tr_b16 v[108:109], v3 offset:576\n"
"v_smfmac_f32_16x16x64_bf16 v[66:69], v[42:45], v[144:151], v2\n"
"ds_read_b64_tr_b16 v[110:111], v3 offset:1024\n"
"ds_read_b64_tr_b16 v[112:113], v3 offset:1088\n"
"ds_read_b128 v[86:89], v0 offset:18432\n"
"ds_read_b128 v[90:93], v0 offset:22528\n"
"ds_read_b64_tr_b16 v[114:115], v3 offset:1536\n"
"s_waitcnt lgkmcnt(8)\n"
"ds_read_b64_tr_b16 v[116:117], v3 offset:1600\n"
"v_smfmac_f32_16x16x64_bf16 v[70:73], v[26:29], a[160:167], v2\n"
"v_smfmac_f32_16x16x64_bf16 v[74:77], v[26:29], v[160:167], v2\n"
"s_waitcnt lgkmcnt(8)\n"
"v_mfma_f32_16x16x32_bf16 v[118:121], v[94:97], v[102:105], 0\n"
"v_permlane32_swap_b32 v62, v66\n"
"v_permlane32_swap_b32 v63, v67\n"
"v_mfma_f32_16x16x32_bf16 v[122:125], v[98:101], v[102:105], 0\n"
"v_permlane32_swap_b32 v64, v68\n"
"v_permlane32_swap_b32 v65, v69\n"
"s_waitcnt lgkmcnt(6)\n"
"v_mfma_f32_16x16x32_bf16 v[118:121], v[54:57], v[106:109], v[118:121]\n"
"v_add_f32 v62, v62, v66\n"
"v_add_f32 v63, v63, v67\n"
"v_mfma_f32_16x16x32_bf16 v[122:125], v[58:61], v[106:109], v[122:125]\n"
"v_add_f32 v64, v64, v68\n"
"v_add_f32 v65, v65, v69\n"
"s_waitcnt lgkmcnt(4)\n"
"v_mfma_f32_16x16x32_bf16 v[118:121], v[78:81], v[110:113], v[118:121]\n"
"v_cvt_pk_bf16_f32 v20, v62, v63\n"
"v_cvt_pk_bf16_f32 v21, v64, v65\n"
"v_mfma_f32_16x16x32_bf16 v[122:125], v[82:85], v[110:113], v[122:125]\n"
"ds_write_b64 v4, v[20:21] offset:2048\n"
"s_waitcnt lgkmcnt(1)\n"
"v_mfma_f32_16x16x32_bf16 v[118:121], v[86:89], v[114:117], v[118:121]\n"
"v_mfma_f32_16x16x32_bf16 v[122:125], v[90:93], v[114:117], v[122:125]\n"
"ds_read_b128 v[78:81], v1 offset:22528\n"
"ds_read_b128 v[82:85], v1 offset:23552\n"
"v_smfmac_f32_16x16x64_bf16 v[70:73], v[30:33], a[168:175], v2\n"
"ds_read_b128 v[86:89], v1 offset:24576\n"
"ds_read_b128 v[90:93], v1 offset:25600\n"
"v_smfmac_f32_16x16x64_bf16 v[74:77], v[30:33], v[168:175], v2\n"
"s_waitcnt lgkmcnt(4)\n"
"s_barrier\n"
"v_smfmac_f32_16x16x64_bf16 v[70:73], v[38:41], a[184:191], v2\n"
"ds_read_b64_tr_b16 v[62:63], v3 offset:2048\n"
"ds_read_b64_tr_b16 v[64:65], v3 offset:2112\n"
"v_smfmac_f32_16x16x64_bf16 v[74:77], v[38:41], v[176:183], v2\n"
"ds_read_b64_tr_b16 v[66:67], v3 offset:2560\n"
"ds_read_b64_tr_b16 v[68:69], v3 offset:2624\n"
"v_smfmac_f32_16x16x64_bf16 v[70:73], v[46:49], a[200:207], v2\n"
"ds_read_b64_tr_b16 v[54:55], v3 offset:3072\n"
"ds_read_b64_tr_b16 v[56:57], v3 offset:3136\n"
"v_smfmac_f32_16x16x64_bf16 v[74:77], v[46:49], v[184:191], v2\n"
"ds_read_b64_tr_b16 v[58:59], v3 offset:3584\n"
"ds_read_b64_tr_b16 v[60:61], v3 offset:3648\n"
"v_smfmac_f32_16x16x64_bf16 v[70:73], v[34:37], a[176:183], v2\n"
"s_waitcnt lgkmcnt(10)\n"
"v_smfmac_f32_16x16x64_bf16 v[74:77], v[34:37], v[78:85], v2\n"
"ds_read_b128 v[78:81], v1 offset:26624\n"
"ds_read_b128 v[82:85], v1 offset:27648\n"
"s_waitcnt lgkmcnt(8)\n"
"v_mfma_f32_16x16x32_bf16 v[94:97], v[62:65], v[102:105], 0\n"
"s_waitcnt lgkmcnt(6)\n"
"v_mfma_f32_16x16x32_bf16 v[94:97], v[66:69], v[106:109], v[94:97]\n"
"s_waitcnt lgkmcnt(4)\n"
"v_mfma_f32_16x16x32_bf16 v[94:97], v[54:57], v[110:113], v[94:97]\n"
"s_waitcnt lgkmcnt(2)\n"
"v_mfma_f32_16x16x32_bf16 v[94:97], v[58:61], v[114:117], v[94:97]\n"
"v_add_u32 v27, v7, v7\n"
"v_sub_u32 v27, v27, v0\n"
"v_smfmac_f32_16x16x64_bf16 v[70:73], v[42:45], a[192:199], v2\n"
"v_max3_f32 v24, v118, v119, v120\n"
"v_max3_f32 v25, v121, v122, v123\n"
"v_smfmac_f32_16x16x64_bf16 v[74:77], v[42:45], v[86:93], v2\n"
"v_max3_f32 v24, v24, v124, v125\n"
"ds_read_b128 v[102:105], v27 offset:23552\n"
"ds_read_b128 v[106:109], v27 offset:24576\n"
"v_smfmac_f32_16x16x64_bf16 v[70:73], v[50:53], a[208:215], v2\n"
"v_max3_f32 v26, v94, v95, v96\n"
"v_max3_f32 v25, v25, v26, v97\n"
"s_waitcnt lgkmcnt(2)\n"
"v_smfmac_f32_16x16x64_bf16 v[74:77], v[50:53], v[78:85], v2\n"
"v_max_f32 v24, v24, v25\n"
"v_mov_b32 v25, v24\n"
"v_mov_b64 v[38:39], 0\n"
"v_mov_b64 v[40:41], 0\n"
"v_permlane16_swap_b32 v24, v25\n"
"v_max_f32 v24, v24, v25\n"
"v_mov_b32 v25, v24\n"
"v_mov_b64 v[46:47], 0\n"
"v_mov_b64 v[48:49], 0\n"
"v_permlane32_swap_b32 v24, v25\n"
"v_max_f32 v24, v24, v25\n"
"v_mul_f32 v25, s33, v24\n"
"v_fma_f32 v118, v118, s33, -v25\n"
"v_fma_f32 v122, v122, s33, -v25\n"
"v_fma_f32 v94, v94, s33, -v25\n"
"v_fma_f32 v119, v119, s33, -v25\n"
"v_fma_f32 v123, v123, s33, -v25\n"
"v_fma_f32 v95, v95, s33, -v25\n"
"v_fma_f32 v120, v120, s33, -v25\n"
"v_fma_f32 v124, v124, s33, -v25\n"
"v_fma_f32 v96, v96, s33, -v25\n"
"v_fma_f32 v121, v121, s33, -v25\n"
"v_fma_f32 v125, v125, s33, -v25\n"
"v_fma_f32 v97, v97, s33, -v25\n"
"v_exp_f32 v118, v118\n"
"v_exp_f32 v122, v122\n"
"v_exp_f32 v119, v119\n"
"v_exp_f32 v123, v123\n"
"v_exp_f32 v120, v120\n"
"v_exp_f32 v124, v124\n"
"v_exp_f32 v121, v121\n"
"v_exp_f32 v125, v125\n"
"v_cvt_pk_bf16_f32 v18, v118, v119\n"
"v_exp_f32 v94, v94\n"
"v_cvt_pk_bf16_f32 v19, v120, v121\n"
"v_exp_f32 v95, v95\n"
"v_cvt_pk_bf16_f32 v20, v122, v123\n"
"v_exp_f32 v96, v96\n"
"v_cvt_pk_bf16_f32 v21, v124, v125\n"
"v_exp_f32 v97, v97\n"
"s_cmp_eq_u32 s88, 63\n"
"s_cbranch_scc1 Llast_%=\n"
"s_waitcnt lgkmcnt(1)\n"
"v_mfma_f32_16x16x32_bf16 v[110:113], v[102:105], v[18:21], 0\n"
"v_permlane32_swap_b32 v70, v74\n"
"v_permlane32_swap_b32 v71, v75\n"
"s_waitcnt lgkmcnt(0)\n"
"v_mfma_f32_16x16x32_bf16 v[114:117], v[106:109], v[18:21], 0\n"
"v_permlane32_swap_b32 v72, v76\n"
"v_permlane32_swap_b32 v73, v77\n"
"v_cvt_pk_bf16_f32 v22, v94, v95\n"
"v_cvt_pk_bf16_f32 v23, v96, v97\n"
"v_mfma_f32_16x16x32_bf16 v[118:121], v[14:17], v[18:21], 0\n"
"v_add_f32 v70, v70, v74\n"
"v_add_f32 v71, v71, v75\n"
"v_add_f32 v72, v72, v76\n"
"v_add_f32 v73, v73, v77\n"
"v_cvt_pk_bf16_f32 v28, v70, v71\n"
"v_cvt_pk_bf16_f32 v29, v72, v73\n"
"v_mov_b32 v32, v28\n"
"v_mov_b32 v33, v29\n"
"v_mov_b64 v[30:31], 0\n"
"v_mfma_f32_16x16x16_bf16 v[118:121], v[12:13], v[22:23], v[118:121]\n"
"s_nop 0\n"
"v_permlane32_swap_b32 v28, v30\n"
"v_permlane32_swap_b32 v29, v31\n"
"ds_read_b128 v[78:81], v1 offset:28672\n"
"s_nop 0\n"
"v_mfma_f32_16x16x16_bf16 v[110:113], v[28:29], v[22:23], v[110:113]\n"
"v_mfma_f32_16x16x16_bf16 v[114:117], v[30:31], v[22:23], v[114:117]\n"
"ds_read_b128 v[82:85], v1 offset:29696\n"
"ds_read_b128 v[86:89], v1 offset:30720\n"
"ds_read_b128 v[90:93], v1 offset:31744\n"
"v_mov_b64 v[34:35], 0\n"
"v_mov_b64 v[36:37], 0\n"
"v_mov_b64 v[42:43], 0\n"
"v_mov_b64 v[44:45], 0\n"
"v_rcp_f32 v118, v118\n"
"v_mov_b64 v[50:51], 0\n"
"v_mov_b64 v[52:53], 0\n"
"v_mul_f32 v110, v110, v118\n"
"v_mul_f32 v111, v111, v118\n"
"v_mul_f32 v112, v112, v118\n"
"v_mul_f32 v113, v113, v118\n"
"v_mul_f32 v114, v114, v118\n"
"v_mul_f32 v115, v115, v118\n"
"v_mul_f32 v116, v116, v118\n"
"v_mul_f32 v117, v117, v118\n"
"v_cvt_pk_bf16_f32 v24, v110, v111\n"
"v_cvt_pk_bf16_f32 v26, v114, v115\n"
"v_cvt_pk_bf16_f32 v25, v112, v113\n"
"v_cvt_pk_bf16_f32 v27, v116, v117\n"
"v_cndmask_b32_e64 v24, v24, v26, s[98:99]\n"
"v_cndmask_b32_e64 v25, v25, v27, s[98:99]\n"
"ds_write_b64 v5, v[24:25]\n"
"s_add_u32 s95, s88, 1\n"
"s_bitcmp1_b64 s[92:93], s95\n"
"s_cbranch_scc1 Lkvokn_%=\n"
"s_mov_b32 s24, 0\n"
"Lkvpn_%=:\n"
"v_lshrrev_b32 v125, 2, v0\n"
"buffer_load_dword v124, v125, s[60:63], s25 offen sc1\n"
"v_add_u32 v125, 8192, v125\n"
"buffer_load_dword v125, v125, s[60:63], s25 offen sc1\n"
"s_waitcnt vmcnt(0)\n"
"v_cmp_eq_u32_e32 vcc, s14, v124\n"
"v_cmp_eq_u32_e64 s[92:93], s14, v125\n"
"s_nop 1\n"
"s_and_b64 s[92:93], s[92:93], vcc\n"
"s_bitcmp1_b64 s[92:93], s95\n"
"s_cbranch_scc1 Lkvokn_%=\n"
"s_cmp_lg_u32 s39, 0\n"
"s_cbranch_scc1 Lkvokn_%=\n"
"s_sleep 2\n"
"s_add_u32 s24, s24, 1\n"
"s_cmp_lt_u32 s24, 65536\n"
"s_cbranch_scc1 Lkvpn_%=\n"
"s_mov_b32 s39, 1\n"
"v_mov_b32 v124, 3000\n"
"v_add_u32 v124, s88, v124\n"
"v_mov_b32 v125, 16384\n"
"buffer_store_dword v124, v125, s[60:63], 0 offen sc1\n"
"Lkvokn_%=:\n"
"s_add_u32 s89, s89, 262144\n"
"s_add_u32 s90, s90, 262144\n"
"buffer_load_dwordx4 v[54:57], v7, s[44:47], s89 offen sc1\n"
"buffer_load_dwordx4 v[58:61], v7, s[44:47], s90 offen sc1\n"
"buffer_load_dwordx4 v[62:65], v7, s[48:51], s89 offen sc1\n"
"buffer_load_dwordx4 v[66:69], v7, s[48:51], s90 offen sc1\n"
"s_waitcnt lgkmcnt(0)\n"
"s_barrier\n"
"ds_read_b128 v[18:21], v0 offset:13312\n"
"ds_read_b128 v[22:25], v0 offset:14336\n"
"s_waitcnt lgkmcnt(1)\n"
"v_smfmac_f32_16x16x64_bf16 v[34:37], v[18:21], v[192:199], v2\n"
"v_smfmac_f32_16x16x64_bf16 v[38:41], v[18:21], v[208:215], v2\n"
"s_waitcnt lgkmcnt(0)\n"
"v_smfmac_f32_16x16x64_bf16 v[34:37], v[22:25], v[200:207], v2\n"
"v_smfmac_f32_16x16x64_bf16 v[38:41], v[22:25], v[216:223], v2\n"
"v_smfmac_f32_16x16x64_bf16 v[42:45], v[18:21], v[224:231], v2\n"
"v_smfmac_f32_16x16x64_bf16 v[46:49], v[18:21], v[240:247], v2\n"
"v_smfmac_f32_16x16x64_bf16 v[42:45], v[22:25], v[232:239], v2\n"
"s_nop 4\n"
"v_permlane32_swap_b32 v34, v38\n"
"v_permlane32_swap_b32 v35, v39\n"
"v_smfmac_f32_16x16x64_bf16 v[46:49], v[22:25], v[248:255], v2\n"
"v_permlane32_swap_b32 v36, v40\n"
"v_permlane32_swap_b32 v37, v41\n"
"s_cmp_lt_u32 s35, 2\n"
"s_cbranch_scc1 Lw4_%=\n"
"v_smfmac_f32_16x16x64_bf16 v[50:53], v[18:21], v[78:85], v2\n"
"v_smfmac_f32_16x16x64_bf16 v[50:53], v[22:25], v[86:93], v2\n"
"Lw4_%=:\n"
"v_add_f32 v34, v34, v38\n"
"v_add_f32 v35, v35, v39\n"
"v_add_f32 v36, v36, v40\n"
"v_add_f32 v37, v37, v41\n"
"v_cvt_pk_bf16_f32 v26, v34, v35\n"
"v_cvt_pk_bf16_f32 v27, v36, v37\n"
"buffer_store_dwordx2 v[26:27], v6, s[40:43], s75 offen sc1\n"
"v_permlane32_swap_b32 v42, v46\n"
"v_permlane32_swap_b32 v43, v47\n"
"v_permlane32_swap_b32 v44, v48\n"
"v_permlane32_swap_b32 v45, v49\n"
"v_add_f32 v42, v42, v46\n"
"v_add_f32 v43, v43, v47\n"
"v_add_f32 v44, v44, v48\n"
"v_add_f32 v45, v45, v49\n"
"v_cvt_pk_bf16_f32 v28, v42, v43\n"
"v_cvt_pk_bf16_f32 v29, v44, v45\n"
"buffer_store_dwordx2 v[28:29], v6, s[40:43], s76 offen sc1\n"
"s_cmp_lt_u32 s35, 2\n"
"s_cbranch_scc1 Lw4b_%=\n"
"v_mov_b32 v34, v50\n"
"v_mov_b32 v35, v51\n"
"v_mov_b32 v36, v52\n"
"v_mov_b32 v37, v53\n"
"v_permlane32_swap_b32 v50, v34\n"
"v_permlane32_swap_b32 v51, v35\n"
"v_permlane32_swap_b32 v52, v36\n"
"v_permlane32_swap_b32 v53, v37\n"
"v_add_f32 v50, v50, v34\n"
"v_add_f32 v51, v51, v35\n"
"v_add_f32 v52, v52, v36\n"
"v_add_f32 v53, v53, v37\n"
"v_cvt_pk_bf16_f32 v30, v50, v51\n"
"v_cvt_pk_bf16_f32 v31, v52, v53\n"
"s_mov_b64 exec, s[96:97]\n"
"buffer_store_dwordx2 v[30:31], v6, s[40:43], s77 offen sc1\n"
"s_mov_b64 exec, -1\n"
"Lw4b_%=:\n"
"s_cmp_eq_u32 s38, 0\n"
"s_cbranch_scc1 Lnored_%=\n"
"v_lshrrev_b32 v100, 4, v0\n"
"v_and_b32 v101, 15, v100\n"
"v_lshrrev_b32 v102, 4, v100\n"
"v_and_b32 v103, 7, v100\n"
"s_cmp_eq_u32 s37, 0\n"
"s_cbranch_scc0 Lnokmr_%=\n"
"v_lshrrev_b32 v104, 5, v100\n"
"v_lshl_add_u32 v104, v104, 4, v101\n"
"v_lshlrev_b32 v104, 4, v104\n"
"v_and_b32 v105, 1, v102\n"
"v_lshl_add_u32 v104, v105, 3, v104\n"
"s_add_u32 s94, s91, s84\n"
"buffer_store_dwordx2 v[32:33], v104, s[56:59], s94 offen sc1\n"
"s_cmp_eq_u32 s35, 3\n"
"s_cbranch_scc0 Lnokm2r_%=\n"
"ds_read_b64_tr_b16 v[106:107], v3 offset:2048\n"
"ds_read_b64_tr_b16 v[108:109], v3 offset:2112\n"
"ds_read_b64_tr_b16 v[110:111], v3 offset:2560\n"
"ds_read_b64_tr_b16 v[112:113], v3 offset:2624\n"
"ds_read_b64_tr_b16 v[114:115], v3 offset:3072\n"
"ds_read_b64_tr_b16 v[116:117], v3 offset:3136\n"
"ds_read_b64_tr_b16 v[118:119], v3 offset:3584\n"
"ds_read_b64_tr_b16 v[120:121], v3 offset:3648\n"
"v_lshlrev_b32 v104, 7, v102\n"
"v_lshl_add_u32 v104, v103, 4, v104\n"
"s_waitcnt lgkmcnt(0)\n"
"s_mov_b64 exec, s[16:17]\n"
"buffer_store_dwordx4 v[106:109], v104, s[52:55], s91 offen sc1\n"
"buffer_store_dwordx4 v[110:113], v104, s[52:55], s91 offen offset:512 sc1\n"
"buffer_store_dwordx4 v[114:117], v104, s[52:55], s91 offen offset:1024 sc1\n"
"buffer_store_dwordx4 v[118:121], v104, s[52:55], s91 offen offset:1536 sc1\n"
"s_mov_b64 exec, -1\n"
"Lnokm2r_%=:\n"
"Lnokmr_%=:\n"
"s_add_u32 s91, s91, 65536\n"
"s_cmp_eq_u32 s88, 0\n"
"s_cbranch_scc1 Lnorsr_%=\n"
"v_mov_b32 v106, -1\n"
"v_mov_b32 v107, -1\n"
"v_mov_b32 v108, -1\n"
"v_mov_b32 v109, -1\n"
"v_lshlrev_b32 v104, 14, v102\n"
"v_lshrrev_b32 v105, 3, v101\n"
"v_lshl_add_u32 v104, v105, 12, v104\n"
"v_lshl_add_u32 v104, v103, 4, v104\n"
"buffer_store_dwordx4 v[106:109], v104, s[40:43], s78 offen sc1\n"
"s_cmp_eq_u32 s35, 3\n"
"s_cbranch_scc0 Lnorsr_%=\n"
"s_mov_b64 exec, s[96:97]\n"
"buffer_store_dwordx4 v[106:109], v104, s[40:43], s79 offen sc1\n"
"s_mov_b64 exec, -1\n"
"Lnorsr_%=:\n"
"s_cmp_eq_u64 s[92:93], -1\n"
"s_cbranch_scc1 Lkrr_%=\n"
"v_lshrrev_b32 v118, 2, v0\n"
"v_add_u32 v119, 8192, v118\n"
"buffer_load_dword v118, v118, s[60:63], s25 offen sc1\n"
"buffer_load_dword v119, v119, s[60:63], s25 offen sc1\n"
"Lkrr_%=:\n"
"ds_read_b128 v[120:123], v0 offset:31744\n"
"s_waitcnt lgkmcnt(0)\n"
"s_cmp_eq_u32 s32, 0\n"
"s_cbranch_scc1 LnsB_%=\n"
"s_mov_b32 s94, s32\n"
"LslB_%=:\n"
"s_sleep 1\n"
"s_sub_u32 s94, s94, 1\n"
"s_cmp_lg_u32 s94, 0\n"
"s_cbranch_scc1 LslB_%=\n"
"LnsB_%=:\n"
"s_mov_b32 s24, 0\n"
"buffer_load_dwordx4 v[18:21], v0, s[40:43], s80 offen sc1\n"
"buffer_load_dwordx4 v[22:25], v0, s[40:43], s80 offen offset:1024 sc1\n"
"buffer_load_dwordx4 v[26:29], v0, s[40:43], s80 offen offset:2048 sc1\n"
"buffer_load_dwordx4 v[30:33], v0, s[40:43], s80 offen offset:3072 sc1\n"
"s_sleep 3\n"
"buffer_load_dwordx4 v[34:37], v0, s[40:43], s80 offen sc1\n"
"buffer_load_dwordx4 v[38:41], v0, s[40:43], s80 offen offset:1024 sc1\n"
"buffer_load_dwordx4 v[42:45], v0, s[40:43], s80 offen offset:2048 sc1\n"
"buffer_load_dwordx4 v[46:49], v0, s[40:43], s80 offen offset:3072 sc1\n"
"s_sleep 3\n"
"buffer_load_dwordx4 v[102:105], v0, s[40:43], s80 offen sc1\n"
"buffer_load_dwordx4 v[106:109], v0, s[40:43], s80 offen offset:1024 sc1\n"
"buffer_load_dwordx4 v[110:113], v0, s[40:43], s80 offen offset:2048 sc1\n"
"buffer_load_dwordx4 v[114:117], v0, s[40:43], s80 offen offset:3072 sc1\n"
"LpollB0_%=:\n"
"s_waitcnt vmcnt(11)\n"
"v_mfma_f32_16x16x32_bf16 v[94:97], v[18:21], v[120:123], 0\n"
"s_waitcnt vmcnt(10)\n"
"v_mfma_f32_16x16x32_bf16 v[98:101], v[22:25], v[120:123], 0\n"
"s_waitcnt vmcnt(9)\n"
"v_mfma_f32_16x16x32_bf16 v[94:97], v[26:29], v[120:123], v[94:97]\n"
"s_waitcnt vmcnt(8)\n"
"v_mfma_f32_16x16x32_bf16 v[98:101], v[30:33], v[120:123], v[98:101]\n"
"s_nop 7\n"
"v_add_f32 v94, v94, v98\n"
"v_add_f32 v95, v95, v99\n"
"v_add_f32 v96, v96, v100\n"
"v_add_f32 v97, v97, v101\n"
"v_add_f32 v124, v94, v95\n"
"v_add_f32 v125, v96, v97\n"
"v_mov_b32 v98, v94\n"
"v_mov_b32 v99, v95\n"
"v_mov_b32 v100, v96\n"
"v_mov_b32 v101, v97\n"
"v_add_f32 v124, v124, v125\n"
"v_cmp_u_f32_e32 vcc, v124, v124\n"
"v_permlane32_swap_b32 v94, v98\n"
"v_permlane32_swap_b32 v95, v99\n"
"s_cbranch_vccz LokB_%=\n"
"s_cmp_lg_u32 s39, 0\n"
"s_cbranch_scc1 LokB_%=\n"
"buffer_load_dwordx4 v[18:21], v0, s[40:43], s80 offen sc1\n"
"buffer_load_dwordx4 v[22:25], v0, s[40:43], s80 offen offset:1024 sc1\n"
"buffer_load_dwordx4 v[26:29], v0, s[40:43], s80 offen offset:2048 sc1\n"
"buffer_load_dwordx4 v[30:33], v0, s[40:43], s80 offen offset:3072 sc1\n"
"LpollB1_%=:\n"
"s_waitcnt vmcnt(11)\n"
"v_mfma_f32_16x16x32_bf16 v[94:97], v[34:37], v[120:123], 0\n"
"s_waitcnt vmcnt(10)\n"
"v_mfma_f32_16x16x32_bf16 v[98:101], v[38:41], v[120:123], 0\n"
"s_waitcnt vmcnt(9)\n"
"v_mfma_f32_16x16x32_bf16 v[94:97], v[42:45], v[120:123], v[94:97]\n"
"s_waitcnt vmcnt(8)\n"
"v_mfma_f32_16x16x32_bf16 v[98:101], v[46:49], v[120:123], v[98:101]\n"
"s_nop 7\n"
"v_add_f32 v94, v94, v98\n"
"v_add_f32 v95, v95, v99\n"
"v_add_f32 v96, v96, v100\n"
"v_add_f32 v97, v97, v101\n"
"v_add_f32 v124, v94, v95\n"
"v_add_f32 v125, v96, v97\n"
"v_mov_b32 v98, v94\n"
"v_mov_b32 v99, v95\n"
"v_mov_b32 v100, v96\n"
"v_mov_b32 v101, v97\n"
"v_add_f32 v124, v124, v125\n"
"v_cmp_u_f32_e32 vcc, v124, v124\n"
"v_permlane32_swap_b32 v94, v98\n"
"v_permlane32_swap_b32 v95, v99\n"
"s_cbranch_vccz LokB_%=\n"
"s_cmp_lg_u32 s39, 0\n"
"s_cbranch_scc1 LokB_%=\n"
"buffer_load_dwordx4 v[34:37], v0, s[40:43], s80 offen sc1\n"
"buffer_load_dwordx4 v[38:41], v0, s[40:43], s80 offen offset:1024 sc1\n"
"buffer_load_dwordx4 v[42:45], v0, s[40:43], s80 offen offset:2048 sc1\n"
"buffer_load_dwordx4 v[46:49], v0, s[40:43], s80 offen offset:3072 sc1\n"
"LpollB2_%=:\n"
"s_waitcnt vmcnt(11)\n"
"v_mfma_f32_16x16x32_bf16 v[94:97], v[102:105], v[120:123], 0\n"
"s_waitcnt vmcnt(10)\n"
"v_mfma_f32_16x16x32_bf16 v[98:101], v[106:109], v[120:123], 0\n"
"s_waitcnt vmcnt(9)\n"
"v_mfma_f32_16x16x32_bf16 v[94:97], v[110:113], v[120:123], v[94:97]\n"
"s_waitcnt vmcnt(8)\n"
"v_mfma_f32_16x16x32_bf16 v[98:101], v[114:117], v[120:123], v[98:101]\n"
"s_nop 7\n"
"v_add_f32 v94, v94, v98\n"
"v_add_f32 v95, v95, v99\n"
"v_add_f32 v96, v96, v100\n"
"v_add_f32 v97, v97, v101\n"
"v_add_f32 v124, v94, v95\n"
"v_add_f32 v125, v96, v97\n"
"v_mov_b32 v98, v94\n"
"v_mov_b32 v99, v95\n"
"v_mov_b32 v100, v96\n"
"v_mov_b32 v101, v97\n"
"v_add_f32 v124, v124, v125\n"
"v_cmp_u_f32_e32 vcc, v124, v124\n"
"v_permlane32_swap_b32 v94, v98\n"
"v_permlane32_swap_b32 v95, v99\n"
"s_cbranch_vccz LokB_%=\n"
"s_cmp_lg_u32 s39, 0\n"
"s_cbranch_scc1 LokB_%=\n"
"buffer_load_dwordx4 v[102:105], v0, s[40:43], s80 offen sc1\n"
"buffer_load_dwordx4 v[106:109], v0, s[40:43], s80 offen offset:1024 sc1\n"
"buffer_load_dwordx4 v[110:113], v0, s[40:43], s80 offen offset:2048 sc1\n"
"buffer_load_dwordx4 v[114:117], v0, s[40:43], s80 offen offset:3072 sc1\n"
"s_add_u32 s24, s24, 1\n"
"s_cmp_lt_u32 s24, 16384\n"
"s_cbranch_scc1 LpollB0_%=\n"
"s_mov_b32 s39, 1\n"
"v_mov_b32 v124, 2000\n"
"v_add_u32 v124, s88, v124\n"
"v_mov_b32 v125, 16384\n"
"buffer_store_dword v124, v125, s[60:63], 0 offen sc1\n"
"s_branch LpollB0_%=\n"
"LokB_%=:\n"
"s_nop 1\n"
"v_permlane32_swap_b32 v96, v100\n"
"v_permlane32_swap_b32 v97, v101\n"
"v_add_f32 v94, v94, v98\n"
"v_add_f32 v95, v95, v99\n"
"v_add_f32 v96, v96, v100\n"
"v_add_f32 v97, v97, v101\n"
"v_cvt_pk_bf16_f32 v124, v94, v95\n"
"v_cvt_pk_bf16_f32 v125, v96, v97\n"
"buffer_store_dwordx2 v[124:125], v127, s[40:43], s81 offen sc1\n"
"s_barrier\n"
"ds_write_b128 v7, v[54:57] offset:15360\n"
"ds_write_b128 v7, v[58:61] offset:19456\n"
"ds_write_b128 v7, v[62:65] offset:23552\n"
"ds_write_b128 v7, v[66:69] offset:27648\n"
"ds_read_b128 v[78:81], v1\n"
"ds_read_b128 v[82:85], v1 offset:1024\n"
"ds_read_b128 v[86:89], v1 offset:10240\n"
"ds_read_b128 v[90:93], v1 offset:11264\n"
"ds_read_b128 v[94:97], v1 offset:20480\n"
"ds_read_b128 v[98:101], v1 offset:21504\n"
"s_add_u32 s88, s88, 1\n"
"s_add_u32 s83, s83, 1\n"
"s_cmp_eq_u32 s83, 3\n"
"s_cselect_b32 s83, 0, s83\n"
"s_lshl_b32 s94, s83, 16\n"
"s_add_u32 s72, s64, s94\n"
"s_add_u32 s73, s72, 4096\n"
"s_lshl_b32 s94, s83, 14\n"
"s_add_u32 s74, s65, s94\n"
"s_mul_i32 s94, s83, 589824\n"
"s_add_u32 s75, s66, s94\n"
"s_add_u32 s76, s75, 32768\n"
"s_add_u32 s77, s75, 65536\n"
"s_add_u32 s80, s69, s94\n"
"s_add_u32 s95, s83, 1\n"
"s_cmp_eq_u32 s95, 3\n"
"s_cselect_b32 s95, 0, s95\n"
"s_mul_i32 s94, s95, s71\n"
"s_add_u32 s81, s70, s94\n"
"s_add_u32 s95, s83, 2\n"
"s_cmp_ge_u32 s95, 3\n"
"s_cselect_b32 s94, 3, 0\n"
"s_sub_u32 s95, s95, s94\n"
"s_mul_i32 s94, s95, s71\n"
"s_add_u32 s82, s70, s94\n"
"s_lshl_b32 s94, s88, 3\n"
"s_add_u32 s94, s94, s26\n"
"s_cmp_eq_u32 s15, 1\n"
"s_cselect_b32 s82, s94, s82\n"
"s_mul_i32 s94, s95, 589824\n"
"s_add_u32 s78, s67, s94\n"
"s_add_u32 s79, s68, s94\n"
"v_mov_b64 v[54:55], 0\n"
"v_mov_b64 v[56:57], 0\n"
"v_mov_b64 v[58:59], 0\n"
"v_mov_b64 v[60:61], 0\n"
"v_mov_b64 v[62:63], 0\n"
"v_mov_b64 v[64:65], 0\n"
"v_mov_b64 v[66:67], 0\n"
"v_mov_b64 v[68:69], 0\n"
"v_mov_b64 v[70:71], 0\n"
"v_mov_b64 v[72:73], 0\n"
"v_mov_b64 v[74:75], 0\n"
"v_mov_b64 v[76:77], 0\n"
"s_waitcnt lgkmcnt(0)\n"
"s_cmp_eq_u64 s[92:93], -1\n"
"s_cbranch_scc1 Lker_%=\n"
"v_cmp_eq_u32_e32 vcc, s14, v118\n"
"v_cmp_eq_u32_e64 s[100:101], s14, v119\n"
"s_nop 1\n"
"s_and_b64 vcc, vcc, s[100:101]\n"
"s_or_b64 s[92:93], s[92:93], vcc\n"
"Lker_%=:\n"
"s_cmp_eq_u32 s86, 0\n"
"s_cbranch_scc1 LnsC_%=\n"
"s_mov_b32 s94, s86\n"
"LslC_%=:\n"
"s_sleep 1\n"
"s_sub_u32 s94, s94, 1\n"
"s_cmp_lg_u32 s94, 0\n"
"s_cbranch_scc1 LslC_%=\n"
"LnsC_%=:\n"
"s_branch Lstep_%=\n"
"Lnored_%=:\n"
"v_lshrrev_b32 v100, 4, v0\n"
"v_and_b32 v101, 15, v100\n"
"v_lshrrev_b32 v102, 4, v100\n"
"v_and_b32 v103, 7, v100\n"
"s_cmp_eq_u32 s37, 0\n"
"s_cbranch_scc0 Lnokmn_%=\n"
"v_lshrrev_b32 v104, 5, v100\n"
"v_lshl_add_u32 v104, v104, 4, v101\n"
"v_lshlrev_b32 v104, 4, v104\n"
"v_and_b32 v105, 1, v102\n"
"v_lshl_add_u32 v104, v105, 3, v104\n"
"s_add_u32 s94, s91, s84\n"
"buffer_store_dwordx2 v[32:33], v104, s[56:59], s94 offen sc1\n"
"s_cmp_eq_u32 s35, 3\n"
"s_cbranch_scc0 Lnokm2n_%=\n"
"ds_read_b64_tr_b16 v[106:107], v3 offset:2048\n"
"ds_read_b64_tr_b16 v[108:109], v3 offset:2112\n"
"ds_read_b64_tr_b16 v[110:111], v3 offset:2560\n"
"ds_read_b64_tr_b16 v[112:113], v3 offset:2624\n"
"ds_read_b64_tr_b16 v[114:115], v3 offset:3072\n"
"ds_read_b64_tr_b16 v[116:117], v3 offset:3136\n"
"ds_read_b64_tr_b16 v[118:119], v3 offset:3584\n"
"ds_read_b64_tr_b16 v[120:121], v3 offset:3648\n"
"v_lshlrev_b32 v104, 7, v102\n"
"v_lshl_add_u32 v104, v103, 4, v104\n"
"s_waitcnt lgkmcnt(0)\n"
"s_mov_b64 exec, s[16:17]\n"
"buffer_store_dwordx4 v[106:109], v104, s[52:55], s91 offen sc1\n"
"buffer_store_dwordx4 v[110:113], v104, s[52:55], s91 offen offset:512 sc1\n"
"buffer_store_dwordx4 v[114:117], v104, s[52:55], s91 offen offset:1024 sc1\n"
"buffer_store_dwordx4 v[118:121], v104, s[52:55], s91 offen offset:1536 sc1\n"
"s_mov_b64 exec, -1\n"
"Lnokm2n_%=:\n"
"Lnokmn_%=:\n"
"s_add_u32 s91, s91, 65536\n"
"s_cmp_eq_u32 s88, 0\n"
"s_cbranch_scc1 Lnorsn_%=\n"
"v_mov_b32 v106, -1\n"
"v_mov_b32 v107, -1\n"
"v_mov_b32 v108, -1\n"
"v_mov_b32 v109, -1\n"
"v_lshlrev_b32 v104, 14, v102\n"
"v_lshrrev_b32 v105, 3, v101\n"
"v_lshl_add_u32 v104, v105, 12, v104\n"
"v_lshl_add_u32 v104, v103, 4, v104\n"
"buffer_store_dwordx4 v[106:109], v104, s[40:43], s78 offen sc1\n"
"s_cmp_eq_u32 s35, 3\n"
"s_cbranch_scc0 Lnorsn_%=\n"
"s_mov_b64 exec, s[96:97]\n"
"buffer_store_dwordx4 v[106:109], v104, s[40:43], s79 offen sc1\n"
"s_mov_b64 exec, -1\n"
"Lnorsn_%=:\n"
"s_cmp_eq_u64 s[92:93], -1\n"
"s_cbranch_scc1 Lkrn_%=\n"
"v_lshrrev_b32 v118, 2, v0\n"
"v_add_u32 v119, 8192, v118\n"
"buffer_load_dword v118, v118, s[60:63], s25 offen sc1\n"
"buffer_load_dword v119, v119, s[60:63], s25 offen sc1\n"
"Lkrn_%=:\n"
"s_waitcnt vmcnt(0)\n"
"s_cmp_eq_u64 s[92:93], -1\n"
"s_cbranch_scc1 Lken_%=\n"
"v_cmp_eq_u32_e32 vcc, s14, v118\n"
"v_cmp_eq_u32_e64 s[100:101], s14, v119\n"
"s_nop 1\n"
"s_and_b64 vcc, vcc, s[100:101]\n"
"s_or_b64 s[92:93], s[92:93], vcc\n"
"Lken_%=:\n"
"ds_write_b128 v7, v[54:57] offset:15360\n"
"ds_write_b128 v7, v[58:61] offset:19456\n"
"ds_write_b128 v7, v[62:65] offset:23552\n"
"ds_write_b128 v7, v[66:69] offset:27648\n"
"ds_read_b128 v[78:81], v1\n"
"ds_read_b128 v[82:85], v1 offset:1024\n"
"ds_read_b128 v[86:89], v1 offset:10240\n"
"ds_read_b128 v[90:93], v1 offset:11264\n"
"ds_read_b128 v[94:97], v1 offset:20480\n"
"ds_read_b128 v[98:101], v1 offset:21504\n"
"s_add_u32 s88, s88, 1\n"
"s_add_u32 s83, s83, 1\n"
"s_cmp_eq_u32 s83, 3\n"
"s_cselect_b32 s83, 0, s83\n"
"s_lshl_b32 s94, s83, 16\n"
"s_add_u32 s72, s64, s94\n"
"s_add_u32 s73, s72, 4096\n"
"s_lshl_b32 s94, s83, 14\n"
"s_add_u32 s74, s65, s94\n"
"s_mul_i32 s94, s83, 589824\n"
"s_add_u32 s75, s66, s94\n"
"s_add_u32 s76, s75, 32768\n"
"s_add_u32 s77, s75, 65536\n"
"s_add_u32 s80, s69, s94\n"
"s_add_u32 s95, s83, 1\n"
"s_cmp_eq_u32 s95, 3\n"
"s_cselect_b32 s95, 0, s95\n"
"s_mul_i32 s94, s95, s71\n"
"s_add_u32 s81, s70, s94\n"
"s_add_u32 s95, s83, 2\n"
"s_cmp_ge_u32 s95, 3\n"
"s_cselect_b32 s94, 3, 0\n"
"s_sub_u32 s95, s95, s94\n"
"s_mul_i32 s94, s95, s71\n"
"s_add_u32 s82, s70, s94\n"
"s_lshl_b32 s94, s88, 3\n"
"s_add_u32 s94, s94, s26\n"
"s_cmp_eq_u32 s15, 1\n"
"s_cselect_b32 s82, s94, s82\n"
"s_mul_i32 s94, s95, 589824\n"
"s_add_u32 s78, s67, s94\n"
"s_add_u32 s79, s68, s94\n"
"v_mov_b64 v[54:55], 0\n"
"v_mov_b64 v[56:57], 0\n"
"v_mov_b64 v[58:59], 0\n"
"v_mov_b64 v[60:61], 0\n"
"v_mov_b64 v[62:63], 0\n"
"v_mov_b64 v[64:65], 0\n"
"v_mov_b64 v[66:67], 0\n"
"v_mov_b64 v[68:69], 0\n"
"v_mov_b64 v[70:71], 0\n"
"v_mov_b64 v[72:73], 0\n"
"v_mov_b64 v[74:75], 0\n"
"v_mov_b64 v[76:77], 0\n"
"s_waitcnt lgkmcnt(0)\n"
"s_barrier\n"
"s_cmp_eq_u32 s15, 1\n"
"s_cbranch_scc0 Lnef_%=\n"
"v_mov_b32 v124, s85\n"
"v_mov_b32 v125, s85\n"
"buffer_store_dwordx2 v[124:125], v127, s[40:43], s82 offen sc1\n"
"Lnef_%=:\n"
"s_cmp_eq_u32 s31, 0\n"
"s_cbranch_scc1 LnsA_%=\n"
"s_mov_b32 s94, s31\n"
"LslA_%=:\n"
"s_sleep 1\n"
"s_sub_u32 s94, s94, 1\n"
"s_cmp_lg_u32 s94, 0\n"
"s_cbranch_scc1 LslA_%=\n"
"LnsA_%=:\n"
"s_branch Lstep_%=\n"
"LretryA_%=:\n"
"s_cmp_lg_u32 s39, 0\n"
"s_cbranch_scc1 LokA_%=\n"
"s_nop 7\n"
"v_mov_b64 v[54:55], 0\n"
"v_mov_b64 v[56:57], 0\n"
"v_mov_b64 v[58:59], 0\n"
"v_mov_b64 v[60:61], 0\n"
"v_mov_b64 v[62:63], 0\n"
"v_mov_b64 v[64:65], 0\n"
"v_mov_b64 v[66:67], 0\n"
"v_mov_b64 v[68:69], 0\n"
"v_mov_b64 v[70:71], 0\n"
"v_mov_b64 v[72:73], 0\n"
"v_mov_b64 v[74:75], 0\n"
"v_mov_b64 v[76:77], 0\n"
"s_waitcnt vmcnt(0)\n"
"v_max3_u32 v102, v104, v105, v106\n"
"v_max3_u32 v103, v107, v108, v109\n"
"v_max3_u32 v102, v102, v110, v111\n"
"v_max3_u32 v103, v103, v112, v113\n"
"v_max3_u32 v102, v102, v114, v115\n"
"v_max_u32 v102, v102, v103\n"
"v_cmp_eq_u32_e32 vcc, -1, v102\n"
"s_nop 0\n"
"s_cbranch_vccnz LretryA2_%=\n"
"v_mov_b64 v[18:19], v[104:105]\n"
"v_mov_b64 v[20:21], v[106:107]\n"
"v_mov_b64 v[22:23], v[108:109]\n"
"v_mov_b64 v[24:25], v[110:111]\n"
"v_mov_b64 v[50:51], v[112:113]\n"
"v_mov_b64 v[52:53], v[114:115]\n"
"s_branch LblkA_%=\n"
"LretryA2_%=:\n"
"buffer_load_dwordx4 v[18:21], v0, s[40:43], s72 offen sc1\n"
"buffer_load_dwordx4 v[22:25], v0, s[40:43], s73 offen sc1\n"
"buffer_load_dwordx4 v[50:53], v11, s[40:43], s74 offen sc1\n"
"s_sleep 3\n"
"buffer_load_dwordx4 v[104:107], v0, s[40:43], s72 offen sc1\n"
"buffer_load_dwordx4 v[108:111], v0, s[40:43], s73 offen sc1\n"
"buffer_load_dwordx4 v[112:115], v11, s[40:43], s74 offen sc1\n"
"s_add_u32 s24, s24, 1\n"
"s_cmp_lt_u32 s24, 16384\n"
"s_cbranch_scc1 LblkA_%=\n"
"s_mov_b32 s39, 1\n"
"v_mov_b32 v124, 1000\n"
"v_add_u32 v124, s88, v124\n"
"v_mov_b32 v125, 16384\n"
"buffer_store_dword v124, v125, s[60:63], 0 offen sc1\n"
"s_branch LblkA_%=\n"
"Llast_%=:\n"
"v_permlane32_swap_b32 v70, v74\n"
"v_permlane32_swap_b32 v71, v75\n"
"v_permlane32_swap_b32 v72, v76\n"
"v_permlane32_swap_b32 v73, v77\n"
"v_add_f32 v70, v70, v74\n"
"v_add_f32 v71, v71, v75\n"
"v_add_f32 v72, v72, v76\n"
"v_add_f32 v73, v73, v77\n"
"v_cvt_pk_bf16_f32 v32, v70, v71\n"
"v_cvt_pk_bf16_f32 v33, v72, v73\n"
"s_nop 1\n"
"v_lshrrev_b32 v100, 4, v0\n"
"v_and_b32 v101, 15, v100\n"
"v_lshrrev_b32 v102, 4, v100\n"
"v_and_b32 v103, 7, v100\n"
"s_cmp_eq_u32 s37, 0\n"
"s_cbranch_scc0 Lnokml_%=\n"
"v_lshrrev_b32 v104, 5, v100\n"
"v_lshl_add_u32 v104, v104, 4, v101\n"
"v_lshlrev_b32 v104, 4, v104\n"
"v_and_b32 v105, 1, v102\n"
"v_lshl_add_u32 v104, v105, 3, v104\n"
"s_add_u32 s94, s91, s84\n"
"buffer_store_dwordx2 v[32:33], v104, s[56:59], s94 offen sc1\n"
"s_cmp_eq_u32 s35, 3\n"
"s_cbranch_scc0 Lnokm2l_%=\n"
"ds_read_b64_tr_b16 v[106:107], v3 offset:2048\n"
"ds_read_b64_tr_b16 v[108:109], v3 offset:2112\n"
"ds_read_b64_tr_b16 v[110:111], v3 offset:2560\n"
"ds_read_b64_tr_b16 v[112:113], v3 offset:2624\n"
"ds_read_b64_tr_b16 v[114:115], v3 offset:3072\n"
"ds_read_b64_tr_b16 v[116:117], v3 offset:3136\n"
"ds_read_b64_tr_b16 v[118:119], v3 offset:3584\n"
"ds_read_b64_tr_b16 v[120:121], v3 offset:3648\n"
"v_lshlrev_b32 v104, 7, v102\n"
"v_lshl_add_u32 v104, v103, 4, v104\n"
"s_waitcnt lgkmcnt(0)\n"
"s_mov_b64 exec, s[16:17]\n"
"buffer_store_dwordx4 v[106:109], v104, s[52:55], s91 offen sc1\n"
"buffer_store_dwordx4 v[110:113], v104, s[52:55], s91 offen offset:512 sc1\n"
"buffer_store_dwordx4 v[114:117], v104, s[52:55], s91 offen offset:1024 sc1\n"
"buffer_store_dwordx4 v[118:121], v104, s[52:55], s91 offen offset:1536 sc1\n"
"s_mov_b64 exec, -1\n"
"Lnokm2l_%=:\n"
"Lnokml_%=:\n"
"s_waitcnt vmcnt(0) lgkmcnt(0)\n"
"s_barrier\n"
"s_cmp_eq_u32 s37, 0\n"
"s_cbranch_scc0 Lend_%=\n"
"s_cmp_eq_u32 s35, 2\n"
"s_cbranch_scc0 Lend_%=\n"
"s_add_u32 s94, s26, 512\n"
"v_mov_b32 v104, 0\n"
"v_mov_b32 v105, s14\n"
"s_mov_b64 exec, 1\n"
"buffer_store_dword v105, v104, s[40:43], s94 offen sc1\n"
"s_mov_b64 exec, -1\n"
"Lend_%=:\n"
"s_waitcnt vmcnt(0) lgkmcnt(0)\n"
      "s_endpgm\n"
      :: "{s[2:3]}"(kp), "{s4}"(wave), "{s5}"(b) :
"v0","v1","v2","v3","v4","v5","v6","v7","v8","v9","v10","v11","v12","v13","v14","v15","v16","v17","v18","v19","v20","v21","v22","v23","v24","v25","v26","v27","v28","v29","v30","v31","v32","v33","v34","v35","v36","v37","v38","v39","v40","v41","v42","v43","v44","v45","v46","v47","v48","v49","v50","v51","v52","v53","v54","v55","v56","v57","v58","v59","v60","v61","v62","v63","v64","v65","v66","v67","v68","v69","v70","v71","v72","v73","v74","v75","v76","v77","v78","v79","v80","v81","v82","v83","v84","v85","v86","v87","v88","v89","v90","v91","v92","v93","v94","v95","v96","v97","v98","v99","v100","v101","v102","v103","v104","v105","v106","v107","v108","v109","v110","v111","v112","v113","v114","v115","v116","v117","v118","v119","v120","v121","v122","v123","v124","v125","v126","v128","v129","v130","v131","v132","v133","v134","v135","v136","v137","v138","v139","v140","v141","v142","v143","v144","v145","v146","v147","v148","v149","v150","v151","v152","v153","v154","v155","v156","v157","v158","v159","v160","v161","v162","v163","v164","v165","v166","v167","v168","v169","v170","v171","v172","v173","v174","v175","v176","v177","v178","v179","v180","v181","v182","v183","v184","v185","v186","v187","v188","v189","v190","v191","v192","v193","v194","v195","v196","v197","v198","v199","v200","v201","v202","v203","v204","v205","v206","v207","v208","v209","v210","v211","v212","v213","v214","v215","v216","v217","v218","v219","v220","v221","v222","v223","v224","v225","v226","v227","v228","v229","v230","v231","v232","v233","v234","v235","v236","v237","v238","v239","v240","v241","v242","v243","v244","v245","v246","v247","v248","v249","v250","v251","v252","v253","v254","v255","a0","a1","a2","a3","a4","a5","a6","a7","a8","a9","a10","a11","a12","a13","a14","a15","a16","a17","a18","a19","a20","a21","a22","a23","a24","a25","a26","a27","a28","a29","a30","a31","a32","a33","a34","a35","a36","a37","a38","a39","a40","a41","a42","a43","a44","a45","a46","a47","a48","a49","a50","a51","a52","a53","a54","a55","a56","a57","a58","a59","a60","a61","a62","a63","a64","a65","a66","a67","a68","a69","a70","a71","a72","a73","a74","a75","a76","a77","a78","a79","a80","a81","a82","a83","a84","a85","a86","a87","a88","a89","a90","a91","a92","a93","a94","a95","a96","a97","a98","a99","a100","a101","a102","a103","a104","a105","a106","a107","a108","a109","a110","a111","a112","a113","a114","a115","a116","a117","a118","a119","a120","a121","a122","a123","a124","a125","a126","a127","a128","a129","a130","a131","a132","a133","a134","a135","a136","a137","a138","a139","a140","a141","a142","a143","a144","a145","a146","a147","a148","a149","a150","a151","a152","a153","a154","a155","a156","a157","a158","a159","a160","a161","a162","a163","a164","a165","a166","a167","a168","a169","a170","a171","a172","a173","a174","a175","a176","a177","a178","a179","a180","a181","a182","a183","a184","a185","a186","a187","a188","a189","a190","a191","a192","a193","a194","a195","a196","a197","a198","a199","a200","a201","a202","a203","a204","a205","a206","a207","a208","a209","a210","a211","a212","a213","a214","a215","a216","a217","a218","a219","a220","a221","a222","a223","a224","a225","a226","a227","a228","a229","a230","a231","a232","a233","a234","a235","a236","a237","a238","a239","a240","a241","a242","a243","a244","a245","a246","a247","a248","a249","a250","a251","a252","a253","a254","a255","s8","s9","s10","s11","s12","s13","s14","s15","s16","s17","s18","s19","s20","s21","s22","s23","s24","s25","s26","s27","s28","s29","s30","s31","s32","s33","s34","s35","s36","s37","s38","s39","s40","s41","s42","s43","s44","s45","s46","s47","s48","s49","s50","s51","s52","s53","s54","s55","s56","s57","s58","s59","s60","s61","s62","s63","s64","s65","s66","s67","s68","s69","s70","s71","s72","s73","s74","s75","s76","s77","s78","s79","s80","s81","s82","s83","s84","s85","s86","s87","s88","s89","s90","s91","s92","s93","s94","s95","s96","s97","s98","s99","vcc","scc","m0","memory"
    );
    __builtin_unreachable();
  }
  Ctx c{mkr(kargs().flags,SZ_FLAGS),fresh_lane(),wave,false,true,0u};
  STAMP(c,0);
  if(xcd<4){
    const int gl=(idx-16)*4+xcd;
    if(gl>=36){
      c.tag=(unsigned)ufl((int)ld4c(c.fl,FL_EPOCH,0))+1u;
      window_fill(c,kargs(),gl-36); STAMP(c,3);
      if(gl>=48) numa_probe(c,kargs(),gl-48);
      STAMP(c,5);
    }
    if(gl>=36) for(int i=0;i<LATE1;i++) __builtin_amdgcn_s_sleep(32);
    ln1_segment(c,kargs(),gl); STAMP(c,1);
    if(gl<36) for(int i=0;i<LATE2;i++) __builtin_amdgcn_s_sleep(32);
    { ModRegs mr; const int t=wave*64+fresh_lane(); c.lane=fresh_lane();
      mod_load(kargs(),36+gl,t,mr); mod_finish(c,kargs(),36+gl,t,mr);
      if(gl<8){ mod_load(kargs(),100+gl,t,mr); mod_finish(c,kargs(),100+gl,t,mr); } }
    STAMP(c,4);
    if(wave==0){
      CArgs& a=kargs(); const int g=(idx-16)*4+xcd, lane=fresh_lane();
      __builtin_amdgcn_s_sleep(127);
      for(int it=0;it<31;it++){
        int i=(it*64+g)*64+lane; if(i>=124416) i=lane;
        const int m=i/41472, j=i-m*41472;
        const char* p=(const char*)((m==0)? a.wg : (m==1)? a.wu : a.wd)+(size_t)j*128;
        asm volatile("s_mov_b32 m0, 0\n\ts_nop 0\n\tglobal_load_lds_dword %0, off" :: "v"(p) : "memory");
        __builtin_amdgcn_s_sleep(WARMSL);
      }
      wait_vm0();
    }
  }
  else {
    const int j=idx, xq=xcd-4;
    if(j==6 || j==7){
      c.tag=(unsigned)ufl((int)ld4c(c.fl,FL_EPOCH,0))+1u;
      if(wave==0) *(LDSP volatile unsigned*)(uintptr_t)L_TAG=c.tag;
      bar_all();
    } else {
      int u,g,n0,n1;
      if(j==23 || j>=30){ u=-1; g=0; n0=0; n1=0; }
      else if(j<6){ u=j; g=0; n0=UA0; n1=UA1; }
      else if(j<14){ u=6+((j-8)>>1); g=(j-8)&1; n0=UB0; n1=UB1; }
      else if(j<20){ u=j-14; g=1; n0=UA0; n1=UA1; }
      else if(j<23){ u=6+(j-20); g=2; n0=UB0; n1=UB1; }
      else { u=j-24; g=2; n0=UA0; n1=UA1; }
      const bool isq=(u<0 || u==8);
      phase1(c,kargs(),isq? 0 : 1+(u>>2),xq*4+(u<0? ((j==30)? 0 : (j==31)? 1 : 2) : (u==8? 3 : (u&3))),g,n0,n1);
    }
    c.lane=fresh_lane();
    c.tag=(unsigned)ufl((int)*(LDSP volatile unsigned*)(uintptr_t)L_TAG);
    STAMP(c,2);
#define ROLEMASK 63
    if(j<6){ if(ROLEMASK&1) role_att(c,kargs(),xq,j*4+wave); }
    else if(j<8){ if(ROLEMASK&4) role_ln2(c,kargs(),xq*2+(j-6)); }
    else if(j>=30){ if(ROLEMASK&32) role_final(c,kargs(),xq*2+(j-30)); }
    else if(j<14){ if(ROLEMASK&2) role_wo(c,kargs(),2*xq+(j-8)/3,(j-8)%3); }
    else if(j<23){ if(ROLEMASK&8) role_f1(c,kargs(),xq*9+(j-14)); }
    else if(j>=24){ if(ROLEMASK&16) role_f2(c,kargs(),2*xq+(j-24)/3,(j-24)%3); }
  }
}

static void launch_all(void* const* d_in,void* d_out,void* d_ws,hipStream_t stream,int dA,int dB,unsigned long long* dbg){
  char* ws=(char*)(((uintptr_t)d_ws+4095)&~(uintptr_t)4095);
  Args a;
  a.Wq=(const u16*)d_in[2]; a.Wk=(const u16*)d_in[3]; a.Wv=(const u16*)d_in[4]; a.Wo=(const u16*)d_in[5];
  a.Kt=ws+O_K; a.Vt=ws+O_V; a.km=ws+O_KM; a.vm=ws+O_VM;
  a.win=(char*)(((uintptr_t)(ws+O_WIN)+(2u<<20)-1)&~(uintptr_t)((2u<<20)-1));
  a.flags=(unsigned*)(ws+O_FLAGS); a.dbg=dbg? dbg : (unsigned long long*)(ws+O_PROBE+2048);
  a.rsv=0; a.dA=dA; a.dB=dB&255; a.dC=dB>>8;
  a.ws=ws; a.x=(const u16*)d_in[0]; a.mem0=(const u16*)d_in[1]; a.wg=(const u16*)d_in[8]; a.wu=(const u16*)d_in[9]; a.wd=(const u16*)d_in[10];
  a.c=(const u16*)d_in[11]; a.wmod=(const u16*)d_in[12]; a.out=(u16*)d_out;
  (void)hipFuncSetAttribute((const void*)k_main,hipFuncAttributeMaxDynamicSharedMemorySize,CH_LDS);
  hipLaunchKernelGGL(k_main,dim3(256),dim3(256),CH_LDS,stream,a);
}

extern "C" void kernel_launch(void* const* d_in, const int* in_sizes, int n_in, void* d_out, int out_size, void* d_ws, size_t ws_size, hipStream_t stream){
  (void)in_sizes; (void)n_in; (void)out_size;
  if(ws_size<WS_NEED) return;
  launch_all(d_in,d_out,d_ws,stream,9,1285,nullptr);
}
```
